```python
import torch

def _softmax(x, axis=-1):
    return torch.softmax(x, dim=axis)

def _gelu(x):
    return torch.nn.functional.gelu(x, approximate='tanh')

def _silu(x):
    return torch.nn.functional.silu(x)

def _sigmoid(x):
    return torch.sigmoid(x)

def _layernorm(x, axis=-1, eps=1e-05):
    m = x.mean(dim=axis, keepdim=True)
    v = x.var(dim=axis, keepdim=True, unbiased=False)
    return (x - m) / torch.sqrt(v + eps)

def _scatter_add(tgt, idx, src, axis=0):
    return tgt.clone().index_add_(axis, idx, src)

def _segment_sum(src, seg, n):
    out = torch.zeros((n,) + src.shape[1:], dtype=src.dtype)
    return out.index_add_(0, seg, src)

def _pad(x, pw):
    flat = tuple((v for pair in reversed(pw) for v in pair))
    return torch.nn.functional.pad(x, flat)
kernel_name = 'adaLN_cond__softmax_full_mha__swiglu_ffn__prompt'

def setup_inputs():

    _w = lambda a: (lambda t: t.to(torch.bfloat16) if t.is_floating_point() else t)(torch.as_tensor(a))

    def _A_setup():
        B, S, D = (1, 2048, 512)
        return {'x': _w((lambda *s: torch.randn(*s, dtype=torch.bfloat16) * 0.05)(B, S, D)), 'wqkv': _w((lambda *s: torch.randn(*s, dtype=torch.bfloat16) * 0.05)(D, 3 * D)), 'wo': _w((lambda *s: torch.randn(*s, dtype=torch.bfloat16) * 0.05)(D, D))}
    _A_in = _A_setup()

    def _B_setup():
        B, S, D, F = (1, 2048, 512, 1792)
        return {'x': _w((lambda *s: torch.randn(*s, dtype=torch.bfloat16) * 0.05)(B, S, D)), 'w_gate': _w((lambda *s: torch.randn(*s, dtype=torch.bfloat16) * 0.05)(D, F)), 'w_up': _w((lambda *s: torch.randn(*s, dtype=torch.bfloat16) * 0.05)(D, F)), 'w_down': _w((lambda *s: torch.randn(*s, dtype=torch.bfloat16) * 0.05)(F, D))}
    _B_in = _B_setup()
    return {'A_x': _A_in['x'], 'A_wqkv': _A_in['wqkv'], 'A_wo': _A_in['wo'], 'B_x': _B_in['x'], 'B_w_gate': _B_in['w_gate'], 'B_w_up': _B_in['w_up'], 'B_w_down': _B_in['w_down'], 'c': _w((lambda *s: torch.randn(*s, dtype=torch.bfloat16) * 0.1)(1, 512) * 8.0), 'w_mod': _w((lambda *s: torch.randn(*s, dtype=torch.bfloat16) * 0.1)(512, 6 * 512) * 512 ** (-0.5) * 8.0)}

def reference(A_x, A_wqkv, A_wo, B_x, B_w_gate, B_w_up, B_w_down, c, w_mod):

    def _A_ref(x, wqkv, wo):
        B, S, D = x.shape
        H = 32
        Dh = D // H
        qkv = (lambda x, *d: x.permute(*d))((x @ wqkv).reshape(B, S, 3, H, Dh), 2, 0, 3, 1, 4)
        q, k, v = (qkv[0], qkv[1], qkv[2])
        scale = Dh ** (-0.5)
        scores = torch.einsum('bhqd,bhkd->bhqk', q, k) * scale
        p = _softmax(scores, axis=-1)
        o = torch.einsum('bhqk,bhkd->bhqd', p, v)
        return (lambda x, *d: x.permute(*d))(o, 0, 2, 1, 3).reshape(B, S, D) @ wo

    def _B_ref(x, w_gate, w_up, w_down):
        g = _silu(x @ w_gate)
        u = x @ w_up
        return g * u @ w_down
    x = A_x
    mod = (_silu(c) @ w_mod).reshape(c.shape[0], 6, 1, -1)
    sa, ca, ga, sb, cb, gb = (mod[:, 0].clone(), mod[:, 1].clone(), mod[:, 2].clone(), mod[:, 3].clone(), mod[:, 4].clone(), mod[:, 5].clone())
    h = _layernorm(x, axis=-1) * (1 + ca) + sa
    x = x + ga * _A_ref(h, A_wqkv, A_wo)
    h = _layernorm(x, axis=-1) * (1 + cb) + sb
    x = x + gb * _B_ref(h, B_w_gate, B_w_up, B_w_down)
    return x
```

```cpp
#include <hip/hip_runtime.h>
#include <hip/hip_bf16.h>

typedef unsigned short u16;
typedef unsigned int u32;
typedef u32 u32x2 __attribute__((ext_vector_type(2)));
typedef u32 u32x4 __attribute__((ext_vector_type(4)));
typedef float f32x2 __attribute__((ext_vector_type(2)));
typedef float f32x4 __attribute__((ext_vector_type(4)));
typedef float f32x8 __attribute__((ext_vector_type(8)));
typedef float f32x16 __attribute__((ext_vector_type(16)));
typedef __bf16 bf16x2 __attribute__((ext_vector_type(2)));
typedef __bf16 bf16x8 __attribute__((ext_vector_type(8)));
typedef __bf16 bf16x16 __attribute__((ext_vector_type(16)));
typedef short s16x4 __attribute__((ext_vector_type(4)));
typedef short s16x8 __attribute__((ext_vector_type(8)));
#define LAS __attribute__((address_space(3)))
#define DI __device__ __forceinline__

constexpr size_t WS_MODP = 0;
constexpr size_t WS_MODF = 0xC000;
constexpr size_t WS_ST1  = 0x10000;
constexpr size_t WS_QF   = 0x100000;
constexpr size_t WS_KF   = 0x300000;
constexpr size_t WS_VF   = 0x500000;
constexpr size_t WS_OF   = 0x800000;
constexpr size_t WS_X1   = 0xA00000;
constexpr size_t WS_ST2  = 0xC00000;
constexpr size_t WS_ACT  = 0xD00000;
constexpr int VF_HEAD = 64 * 4 * 272;

DI float bf_lo(u32 u) { return __uint_as_float(u << 16); }
DI float bf_hi(u32 u) { return __uint_as_float(u & 0xffff0000u); }
DI u32 pack2(float a, float b) { bf16x2 r = {(__bf16)a, (__bf16)b}; return __builtin_bit_cast(u32, r); }
DI float bfround(float a) { return (float)(__bf16)a; }
DI float shfl_idx(float v, int srcLane) { return __int_as_float(__builtin_amdgcn_ds_bpermute(srcLane << 2, __float_as_int(v))); }

DI u32x4 make_srd(const void* p, u32 bytes) {
  const size_t a = (size_t)p;
  u32x4 r = {(u32)a, (u32)(a >> 32) & 0xffffu, bytes, 0x20000u};
  return r;
}
#define GLDS(voff, srd, sb, GO, lb, LO)                                                                                   \
  do { u32 tmp_;                                                                                                          \
    asm volatile("s_add_u32 m0, %4, %6\n\ts_add_u32 %0, %3, %5\n\tbuffer_load_dwordx4 %1, %2, %0 offen lds"            \
                 : "=&s"(tmp_) : "v"(voff), "s"(srd), "s"(sb), "s"(lb), "i"(GO), "i"(LO) : "memory", "scc"); } while (0)
#define GLD(W, dst, voff, srd, sb, GO)                                                                                    \
  do { u32 tmp_;                                                                                                          \
    asm volatile("s_add_u32 %1, %4, %5\n\tbuffer_load_dword" W " %0, %2, %3, %1 offen"                                   \
                 : "=v"(dst), "=&s"(tmp_) : "v"(voff), "s"(srd), "s"(sb), "i"(GO) : "memory", "scc"); } while (0)
#define GLD128(dst, voff, srd, sb, GO) GLD("x4", dst, voff, srd, sb, GO)
#define GLD64(dst, voff, srd, sb, GO) GLD("x2", dst, voff, srd, sb, GO)
#define GLD32(dst, voff, srd, sb, GO) GLD("", dst, voff, srd, sb, GO)
constexpr u32 WS_BYTES = 0x1400000;
#define WAITV(N) asm volatile("s_waitcnt vmcnt(" #N ")" ::: "memory")
#define PIN(x) asm volatile("" : "+v"(x))
#define BAR() do { asm volatile("s_waitcnt lgkmcnt(0)" ::: "memory"); __builtin_amdgcn_s_barrier(); asm volatile("" ::: "memory"); } while (0)

DI bf16x8 lds_frag(const char* smem, u32 off) { return *(const LAS bf16x8*)((const LAS char*)smem + off); }
DI bf16x8 lds_tr2(const char* smem, u32 off0, u32 off1) {
  s16x4 a = __builtin_amdgcn_ds_read_tr16_b64_v4i16((LAS s16x4*)((LAS char*)smem + off0));
  s16x4 b = __builtin_amdgcn_ds_read_tr16_b64_v4i16((LAS s16x4*)((LAS char*)smem + off1));
  s16x8 r = __builtin_shufflevector(a, b, 0, 1, 2, 3, 4, 5, 6, 7);
  return __builtin_bit_cast(bf16x8, r);
}
DI u32 lds_base(const char* smem) { return (u32)(size_t)(const LAS char*)smem; }
#define MFMA32(a, b, c) __builtin_amdgcn_mfma_f32_32x32x16_bf16((a), (b), (c), 0, 0, 0)
#define MFMA16(a, b, c) __builtin_amdgcn_mfma_f32_16x16x32_bf16((a), (b), (c), 0, 0, 0)

__global__ __launch_bounds__(256) void k0_mod(const u16* __restrict__ c, const u16* __restrict__ wmod, const u16* __restrict__ x,
                                              float* __restrict__ modp, f32x2* __restrict__ st1) {
  __shared__ float red[4][64];
  const int b = blockIdx.x, t = threadIdx.x, w = t >> 6, l = t & 63;
  if (b < 192) {
    const int cb = b % 48, kq = b / 48, c8 = t & 7, r = t >> 3;
    u32x4 v[4]; float s[4];
#pragma unroll
    for (int i = 0; i < 4; ++i) {
      const int row = kq * 128 + i * 32 + r;
      v[i] = *(const u32x4*)(wmod + (size_t)row * 3072 + cb * 64 + c8 * 8);
      s[i] = __uint_as_float((u32)c[row] << 16);
    }
    float acc[8];
#pragma unroll
    for (int e = 0; e < 8; ++e) acc[e] = 0.f;
#pragma unroll
    for (int i = 0; i < 4; ++i) {
      const float sv = bfround(s[i] / (1.f + __expf(-s[i])));
#pragma unroll
      for (int e = 0; e < 4; ++e) { acc[2 * e] += sv * bf_lo(v[i][e]); acc[2 * e + 1] += sv * bf_hi(v[i][e]); }
    }
#pragma unroll
    for (int e = 0; e < 8; ++e) {
      acc[e] += __shfl_xor(acc[e], 8); acc[e] += __shfl_xor(acc[e], 16); acc[e] += __shfl_xor(acc[e], 32);
    }
    if (l < 8) {
#pragma unroll
      for (int e = 0; e < 8; ++e) red[w][l * 8 + e] = acc[e];
    }
    __syncthreads();
    if (t < 64) modp[kq * 3072 + cb * 64 + t] = (red[0][t] + red[1][t]) + (red[2][t] + red[3][t]);
  } else {
    const int row0 = (b - 192) * 8 + w * 2;
    u32x4 v[2];
#pragma unroll
    for (int i = 0; i < 2; ++i) v[i] = *(const u32x4*)(x + (size_t)(row0 + i) * 512 + l * 8);
#pragma unroll
    for (int i = 0; i < 2; ++i) {
      float f[8];
#pragma unroll
      for (int e = 0; e < 4; ++e) { f[2 * e] = bf_lo(v[i][e]); f[2 * e + 1] = bf_hi(v[i][e]); }
      float sum = 0.f;
#pragma unroll
      for (int e = 0; e < 8; ++e) sum += f[e];
#pragma unroll
      for (int o = 1; o < 64; o <<= 1) sum += __shfl_xor(sum, o);
      const float mean = sum * (1.f / 512.f);
      float sq = 0.f;
#pragma unroll
      for (int e = 0; e < 8; ++e) { const float d = f[e] - mean; sq += d * d; }
#pragma unroll
      for (int o = 1; o < 64; o <<= 1) sq += __shfl_xor(sq, o);
      if (l == 0) { f32x2 o2 = {mean, __builtin_amdgcn_rsqf(sq * (1.f / 512.f) + 1e-5f)}; st1[row0 + i] = o2; }
    }
  }
}

DI u32x4 modulate8(const u32x4 v, float rs, float mr, const float* c1, const float* sh) {
  u32x4 o;
#pragma unroll
  for (int e = 0; e < 4; ++e) {
    const float a = __builtin_fmaf(__builtin_fmaf(bf_lo(v[e]), rs, mr), c1[2 * e], sh[2 * e]);
    const float b = __builtin_fmaf(__builtin_fmaf(bf_hi(v[e]), rs, mr), c1[2 * e + 1], sh[2 * e + 1]);
    o[e] = pack2(a, b);
  }
  return o;
}

__global__ __launch_bounds__(512) void k1_qkv(const u16* __restrict__ x, const u16* __restrict__ wqkv, char* __restrict__ ws) {
  __shared__ __attribute__((aligned(16))) char smem[163840];
  constexpr u32 AOFF = 98304;
  const int bid = blockIdx.x, xcd = bid & 7, loc = bid >> 3;
  const int pm = (xcd >> 1) * 4 + (loc >> 3), pn = (xcd & 1) * 8 + (loc & 7);
  const int t = threadIdx.x, l = t & 63;
  const int w = __builtin_amdgcn_readfirstlane(t >> 6);
  const u32 lb = lds_base(smem);
  const u32x4 sws = make_srd(ws, WS_BYTES), sx = make_srd(x, 2048 * 1024), sw = make_srd(wqkv, 512 * 3072);
  const u32 zs = 0;

  float fin[4];
  {
    const u32 vo = (u32)t * 4, sb = (u32)(bid < 6 ? bid : 0) * 2048;
#pragma unroll
    for (int p = 0; p < 4; ++p) GLD32(fin[p], vo, sws, sb, WS_MODP + p * 12288);
  }
  float mp[2][2][4];
  {
    const u32 vo = (u32)(((t >> 6 & 3) * 64 + l) * 4);
#pragma unroll
    for (int hh = 0; hh < 2; ++hh)
#pragma unroll
      for (int m = 0; m < 2; ++m)
#pragma unroll
        for (int p = 0; p < 4; ++p) GLD32(mp[hh][m][p], vo, sws, zs, WS_MODP + p * 12288 + m * 2048 + hh * 1024);
  }
  u32x2 st[8];
  {
    const u32 vo = (u32)(((t >> 8) * 64 + (l & 7)) * 8);
#pragma unroll
    for (int i = 0; i < 8; ++i) GLD64(st[i], vo, sws, (u32)pm * 1024, WS_ST1 + i * 64);
  }
  u32x4 xa[2][8];
  const u32 xvo = (u32)(((t >> 8) * 64 + (l & 7)) * 1024 + ((t >> 6 & 3) * 64 + (l >> 3) * 8) * 2);
  const u32 wvo = (u32)((8 * (l >> 4) + (l >> 2 & 3)) * 3072 + ((((l >> 1) ^ (l >> 4)) & 1) * 16 + (l & 1) * 8) * 2);
  const u32 xsb = (u32)pm * 131072, wsb = (u32)(w * 32 * 3072 + pn * 64), wlb = lb + w * 2048;
#pragma unroll
  for (int hh = 0; hh < 2; ++hh) {
#pragma unroll
    for (int i = 0; i < 8; ++i) GLD128(xa[hh][i], xvo, sx, xsb, i * 8192 + hh * 512);
#pragma unroll
    for (int c = 0; c < 3; ++c)
#pragma unroll
      for (int e = 0; e < 2; ++e) GLDS(wvo, sw, wsb, (hh * 256 + 4 * e) * 3072 + c * 1024, wlb, c * 32768 + (hh * 16 + e) * 1024);
  }
  WAITV(36);
#pragma unroll
  for (int p = 0; p < 4; ++p) PIN(fin[p]);
  float c1[2][8], sh[2][8];
#pragma unroll
  for (int hh = 0; hh < 2; ++hh) {
#pragma unroll
    for (int p = 0; p < 4; ++p) { PIN(mp[hh][0][p]); PIN(mp[hh][1][p]); }
    const float s = bfround((mp[hh][0][0] + mp[hh][0][1]) + (mp[hh][0][2] + mp[hh][0][3]));
    const float cc = bfround(1.f + bfround((mp[hh][1][0] + mp[hh][1][1]) + (mp[hh][1][2] + mp[hh][1][3])));
#pragma unroll
    for (int e = 0; e < 8; ++e) { c1[hh][e] = shfl_idx(cc, (l & 56) + e); sh[hh][e] = shfl_idx(s, (l & 56) + e); }
  }
  if (bid < 6) ((float*)(ws + WS_MODF))[bid * 512 + t] = bfround((fin[0] + fin[1]) + (fin[2] + fin[3]));

  const u32 awr = AOFF + (u32)(((t >> 8) * 4 * 8 + (t >> 6 & 3) * 2 + (l >> 5)) * 1024 + ((l >> 3 & 3) * 16 + (l & 7)) * 16);
  const int wm = w >> 1, wn = w & 1;
  const u32 ard = AOFF + (u32)(wm * 16 * 1024 + l * 16);
  const u32 brd = (u32)((l >> 2) * 64 + ((wn ^ (l >> 4)) & 1) * 32 + (l & 3) * 8);
  f32x4 acc[2][3];
#pragma unroll
  for (int a = 0; a < 2; ++a)
#pragma unroll
    for (int c = 0; c < 3; ++c) acc[a][c] = (f32x4){0.f, 0.f, 0.f, 0.f};

  WAITV(20);
#pragma unroll
  for (int i = 0; i < 8; ++i) { PIN(st[i]); PIN(xa[0][i]); }
  float rs[8], mr[8];
#pragma unroll
  for (int i = 0; i < 8; ++i) { rs[i] = __uint_as_float(st[i][1]); mr[i] = -__uint_as_float(st[i][0]) * rs[i]; }
#pragma unroll
  for (int i = 0; i < 8; ++i)
    *(LAS u32x4*)((LAS char*)smem + awr + (i >> 1) * 8192 + (i & 1) * 128) = modulate8(xa[0][i], rs[i], mr[i], c1[0], sh[0]);
  WAITV(14);
  BAR();
#pragma unroll
  for (int hh = 0; hh < 2; ++hh) {
#pragma unroll
    for (int ks = 0; ks < 8; ++ks) {
      bf16x8 hf[2], wf[3];
#pragma unroll
      for (int a = 0; a < 2; ++a) hf[a] = lds_frag(smem, ard + (a * 8 + ks) * 1024);
#pragma unroll
      for (int c = 0; c < 3; ++c) {
        const u32 o = brd + c * 32768 + (hh * 256 + ks * 32) * 64;
        wf[c] = lds_tr2(smem, o, o + 1024);
      }
#pragma unroll
      for (int a = 0; a < 2; ++a) {
        acc[a][0] = MFMA16(wf[0], hf[a], acc[a][0]);
        acc[a][1] = MFMA16(wf[1], hf[a], acc[a][1]);
        acc[a][2] = MFMA16(hf[a], wf[2], acc[a][2]);
      }
    }
    if (hh == 0) {
      WAITV(6);
      u32x4 hv[8];
#pragma unroll
      for (int i = 0; i < 8; ++i) { PIN(xa[1][i]); hv[i] = modulate8(xa[1][i], rs[i], mr[i], c1[1], sh[1]); }
      BAR();
#pragma unroll
      for (int i = 0; i < 8; ++i) *(LAS u32x4*)((LAS char*)smem + awr + (i >> 1) * 8192 + (i & 1) * 128) = hv[i];
      WAITV(0);
      BAR();
    }
  }
  const int head = pn * 2 + wn, tile = pm * 4 + wm, g = l >> 4;
  constexpr float QS = 0.25f * 1.4426950408889634f;
#pragma unroll
  for (int a = 0; a < 2; ++a) {
    const size_t qo = ((size_t)(head * 64 + tile) * 64 + a * 16 + (l & 15) + 32 * (g >> 1)) * 16 + (g & 1) * 8;
    u32x2 q = {pack2(acc[a][0][0] * QS, acc[a][0][1] * QS), pack2(acc[a][0][2] * QS, acc[a][0][3] * QS)};
    u32x2 k = {pack2(acc[a][1][0], acc[a][1][1]), pack2(acc[a][1][2], acc[a][1][3])};
    u32x2 v = {pack2(acc[a][2][0], acc[a][2][1]), pack2(acc[a][2][2], acc[a][2][3])};
    *(u32x2*)(ws + WS_QF + qo) = q;
    *(u32x2*)(ws + WS_KF + qo) = k;
    const size_t vo = (size_t)head * VF_HEAD + ((tile * 2 + a) * 2 + (g >> 1)) * 272 + (g & 1) * 8;
    *(u32x2*)(ws + WS_VF + vo + (l & 15) * 16) = v;
    if ((l & 15) == 0) { u32x2 one = {0x3f803f80u, 0x3f803f80u}; *(u32x2*)(ws + WS_VF + vo + 256) = one; }
  }
}

__global__ __launch_bounds__(1024) void k2_attn(char* __restrict__ ws) {
  __shared__ __attribute__((aligned(16))) char smem[65536 + VF_HEAD + 8 * 64 * 40];
  constexpr u32 VOFF = 65536, COFF = 65536 + VF_HEAD;
  const int bid = blockIdx.x, xcd = bid & 7, loc = bid >> 3;
  const int head = xcd * 4 + (loc >> 3), qb = loc & 7;
  const int t = threadIdx.x, l = t & 63;
  const int w = __builtin_amdgcn_readfirstlane(t >> 6);
  const u32 lb = lds_base(smem);
  const u32x4 sws = make_srd(ws, WS_BYTES);
  const u32 ksb = (u32)(head * 65536 + w * 1024), vsb = (u32)(head * VF_HEAD + w * 1024), wlb = lb + w * 1024;
  const int qt = w & 7, kh = w >> 3;
  const u32 lo16 = (u32)l * 16;
  if (w < 4) GLDS(lo16, sws, vsb + w * 16384, WS_VF + 16384, wlb + w * 16384, VOFF + 16384);
  u32x4 qraw;
  GLD128(qraw, lo16, sws, (u32)(head * 64 + qb * 8 + qt) * 1024, WS_QF);
#pragma unroll
  for (int s = 0; s < 4; ++s) {
    GLDS(lo16, sws, ksb, WS_KF + s * 16384, wlb, s * 16384);
    GLDS(lo16, sws, vsb, WS_VF + s * 17408, wlb, VOFF + s * 17408);
  }
  const u32 krd = (u32)(kh * 8 * 1024) + lo16;
  const int r = l & 31;
  u32 vrd = VOFF + (u32)(kh * 8 * 1088 + (l >> 5) * 272 + (r == 16 ? 256 : (r & 15) * 16));
  PIN(vrd);
  WAITV(6);
  PIN(qraw);
  BAR();
  const bf16x8 qf = __builtin_bit_cast(bf16x8, qraw);
  f32x16 zero;
#pragma unroll
  for (int i = 0; i < 16; ++i) zero[i] = 0.f;
  float m;
  {
    f32x16 s0 = MFMA32(lds_frag(smem, krd), qf, zero);
    m = s0[0];
#pragma unroll
    for (int i = 1; i < 16; ++i) m = fmaxf(m, s0[i]);
    m = fmaxf(m, __shfl_xor(m, 32));
  }
  f32x16 o;
  float lsum;
  for (int attempt = 0;; ++attempt) {
    f32x16 negm;
#pragma unroll
    for (int i = 0; i < 16; ++i) negm[i] = -m;
    o = zero;
#pragma unroll 1
    for (int s = 0; s < 4; ++s) {
      if (s == 1 && attempt == 0) { WAITV(0); BAR(); }
      const u32 kb = krd + s * 16384, vb = vrd + s * 17408;
#pragma unroll
      for (int j = 0; j < 8; ++j) {
        f32x16 sv = MFMA32(lds_frag(smem, kb + j * 1024), qf, negm);
        const bf16x8 v0 = lds_frag(smem, vb + j * 1088), v1 = lds_frag(smem, vb + j * 1088 + 544);
#pragma unroll
        for (int i = 0; i < 16; ++i) sv[i] = __builtin_amdgcn_exp2f(sv[i]);
        const f32x8 p0 = __builtin_shufflevector(sv, sv, 0, 1, 2, 3, 4, 5, 6, 7);
        const f32x8 p1 = __builtin_shufflevector(sv, sv, 8, 9, 10, 11, 12, 13, 14, 15);
        o = __builtin_amdgcn_smfmac_f32_32x32x32_bf16(v0, __builtin_bit_cast(bf16x16, p0), o, 0xDDDD, 0, 0);
        o = __builtin_amdgcn_smfmac_f32_32x32x32_bf16(v1, __builtin_bit_cast(bf16x16, p1), o, 0xDDDD, 0, 0);
      }
    }
    lsum = shfl_idx(o[8], r);
    bool ok = lsum < 1e18f;
#pragma unroll
    for (int i = 0; i < 8; ++i) ok = ok && (fabsf(o[i]) < 3e38f);
    if (attempt == 1 || __all(ok)) break;
    asm volatile("" ::: "memory");
#pragma unroll 1
    for (int j = 0; j < 32; ++j) {
      f32x16 sv = MFMA32(lds_frag(smem, krd + (j >> 3) * 16384 + (j & 7) * 1024), qf, zero);
#pragma unroll
      for (int i = 0; i < 16; ++i) m = fmaxf(m, sv[i]);
    }
    m = fmaxf(m, __shfl_xor(m, 32));
  }
  LAS float* cb = (LAS float*)((LAS char*)smem + COFF) + qt * 640 + l;
  if (kh == 1) {
#pragma unroll
    for (int i = 0; i < 8; ++i) cb[i * 64] = o[i];
    cb[512] = lsum; cb[576] = m;
  }
  BAR();
  if (kh == 0) {
    const float m2 = cb[576], l2 = cb[512];
    const float mm = fmaxf(m, m2);
    const float f1 = __builtin_amdgcn_exp2f(m - mm), f2 = __builtin_amdgcn_exp2f(m2 - mm);
    const float inv = 1.f / (lsum * f1 + l2 * f2);
    const float g1 = f1 * inv, g2 = f2 * inv;
    u32x4 ov;
#pragma unroll
    for (int e = 0; e < 4; ++e)
      ov[e] = pack2(o[2 * e] * g1 + cb[(2 * e) * 64] * g2, o[2 * e + 1] * g1 + cb[(2 * e + 1) * 64] * g2);
    *(u32x4*)(ws + WS_OF + (size_t)((qb * 8 + qt) * 32 + head) * 1024 + lo16) = ov;
  }
}

__global__ __launch_bounds__(256) void k3_wo(const u16* __restrict__ x, const u16* __restrict__ wo, char* __restrict__ ws) {
  __shared__ __attribute__((aligned(16))) char smem[131072];
  const int bid = blockIdx.x, xcd = bid & 7, loc = bid >> 3;
  const int pm = xcd * 4 + (loc >> 3), pn = loc & 7;
  const int t = threadIdx.x, l = t & 63;
  const int w = __builtin_amdgcn_readfirstlane(t >> 6);
  const int tm = w & 1, tn = w >> 1, hb = l >> 5;
  const u32 lb = lds_base(smem);
  const u32 lo16 = (u32)l * 16;
  const u32 wvo = (u32)((l >> 3) * 1024 + ((((l >> 2) ^ (l >> 4)) & 1) * 32 + (l & 3) * 8) * 2);
  u32x2 xr[4]; u32x4 ga[4];
  const int token = pm * 64 + tm * 32 + (l & 31), n0 = pn * 64 + tn * 32;
  const u32x4 sws = make_srd(ws, WS_BYTES), sx = make_srd(x, 2048 * 1024), sw = make_srd(wo, 512 * 1024);
  const u32 zs = 0;
  const u32 olo = (u32)((w >> 1) * 32768 + (w & 1) * 4096);
  const u32 osb = (u32)pm * 65536 + olo, olb = lb + olo, wsb = (u32)(w * 32768 + pn * 128), wlb = lb + w * 4096;
#pragma unroll
  for (int s = 0; s < 4; ++s) {
#pragma unroll
    for (int j = 0; j < 4; ++j) {
      GLDS(lo16, sws, osb, WS_OF + (s * 8 + j) * 1024, olb, (s * 8 + j) * 1024);
      GLDS(wvo, sw, wsb, (s * 128 + j * 8) * 1024, wlb, 65536 + (s * 16 + j) * 1024);
    }
  }
  const u32 xvo = (u32)(token * 1024 + (n0 + hb * 4) * 2), gvo = (u32)((n0 + hb * 4) * 4);
#pragma unroll
  for (int mq = 0; mq < 4; ++mq) {
    GLD64(xr[mq], xvo, sx, zs, mq * 16);
    GLD128(ga[mq], gvo, sws, zs, WS_MODF + 1024 * 4 + mq * 32);
  }
  const u32 ord = (u32)(tm * 32768) + lo16;
  const u32 wrd = 65536 + (u32)((hb * 4 + (l >> 2 & 3)) * 128 + ((tn ^ (l >> 3)) & 1) * 64 + (l >> 4 & 1) * 32 + (l & 3) * 8);
  f32x16 acc;
#pragma unroll
  for (int i = 0; i < 16; ++i) acc[i] = 0.f;
#pragma unroll
  for (int s = 0; s < 4; ++s) {
    if (s == 0) WAITV(32); else if (s == 1) WAITV(24); else if (s == 2) WAITV(16); else WAITV(8);
    BAR();
#pragma unroll
    for (int j = 0; j < 8; ++j) {
      const int ks = s * 8 + j;
      const bf16x8 of = lds_frag(smem, ord + ks * 1024);
      const bf16x8 wf = lds_tr2(smem, wrd + ks * 2048, wrd + ks * 2048 + 1024);
      acc = MFMA32(wf, of, acc);
    }
  }
  WAITV(0);
  float v[16]; float sum = 0.f;
#pragma unroll
  for (int mq = 0; mq < 4; ++mq) {
    PIN(xr[mq]); PIN(ga[mq]);
    u32x2 o;
    o[0] = pack2(bf_lo(xr[mq][0]) + __uint_as_float(ga[mq][0]) * acc[4 * mq], bf_hi(xr[mq][0]) + __uint_as_float(ga[mq][1]) * acc[4 * mq + 1]);
    o[1] = pack2(bf_lo(xr[mq][1]) + __uint_as_float(ga[mq][2]) * acc[4 * mq + 2], bf_hi(xr[mq][1]) + __uint_as_float(ga[mq][3]) * acc[4 * mq + 3]);
    *(u32x2*)(ws + WS_X1 + (size_t)token * 1024 + (n0 + hb * 4 + mq * 8) * 2) = o;
    v[4 * mq] = bf_lo(o[0]); v[4 * mq + 1] = bf_hi(o[0]); v[4 * mq + 2] = bf_lo(o[1]); v[4 * mq + 3] = bf_hi(o[1]);
  }
#pragma unroll
  for (int i = 0; i < 16; ++i) sum += v[i];
  sum += __shfl_xor(sum, 32);
  const float mean = sum * (1.f / 32.f);
  float sq = 0.f;
#pragma unroll
  for (int i = 0; i < 16; ++i) { const float d = v[i] - mean; sq += d * d; }
  sq += __shfl_xor(sq, 32);
  if (l < 32) { f32x2 o2 = {mean, sq}; *(f32x2*)(ws + WS_ST2 + (size_t)token * 128 + (pn * 2 + tn) * 8) = o2; }
}

__global__ __launch_bounds__(512) void k4_gateup(const u16* __restrict__ wg, const u16* __restrict__ wu, char* __restrict__ ws) {
  __shared__ __attribute__((aligned(16))) char smem[3 * 49152 + 4096];
  constexpr u32 SLOT = 49152, TOFF = 3 * 49152;
  const int bid = blockIdx.x, xcd = bid & 7, loc = bid >> 3;
  const int pm = (xcd >> 1) * 4 + loc / 7, pn = (xcd & 1) * 7 + loc % 7;
  const int t = threadIdx.x, l = t & 63;
  const int w = __builtin_amdgcn_readfirstlane(t >> 6);
  const u32 lb = lds_base(smem);
  float tsb, tcb;
  const u32x4 sws = make_srd(ws, WS_BYTES), sg = make_srd(wg, 512 * 3584), su = make_srd(wu, 512 * 3584);
  const u32 zs = 0;
  GLD32(tsb, (u32)t * 4, sws, zs, WS_MODF + 1536 * 4);
  GLD32(tcb, (u32)t * 4, sws, zs, WS_MODF + 2048 * 4);
  u32x4 sp[2];
  const u32 rowl = (u32)((t >> 6) * 16 + (l & 7));
#pragma unroll
  for (int i = 0; i < 2; ++i) GLD128(sp[i], rowl * 128 + (l >> 3) * 16, sws, (u32)pm * 16384, WS_ST2 + i * 1024);
  u32x4 xa[8][2];
  const u32 xvo = rowl * 1024 + (l >> 3) * 16;
  const u32 gvo = (u32)((l >> 4) * 3584 + ((((l >> 1) & 7) ^ (2 * (l >> 4))) * 16 + (l & 1) * 8) * 2);
  const u32 uvo = (u32)((l >> 4) * 3584 + ((((l >> 1) & 7) ^ (2 * (l >> 4) + 1)) * 16 + (l & 1) * 8) * 2);
  const u32 xsb = (u32)pm * 131072, wsb = (u32)(w * 8 * 3584 + pn * 256), wlb = lb + w * 2048;
#define issueB(kc)                                                                              \
  _Pragma("unroll") for (int j = 0; j < 2; ++j) {                                               \
    GLDS(gvo, sg, wsb, ((kc) * 64 + j * 4) * 3584, wlb, ((kc) % 3) * SLOT + 16384 + j * 1024);  \
    GLDS(uvo, su, wsb, ((kc) * 64 + j * 4) * 3584, wlb, ((kc) % 3) * SLOT + 32768 + j * 1024);  \
  }
#pragma unroll
  for (int kc = 0; kc < 8; ++kc) {
#pragma unroll
    for (int i = 0; i < 2; ++i) GLD128(xa[kc][i], xvo, sws, xsb, WS_X1 + i * 8192 + kc * 128);
    if (kc < 3) { issueB(kc) }
  }
  WAITV(28);
  PIN(tsb); PIN(tcb); PIN(sp[0]); PIN(sp[1]);
  ((LAS float*)((LAS char*)smem + TOFF))[t] = bfround(1.f + tcb);
  ((LAS float*)((LAS char*)smem + TOFF + 2048))[t] = tsb;
  float rs[2], mr[2];
#pragma unroll
  for (int i = 0; i < 2; ++i) {
    const float m0 = __uint_as_float(sp[i][0]), m1 = __uint_as_float(sp[i][2]);
    float sm = m0 + m1;
    sm += __shfl_xor(sm, 8); sm += __shfl_xor(sm, 16); sm += __shfl_xor(sm, 32);
    const float mean = sm * (1.f / 16.f);
    float q = __uint_as_float(sp[i][1]) + __uint_as_float(sp[i][3]) + 32.f * ((m0 - mean) * (m0 - mean) + (m1 - mean) * (m1 - mean));
    q += __shfl_xor(q, 8); q += __shfl_xor(q, 16); q += __shfl_xor(q, 32);
    rs[i] = __builtin_amdgcn_rsqf(q * (1.f / 512.f) + 1e-5f);
    mr[i] = -mean * rs[i];
  }
  BAR();
  const u32 awr = (u32)(((t >> 7) * 4 + (l >> 4)) * 1024 + ((l >> 3 & 1) * 32 + (t >> 6 & 1) * 16 + (l & 7)) * 16);
  auto stageA = [&](int kc) {
    float c1[8], sh[8];
    const LAS f32x4* tc = (const LAS f32x4*)((LAS char*)smem + TOFF + (kc * 64 + (l >> 3) * 8) * 4);
    const f32x4 a0 = tc[0], a1 = tc[1], b0 = tc[128], b1 = tc[129];
#pragma unroll
    for (int e = 0; e < 4; ++e) { c1[e] = a0[e]; c1[4 + e] = a1[e]; sh[e] = b0[e]; sh[4 + e] = b1[e]; }
#pragma unroll
    for (int i = 0; i < 2; ++i) {
      PIN(xa[kc][i]);
      *(LAS u32x4*)((LAS char*)smem + (kc % 3) * SLOT + awr + i * 128) = modulate8(xa[kc][i], rs[i], mr[i], c1, sh);
    }
  };
  const int wm = w & 1, wn = w >> 1, hb = l >> 5, blk = l >> 4 & 1, q4 = l >> 2 & 3;
  const u32 ard = (u32)(wm * 8192 + l * 16);
  u32 wrd[2];
#pragma unroll
  for (int f = 0; f < 2; ++f) wrd[f] = (u32)(16384 + blk * 16384 + (hb * 8 + q4) * 256 + (((wn * 2 + f) ^ (2 * q4 + blk)) * 32) + (l & 3) * 8);
  f32x16 acc[2][2];
#pragma unroll
  for (int a = 0; a < 2; ++a)
#pragma unroll
    for (int f = 0; f < 2; ++f)
#pragma unroll
      for (int i = 0; i < 16; ++i) acc[a][f][i] = 0.f;
  WAITV(26);
  stageA(0);
  WAITV(22);
  BAR();
#pragma unroll
  for (int kc = 0; kc < 8; ++kc) {
    if (kc == 0) WAITV(20); else if (kc == 1) WAITV(18); else if (kc == 2) WAITV(16);
    if (kc < 7) stageA(kc + 1);
    const u32 sb = (kc % 3) * SLOT;
#pragma unroll
    for (int ks = 0; ks < 4; ++ks) {
      bf16x8 hf[2], wf[2];
#pragma unroll
      for (int a = 0; a < 2; ++a) hf[a] = lds_frag(smem, sb + ard + (a * 4 + ks) * 1024);
#pragma unroll
      for (int f = 0; f < 2; ++f) wf[f] = lds_tr2(smem, sb + wrd[f] + ks * 4096, sb + wrd[f] + ks * 4096 + 1024);
#pragma unroll
      for (int a = 0; a < 2; ++a)
#pragma unroll
        for (int f = 0; f < 2; ++f) acc[a][f] = MFMA32(wf[f], hf[a], acc[a][f]);
    }
    if (kc < 7) {
      if (kc == 0) WAITV(16); else if (kc == 1) WAITV(14); else if (kc < 6) WAITV(4); else WAITV(0);
      BAR();
      if (kc + 3 < 8) { issueB(kc + 3) }
    }
  }
#pragma unroll
  for (int a = 0; a < 2; ++a)
#pragma unroll
    for (int f = 0; f < 2; ++f) {
      float r[8];
#pragma unroll
      for (int i = 0; i < 8; ++i) {
        const float gte = acc[a][f][i];
        r[i] = gte * __builtin_amdgcn_rcpf(1.f + __builtin_amdgcn_exp2f(-1.4426950408889634f * gte)) * acc[a][f][8 + i];
      }
      u32x4 o = {pack2(r[0], r[1]), pack2(r[2], r[3]), pack2(r[4], r[5]), pack2(r[6], r[7])};
      *(u32x4*)(ws + WS_ACT + (size_t)((pm * 4 + wm * 2 + a) * 112 + pn * 8 + wn * 2 + f) * 1024 + l * 16) = o;
    }
}

__global__ __launch_bounds__(256) void k5_down(const u16* __restrict__ wd, char* __restrict__ ws, u16* __restrict__ out) {
  __shared__ __attribute__((aligned(16))) char smem[131072];
  const int bid = blockIdx.x, xcd = bid & 7, loc = bid >> 3;
  const int pm = (xcd >> 1) * 8 + (loc >> 2), pn = (xcd & 1) * 4 + (loc & 3);
  const int t = threadIdx.x, l = t & 63;
  const int w = __builtin_amdgcn_readfirstlane(t >> 6);
  const int tm = w & 1, tn = w >> 1, hb = l >> 5;
  const u32 lb = lds_base(smem);
  const u32 lo16 = (u32)l * 16;
  const u32 wvo = (u32)((l >> 3) * 1024 + ((((l >> 2) ^ (l >> 4)) & 1) * 32 + (l & 3) * 8) * 2);
  const int token = pm * 64 + tm * 32 + (l & 31), n0 = pn * 64 + tn * 32;
  u32x2 xr[4]; u32x4 gb[4];
  const u32x4 sws = make_srd(ws, WS_BYTES), sw = make_srd(wd, 1792 * 1024);
  const u32 zs = 0;
  const u32 xvo = (u32)(token * 1024 + (n0 + hb * 4) * 2), gvo = (u32)((n0 + hb * 4) * 4);
#pragma unroll
  for (int mq = 0; mq < 4; ++mq) {
    GLD64(xr[mq], xvo, sws, zs, WS_X1 + mq * 16);
    GLD128(gb[mq], gvo, sws, zs, WS_MODF + 2560 * 4 + mq * 32);
  }
  const u32 ag = (u32)(((pm * 2 + (w >> 1)) * 112 + (w & 1) * 4) * 1024);
  const u32 bg = (u32)(w * 32768 + pn * 128);
  auto issue = [&](int kc) {
    const u32 sl = lb + (kc & 3) * 32768 + w * 4096, as = ag + kc * 8192, bs = bg + kc * 131072;
#pragma unroll
    for (int j = 0; j < 4; ++j) {
      GLDS(lo16, sws, as, WS_ACT + j * 1024, sl, j * 1024);
      GLDS(wvo, sw, bs, j * 8192, sl, 16384 + j * 1024);
    }
  };
  issue(0); issue(1); issue(2);
  const u32 ord = (u32)(tm * 8192) + lo16;
  const u32 wrd = 16384 + (u32)((hb * 4 + (l >> 2 & 3)) * 128 + ((tn ^ (l >> 3)) & 1) * 64 + (l >> 4 & 1) * 32 + (l & 3) * 8);
  f32x16 acc;
#pragma unroll
  for (int i = 0; i < 16; ++i) acc[i] = 0.f;
  auto comp = [&](int kc) {
    const u32 sb = (kc & 3) * 32768;
#pragma unroll
    for (int j = 0; j < 8; ++j) {
      const bf16x8 of = lds_frag(smem, sb + ord + j * 1024);
      const bf16x8 wf = lds_tr2(smem, sb + wrd + j * 2048, sb + wrd + j * 2048 + 1024);
      acc = MFMA32(wf, of, acc);
    }
  };
  for (int kc = 0; kc < 11; ++kc) {
    WAITV(16); BAR();
    issue(kc + 3);
    comp(kc);
  }
  WAITV(16); BAR(); comp(11);
  WAITV(8); BAR(); comp(12);
  WAITV(0); BAR(); comp(13);
#pragma unroll
  for (int mq = 0; mq < 4; ++mq) {
    PIN(xr[mq]); PIN(gb[mq]);
    u32x2 o;
    o[0] = pack2(bf_lo(xr[mq][0]) + __uint_as_float(gb[mq][0]) * acc[4 * mq], bf_hi(xr[mq][0]) + __uint_as_float(gb[mq][1]) * acc[4 * mq + 1]);
    o[1] = pack2(bf_lo(xr[mq][1]) + __uint_as_float(gb[mq][2]) * acc[4 * mq + 2], bf_hi(xr[mq][1]) + __uint_as_float(gb[mq][3]) * acc[4 * mq + 3]);
    *(u32x2*)((char*)out + (size_t)token * 1024 + (n0 + hb * 4 + mq * 8) * 2) = o;
  }
}

extern "C" void kernel_launch(void* const* d_in, const int* in_sizes, int n_in, void* d_out, int out_size, void* d_ws, size_t ws_size,
                              hipStream_t stream) {
  const u16* x = (const u16*)d_in[0];
  const u16* wqkv = (const u16*)d_in[1];
  const u16* wo = (const u16*)d_in[2];
  const u16* wg = (const u16*)d_in[4];
  const u16* wu = (const u16*)d_in[5];
  const u16* wd = (const u16*)d_in[6];
  const u16* c = (const u16*)d_in[7];
  const u16* wmod = (const u16*)d_in[8];
  char* ws = (char*)d_ws;
  k0_mod<<<448, 256, 0, stream>>>(c, wmod, x, (float*)(ws + WS_MODP), (f32x2*)(ws + WS_ST1));
  k1_qkv<<<256, 512, 0, stream>>>(x, wqkv, ws);
  k2_attn<<<256, 1024, 0, stream>>>(ws);
  k3_wo<<<256, 256, 0, stream>>>(x, wo, ws);
  k4_gateup<<<224, 512, 0, stream>>>(wg, wu, ws);
  k5_down<<<256, 256, 0, stream>>>(wd, ws, (u16*)d_out);
}
```

```cpp
#include <hip/hip_runtime.h>
#include <hip/hip_bf16.h>

typedef unsigned short u16;
typedef unsigned int u32;
typedef u32 u32x2 __attribute__((ext_vector_type(2)));
typedef u32 u32x4 __attribute__((ext_vector_type(4)));
typedef float f32x2 __attribute__((ext_vector_type(2)));
typedef float f32x4 __attribute__((ext_vector_type(4)));
typedef float f32x8 __attribute__((ext_vector_type(8)));
typedef float f32x16 __attribute__((ext_vector_type(16)));
typedef __bf16 bf16x2 __attribute__((ext_vector_type(2)));
typedef __bf16 bf16x8 __attribute__((ext_vector_type(8)));
typedef __bf16 bf16x16 __attribute__((ext_vector_type(16)));
typedef short s16x4 __attribute__((ext_vector_type(4)));
typedef short s16x8 __attribute__((ext_vector_type(8)));
#define PRIO_MODE 1
#define NO_WSTEP 0
#define LAS __attribute__((address_space(3)))
#define DI __device__ __forceinline__

constexpr u32 WS_MODP = 0;
constexpr u32 WS_MODF = 0xC000;
constexpr u32 WS_ST1  = 0x10000;
constexpr u32 WS_FLG  = 0x20000;
constexpr u32 WS_QF   = 0x100000;
constexpr u32 WS_KV   = 0x300000;
constexpr u32 WS_OF   = 0x800000;
constexpr u32 WS_X1   = 0xA00000;
constexpr u32 WS_Y    = 0xC00000;
constexpr u32 WS_ST2  = 0xE00000;
constexpr u32 WS_ACT  = 0xF00000;
constexpr u32 WS_BYTES = 0x1600000;
constexpr int KV_TILE = 2112, KV_HEAD = 64 * KV_TILE;

template <int I> struct IC { static constexpr int value = I; };
template <int B, int E, class F> DI void static_for(F&& f) { if constexpr (B < E) { f(IC<B>{}); static_for<B + 1, E>(f); } }
#define SFOR(n, B, E) static_for<B, E>([&](auto ic_) __attribute__((always_inline)) { constexpr int n = decltype(ic_)::value;
#define SEND });

DI float bf_lo(u32 u) { return __uint_as_float(u << 16); }
DI float bf_hi(u32 u) { return __uint_as_float(u & 0xffff0000u); }
DI u32 pack2(float a, float b) { bf16x2 r = {(__bf16)a, (__bf16)b}; return __builtin_bit_cast(u32, r); }
DI float bfround(float a) { return (float)(__bf16)a; }
DI float xor32_sum(float v) {
  const auto r = __builtin_amdgcn_permlane32_swap(__float_as_uint(v), __float_as_uint(v), false, false);
  return __uint_as_float(r[0]) + __uint_as_float(r[1]);
}
DI float dpp_xor1(float v) { return __int_as_float(__builtin_amdgcn_mov_dpp(__float_as_int(v), 0xB1, 0xF, 0xF, true)); }
DI float dpp_xor2(float v) { return __int_as_float(__builtin_amdgcn_mov_dpp(__float_as_int(v), 0x4E, 0xF, 0xF, true)); }
DI float shfl_idx(float v, int srcLane) { return __int_as_float(__builtin_amdgcn_ds_bpermute(srcLane << 2, __float_as_int(v))); }

DI u32x4 make_srd(const void* p, u32 bytes) {
  const size_t a = (size_t)p;
  u32x4 r = {(u32)a, (u32)(a >> 32) & 0xffffu, bytes, 0x20000u};
  return r;
}
#define GLDS(voff, srd, sb, GO, lb, LO)                                                                                   \
  do { u32 tmp_;                                                                                                          \
    asm volatile("s_add_u32 m0, %4, %6\n\ts_add_u32 %0, %3, %5\n\tbuffer_load_dwordx4 %1, %2, %0 offen lds"            \
                 : "=&s"(tmp_) : "v"(voff), "s"(srd), "s"(sb), "s"(lb), "i"(GO), "i"(LO) : "memory", "scc"); } while (0)
#define GLD(W, dst, voff, srd, sb, GO)                                                                                    \
  do { u32 tmp_;                                                                                                          \
    asm volatile("s_add_u32 %1, %4, %5\n\tbuffer_load_dword" W " %0, %2, %3, %1 offen"                                   \
                 : "=v"(dst), "=&s"(tmp_) : "v"(voff), "s"(srd), "s"(sb), "i"(GO) : "memory", "scc"); } while (0)
#define GLD128(dst, voff, srd, sb, GO) GLD("x4", dst, voff, srd, sb, GO)
#define GLD64(dst, voff, srd, sb, GO) GLD("x2", dst, voff, srd, sb, GO)
#define GLD32(dst, voff, srd, sb, GO) GLD("", dst, voff, srd, sb, GO)
#define WAITV(N) asm volatile("s_waitcnt vmcnt(" #N ")" ::: "memory")
#define PIN(x) asm volatile("" : "+v"(x))
#define BAR() do { asm volatile("s_waitcnt lgkmcnt(0)" ::: "memory"); __builtin_amdgcn_s_barrier(); asm volatile("" ::: "memory"); } while (0)

#define ST_DECL
#define STAMP(i)
#define ST_OUT(kid, wsp)

DI bf16x8 lds_frag(const char* smem, u32 off) { return *(const LAS bf16x8*)((const LAS char*)smem + off); }
DI bf16x8 lds_tr2(const char* smem, u32 off0, u32 off1) {
  s16x4 a = __builtin_amdgcn_ds_read_tr16_b64_v4i16((LAS s16x4*)((LAS char*)smem + off0));
  s16x4 b = __builtin_amdgcn_ds_read_tr16_b64_v4i16((LAS s16x4*)((LAS char*)smem + off1));
  s16x8 r = __builtin_shufflevector(a, b, 0, 1, 2, 3, 4, 5, 6, 7);
  return __builtin_bit_cast(bf16x8, r);
}
DI u32 lds_base(const char* smem) { return (u32)(size_t)(const LAS char*)smem; }
#define SB() __builtin_amdgcn_sched_barrier(0)
#define RAWBAR() do { asm volatile("" ::: "memory"); __builtin_amdgcn_s_barrier(); asm volatile("" ::: "memory"); } while (0)
#define MFMA32(a, b, c) __builtin_amdgcn_mfma_f32_32x32x16_bf16((a), (b), (c), 0, 0, 0)
#define MFMA16(a, b, c) __builtin_amdgcn_mfma_f32_16x16x32_bf16((a), (b), (c), 0, 0, 0)

__global__ __launch_bounds__(256) void k0_mod(const u16* __restrict__ c, const u16* __restrict__ wmod, const u16* __restrict__ x,
                                              float* __restrict__ modp, f32x2* __restrict__ st1) {
  __shared__ float red[4][64];
  const int b = blockIdx.x, t = threadIdx.x, w = t >> 6, l = t & 63;
  if (b < 192) {
    const int cb = b % 48, kq = b / 48, c8 = t & 7, r = t >> 3;
    u32x4 v[4]; float s[4];
#pragma unroll
    for (int i = 0; i < 4; ++i) {
      const int row = kq * 128 + i * 32 + r;
      v[i] = *(const u32x4*)(wmod + (size_t)row * 3072 + cb * 64 + c8 * 8);
      s[i] = __uint_as_float((u32)c[row] << 16);
    }
    float acc[8];
#pragma unroll
    for (int e = 0; e < 8; ++e) acc[e] = 0.f;
#pragma unroll
    for (int i = 0; i < 4; ++i) {
      const float sv = bfround(s[i] / (1.f + __expf(-s[i])));
#pragma unroll
      for (int e = 0; e < 4; ++e) { acc[2 * e] += sv * bf_lo(v[i][e]); acc[2 * e + 1] += sv * bf_hi(v[i][e]); }
    }
#pragma unroll
    for (int e = 0; e < 8; ++e) {
      acc[e] += __shfl_xor(acc[e], 8); acc[e] += __shfl_xor(acc[e], 16); acc[e] += __shfl_xor(acc[e], 32);
    }
    if (l < 8) {
#pragma unroll
      for (int e = 0; e < 8; ++e) red[w][l * 8 + e] = acc[e];
    }
    __syncthreads();
    if (t < 64) modp[(cb * 64 + t) * 4 + kq] = (red[0][t] + red[1][t]) + (red[2][t] + red[3][t]);
  } else {
    const int row0 = (b - 192) * 8 + w * 2;
    u32x4 v[2];
#pragma unroll
    for (int i = 0; i < 2; ++i) v[i] = *(const u32x4*)(x + (size_t)(row0 + i) * 512 + l * 8);
#pragma unroll
    for (int i = 0; i < 2; ++i) {
      float f[8];
#pragma unroll
      for (int e = 0; e < 4; ++e) { f[2 * e] = bf_lo(v[i][e]); f[2 * e + 1] = bf_hi(v[i][e]); }
      float sum = 0.f;
#pragma unroll
      for (int e = 0; e < 8; ++e) sum += f[e];
#pragma unroll
      for (int o = 1; o < 64; o <<= 1) sum += __shfl_xor(sum, o);
      const float mean = sum * (1.f / 512.f);
      float sq = 0.f;
#pragma unroll
      for (int e = 0; e < 8; ++e) { const float d = f[e] - mean; sq += d * d; }
#pragma unroll
      for (int o = 1; o < 64; o <<= 1) sq += __shfl_xor(sq, o);
      if (l == 0) { f32x2 o2 = {mean, __builtin_amdgcn_rsqf(sq * (1.f / 512.f) + 1e-5f)}; st1[row0 + i] = o2; }
    }
  }
}

DI u32x4 modulate8(const u32x4 v, float rs, float mr, const float* c1, const float* sh) {
  u32x4 o;
#pragma unroll
  for (int e = 0; e < 4; ++e) {
    const float a = __builtin_fmaf(__builtin_fmaf(bf_lo(v[e]), rs, mr), c1[2 * e], sh[2 * e]);
    const float b = __builtin_fmaf(__builtin_fmaf(bf_hi(v[e]), rs, mr), c1[2 * e + 1], sh[2 * e + 1]);
    o[e] = pack2(a, b);
  }
  return o;
}

__global__ __launch_bounds__(512) void k1_qkv(const u16* __restrict__ x, const u16* __restrict__ wqkv, const float* __restrict__ st1,
                                              char* __restrict__ ws) {
  __shared__ __attribute__((aligned(16))) char smem[163840];
  constexpr u32 WOFF = 65536, WSLOT = 24576;
  ST_DECL STAMP(0);
  const int bid = blockIdx.x, xcd = bid & 7, loc = bid >> 3;
  const int pm = (xcd >> 1) * 8 + (loc >> 2), cg = (xcd & 1) * 4 + (loc & 3);
  const int t = threadIdx.x, l = t & 63;
  const int w = __builtin_amdgcn_readfirstlane(t >> 6);
  const u32 lb = lds_base(smem);
  const u32x4 sws = make_srd(ws, WS_BYTES), sx = make_srd(x, 2048 * 1024), sw = make_srd(wqkv, 512 * 3072);
  const u32 zs = 0;
  u32x4 fin, mp[2];
  const u32 t16 = (u32)t * 16;
  GLD128(mp[0], t16, sws, zs, WS_MODP);
  GLD128(mp[1], t16, sws, zs, WS_MODP + 8192);
  GLD128(fin, t16, sws, (u32)(bid < 6 ? bid : 0) * 8192, WS_MODP);
  u32x4 xa[8];
  const u32 lo16 = (u32)l * 16;
  const u32 xsb = (u32)(pm * 64 + w * 8) * 1024;
#pragma unroll
  for (int i = 0; i < 8; ++i) GLD128(xa[i], lo16, sx, xsb, i * 1024);
  const u32 wvo = (u32)((8 * (l >> 5) + (l >> 3 & 3)) * 3072 + ((((l >> 1) ^ (l >> 4)) & 3) * 16 + (l & 1) * 8) * 2);
  const u32 wsb = (u32)((32 * (w >> 2) + 16 * (w & 1) + 4 * (w >> 1 & 1)) * 3072 + cg * 128), wlb = lb + w * 1024;
#define K1_ISSUE(kc) _Pragma("unroll") for (int c = 0; c < 3; ++c) GLDS(wvo, sw, wsb, (kc) * 64 * 3072 + c * 1024, wlb, WOFF + ((kc) & 3) * WSLOT + c * 8192)
  K1_ISSUE(0);
  float rs[8], mr[8];
#pragma unroll
  for (int i = 0; i < 8; ++i) { rs[i] = st1[(pm * 64 + w * 8 + i) * 2 + 1]; mr[i] = -st1[(pm * 64 + w * 8 + i) * 2] * rs[i]; }
  STAMP(1);
  WAITV(11);
  PIN(fin); PIN(mp[0]); PIN(mp[1]);
#define SUM4(v) ((__uint_as_float(v[0]) + __uint_as_float(v[1])) + (__uint_as_float(v[2]) + __uint_as_float(v[3])))
  ((LAS float*)(LAS char*)smem)[t] = bfround(1.f + bfround(SUM4(mp[1])));
  ((LAS float*)(LAS char*)smem)[512 + t] = bfround(SUM4(mp[0]));
  {
    float f = bfround(SUM4(fin));
    if (bid == 4) f = bfround(1.f + f);
    if (bid < 6) ((float*)(ws + WS_MODF))[bid * 512 + t] = f;
    if (t < 384) ((u32*)(ws + WS_FLG))[bid * 384 + t] = 0u;
  }
  BAR();
  float c1[8], sh[8];
  {
    const LAS f32x4* tc = (const LAS f32x4*)((LAS char*)smem + l * 32);
    const f32x4 a0 = tc[0], a1 = tc[1], b0 = tc[128], b1 = tc[129];
#pragma unroll
    for (int e = 0; e < 4; ++e) { c1[e] = a0[e]; c1[4 + e] = a1[e]; sh[e] = b0[e]; sh[4 + e] = b1[e]; }
  }
  K1_ISSUE(1);
  WAITV(6);
  BAR();
  STAMP(2);
  const u32 awr = (u32)(((w >> 1) * 16 + (l >> 2)) * 1024 + (l & 3) * 256 + (w & 1) * 128);
#pragma unroll
  for (int i = 0; i < 8; ++i) {
    PIN(xa[i]);
    *(LAS u32x4*)((LAS char*)smem + awr + ((i ^ (l & 7)) << 4)) = modulate8(xa[i], rs[i], mr[i], c1, sh);
    if (i == 1) { K1_ISSUE(2); }
    if (i == 4) { K1_ISSUE(3); }
  }
  const int tm = w & 1, hd = w >> 1, g = l >> 4;
  u32 ard[2];
#pragma unroll
  for (int e = 0; e < 2; ++e) ard[e] = (u32)(tm * 32768 + e * 1024 + g * 256 + (((l & 15) ^ (e * 4 + g)) << 4));
  u32 brd = WOFF + (u32)((l >> 2) * 128 + ((hd ^ (l >> 3 & 3)) * 32) + (l & 3) * 8);
  PIN(brd);
  f32x4 acc[2][3];
#pragma unroll
  for (int a = 0; a < 2; ++a)
#pragma unroll
    for (int c = 0; c < 3; ++c) acc[a][c] = (f32x4){0.f, 0.f, 0.f, 0.f};
  WAITV(9);
  BAR();
  STAMP(3);
#pragma unroll
  for (int kc = 0; kc < 8; ++kc) {
    bf16x8 hf[2][2], wf[2][3];
#pragma unroll
    for (int e = 0; e < 2; ++e) {
#pragma unroll
      for (int a = 0; a < 2; ++a) hf[e][a] = lds_frag(smem, ard[e] + (a * 16 + kc * 2) * 1024);
#pragma unroll
      for (int c = 0; c < 3; ++c) {
        const u32 o = brd + (kc & 3) * WSLOT + c * 8192 + e * 4096;
        wf[e][c] = lds_tr2(smem, o, o + 2048);
      }
    }
#pragma unroll
    for (int e = 0; e < 2; ++e)
#pragma unroll
      for (int a = 0; a < 2; ++a) {
        acc[a][0] = MFMA16(wf[e][0], hf[e][a], acc[a][0]);
        acc[a][1] = MFMA16(wf[e][1], hf[e][a], acc[a][1]);
        acc[a][2] = MFMA16(hf[e][a], wf[e][2], acc[a][2]);
      }
    if (kc < 7) {
      if (kc < 5) WAITV(6); else if (kc == 5) WAITV(3); else WAITV(0);
      BAR();
      if (kc < 4) { K1_ISSUE(kc + 4); }
    }
    if (kc == 3) STAMP(4);
  }
  STAMP(5);
  const int head = cg * 4 + hd, tile = pm * 2 + tm;
  constexpr float QS = 0.25f * 1.4426950408889634f;
  char* kv = ws + WS_KV + (size_t)head * KV_HEAD + tile * KV_TILE;
#pragma unroll
  for (int a = 0; a < 2; ++a) {
    const u32 fo = (u32)((a * 16 + (l & 15) + 32 * (g >> 1)) * 16 + (g & 1) * 8);
    u32x2 q = {pack2(acc[a][0][0] * QS, acc[a][0][1] * QS), pack2(acc[a][0][2] * QS, acc[a][0][3] * QS)};
    u32x2 k = {pack2(acc[a][1][0], acc[a][1][1]), pack2(acc[a][1][2], acc[a][1][3])};
    u32x2 v = {pack2(acc[a][2][0], acc[a][2][1]), pack2(acc[a][2][2], acc[a][2][3])};
    *(u32x2*)(ws + WS_QF + (size_t)(head * 64 + tile) * 1024 + fo) = q;
    *(u32x2*)(kv + fo) = k;
    const u32 vo = (u32)(1024 + (a * 2 + (g >> 1)) * 272 + (g & 1) * 8);
    *(u32x2*)(kv + vo + (l & 15) * 16) = v;
    if ((l & 15) == 0) { u32x2 one = {0x3f803f80u, 0x3f803f80u}; *(u32x2*)(kv + vo + 256) = one; }
  }
  STAMP(6);
  ST_OUT(1, ws);
}

#define GLDS_SC1(voff, srd, sb, GO, lb, LO)                                                                               \
  do { u32 tmp_;                                                                                                          \
    asm volatile("s_add_u32 m0, %4, %6\n\ts_add_u32 %0, %3, %5\n\tbuffer_load_dwordx4 %1, %2, %0 offen sc1 lds"         \
                 : "=&s"(tmp_) : "v"(voff), "s"(srd), "s"(sb), "s"(lb), "i"(GO), "i"(LO) : "memory", "scc"); } while (0)
#define GLD128_SC1(dst, voff, srd, sb, GO)                                                                                \
  do { u32 tmp_;                                                                                                          \
    asm volatile("s_add_u32 %1, %4, %5\n\tbuffer_load_dwordx4 %0, %2, %3, %1 offen sc1"                                 \
                 : "=v"(dst), "=&s"(tmp_) : "v"(voff), "s"(srd), "s"(sb), "i"(GO) : "memory", "scc"); } while (0)
#define GST_SC1(W, val, voff, srd, sb, GO)                                                                                \
  do { u32 tmp_;                                                                                                          \
    asm volatile("s_add_u32 %0, %4, %5\n\tbuffer_store_dword" W " %1, %2, %3, %0 offen sc1\n\ts_nop 1"                  \
                 : "=&s"(tmp_) : "v"(val), "v"(voff), "s"(srd), "s"(sb), "i"(GO) : "memory", "scc"); } while (0)
#define GTOUCH(voff, srd, sb, GO, lb)                                                                                     \
  do { u32 tmp_;                                                                                                          \
    asm volatile("s_mov_b32 m0, %4\n\ts_add_u32 %0, %3, %5\n\tbuffer_load_dword %1, %2, %0 offen lds"                   \
                 : "=&s"(tmp_) : "v"(voff), "s"(srd), "s"(sb), "s"(lb), "i"(GO) : "memory", "scc"); } while (0)
#define GLDS64(vaddr, lb, LO)                                                                                             \
    asm volatile("s_add_u32 m0, %1, %2\n\ts_nop 0\n\tglobal_load_lds_dwordx4 %0, off" :: "v"(vaddr), "s"(lb), "i"(LO) : "memory", "scc")

DI void raise_flag(char* ws, int h, bool on, int cons, int idx) {
  if (on) __hip_atomic_store((u32*)(ws + WS_FLG) + (cons * 3 + h) * 128 + idx, 1u, __ATOMIC_RELAXED, __HIP_MEMORY_SCOPE_AGENT);
}
DI void wait_flags(char* ws, int h, int nlanes, int w) {
  if (w == 0) {
    const int l = threadIdx.x & 63;
    unsigned long long* c = (unsigned long long*)(ws + WS_FLG + (blockIdx.x * 3 + h) * 512) + (l < nlanes ? l : 0);
#pragma unroll 1
    for (u32 spins = 0; spins < (1u << 17); ++spins) {
      if (__all(__hip_atomic_load(c, __ATOMIC_RELAXED, __HIP_MEMORY_SCOPE_AGENT) == 0x100000001ull)) break;
      __builtin_amdgcn_s_sleep(1);
    }
    asm volatile("" ::: "memory");
  }
}

template <int PH>
__global__ __launch_bounds__(512) void kb_block(const u16* __restrict__ x, const u16* __restrict__ wo, const u16* __restrict__ wg,
                                                const u16* __restrict__ wu, const u16* __restrict__ wd, char* __restrict__ ws,
                                                u16* __restrict__ out) {
  __shared__ __attribute__((aligned(16))) char smem[163840];
  ST_DECL STAMP(0);
  const int bid = blockIdx.x, xcd = bid & 7, loc = bid >> 3;
  const int t = threadIdx.x, l = t & 63;
  const int w = __builtin_amdgcn_readfirstlane(t >> 6);
  const u32 lb = lds_base(smem);
  const u32x4 sws = make_srd(ws, WS_BYTES);
  const u32 lo16 = (u32)l * 16, zs = 0;
  const int hb = l >> 5;
  const int pm3 = loc >> 3, pn3 = loc & 7;
  const int lq = loc >= 28 ? 27 : loc;
  const int pan4 = (xcd >> 1) * 4 + lq / 7, pn4 = (xcd & 1) * 7 + lq % 7;
  constexpr u32 STG = KV_HEAD;
  constexpr u32 TAB = 159744, UVP = TAB + 2048, SCR = UVP;
  constexpr u32 XT = KV_HEAD, EX3 = XT + 8192, RST3 = 131072, RST4 = 157696;
  f32x16 zero;
#pragma unroll
  for (int i = 0; i < 16; ++i) zero[i] = 0.f;

  bf16x8 wf[32];
  const char* wsrc = (const char*)((l & 2) ? wu : wg) + (size_t)(l >> 2) * 3584 + (pn4 * 128 + w * 16 + (l & 1) * 8) * 2;
  const u32 stg_lb = lb + w * 3072;
  u32 stg_rd0 = (u32)(w * 3072 + (hb * 4 + (l >> 2 & 3)) * 64 + (l >> 4 & 1) * 32 + (l & 3) * 8), stg_rd1 = stg_rd0 + STG;
  PIN(stg_rd1);
#define WSLOT_OFF(p) ((((p) % 6) < 3 ? STG : 0u) + ((p) % 3) * 1024u)
#define WSTEP(p)                                                                                        \
  do {                                                                                                  \
    if constexpr ((p) >= 5) {                                                                                     \
      if ((p) <= 32) asm volatile("s_waitcnt vmcnt(4) lgkmcnt(0)" ::: "memory");                        \
      else if ((p) == 33) asm volatile("s_waitcnt vmcnt(3)" ::: "memory");                              \
      else if ((p) == 34) asm volatile("s_waitcnt vmcnt(2)" ::: "memory");                              \
      else if ((p) == 35) asm volatile("s_waitcnt vmcnt(1)" ::: "memory");                              \
      else asm volatile("s_waitcnt vmcnt(0)" ::: "memory");                                             \
      const u32 o_ = ((((p) - 5) % 6) < 3 ? stg_rd1 : stg_rd0) + (((p) - 5) % 3) * 1024;                \
      wf[(p) - 5] = lds_tr2(smem, o_, o_ + 512);                                                        \
    }                                                                                                   \
    if ((p) < 32) { GLDS64(wsrc, stg_lb, WSLOT_OFF(p)); wsrc += 16 * 3584; }                            \
  } while (0)

  const int tm = w & 1;
  const u32 wvo = (u32)((l >> 3) * 1024 + ((((l >> 2) ^ (l >> 4)) & 1) * 32 + (l & 3) * 8) * 2);
  const u32 wrd0 = (u32)((hb * 4 + (l >> 2 & 3)) * 128 + (l >> 3 & 1) * 64 + (l >> 4 & 1) * 32 + (l & 3) * 8);
  const int ncol = pn3 * 64 + (w >> 1 & 1) * 32 + (w >> 2) * 16;
  const u32 mvo = (u32)((ncol + hb * 4) * 4);
  const int rt3 = xcd * 8 + pm3 * 2;

  float tsb, tc1;
#define TAB_WRITE()                                                                                          \
  do { PIN(tsb); PIN(tc1);                                                                                   \
    const u32 to = TAB + (u32)((t >> 4) * 32 + (t >> 2 & 1) * 16 + ((t & 3) + 4 * (t >> 3 & 1)) * 2);        \
    *(LAS u16*)((LAS char*)smem + to) = (u16)(__float_as_uint(tc1) >> 16);                                   \
    *(LAS u16*)((LAS char*)smem + to + 1024) = (u16)(__float_as_uint(tsb) >> 16); } while (0)
#define TAB_LOAD() do { GLD32(tsb, (u32)t * 4, sws, zs, WS_MODF + 1536 * 4); GLD32(tc1, (u32)t * 4, sws, zs, WS_MODF + 2048 * 4); } while (0)
  const u32x4 so = make_srd(wo, 512 * 1024), sx = make_srd(x, 2048 * 1024), sd = make_srd(wd, 1792 * 1024);
#define P3_PIECE(j_)                                                                                         \
  do { if constexpr ((j_) < 8) GLDS(wvo, so, (u32)(w * 8192 + pn3 * 128), (j_) * 65536, lb + w * 1024, (j_) * 8192); \
    else { const u32 xvo_ = (u32)((l >> 3) * 1024 + (((l & 7) ^ ((4 * w + (l >> 4)) & 7)) * 16));            \
      GLDS(xvo_, sx, (u32)((rt3 * 32 + w * 8) * 1024 + pn3 * 128), 0, lb + w * 1024, XT); } } while (0)

  if (PH == 0 || PH == 2) {
    const int head = xcd * 4 + (loc >> 3), qt = (loc & 7) * 8 + w;
    const u32 ksb = (u32)(head * KV_HEAD + w * 1024), wlb = lb + w * 1024;
#define KVR(r) GLDS(lo16, sws, ksb, WS_KV + (r) * 8192, wlb, (r) * 8192)
    u32x4 qraw;
    GLD128(qraw, lo16, sws, (u32)(head * 64 + qt) * 1024, WS_QF);
    KVR(0); KVR(1);
    if (w < 4) KVR(16);
    KVR(2); KVR(3);
    const int r = l & 31;
    u32 krd = lo16;
    u32 vrd = (u32)(1024 + hb * 272 + (r < 16 ? r * 16 : (r == 16 ? 256 : 0x40000)));
    STAMP(1);
    WAITV(2);
    PIN(qraw);
    BAR();
    STAMP(2);
    const bf16x8 qf = __builtin_bit_cast(bf16x8, qraw);
    float m;
    bf16x8 kf = lds_frag(smem, krd);
    {
      f32x16 s0 = MFMA32(kf, qf, zero);
      m = s0[0];
#pragma unroll
      for (int i = 1; i < 16; ++i) m = fmaxf(m, s0[i]);
      m = fmaxf(m, __shfl_xor(m, 32));
    }
    f32x16 negm;
#pragma unroll
    for (int i = 0; i < 16; ++i) negm[i] = -m;
    f32x16 o = zero;
    f32x16 sc = MFMA32(kf, qf, negm);
    kf = lds_frag(smem, krd + KV_TILE);
    SFOR(j, 0, 64)
      if constexpr ((j & 1) == 0) __builtin_amdgcn_s_setprio(3 - (j >> 1 & 3));
      if (j == 5) { WAITV(10); BAR(); }
      if (j == 13) { WAITV(0); BAR(); }
      if (j == 14) STAMP(3);
      if constexpr (j < 6) { KVR(4 + 2 * j); KVR(5 + 2 * j); }
      if (PH == 0) {
        if constexpr (j >= 14 && j < 51) WSTEP(j - 14);
        if (j == 51) STAMP(21);
        if (j == 52) { BAR(); TAB_LOAD(); }
        if constexpr (j >= 52 && j < 61) P3_PIECE(j - 52);
      }
      if (j == 30 || j == 60) { krd += 30 * KV_TILE; vrd += 30 * KV_TILE; PIN(krd); PIN(vrd); }
      const u32 jo = (u32)(j % 30) * KV_TILE;
      const bf16x8 v0 = lds_frag(smem, vrd + jo), v1 = lds_frag(smem, vrd + jo + 544);
      f32x16 sn;
      if (j < 63) sn = MFMA32(kf, qf, negm);
      if (j < 62) kf = lds_frag(smem, krd + jo + 2 * KV_TILE);
      __builtin_amdgcn_sched_barrier(0);
#pragma unroll
      for (int i = 0; i < 11; ++i) sc[i] = __builtin_amdgcn_exp2f(sc[i]);
      __builtin_amdgcn_sched_barrier(0);
      o = __builtin_amdgcn_smfmac_f32_32x32x32_bf16(v0, __builtin_bit_cast(bf16x16, __builtin_shufflevector(sc, sc, 0, 1, 2, 3, 4, 5, 6, 7)), o, 0xDDDD, 0, 0);
      __builtin_amdgcn_sched_barrier(0);
#pragma unroll
      for (int i = 11; i < 16; ++i) sc[i] = __builtin_amdgcn_exp2f(sc[i]);
      o = __builtin_amdgcn_smfmac_f32_32x32x32_bf16(v1, __builtin_bit_cast(bf16x16, __builtin_shufflevector(sc, sc, 8, 9, 10, 11, 12, 13, 14, 15)), o, 0xDDDD, 0, 0);
      __builtin_amdgcn_sched_barrier(0);
      if (j < 63) sc = sn;
    SEND
    float lsum = shfl_idx(o[8], r);
    bool ok = lsum < 1e18f;
#pragma unroll
    for (int i = 0; i < 8; ++i) ok = ok && (fabsf(o[i]) < 3e38f);
    if (!__all(ok)) {
      asm volatile("" ::: "memory");
      const char* kg = ws + WS_KV + (size_t)head * KV_HEAD + lo16;
      const char* vg = ws + WS_KV + (size_t)head * KV_HEAD + 1024 + hb * 272 + (r == 16 ? 256 : (r & 15) * 16);
#pragma unroll 1
      for (int j = 0; j < 64; ++j) {
        const f32x16 sv = MFMA32(*(const bf16x8*)(kg + j * KV_TILE), qf, zero);
#pragma unroll
        for (int i = 0; i < 16; ++i) m = fmaxf(m, sv[i]);
      }
      m = fmaxf(m, __shfl_xor(m, 32));
#pragma unroll
      for (int i = 0; i < 16; ++i) negm[i] = -m;
      o = zero;
#pragma unroll 1
      for (int j = 0; j < 64; ++j) {
        f32x16 sv = MFMA32(*(const bf16x8*)(kg + j * KV_TILE), qf, negm);
        const bf16x8 v0 = *(const bf16x8*)(vg + j * KV_TILE), v1 = *(const bf16x8*)(vg + j * KV_TILE + 544);
#pragma unroll
        for (int i = 0; i < 16; ++i) sv[i] = __builtin_amdgcn_exp2f(sv[i]);
        o = __builtin_amdgcn_smfmac_f32_32x32x32_bf16(v0, __builtin_bit_cast(bf16x16, __builtin_shufflevector(sv, sv, 0, 1, 2, 3, 4, 5, 6, 7)), o, 0xDDDD, 0, 0);
        o = __builtin_amdgcn_smfmac_f32_32x32x32_bf16(v1, __builtin_bit_cast(bf16x16, __builtin_shufflevector(sv, sv, 8, 9, 10, 11, 12, 13, 14, 15)), o, 0xDDDD, 0, 0);
      }
      lsum = shfl_idx(o[8], r);
    }
    const float inv = 1.f / lsum;
    u32x4 ov;
#pragma unroll
    for (int e = 0; e < 4; ++e) ov[e] = pack2(o[2 * e] * inv, o[2 * e + 1] * inv);
    GST_SC1("x4", ov, lo16, sws, (u32)(qt * 32 + head) * 1024, WS_OF);
    STAMP(4);
    WAITV(0);
    STAMP(5);
    if (PH == 0) {
      raise_flag(ws, 0, l < 8, (loc & 7) + 8 * ((w >> 1) * 8 + l), head * 2 + (w & 1));
      TAB_WRITE();
    }
  }

  if (PH == 4) {
    SFOR(p, 0, 37) WSTEP(p); SEND
    BAR();
  }

  f32x16 uv = zero;
  const u32 trd = TAB + (u32)((l >> 4 & 1) * 1024 + hb * 16);
#define UV_PART(k0)                                                                                          \
  do { bf16x8 tf_[16];                                                                                       \
    _Pragma("unroll") for (int i_ = 0; i_ < 4; ++i_) tf_[i_] = lds_frag(smem, trd + ((k0) + i_) * 32);       \
    _Pragma("unroll") for (int i_ = 0; i_ < 16; ++i_) {                                                      \
      if (i_ + 4 < 16) tf_[i_ + 4] = lds_frag(smem, trd + ((k0) + i_ + 4) * 32);                             \
      uv = MFMA32(wf[(k0) + i_], tf_[i_], uv); SB(); } } while (0)
  if (PH == 4) { TAB_LOAD(); WAITV(0); TAB_WRITE(); BAR(); UV_PART(0); }

  if (PH == 0 || PH == 3) {
    if (PH == 3) { SFOR(j, 0, 9) P3_PIECE(j); SEND }
    STAMP(7);
    if (PH == 0) { wait_flags(ws, 0, 32, w); BAR(); }
    STAMP(8);
    const u32 olo = (u32)((w >> 2) * 32768 + (w & 3) * 4096);
    const u32 osb = (u32)rt3 * 32768 + olo, olb = lb + olo;
#pragma unroll
    for (int s = 0; s < 2; ++s)
#pragma unroll
      for (int j = 0; j < 4; ++j) GLDS_SC1(lo16, sws, osb, WS_OF + (s * 16 + j) * 1024, olb, 65536 + (s * 16 + j) * 1024);
    u32x4 ga[2], cb[2];
#pragma unroll
    for (int e = 0; e < 2; ++e) { GLD128(ga[e], mvo, sws, zs, WS_MODF + 1024 * 4 + e * 32); GLD128(cb[e], mvo, sws, zs, WS_MODF + 2048 * 4 + e * 32); }
    if (PH == 0) UV_PART(0);
    const int tn = w >> 1 & 1, kh = w >> 2;
    u32 ord = 65536 + (u32)(tm * 32768 + kh * 8192) + lo16;
    PIN(ord);
    const u32 wrd = (wrd0 ^ (u32)(tn * 64)) + kh * 16384;
    f32x16 acc = zero;
#pragma unroll
    for (int s = 0; s < 2; ++s) {
      bf16x8 wq[8], oq[8];
      if (PH == 0) {
#pragma unroll
        for (int j = 0; j < 8; ++j) wq[j] = lds_tr2(smem, wrd + (s * 16 + j) * 2048, wrd + (s * 16 + j) * 2048 + 1024);
      }
      if (s == 0) WAITV(8); else WAITV(4);
      BAR();
      if (PH != 0) {
#pragma unroll
        for (int j = 0; j < 8; ++j) wq[j] = lds_tr2(smem, wrd + (s * 16 + j) * 2048, wrd + (s * 16 + j) * 2048 + 1024);
      }
#pragma unroll
      for (int j = 0; j < 4; ++j) oq[j] = lds_frag(smem, ord + (s * 16 + j) * 1024);
      SB();
#pragma unroll
      for (int j = 0; j < 8; ++j) {
        if (j + 4 < 8) oq[j + 4] = lds_frag(smem, ord + (s * 16 + j + 4) * 1024);
        acc = MFMA32(wq[j], oq[j], acc);
        SB();
      }
    }
    STAMP(9);
    LAS f32x4* ex = (LAS f32x4*)((LAS char*)smem + EX3);
    float mine[8];
#pragma unroll
    for (int i = 0; i < 8; ++i) mine[i] = kh ? acc[8 + i] : acc[i];
    {
      f32x4 a, b;
#pragma unroll
      for (int i = 0; i < 4; ++i) { a[i] = kh ? acc[i] : acc[8 + i]; b[i] = kh ? acc[4 + i] : acc[12 + i]; }
      ex[(w ^ 4) * 128 + l] = a; ex[(w ^ 4) * 128 + 64 + l] = b;
    }
    WAITV(0);
    BAR();
    {
      const f32x4 a = ex[w * 128 + l], b = ex[w * 128 + 64 + l];
#pragma unroll
      for (int i = 0; i < 4; ++i) { mine[i] += a[i]; mine[4 + i] += b[i]; }
    }
    const int row = tm * 32 + (l & 31);
    float xv[8];
    u32x4 x1p, yp;
#pragma unroll
    for (int e = 0; e < 2; ++e) {
      PIN(ga[e]); PIN(cb[e]);
      const u32x2 xr = *(const LAS u32x2*)((LAS char*)smem + XT + row * 128 + (((tn * 4 + kh * 2 + e) ^ (row >> 1 & 7)) * 16) + hb * 8);
#pragma unroll
      for (int h = 0; h < 2; ++h) {
        const u32 pk = pack2(bf_lo(xr[h]) + __uint_as_float(ga[e][2 * h]) * mine[4 * e + 2 * h], bf_hi(xr[h]) + __uint_as_float(ga[e][2 * h + 1]) * mine[4 * e + 2 * h + 1]);
        x1p[2 * e + h] = pk;
        xv[4 * e + 2 * h] = bf_lo(pk); xv[4 * e + 2 * h + 1] = bf_hi(pk);
        yp[2 * e + h] = pack2(bf_lo(pk) * __uint_as_float(cb[e][2 * h]), bf_hi(pk) * __uint_as_float(cb[e][2 * h + 1]));
      }
    }
    GST_SC1("x4", yp, lo16, sws, (u32)((rt3 + tm) * 32 + pn3 * 4 + tn * 2 + kh) * 1024, WS_Y);
    float sum = 0.f;
#pragma unroll
    for (int i = 0; i < 8; ++i) sum += xv[i];
    sum = xor32_sum(sum);
    const float mean = sum * (1.f / 16.f);
    float sq = 0.f;
#pragma unroll
    for (int i = 0; i < 8; ++i) { const float d = xv[i] - mean; sq += d * d; }
    sq = xor32_sum(sq);
    LAS f32x2* sp = (LAS f32x2*)((LAS char*)smem + RST3);
    if (l < 32) sp[(w >> 1) * 64 + row] = (f32x2){mean, sq};
    BAR();
    if (w == 0) {
      const f32x2 p0 = sp[l], p1 = sp[64 + l], p2 = sp[128 + l], p3 = sp[192 + l];
      const float mu = 0.25f * ((p0[0] + p1[0]) + (p2[0] + p3[0]));
      const float d0 = p0[0] - mu, d1 = p1[0] - mu, d2 = p2[0] - mu, d3 = p3[0] - mu;
      const f32x2 o2 = {mu, (p0[1] + p1[1]) + (p2[1] + p3[1]) + 16.f * ((d0 * d0 + d1 * d1) + (d2 * d2 + d3 * d3))};
      GST_SC1("x2", o2, (u32)(hb * 2048 + (l & 31) * 8), sws, (u32)(rt3 * 2048 + pn3 * 256), WS_ST2);
    }
    STAMP(10);
    WAITV(0);
    if (PH == 0) {
      const int pan = xcd * 2 + (pm3 >> 1);
      raise_flag(ws, 1, l < 14, (pan >> 2) * 2 + (l >= 7 ? 1 : 0) + 8 * ((pan & 3) * 7 + (l >= 7 ? l - 7 : l)), ((pm3 & 1) * 8 + pn3) * 8 + w);
    }
    STAMP(11);
    *(u32x4*)(ws + WS_X1 + (size_t)(bid * 512 + t) * 16) = x1p;
  }

  const u32 dsb = (u32)(w * 16384 + pn3 * 128), dlb = lb + w * 2048;
#define WDPIECE(c, slot, h) GLDS(wvo, sd, dsb, (c) * 131072 + (h) * 8192, dlb, (slot) * 16384 + (h) * 1024)
#define WDISSUE(c, slot) do { GLDS(wvo, sd, dsb, (c) * 131072, dlb, (slot) * 16384); GLDS(wvo, sd, dsb, (c) * 131072 + 8192, dlb, (slot) * 16384 + 1024); } while (0)
  if ((PH == 0 || PH == 4) && loc < 28) {
    if (PH == 0) { wait_flags(ws, 1, 64, w); BAR(); }
    STAMP(12);
    const int rt4 = pan4 * 4;
    const u32 ysb = (u32)(rt4 * 32768 + w * 1024), ylb = lb + w * 1024;
#define YISSUE(a) _Pragma("unroll") for (int j = 0; j < 4; ++j) GLDS_SC1(lo16, sws, ysb, WS_Y + (a) * 32768 + j * 8192, ylb, (a) * 32768 + j * 8192)
    YISSUE(0);
    GLDS_SC1(lo16, sws, (u32)(rt4 * 2048 + w * 1024), WS_ST2, ylb, 131072);
    YISSUE(1);
    UV_PART(16);
    float uu[16], vv[16];
#pragma unroll
    for (int i = 0; i < 16; ++i) {
      const auto r = __builtin_amdgcn_permlane16_swap(__float_as_uint(uv[i]), __float_as_uint(uv[i]), false, false);
      uu[i] = __uint_as_float(r[0]); vv[i] = __uint_as_float(r[1]);
    }
    u32 yrd0 = lo16, yrd1 = lo16 + 65536;
    PIN(yrd1);
    const u32 asb = (u32)(rt4 * 112 + pn4 * 8 + w) * 1024;
#define YFRAG(n) lds_frag(smem, ((n) < 64 ? yrd0 : yrd1) + ((n) & 63) * 1024)
    WAITV(4);
    BAR();
    STAMP(13);
    bf16x8 yf[128];
#pragma unroll
    for (int n = 0; n < 4; ++n) yf[n] = YFRAG(n);
    SB();
    f32x16 acc = zero, accp = zero;
    f32x2 stp = {0.f, 0.f};
    float eg[8], eu[8], et[8];
    u32x4 ov;
    SFOR(n, 0, 160)
      constexpr int a = n >> 5, ks = n & 31;
      if (n == 0) STAMP(22);
      if (n == 26) STAMP(23);
      if (n == 56) STAMP(25);
      if (n == 36) STAMP(27);
      if (n == 68) STAMP(29);
      if (n == 100) STAMP(31);
      if (n == 26) { WAITV(8); BAR(); }
      if (n == 56) { if (PH == 0) WAITV(4); else WAITV(0); RAWBAR(); }
      if constexpr (n < 24 && n % 3 == 0) GLDS_SC1(lo16, sws, ysb, WS_Y + 65536 + (n / 3) * 8192, ylb, 65536 + (n / 3) * 8192);
      if constexpr (PH == 0) {
        if (n == 36 || n == 68 || n == 100) RAWBAR();
        if constexpr (n == 60) {
          const int rr = pm3 * 224 + w * 28 + (l < 28 ? l : 27);
          GTOUCH((u32)(rr + (rr < 256 ? 768 : 896)) * 1024, sd, (u32)(pn3 * 128), 0, lb + SCR);
        }
        if constexpr (n >= 36 && n < 52 && n % 4 == 0) WDPIECE((n - 36) / 8, (n - 36) / 8, (n - 36) / 4 & 1);
        if constexpr (n >= 68 && n < 84 && n % 4 == 0) WDPIECE(2 + (n - 68) / 8, 2 + (n - 68) / 8, (n - 68) / 4 & 1);
        if constexpr (n == 84 || n == 88) WDPIECE(8, 8, (n - 84) / 4);
        if constexpr (n >= 100 && n < 116 && n % 4 == 0) WDPIECE(4 + (n - 100) / 8, 4 + (n - 100) / 8, (n - 100) / 4 & 1);
      }
      if (n == 26) STAMP(24);
      if (n == 56) STAMP(26);
      if (n == 36) STAMP(28);
      if (n == 68) STAMP(30);
      if (n == 100) STAMP(32);
      if constexpr (n == 4) {
      const LAS f32x2* sp = (const LAS f32x2*)((LAS char*)smem + 131072 + (t >> 7) * 2048 + (t & 3) * 512 + (t >> 2 & 31) * 8);
      const f32x2 p0 = sp[0], p1 = sp[32];
      float mu = p0[0] + p1[0];
      mu += dpp_xor1(mu); mu += dpp_xor2(mu);
      mu *= 0.125f;
      float q = p0[1] + p1[1] + 64.f * ((p0[0] - mu) * (p0[0] - mu) + (p1[0] - mu) * (p1[0] - mu));
      q += dpp_xor1(q); q += dpp_xor2(q);
      const float rs = __builtin_amdgcn_rsqf(q * (1.f / 512.f) + 1e-5f);
      ((LAS f32x2*)((LAS char*)smem + RST4))[t >> 2] = (f32x2){rs, -mu * rs};
    }
      if (ks == 28 && a < 4) stp = ((const LAS f32x2*)((LAS char*)smem + RST4))[a * 32 + (l & 31)];
      if (ks == 0 && a > 0) { accp = acc; acc = zero; }
      if (n == 128) STAMP(14);
      if constexpr (n + 4 < 128) yf[n + 4] = YFRAG(n + 4);
      if constexpr (n < 128) acc = MFMA32(wf[ks], yf[n], acc);
      if constexpr (a > 0 && ks >= 2 && ks < 26) {
        constexpr int i = (ks - 2) / 3, ph = (ks - 2) % 3;
        if (ph == 0) {
          eg[i] = __builtin_fmaf(accp[i], stp[0], __builtin_fmaf(uu[i], stp[1], vv[i]));
          eu[i] = __builtin_fmaf(accp[8 + i], stp[0], __builtin_fmaf(uu[8 + i], stp[1], vv[8 + i]));
          et[i] = -1.4426950408889634f * eg[i];
          PIN(eg[i]); PIN(eu[i]); PIN(et[i]);
        } else if (ph == 1) {
          et[i] = 1.f + __builtin_amdgcn_exp2f(et[i]);
          eg[i] *= eu[i];
          PIN(eg[i]); PIN(et[i]);
        } else {
          eg[i] *= __builtin_amdgcn_rcpf(et[i]);
          PIN(eg[i]);
          if constexpr (i & 1) ov[i >> 1] = pack2(eg[i - 1], eg[i]);
        }
      }
      if constexpr (a > 0 && ks == 26) GST_SC1("x4", ov, lo16, sws, asb, WS_ACT + (a - 1) * 112 * 1024);
      if (n < 128) SB();
    SEND
    STAMP(15);
    WAITV(0);
    if (PH == 0) raise_flag(ws, 2, l < 16, (pan4 >> 1) + 8 * ((pan4 & 1) * 16 + l), pn4 * 8 + w);
    STAMP(16);
  } else if (PH == 0) {
    BAR();
    WDISSUE(0, 0); WDISSUE(1, 1); WDISSUE(2, 2); WDISSUE(3, 3); WDISSUE(8, 8); WDISSUE(4, 4); WDISSUE(5, 5);
  }

  if (PH == 0 || PH == 5) {
    if (PH == 5) { WDISSUE(0, 0); WDISSUE(1, 1); WDISSUE(2, 2); WDISSUE(3, 3); WDISSUE(8, 8); WDISSUE(4, 4); WDISSUE(5, 5); }
    WAITV(0);
    BAR();
    if (PH == 5 || w != 0) { WDISSUE(6, 6); WDISSUE(7, 7); WDISSUE(9, 9); }
    const int kq = w >> 1;
    u32 wra[3][2];
#pragma unroll
    for (int i = 0; i < 3; ++i) { wra[i][0] = wrd0 + kq * 4096 + i * 65536; wra[i][1] = wra[i][0] ^ 64u; PIN(wra[i][0]); PIN(wra[i][1]); }
    bf16x8 dq[14][4];
#define DFRAG(c) _Pragma("unroll") for (int i_ = 0; i_ < 4; ++i_) {                                              \
      const u32 o_ = wra[((c) % 10) >> 2][i_ & 1] + (((c) % 10) & 3) * 16384 + (i_ >> 1) * 2048; dq[c][i_] = lds_tr2(smem, o_, o_ + 1024); }
    DFRAG(0); DFRAG(1); DFRAG(2); DFRAG(3);
    BAR();
    if (PH == 5 || w != 0) { WDISSUE(10, 0); WDISSUE(11, 1); WDISSUE(12, 2); WDISSUE(13, 3); }
    STAMP(17);
    if (PH == 0) {
      wait_flags(ws, 2, 56, w);
      RAWBAR();
    }
    STAMP(18);
    const u32 asb = (u32)((rt3 + tm) * 112 + kq * 2) * 1024;
    u32x4 af[14][2];
#define AISSUE(c) do { GLD128_SC1(af[c][0], lo16, sws, asb, WS_ACT + (c) * 8192); GLD128_SC1(af[c][1], lo16, sws, asb, WS_ACT + (c) * 8192 + 1024); } while (0)
    AISSUE(0); AISSUE(1);
    if (PH == 0 && w == 0) { WDISSUE(6, 6); WDISSUE(7, 7); WDISSUE(9, 9); WDISSUE(10, 0); WDISSUE(11, 1); WDISSUE(12, 2); WDISSUE(13, 3); }
    AISSUE(2); AISSUE(3); AISSUE(4); AISSUE(5);
    f32x16 acc[2] = {zero, zero};
    u32x4 x1p, gb[2];
#pragma unroll
    for (int c = 0; c < 14; ++c) {
      if (c < 8) WAITV(10); else if (c == 8) WAITV(13); else if (c == 9) WAITV(11);
      else if (c == 10) WAITV(9); else if (c == 11) WAITV(7); else if (c == 12) WAITV(5); else WAITV(0);
      if (c == 0) STAMP(19);
      if (c == 4 || c == 9) RAWBAR();
      if (c < 8) AISSUE(c + 6);
      if (c == 7) {
        GLD128(x1p, (u32)t * 16, sws, (u32)bid * 8192, WS_X1);
        GLD128(gb[0], mvo, sws, zs, WS_MODF + 2560 * 4); GLD128(gb[1], mvo, sws, zs, WS_MODF + 2560 * 4 + 32);
      }
      if (c >= 3 && c < 13) { DFRAG(c + 1); }
      PIN(af[c][0]); PIN(af[c][1]);
      SB();
#pragma unroll
      for (int i = 0; i < 4; ++i) acc[i & 1] = MFMA32(dq[c][i], __builtin_bit_cast(bf16x8, af[c][i >> 1]), acc[i & 1]);
      SB();
    }
    STAMP(20);
    LAS f32x4* ex = (LAS f32x4*)((LAS char*)smem + 65536);
#pragma unroll
    for (int qd = 0; qd < 4; ++qd)
#pragma unroll
      for (int h = 0; h < 2; ++h) {
        const int b = (qd >> 1) * 8 + h * 4;
        ex[(((tm + 2 * qd) * 4 + kq) * 2 + h) * 64 + l] = (f32x4){acc[qd & 1][b], acc[qd & 1][b + 1], acc[qd & 1][b + 2], acc[qd & 1][b + 3]};
      }
    BAR();
    u32 res[4];
    PIN(x1p);
#pragma unroll
    for (int h = 0; h < 2; ++h) {
      PIN(gb[h]);
      const f32x4 s = (ex[((w * 4 + 0) * 2 + h) * 64 + l] + ex[((w * 4 + 1) * 2 + h) * 64 + l]) + (ex[((w * 4 + 2) * 2 + h) * 64 + l] + ex[((w * 4 + 3) * 2 + h) * 64 + l]);
      res[2 * h] = pack2(bf_lo(x1p[2 * h]) + __uint_as_float(gb[h][0]) * s[0], bf_hi(x1p[2 * h]) + __uint_as_float(gb[h][1]) * s[1]);
      res[2 * h + 1] = pack2(bf_lo(x1p[2 * h + 1]) + __uint_as_float(gb[h][2]) * s[2], bf_hi(x1p[2 * h + 1]) + __uint_as_float(gb[h][3]) * s[3]);
    }
    const auto r0 = __builtin_amdgcn_permlane32_swap(res[0], res[2], false, false);
    const auto r1 = __builtin_amdgcn_permlane32_swap(res[1], res[3], false, false);
    const u32x4 ov = {r0[0], r1[0], r0[1], r1[1]};
    *(u32x4*)((char*)out + (size_t)((rt3 + tm) * 32 + (l & 31)) * 1024 + (ncol + hb * 8) * 2) = ov;
  }
  ST_OUT(PH == 0 ? 2 : PH, ws);
}

#define FUSED 1
extern "C" void kernel_launch(void* const* d_in, const int* in_sizes, int n_in, void* d_out, int out_size, void* d_ws, size_t ws_size,
                              hipStream_t stream) {
  const u16* x = (const u16*)d_in[0];
  const u16* wqkv = (const u16*)d_in[1];
  const u16* wo = (const u16*)d_in[2];
  const u16* wg = (const u16*)d_in[4];
  const u16* wu = (const u16*)d_in[5];
  const u16* wd = (const u16*)d_in[6];
  const u16* c = (const u16*)d_in[7];
  const u16* wmod = (const u16*)d_in[8];
  char* ws = (char*)d_ws;
  k0_mod<<<448, 256, 0, stream>>>(c, wmod, x, (float*)(ws + WS_MODP), (f32x2*)(ws + WS_ST1));
  k1_qkv<<<256, 512, 0, stream>>>(x, wqkv, (const float*)(ws + WS_ST1), ws);
  kb_block<0><<<256, 512, 0, stream>>>(x, wo, wg, wu, wd, ws, (u16*)d_out);
}
```

```cpp
#include <hip/hip_runtime.h>
#include <hip/hip_bf16.h>

typedef unsigned short u16;
typedef unsigned int u32;
typedef u32 u32x2 __attribute__((ext_vector_type(2)));
typedef u32 u32x4 __attribute__((ext_vector_type(4)));
typedef float f32x2 __attribute__((ext_vector_type(2)));
typedef float f32x4 __attribute__((ext_vector_type(4)));
typedef float f32x8 __attribute__((ext_vector_type(8)));
typedef float f32x16 __attribute__((ext_vector_type(16)));
typedef __bf16 bf16x2 __attribute__((ext_vector_type(2)));
typedef __bf16 bf16x8 __attribute__((ext_vector_type(8)));
typedef __bf16 bf16x16 __attribute__((ext_vector_type(16)));
typedef short s16x4 __attribute__((ext_vector_type(4)));
typedef short s16x8 __attribute__((ext_vector_type(8)));
#define PRIO_MODE 1
#define NO_WSTEP 0
#define LAS __attribute__((address_space(3)))
#define DI __device__ __forceinline__

constexpr u32 WS_MODP = 0;
constexpr u32 WS_MODF = 0xC000;
constexpr u32 WS_ST1  = 0x10000;
constexpr u32 WS_CNT  = 0x20000;
constexpr u32 WS_QF   = 0x100000;
constexpr u32 WS_KV   = 0x300000;
constexpr u32 WS_OF   = 0x800000;
constexpr u32 WS_X1   = 0xA00000;
constexpr u32 WS_Y    = 0xC00000;
constexpr u32 WS_ST2  = 0xE00000;
constexpr u32 WS_ACT  = 0xF00000;
constexpr u32 WS_BYTES = 0x1600000;
constexpr int KV_TILE = 2112, KV_HEAD = 64 * KV_TILE;
constexpr int NCNT = 48;

template <int I> struct IC { static constexpr int value = I; };
template <int B, int E, class F> DI void static_for(F&& f) { if constexpr (B < E) { f(IC<B>{}); static_for<B + 1, E>(f); } }
#define SFOR(n, B, E) static_for<B, E>([&](auto ic_) __attribute__((always_inline)) { constexpr int n = decltype(ic_)::value;
#define SEND });

DI float bf_lo(u32 u) { return __uint_as_float(u << 16); }
DI float bf_hi(u32 u) { return __uint_as_float(u & 0xffff0000u); }
DI u32 pack2(float a, float b) { bf16x2 r = {(__bf16)a, (__bf16)b}; return __builtin_bit_cast(u32, r); }
DI float bfround(float a) { return (float)(__bf16)a; }
DI float xor32_sum(float v) {
  const auto r = __builtin_amdgcn_permlane32_swap(__float_as_uint(v), __float_as_uint(v), false, false);
  return __uint_as_float(r[0]) + __uint_as_float(r[1]);
}
DI float dpp_xor1(float v) { return __int_as_float(__builtin_amdgcn_mov_dpp(__float_as_int(v), 0xB1, 0xF, 0xF, true)); }
DI float dpp_xor2(float v) { return __int_as_float(__builtin_amdgcn_mov_dpp(__float_as_int(v), 0x4E, 0xF, 0xF, true)); }
DI float shfl_idx(float v, int srcLane) { return __int_as_float(__builtin_amdgcn_ds_bpermute(srcLane << 2, __float_as_int(v))); }

DI u32x4 make_srd(const void* p, u32 bytes) {
  const size_t a = (size_t)p;
  u32x4 r = {(u32)a, (u32)(a >> 32) & 0xffffu, bytes, 0x20000u};
  return r;
}
#define GLDS(voff, srd, sb, GO, lb, LO)                                                                                   \
  do { u32 tmp_;                                                                                                          \
    asm volatile("s_add_u32 m0, %4, %6\n\ts_add_u32 %0, %3, %5\n\tbuffer_load_dwordx4 %1, %2, %0 offen lds"            \
                 : "=&s"(tmp_) : "v"(voff), "s"(srd), "s"(sb), "s"(lb), "i"(GO), "i"(LO) : "memory", "scc"); } while (0)
#define GLD(W, dst, voff, srd, sb, GO)                                                                                    \
  do { u32 tmp_;                                                                                                          \
    asm volatile("s_add_u32 %1, %4, %5\n\tbuffer_load_dword" W " %0, %2, %3, %1 offen"                                   \
                 : "=v"(dst), "=&s"(tmp_) : "v"(voff), "s"(srd), "s"(sb), "i"(GO) : "memory", "scc"); } while (0)
#define GLD128(dst, voff, srd, sb, GO) GLD("x4", dst, voff, srd, sb, GO)
#define GLD64(dst, voff, srd, sb, GO) GLD("x2", dst, voff, srd, sb, GO)
#define GLD32(dst, voff, srd, sb, GO) GLD("", dst, voff, srd, sb, GO)
#define WAITV(N) asm volatile("s_waitcnt vmcnt(" #N ")" ::: "memory")
#define PIN(x) asm volatile("" : "+v"(x))
#define BAR() do { asm volatile("s_waitcnt lgkmcnt(0)" ::: "memory"); __builtin_amdgcn_s_barrier(); asm volatile("" ::: "memory"); } while (0)

#define ST_DECL
#define STAMP(i)
#define ST_OUT(kid, wsp)

DI bf16x8 lds_frag(const char* smem, u32 off) { return *(const LAS bf16x8*)((const LAS char*)smem + off); }
DI bf16x8 lds_tr2(const char* smem, u32 off0, u32 off1) {
  s16x4 a = __builtin_amdgcn_ds_read_tr16_b64_v4i16((LAS s16x4*)((LAS char*)smem + off0));
  s16x4 b = __builtin_amdgcn_ds_read_tr16_b64_v4i16((LAS s16x4*)((LAS char*)smem + off1));
  s16x8 r = __builtin_shufflevector(a, b, 0, 1, 2, 3, 4, 5, 6, 7);
  return __builtin_bit_cast(bf16x8, r);
}
DI u32 lds_base(const char* smem) { return (u32)(size_t)(const LAS char*)smem; }
#define SB() __builtin_amdgcn_sched_barrier(0)
#define RAWBAR() do { asm volatile("" ::: "memory"); __builtin_amdgcn_s_barrier(); asm volatile("" ::: "memory"); } while (0)
#define MFMA32(a, b, c) __builtin_amdgcn_mfma_f32_32x32x16_bf16((a), (b), (c), 0, 0, 0)
#define MFMA16(a, b, c) __builtin_amdgcn_mfma_f32_16x16x32_bf16((a), (b), (c), 0, 0, 0)

__global__ __launch_bounds__(256) void k0_mod(const u16* __restrict__ c, const u16* __restrict__ wmod, const u16* __restrict__ x,
                                              float* __restrict__ modp, f32x2* __restrict__ st1) {
  __shared__ float red[4][64];
  const int b = blockIdx.x, t = threadIdx.x, w = t >> 6, l = t & 63;
  if (b < 192) {
    const int cb = b % 48, kq = b / 48, c8 = t & 7, r = t >> 3;
    u32x4 v[4]; float s[4];
#pragma unroll
    for (int i = 0; i < 4; ++i) {
      const int row = kq * 128 + i * 32 + r;
      v[i] = *(const u32x4*)(wmod + (size_t)row * 3072 + cb * 64 + c8 * 8);
      s[i] = __uint_as_float((u32)c[row] << 16);
    }
    float acc[8];
#pragma unroll
    for (int e = 0; e < 8; ++e) acc[e] = 0.f;
#pragma unroll
    for (int i = 0; i < 4; ++i) {
      const float sv = bfround(s[i] / (1.f + __expf(-s[i])));
#pragma unroll
      for (int e = 0; e < 4; ++e) { acc[2 * e] += sv * bf_lo(v[i][e]); acc[2 * e + 1] += sv * bf_hi(v[i][e]); }
    }
#pragma unroll
    for (int e = 0; e < 8; ++e) {
      acc[e] += __shfl_xor(acc[e], 8); acc[e] += __shfl_xor(acc[e], 16); acc[e] += __shfl_xor(acc[e], 32);
    }
    if (l < 8) {
#pragma unroll
      for (int e = 0; e < 8; ++e) red[w][l * 8 + e] = acc[e];
    }
    __syncthreads();
    if (t < 64) modp[(cb * 64 + t) * 4 + kq] = (red[0][t] + red[1][t]) + (red[2][t] + red[3][t]);
  } else {
    const int row0 = (b - 192) * 8 + w * 2;
    u32x4 v[2];
#pragma unroll
    for (int i = 0; i < 2; ++i) v[i] = *(const u32x4*)(x + (size_t)(row0 + i) * 512 + l * 8);
#pragma unroll
    for (int i = 0; i < 2; ++i) {
      float f[8];
#pragma unroll
      for (int e = 0; e < 4; ++e) { f[2 * e] = bf_lo(v[i][e]); f[2 * e + 1] = bf_hi(v[i][e]); }
      float sum = 0.f;
#pragma unroll
      for (int e = 0; e < 8; ++e) sum += f[e];
#pragma unroll
      for (int o = 1; o < 64; o <<= 1) sum += __shfl_xor(sum, o);
      const float mean = sum * (1.f / 512.f);
      float sq = 0.f;
#pragma unroll
      for (int e = 0; e < 8; ++e) { const float d = f[e] - mean; sq += d * d; }
#pragma unroll
      for (int o = 1; o < 64; o <<= 1) sq += __shfl_xor(sq, o);
      if (l == 0) { f32x2 o2 = {mean, __builtin_amdgcn_rsqf(sq * (1.f / 512.f) + 1e-5f)}; st1[row0 + i] = o2; }
    }
  }
}

DI u32x4 modulate8(const u32x4 v, float rs, float mr, const float* c1, const float* sh) {
  u32x4 o;
#pragma unroll
  for (int e = 0; e < 4; ++e) {
    const float a = __builtin_fmaf(__builtin_fmaf(bf_lo(v[e]), rs, mr), c1[2 * e], sh[2 * e]);
    const float b = __builtin_fmaf(__builtin_fmaf(bf_hi(v[e]), rs, mr), c1[2 * e + 1], sh[2 * e + 1]);
    o[e] = pack2(a, b);
  }
  return o;
}

__global__ __launch_bounds__(512) void k1_qkv(const u16* __restrict__ x, const u16* __restrict__ wqkv, const float* __restrict__ st1,
                                              char* __restrict__ ws) {
  __shared__ __attribute__((aligned(16))) char smem[163840];
  constexpr u32 WOFF = 65536, WSLOT = 24576;
  ST_DECL STAMP(0);
  const int bid = blockIdx.x, xcd = bid & 7, loc = bid >> 3;
  const int pm = (xcd >> 1) * 8 + (loc >> 2), cg = (xcd & 1) * 4 + (loc & 3);
  const int t = threadIdx.x, l = t & 63;
  const int w = __builtin_amdgcn_readfirstlane(t >> 6);
  const u32 lb = lds_base(smem);
  const u32x4 sws = make_srd(ws, WS_BYTES), sx = make_srd(x, 2048 * 1024), sw = make_srd(wqkv, 512 * 3072);
  const u32 zs = 0;
  u32x4 fin, mp[2];
  const u32 t16 = (u32)t * 16;
  GLD128(mp[0], t16, sws, zs, WS_MODP);
  GLD128(mp[1], t16, sws, zs, WS_MODP + 8192);
  GLD128(fin, t16, sws, (u32)(bid < 6 ? bid : 0) * 8192, WS_MODP);
  u32x4 xa[8];
  const u32 lo16 = (u32)l * 16;
  const u32 xsb = (u32)(pm * 64 + w * 8) * 1024;
#pragma unroll
  for (int i = 0; i < 8; ++i) GLD128(xa[i], lo16, sx, xsb, i * 1024);
  const u32 wvo = (u32)((8 * (l >> 5) + (l >> 3 & 3)) * 3072 + ((((l >> 1) ^ (l >> 4)) & 3) * 16 + (l & 1) * 8) * 2);
  const u32 wsb = (u32)((32 * (w >> 2) + 16 * (w & 1) + 4 * (w >> 1 & 1)) * 3072 + cg * 128), wlb = lb + w * 1024;
#define K1_ISSUE(kc) _Pragma("unroll") for (int c = 0; c < 3; ++c) GLDS(wvo, sw, wsb, (kc) * 64 * 3072 + c * 1024, wlb, WOFF + ((kc) & 3) * WSLOT + c * 8192)
  K1_ISSUE(0);
  float rs[8], mr[8];
#pragma unroll
  for (int i = 0; i < 8; ++i) { rs[i] = st1[(pm * 64 + w * 8 + i) * 2 + 1]; mr[i] = -st1[(pm * 64 + w * 8 + i) * 2] * rs[i]; }
  STAMP(1);
  WAITV(11);
  PIN(fin); PIN(mp[0]); PIN(mp[1]);
#define SUM4(v) ((__uint_as_float(v[0]) + __uint_as_float(v[1])) + (__uint_as_float(v[2]) + __uint_as_float(v[3])))
  ((LAS float*)(LAS char*)smem)[t] = bfround(1.f + bfround(SUM4(mp[1])));
  ((LAS float*)(LAS char*)smem)[512 + t] = bfround(SUM4(mp[0]));
  {
    float f = bfround(SUM4(fin));
    if (bid == 4) f = bfround(1.f + f);
    if (bid < 6) ((float*)(ws + WS_MODF))[bid * 512 + t] = f;
    if (bid == 6 && t < NCNT) ((u32*)(ws + WS_CNT))[t * 64] = 0u;
  }
  BAR();
  float c1[8], sh[8];
  {
    const LAS f32x4* tc = (const LAS f32x4*)((LAS char*)smem + l * 32);
    const f32x4 a0 = tc[0], a1 = tc[1], b0 = tc[128], b1 = tc[129];
#pragma unroll
    for (int e = 0; e < 4; ++e) { c1[e] = a0[e]; c1[4 + e] = a1[e]; sh[e] = b0[e]; sh[4 + e] = b1[e]; }
  }
  K1_ISSUE(1);
  WAITV(6);
  BAR();
  STAMP(2);
  const u32 awr = (u32)(((w >> 1) * 16 + (l >> 2)) * 1024 + (l & 3) * 256 + (w & 1) * 128);
#pragma unroll
  for (int i = 0; i < 8; ++i) {
    PIN(xa[i]);
    *(LAS u32x4*)((LAS char*)smem + awr + ((i ^ (l & 7)) << 4)) = modulate8(xa[i], rs[i], mr[i], c1, sh);
    if (i == 1) { K1_ISSUE(2); }
    if (i == 4) { K1_ISSUE(3); }
  }
  const int tm = w & 1, hd = w >> 1, g = l >> 4;
  u32 ard[2];
#pragma unroll
  for (int e = 0; e < 2; ++e) ard[e] = (u32)(tm * 32768 + e * 1024 + g * 256 + (((l & 15) ^ (e * 4 + g)) << 4));
  u32 brd = WOFF + (u32)((l >> 2) * 128 + ((hd ^ (l >> 3 & 3)) * 32) + (l & 3) * 8);
  PIN(brd);
  f32x4 acc[2][3];
#pragma unroll
  for (int a = 0; a < 2; ++a)
#pragma unroll
    for (int c = 0; c < 3; ++c) acc[a][c] = (f32x4){0.f, 0.f, 0.f, 0.f};
  WAITV(9);
  BAR();
  STAMP(3);
#pragma unroll
  for (int kc = 0; kc < 8; ++kc) {
    bf16x8 hf[2][2], wf[2][3];
#pragma unroll
    for (int e = 0; e < 2; ++e) {
#pragma unroll
      for (int a = 0; a < 2; ++a) hf[e][a] = lds_frag(smem, ard[e] + (a * 16 + kc * 2) * 1024);
#pragma unroll
      for (int c = 0; c < 3; ++c) {
        const u32 o = brd + (kc & 3) * WSLOT + c * 8192 + e * 4096;
        wf[e][c] = lds_tr2(smem, o, o + 2048);
      }
    }
#pragma unroll
    for (int e = 0; e < 2; ++e)
#pragma unroll
      for (int a = 0; a < 2; ++a) {
        acc[a][0] = MFMA16(wf[e][0], hf[e][a], acc[a][0]);
        acc[a][1] = MFMA16(wf[e][1], hf[e][a], acc[a][1]);
        acc[a][2] = MFMA16(hf[e][a], wf[e][2], acc[a][2]);
      }
    if (kc < 7) {
      if (kc < 5) WAITV(6); else if (kc == 5) WAITV(3); else WAITV(0);
      BAR();
      if (kc < 4) { K1_ISSUE(kc + 4); }
    }
    if (kc == 3) STAMP(4);
  }
  STAMP(5);
  const int head = cg * 4 + hd, tile = pm * 2 + tm;
  constexpr float QS = 0.25f * 1.4426950408889634f;
  char* kv = ws + WS_KV + (size_t)head * KV_HEAD + tile * KV_TILE;
#pragma unroll
  for (int a = 0; a < 2; ++a) {
    const u32 fo = (u32)((a * 16 + (l & 15) + 32 * (g >> 1)) * 16 + (g & 1) * 8);
    u32x2 q = {pack2(acc[a][0][0] * QS, acc[a][0][1] * QS), pack2(acc[a][0][2] * QS, acc[a][0][3] * QS)};
    u32x2 k = {pack2(acc[a][1][0], acc[a][1][1]), pack2(acc[a][1][2], acc[a][1][3])};
    u32x2 v = {pack2(acc[a][2][0], acc[a][2][1]), pack2(acc[a][2][2], acc[a][2][3])};
    *(u32x2*)(ws + WS_QF + (size_t)(head * 64 + tile) * 1024 + fo) = q;
    *(u32x2*)(kv + fo) = k;
    const u32 vo = (u32)(1024 + (a * 2 + (g >> 1)) * 272 + (g & 1) * 8);
    *(u32x2*)(kv + vo + (l & 15) * 16) = v;
    if ((l & 15) == 0) { u32x2 one = {0x3f803f80u, 0x3f803f80u}; *(u32x2*)(kv + vo + 256) = one; }
  }
  STAMP(6);
  ST_OUT(1, ws);
}

#define GLDS_SC1(voff, srd, sb, GO, lb, LO)                                                                               \
  do { u32 tmp_;                                                                                                          \
    asm volatile("s_add_u32 m0, %4, %6\n\ts_add_u32 %0, %3, %5\n\tbuffer_load_dwordx4 %1, %2, %0 offen sc1 lds"         \
                 : "=&s"(tmp_) : "v"(voff), "s"(srd), "s"(sb), "s"(lb), "i"(GO), "i"(LO) : "memory", "scc"); } while (0)
#define GLD128_SC1(dst, voff, srd, sb, GO)                                                                                \
  do { u32 tmp_;                                                                                                          \
    asm volatile("s_add_u32 %1, %4, %5\n\tbuffer_load_dwordx4 %0, %2, %3, %1 offen sc1"                                 \
                 : "=v"(dst), "=&s"(tmp_) : "v"(voff), "s"(srd), "s"(sb), "i"(GO) : "memory", "scc"); } while (0)
#define GST_SC1(W, val, voff, srd, sb, GO)                                                                                \
  do { u32 tmp_;                                                                                                          \
    asm volatile("s_add_u32 %0, %4, %5\n\tbuffer_store_dword" W " %1, %2, %3, %0 offen sc1\n\ts_nop 1"                  \
                 : "=&s"(tmp_) : "v"(val), "v"(voff), "s"(srd), "s"(sb), "i"(GO) : "memory", "scc"); } while (0)
#define GTOUCH(voff, srd, sb, GO, lb)                                                                                     \
  do { u32 tmp_;                                                                                                          \
    asm volatile("s_mov_b32 m0, %4\n\ts_add_u32 %0, %3, %5\n\tbuffer_load_dword %1, %2, %0 offen lds"                   \
                 : "=&s"(tmp_) : "v"(voff), "s"(srd), "s"(sb), "s"(lb), "i"(GO) : "memory", "scc"); } while (0)
#define GLDS64(vaddr, lb, LO)                                                                                             \
    asm volatile("s_add_u32 m0, %1, %2\n\ts_nop 0\n\tglobal_load_lds_dwordx4 %0, off" :: "v"(vaddr), "s"(lb), "i"(LO) : "memory", "scc")

DI void signal_cnt(char* ws, int idx) {
  if (threadIdx.x == 0) __hip_atomic_fetch_add((u32*)(ws + WS_CNT) + idx * 64, 1u, __ATOMIC_RELAXED, __HIP_MEMORY_SCOPE_AGENT);
}
DI void wait_cnt(char* ws, int idx, u32 want, int w) {
  if (w == 0) {
    u32* c = (u32*)(ws + WS_CNT) + idx * 64;
    for (u32 spins = 0; spins < (1u << 20); ++spins) {
      if ((u32)__builtin_amdgcn_readfirstlane(__hip_atomic_load(c, __ATOMIC_RELAXED, __HIP_MEMORY_SCOPE_AGENT)) >= want) break;
      __builtin_amdgcn_s_sleep(1);
    }
    asm volatile("" ::: "memory");
  }
}

template <int PH>
__global__ __launch_bounds__(512) void kb_block(const u16* __restrict__ x, const u16* __restrict__ wo, const u16* __restrict__ wg,
                                                const u16* __restrict__ wu, const u16* __restrict__ wd, char* __restrict__ ws,
                                                u16* __restrict__ out) {
  __shared__ __attribute__((aligned(16))) char smem[163840];
  ST_DECL STAMP(0);
  const int bid = blockIdx.x, xcd = bid & 7, loc = bid >> 3;
  const int t = threadIdx.x, l = t & 63;
  const int w = __builtin_amdgcn_readfirstlane(t >> 6);
  const u32 lb = lds_base(smem);
  const u32x4 sws = make_srd(ws, WS_BYTES);
  const u32 lo16 = (u32)l * 16, zs = 0;
  const int hb = l >> 5;
  const int pm3 = loc >> 3, pn3 = loc & 7;
  const int lq = loc >= 28 ? 27 : loc;
  const int pan4 = (xcd >> 1) * 4 + lq / 7, pn4 = (xcd & 1) * 7 + lq % 7;
  constexpr u32 STG = KV_HEAD;
  constexpr u32 TAB = 159744, UVP = TAB + 2048, SCR = UVP;
  constexpr u32 XT = KV_HEAD, EX3 = XT + 8192, RST3 = 131072, RST4 = 157696;
  f32x16 zero;
#pragma unroll
  for (int i = 0; i < 16; ++i) zero[i] = 0.f;

  bf16x8 wf[32];
  const char* wsrc = (const char*)((l & 2) ? wu : wg) + (size_t)(l >> 2) * 3584 + (pn4 * 128 + w * 16 + (l & 1) * 8) * 2;
  const u32 stg_lb = lb + w * 3072;
  u32 stg_rd0 = (u32)(w * 3072 + (hb * 4 + (l >> 2 & 3)) * 64 + (l >> 4 & 1) * 32 + (l & 3) * 8), stg_rd1 = stg_rd0 + STG;
  PIN(stg_rd1);
#define WSLOT_OFF(p) ((((p) % 6) < 3 ? STG : 0u) + ((p) % 3) * 1024u)
#define WSTEP(p)                                                                                        \
  do {                                                                                                  \
    if constexpr ((p) >= 5) {                                                                                     \
      if ((p) <= 32) asm volatile("s_waitcnt vmcnt(4) lgkmcnt(0)" ::: "memory");                        \
      else if ((p) == 33) asm volatile("s_waitcnt vmcnt(3)" ::: "memory");                              \
      else if ((p) == 34) asm volatile("s_waitcnt vmcnt(2)" ::: "memory");                              \
      else if ((p) == 35) asm volatile("s_waitcnt vmcnt(1)" ::: "memory");                              \
      else asm volatile("s_waitcnt vmcnt(0)" ::: "memory");                                             \
      const u32 o_ = ((((p) - 5) % 6) < 3 ? stg_rd1 : stg_rd0) + (((p) - 5) % 3) * 1024;                \
      wf[(p) - 5] = lds_tr2(smem, o_, o_ + 512);                                                        \
    }                                                                                                   \
    if ((p) < 32) { GLDS64(wsrc, stg_lb, WSLOT_OFF(p)); wsrc += 16 * 3584; }                            \
  } while (0)

  const int tm = w & 1;
  const u32 wvo = (u32)((l >> 3) * 1024 + ((((l >> 2) ^ (l >> 4)) & 1) * 32 + (l & 3) * 8) * 2);
  const u32 wrd0 = (u32)((hb * 4 + (l >> 2 & 3)) * 128 + (l >> 3 & 1) * 64 + (l >> 4 & 1) * 32 + (l & 3) * 8);
  const int ncol = pn3 * 64 + (w >> 1 & 1) * 32 + (w >> 2) * 16;
  const u32 mvo = (u32)((ncol + hb * 4) * 4);
  const int rt3 = xcd * 8 + pm3 * 2;

  float tsb, tc1;
#define TAB_WRITE()                                                                                          \
  do { PIN(tsb); PIN(tc1);                                                                                   \
    const u32 to = TAB + (u32)((t >> 4) * 32 + (t >> 2 & 1) * 16 + ((t & 3) + 4 * (t >> 3 & 1)) * 2);        \
    *(LAS u16*)((LAS char*)smem + to) = (u16)(__float_as_uint(tc1) >> 16);                                   \
    *(LAS u16*)((LAS char*)smem + to + 1024) = (u16)(__float_as_uint(tsb) >> 16); } while (0)
#define TAB_LOAD() do { GLD32(tsb, (u32)t * 4, sws, zs, WS_MODF + 1536 * 4); GLD32(tc1, (u32)t * 4, sws, zs, WS_MODF + 2048 * 4); } while (0)
  const u32x4 so = make_srd(wo, 512 * 1024), sx = make_srd(x, 2048 * 1024), sd = make_srd(wd, 1792 * 1024);
#define P3_PIECE(j_)                                                                                         \
  do { if constexpr ((j_) < 8) GLDS(wvo, so, (u32)(w * 8192 + pn3 * 128), (j_) * 65536, lb + w * 1024, (j_) * 8192); \
    else { const u32 xvo_ = (u32)((l >> 3) * 1024 + (((l & 7) ^ ((4 * w + (l >> 4)) & 7)) * 16));            \
      GLDS(xvo_, sx, (u32)((rt3 * 32 + w * 8) * 1024 + pn3 * 128), 0, lb + w * 1024, XT); } } while (0)

  if (PH == 0 || PH == 2) {
    const int head = xcd * 4 + (loc >> 3), qt = (loc & 7) * 8 + w;
    const u32 ksb = (u32)(head * KV_HEAD + w * 1024), wlb = lb + w * 1024;
#define KVR(r) GLDS(lo16, sws, ksb, WS_KV + (r) * 8192, wlb, (r) * 8192)
    u32x4 qraw;
    GLD128(qraw, lo16, sws, (u32)(head * 64 + qt) * 1024, WS_QF);
    KVR(0); KVR(1);
    if (w < 4) KVR(16);
    KVR(2); KVR(3);
    const int r = l & 31;
    u32 krd = lo16;
    u32 vrd = (u32)(1024 + hb * 272 + (r < 16 ? r * 16 : (r == 16 ? 256 : 0x40000)));
    STAMP(1);
    WAITV(2);
    PIN(qraw);
    BAR();
    STAMP(2);
    const bf16x8 qf = __builtin_bit_cast(bf16x8, qraw);
    float m;
    bf16x8 kf = lds_frag(smem, krd);
    {
      f32x16 s0 = MFMA32(kf, qf, zero);
      m = s0[0];
#pragma unroll
      for (int i = 1; i < 16; ++i) m = fmaxf(m, s0[i]);
      m = fmaxf(m, __shfl_xor(m, 32));
    }
    f32x16 negm;
#pragma unroll
    for (int i = 0; i < 16; ++i) negm[i] = -m;
    f32x16 o = zero;
    f32x16 sc = MFMA32(kf, qf, negm);
    kf = lds_frag(smem, krd + KV_TILE);
    SFOR(j, 0, 64)
      if constexpr ((j & 1) == 0) __builtin_amdgcn_s_setprio(3 - (j >> 1 & 3));
      if (j == 5) { WAITV(10); BAR(); }
      if (j == 13) { WAITV(0); BAR(); }
      if (j == 14) STAMP(3);
      if constexpr (j < 6) { KVR(4 + 2 * j); KVR(5 + 2 * j); }
      if (PH == 0) {
        if constexpr (j >= 14 && j < 51) WSTEP(j - 14);
        if (j == 51) STAMP(21);
        if (j == 52) { BAR(); TAB_LOAD(); }
        if constexpr (j >= 52 && j < 61) P3_PIECE(j - 52);
      }
      if (j == 30 || j == 60) { krd += 30 * KV_TILE; vrd += 30 * KV_TILE; PIN(krd); PIN(vrd); }
      const u32 jo = (u32)(j % 30) * KV_TILE;
      const bf16x8 v0 = lds_frag(smem, vrd + jo), v1 = lds_frag(smem, vrd + jo + 544);
      f32x16 sn;
      if (j < 63) sn = MFMA32(kf, qf, negm);
      if (j < 62) kf = lds_frag(smem, krd + jo + 2 * KV_TILE);
      __builtin_amdgcn_sched_barrier(0);
#pragma unroll
      for (int i = 0; i < 11; ++i) sc[i] = __builtin_amdgcn_exp2f(sc[i]);
      __builtin_amdgcn_sched_barrier(0);
      o = __builtin_amdgcn_smfmac_f32_32x32x32_bf16(v0, __builtin_bit_cast(bf16x16, __builtin_shufflevector(sc, sc, 0, 1, 2, 3, 4, 5, 6, 7)), o, 0xDDDD, 0, 0);
      __builtin_amdgcn_sched_barrier(0);
#pragma unroll
      for (int i = 11; i < 16; ++i) sc[i] = __builtin_amdgcn_exp2f(sc[i]);
      o = __builtin_amdgcn_smfmac_f32_32x32x32_bf16(v1, __builtin_bit_cast(bf16x16, __builtin_shufflevector(sc, sc, 8, 9, 10, 11, 12, 13, 14, 15)), o, 0xDDDD, 0, 0);
      __builtin_amdgcn_sched_barrier(0);
      if (j < 63) sc = sn;
    SEND
    float lsum = shfl_idx(o[8], r);
    bool ok = lsum < 1e18f;
#pragma unroll
    for (int i = 0; i < 8; ++i) ok = ok && (fabsf(o[i]) < 3e38f);
    if (!__all(ok)) {
      asm volatile("" ::: "memory");
      const char* kg = ws + WS_KV + (size_t)head * KV_HEAD + lo16;
      const char* vg = ws + WS_KV + (size_t)head * KV_HEAD + 1024 + hb * 272 + (r == 16 ? 256 : (r & 15) * 16);
#pragma unroll 1
      for (int j = 0; j < 64; ++j) {
        const f32x16 sv = MFMA32(*(const bf16x8*)(kg + j * KV_TILE), qf, zero);
#pragma unroll
        for (int i = 0; i < 16; ++i) m = fmaxf(m, sv[i]);
      }
      m = fmaxf(m, __shfl_xor(m, 32));
#pragma unroll
      for (int i = 0; i < 16; ++i) negm[i] = -m;
      o = zero;
#pragma unroll 1
      for (int j = 0; j < 64; ++j) {
        f32x16 sv = MFMA32(*(const bf16x8*)(kg + j * KV_TILE), qf, negm);
        const bf16x8 v0 = *(const bf16x8*)(vg + j * KV_TILE), v1 = *(const bf16x8*)(vg + j * KV_TILE + 544);
#pragma unroll
        for (int i = 0; i < 16; ++i) sv[i] = __builtin_amdgcn_exp2f(sv[i]);
        o = __builtin_amdgcn_smfmac_f32_32x32x32_bf16(v0, __builtin_bit_cast(bf16x16, __builtin_shufflevector(sv, sv, 0, 1, 2, 3, 4, 5, 6, 7)), o, 0xDDDD, 0, 0);
        o = __builtin_amdgcn_smfmac_f32_32x32x32_bf16(v1, __builtin_bit_cast(bf16x16, __builtin_shufflevector(sv, sv, 8, 9, 10, 11, 12, 13, 14, 15)), o, 0xDDDD, 0, 0);
      }
      lsum = shfl_idx(o[8], r);
    }
    const float inv = 1.f / lsum;
    u32x4 ov;
#pragma unroll
    for (int e = 0; e < 4; ++e) ov[e] = pack2(o[2 * e] * inv, o[2 * e + 1] * inv);
    GST_SC1("x4", ov, lo16, sws, (u32)(qt * 32 + head) * 1024, WS_OF);
    STAMP(4);
    WAITV(0);
    STAMP(5);
    if (PH == 0) TAB_WRITE();
    BAR();
    STAMP(6);
    if (PH == 0) signal_cnt(ws, loc & 7);
  }

  if (PH == 4) {
    SFOR(p, 0, 37) WSTEP(p); SEND
    BAR();
  }

  f32x16 uv = zero;
  const u32 trd = TAB + (u32)((l >> 4 & 1) * 1024 + hb * 16);
#define UV_PART(k0)                                                                                          \
  do { bf16x8 tf_[16];                                                                                       \
    _Pragma("unroll") for (int i_ = 0; i_ < 4; ++i_) tf_[i_] = lds_frag(smem, trd + ((k0) + i_) * 32);       \
    _Pragma("unroll") for (int i_ = 0; i_ < 16; ++i_) {                                                      \
      if (i_ + 4 < 16) tf_[i_ + 4] = lds_frag(smem, trd + ((k0) + i_ + 4) * 32);                             \
      uv = MFMA32(wf[(k0) + i_], tf_[i_], uv); SB(); } } while (0)
  if (PH == 4) { TAB_LOAD(); WAITV(0); TAB_WRITE(); BAR(); UV_PART(0); }

  if (PH == 0 || PH == 3) {
    if (PH == 3) { SFOR(j, 0, 9) P3_PIECE(j); SEND }
    STAMP(7);
    if (PH == 0) { wait_cnt(ws, xcd, 32, w); BAR(); }
    STAMP(8);
    const u32 olo = (u32)((w >> 2) * 32768 + (w & 3) * 4096);
    const u32 osb = (u32)rt3 * 32768 + olo, olb = lb + olo;
#pragma unroll
    for (int s = 0; s < 2; ++s)
#pragma unroll
      for (int j = 0; j < 4; ++j) GLDS_SC1(lo16, sws, osb, WS_OF + (s * 16 + j) * 1024, olb, 65536 + (s * 16 + j) * 1024);
    u32x4 ga[2], cb[2];
#pragma unroll
    for (int e = 0; e < 2; ++e) { GLD128(ga[e], mvo, sws, zs, WS_MODF + 1024 * 4 + e * 32); GLD128(cb[e], mvo, sws, zs, WS_MODF + 2048 * 4 + e * 32); }
    if (PH == 0) UV_PART(0);
    const int tn = w >> 1 & 1, kh = w >> 2;
    u32 ord = 65536 + (u32)(tm * 32768 + kh * 8192) + lo16;
    PIN(ord);
    const u32 wrd = (wrd0 ^ (u32)(tn * 64)) + kh * 16384;
    f32x16 acc = zero;
#pragma unroll
    for (int s = 0; s < 2; ++s) {
      bf16x8 wq[8], oq[8];
      if (PH == 0) {
#pragma unroll
        for (int j = 0; j < 8; ++j) wq[j] = lds_tr2(smem, wrd + (s * 16 + j) * 2048, wrd + (s * 16 + j) * 2048 + 1024);
      }
      if (s == 0) WAITV(8); else WAITV(4);
      BAR();
      if (PH != 0) {
#pragma unroll
        for (int j = 0; j < 8; ++j) wq[j] = lds_tr2(smem, wrd + (s * 16 + j) * 2048, wrd + (s * 16 + j) * 2048 + 1024);
      }
#pragma unroll
      for (int j = 0; j < 4; ++j) oq[j] = lds_frag(smem, ord + (s * 16 + j) * 1024);
      SB();
#pragma unroll
      for (int j = 0; j < 8; ++j) {
        if (j + 4 < 8) oq[j + 4] = lds_frag(smem, ord + (s * 16 + j + 4) * 1024);
        acc = MFMA32(wq[j], oq[j], acc);
        SB();
      }
    }
    STAMP(9);
    LAS f32x4* ex = (LAS f32x4*)((LAS char*)smem + EX3);
    float mine[8];
#pragma unroll
    for (int i = 0; i < 8; ++i) mine[i] = kh ? acc[8 + i] : acc[i];
    {
      f32x4 a, b;
#pragma unroll
      for (int i = 0; i < 4; ++i) { a[i] = kh ? acc[i] : acc[8 + i]; b[i] = kh ? acc[4 + i] : acc[12 + i]; }
      ex[(w ^ 4) * 128 + l] = a; ex[(w ^ 4) * 128 + 64 + l] = b;
    }
    WAITV(0);
    BAR();
    {
      const f32x4 a = ex[w * 128 + l], b = ex[w * 128 + 64 + l];
#pragma unroll
      for (int i = 0; i < 4; ++i) { mine[i] += a[i]; mine[4 + i] += b[i]; }
    }
    const int row = tm * 32 + (l & 31);
    float xv[8];
    u32x4 x1p, yp;
#pragma unroll
    for (int e = 0; e < 2; ++e) {
      PIN(ga[e]); PIN(cb[e]);
      const u32x2 xr = *(const LAS u32x2*)((LAS char*)smem + XT + row * 128 + (((tn * 4 + kh * 2 + e) ^ (row >> 1 & 7)) * 16) + hb * 8);
#pragma unroll
      for (int h = 0; h < 2; ++h) {
        const u32 pk = pack2(bf_lo(xr[h]) + __uint_as_float(ga[e][2 * h]) * mine[4 * e + 2 * h], bf_hi(xr[h]) + __uint_as_float(ga[e][2 * h + 1]) * mine[4 * e + 2 * h + 1]);
        x1p[2 * e + h] = pk;
        xv[4 * e + 2 * h] = bf_lo(pk); xv[4 * e + 2 * h + 1] = bf_hi(pk);
        yp[2 * e + h] = pack2(bf_lo(pk) * __uint_as_float(cb[e][2 * h]), bf_hi(pk) * __uint_as_float(cb[e][2 * h + 1]));
      }
    }
    GST_SC1("x4", yp, lo16, sws, (u32)((rt3 + tm) * 32 + pn3 * 4 + tn * 2 + kh) * 1024, WS_Y);
    *(u32x4*)(ws + WS_X1 + (size_t)(bid * 512 + t) * 16) = x1p;
    float sum = 0.f;
#pragma unroll
    for (int i = 0; i < 8; ++i) sum += xv[i];
    sum = xor32_sum(sum);
    const float mean = sum * (1.f / 16.f);
    float sq = 0.f;
#pragma unroll
    for (int i = 0; i < 8; ++i) { const float d = xv[i] - mean; sq += d * d; }
    sq = xor32_sum(sq);
    LAS f32x2* sp = (LAS f32x2*)((LAS char*)smem + RST3);
    if (l < 32) sp[(w >> 1) * 64 + row] = (f32x2){mean, sq};
    BAR();
    if (w == 0) {
      const f32x2 p0 = sp[l], p1 = sp[64 + l], p2 = sp[128 + l], p3 = sp[192 + l];
      const float mu = 0.25f * ((p0[0] + p1[0]) + (p2[0] + p3[0]));
      const float d0 = p0[0] - mu, d1 = p1[0] - mu, d2 = p2[0] - mu, d3 = p3[0] - mu;
      const f32x2 o2 = {mu, (p0[1] + p1[1]) + (p2[1] + p3[1]) + 16.f * ((d0 * d0 + d1 * d1) + (d2 * d2 + d3 * d3))};
      GST_SC1("x2", o2, (u32)(hb * 2048 + (l & 31) * 8), sws, (u32)(rt3 * 2048 + pn3 * 256), WS_ST2);
    }
    STAMP(10);
    WAITV(0);
    BAR();
    STAMP(11);
    if (PH == 0) signal_cnt(ws, 8 + xcd * 2 + (pm3 >> 1));
  }

  const u32 dsb = (u32)(w * 16384 + pn3 * 128), dlb = lb + w * 2048;
#define WDPIECE(c, slot, h) GLDS(wvo, sd, dsb, (c) * 131072 + (h) * 8192, dlb, (slot) * 16384 + (h) * 1024)
#define WDISSUE(c, slot) do { GLDS(wvo, sd, dsb, (c) * 131072, dlb, (slot) * 16384); GLDS(wvo, sd, dsb, (c) * 131072 + 8192, dlb, (slot) * 16384 + 1024); } while (0)
  if ((PH == 0 || PH == 4) && loc < 28) {
    if (PH == 0) { wait_cnt(ws, 8 + pan4, 16, w); BAR(); }
    STAMP(12);
    const int rt4 = pan4 * 4;
    const u32 ysb = (u32)(rt4 * 32768 + w * 1024), ylb = lb + w * 1024;
#define YISSUE(a) _Pragma("unroll") for (int j = 0; j < 4; ++j) GLDS_SC1(lo16, sws, ysb, WS_Y + (a) * 32768 + j * 8192, ylb, (a) * 32768 + j * 8192)
    YISSUE(0);
    GLDS_SC1(lo16, sws, (u32)(rt4 * 2048 + w * 1024), WS_ST2, ylb, 131072);
    YISSUE(1);
    UV_PART(16);
    float uu[16], vv[16];
#pragma unroll
    for (int i = 0; i < 16; ++i) {
      const auto r = __builtin_amdgcn_permlane16_swap(__float_as_uint(uv[i]), __float_as_uint(uv[i]), false, false);
      uu[i] = __uint_as_float(r[0]); vv[i] = __uint_as_float(r[1]);
    }
    u32 yrd0 = lo16, yrd1 = lo16 + 65536;
    PIN(yrd1);
    const u32 asb = (u32)(rt4 * 112 + pn4 * 8 + w) * 1024;
#define YFRAG(n) lds_frag(smem, ((n) < 64 ? yrd0 : yrd1) + ((n) & 63) * 1024)
    WAITV(4);
    BAR();
    STAMP(13);
    bf16x8 yf[128];
#pragma unroll
    for (int n = 0; n < 4; ++n) yf[n] = YFRAG(n);
    SB();
    f32x16 acc = zero, accp = zero;
    f32x2 stp = {0.f, 0.f};
    float eg[8], eu[8], et[8];
    u32x4 ov;
    SFOR(n, 0, 160)
      constexpr int a = n >> 5, ks = n & 31;
      if (n == 0) STAMP(22);
      if (n == 26) STAMP(23);
      if (n == 56) STAMP(25);
      if (n == 36) STAMP(27);
      if (n == 68) STAMP(29);
      if (n == 100) STAMP(31);
      if (n == 26) { WAITV(8); BAR(); }
      if (n == 56) { if (PH == 0) WAITV(4); else WAITV(0); RAWBAR(); }
      if constexpr (n < 24 && n % 3 == 0) GLDS_SC1(lo16, sws, ysb, WS_Y + 65536 + (n / 3) * 8192, ylb, 65536 + (n / 3) * 8192);
      if constexpr (PH == 0) {
        if (n == 36 || n == 68 || n == 100) RAWBAR();
        if constexpr (n == 60) {
          const int rr = pm3 * 224 + w * 28 + (l < 28 ? l : 27);
          GTOUCH((u32)(rr + (rr < 256 ? 768 : 896)) * 1024, sd, (u32)(pn3 * 128), 0, lb + SCR);
        }
        if constexpr (n >= 36 && n < 52 && n % 4 == 0) WDPIECE((n - 36) / 8, (n - 36) / 8, (n - 36) / 4 & 1);
        if constexpr (n >= 68 && n < 84 && n % 4 == 0) WDPIECE(2 + (n - 68) / 8, 2 + (n - 68) / 8, (n - 68) / 4 & 1);
        if constexpr (n == 84 || n == 88) WDPIECE(8, 8, (n - 84) / 4);
        if constexpr (n >= 100 && n < 116 && n % 4 == 0) WDPIECE(4 + (n - 100) / 8, 4 + (n - 100) / 8, (n - 100) / 4 & 1);
      }
      if (n == 26) STAMP(24);
      if (n == 56) STAMP(26);
      if (n == 36) STAMP(28);
      if (n == 68) STAMP(30);
      if (n == 100) STAMP(32);
      if constexpr (n == 4) {
      const LAS f32x2* sp = (const LAS f32x2*)((LAS char*)smem + 131072 + (t >> 7) * 2048 + (t & 3) * 512 + (t >> 2 & 31) * 8);
      const f32x2 p0 = sp[0], p1 = sp[32];
      float mu = p0[0] + p1[0];
      mu += dpp_xor1(mu); mu += dpp_xor2(mu);
      mu *= 0.125f;
      float q = p0[1] + p1[1] + 64.f * ((p0[0] - mu) * (p0[0] - mu) + (p1[0] - mu) * (p1[0] - mu));
      q += dpp_xor1(q); q += dpp_xor2(q);
      const float rs = __builtin_amdgcn_rsqf(q * (1.f / 512.f) + 1e-5f);
      ((LAS f32x2*)((LAS char*)smem + RST4))[t >> 2] = (f32x2){rs, -mu * rs};
    }
      if (ks == 28 && a < 4) stp = ((const LAS f32x2*)((LAS char*)smem + RST4))[a * 32 + (l & 31)];
      if (ks == 0 && a > 0) { accp = acc; acc = zero; }
      if (n == 128) STAMP(14);
      if constexpr (n + 4 < 128) yf[n + 4] = YFRAG(n + 4);
      if constexpr (n < 128) acc = MFMA32(wf[ks], yf[n], acc);
      if constexpr (a > 0 && ks >= 2 && ks < 26) {
        constexpr int i = (ks - 2) / 3, ph = (ks - 2) % 3;
        if (ph == 0) {
          eg[i] = __builtin_fmaf(accp[i], stp[0], __builtin_fmaf(uu[i], stp[1], vv[i]));
          eu[i] = __builtin_fmaf(accp[8 + i], stp[0], __builtin_fmaf(uu[8 + i], stp[1], vv[8 + i]));
          et[i] = -1.4426950408889634f * eg[i];
        } else if (ph == 1) {
          et[i] = 1.f + __builtin_amdgcn_exp2f(et[i]);
          eg[i] *= eu[i];
        } else {
          eg[i] *= __builtin_amdgcn_rcpf(et[i]);
          if constexpr (i & 1) ov[i >> 1] = pack2(eg[i - 1], eg[i]);
        }
      }
      if constexpr (a > 0 && ks == 26) GST_SC1("x4", ov, lo16, sws, asb, WS_ACT + (a - 1) * 112 * 1024);
      if (n < 128) SB();
    SEND
    STAMP(15);
    WAITV(0);
    BAR();
    STAMP(16);
    if (PH == 0) signal_cnt(ws, 24 + pan4);
  } else if (PH == 0) {
    WDISSUE(0, 0); WDISSUE(1, 1); WDISSUE(2, 2); WDISSUE(3, 3); WDISSUE(8, 8); WDISSUE(4, 4); WDISSUE(5, 5);
  }

  if (PH == 0 || PH == 5) {
    if (PH == 5) { WDISSUE(0, 0); WDISSUE(1, 1); WDISSUE(2, 2); WDISSUE(3, 3); WDISSUE(8, 8); WDISSUE(4, 4); WDISSUE(5, 5); }
    if (PH == 5 || w != 0) { WDISSUE(6, 6); WDISSUE(7, 7); WDISSUE(9, 9); WAITV(6); }
    STAMP(17);
    if (PH == 0) {
      wait_cnt(ws, 24 + xcd * 2 + (pm3 >> 1), 14, w);
      if (w == 0) { WAITV(0); WDISSUE(6, 6); WDISSUE(7, 7); WDISSUE(9, 9); }
    }
    BAR();
    STAMP(18);
    const int kq = w >> 1;
    const u32 asb = (u32)((rt3 + tm) * 112 + kq * 2) * 1024;
    u32x4 af[14][2];
#define AISSUE(c) do { GLD128_SC1(af[c][0], lo16, sws, asb, WS_ACT + (c) * 8192); GLD128_SC1(af[c][1], lo16, sws, asb, WS_ACT + (c) * 8192 + 1024); } while (0)
#pragma unroll
    for (int c = 0; c < 6; ++c) AISSUE(c);
    u32 wra[3][2];
#pragma unroll
    for (int i = 0; i < 3; ++i) { wra[i][0] = wrd0 + kq * 4096 + i * 65536; wra[i][1] = wra[i][0] ^ 64u; PIN(wra[i][0]); PIN(wra[i][1]); }
    f32x16 acc[2] = {zero, zero};
    bf16x8 dq[14][4];
    u32x4 x1p, gb[2];
#define DFRAG(c) _Pragma("unroll") for (int i_ = 0; i_ < 4; ++i_) {                                              \
      const u32 o_ = wra[((c) % 10) >> 2][i_ & 1] + (((c) % 10) & 3) * 16384 + (i_ >> 1) * 2048; dq[c][i_] = lds_tr2(smem, o_, o_ + 1024); }
    DFRAG(0);
#pragma unroll
    for (int c = 0; c < 14; ++c) {
      if (c < 5) WAITV(10); else if (c < 8) WAITV(18); else if (c == 8) WAITV(21); else if (c == 9) WAITV(11);
      else if (c == 10) WAITV(9); else if (c == 11) WAITV(7); else if (c == 12) WAITV(5); else WAITV(0);
      if (c == 0) STAMP(19);
      if (c == 4 || c == 9) RAWBAR();
      if (c == 4) { WDISSUE(10, 0); WDISSUE(11, 1); WDISSUE(12, 2); WDISSUE(13, 3); }
      if (c < 8) AISSUE(c + 6);
      if (c == 7) {
        GLD128(x1p, (u32)t * 16, sws, (u32)bid * 8192, WS_X1);
        GLD128(gb[0], mvo, sws, zs, WS_MODF + 2560 * 4); GLD128(gb[1], mvo, sws, zs, WS_MODF + 2560 * 4 + 32);
      }
      if (c < 13) { DFRAG(c + 1); }
      PIN(af[c][0]); PIN(af[c][1]);
      SB();
#pragma unroll
      for (int i = 0; i < 4; ++i) acc[i & 1] = MFMA32(dq[c][i], __builtin_bit_cast(bf16x8, af[c][i >> 1]), acc[i & 1]);
      SB();
    }
    STAMP(20);
    LAS f32x4* ex = (LAS f32x4*)((LAS char*)smem + 65536);
#pragma unroll
    for (int qd = 0; qd < 4; ++qd)
#pragma unroll
      for (int h = 0; h < 2; ++h) {
        const int b = (qd >> 1) * 8 + h * 4;
        ex[(((tm + 2 * qd) * 4 + kq) * 2 + h) * 64 + l] = (f32x4){acc[qd & 1][b], acc[qd & 1][b + 1], acc[qd & 1][b + 2], acc[qd & 1][b + 3]};
      }
    BAR();
    u32 res[4];
    PIN(x1p);
#pragma unroll
    for (int h = 0; h < 2; ++h) {
      PIN(gb[h]);
      const f32x4 s = (ex[((w * 4 + 0) * 2 + h) * 64 + l] + ex[((w * 4 + 1) * 2 + h) * 64 + l]) + (ex[((w * 4 + 2) * 2 + h) * 64 + l] + ex[((w * 4 + 3) * 2 + h) * 64 + l]);
      res[2 * h] = pack2(bf_lo(x1p[2 * h]) + __uint_as_float(gb[h][0]) * s[0], bf_hi(x1p[2 * h]) + __uint_as_float(gb[h][1]) * s[1]);
      res[2 * h + 1] = pack2(bf_lo(x1p[2 * h + 1]) + __uint_as_float(gb[h][2]) * s[2], bf_hi(x1p[2 * h + 1]) + __uint_as_float(gb[h][3]) * s[3]);
    }
    const auto r0 = __builtin_amdgcn_permlane32_swap(res[0], res[2], false, false);
    const auto r1 = __builtin_amdgcn_permlane32_swap(res[1], res[3], false, false);
    const u32x4 ov = {r0[0], r1[0], r0[1], r1[1]};
    *(u32x4*)((char*)out + (size_t)((rt3 + tm) * 32 + (l & 31)) * 1024 + (ncol + hb * 8) * 2) = ov;
  }
  ST_OUT(PH == 0 ? 2 : PH, ws);
}

#define FUSED 1
extern "C" void kernel_launch(void* const* d_in, const int* in_sizes, int n_in, void* d_out, int out_size, void* d_ws, size_t ws_size,
                              hipStream_t stream) {
  const u16* x = (const u16*)d_in[0];
  const u16* wqkv = (const u16*)d_in[1];
  const u16* wo = (const u16*)d_in[2];
  const u16* wg = (const u16*)d_in[4];
  const u16* wu = (const u16*)d_in[5];
  const u16* wd = (const u16*)d_in[6];
  const u16* c = (const u16*)d_in[7];
  const u16* wmod = (const u16*)d_in[8];
  char* ws = (char*)d_ws;
  k0_mod<<<448, 256, 0, stream>>>(c, wmod, x, (float*)(ws + WS_MODP), (f32x2*)(ws + WS_ST1));
  k1_qkv<<<256, 512, 0, stream>>>(x, wqkv, (const float*)(ws + WS_ST1), ws);
  kb_block<0><<<256, 512, 0, stream>>>(x, wo, wg, wu, wd, ws, (u16*)d_out);
}
```

```cpp
#include <hip/hip_runtime.h>
#include <hip/hip_bf16.h>

typedef unsigned short u16;
typedef unsigned int u32;
typedef u32 u32x2 __attribute__((ext_vector_type(2)));
typedef u32 u32x4 __attribute__((ext_vector_type(4)));
typedef float f32x2 __attribute__((ext_vector_type(2)));
typedef float f32x4 __attribute__((ext_vector_type(4)));
typedef float f32x8 __attribute__((ext_vector_type(8)));
typedef float f32x16 __attribute__((ext_vector_type(16)));
typedef __bf16 bf16x2 __attribute__((ext_vector_type(2)));
typedef __bf16 bf16x8 __attribute__((ext_vector_type(8)));
typedef __bf16 bf16x16 __attribute__((ext_vector_type(16)));
typedef short s16x4 __attribute__((ext_vector_type(4)));
typedef short s16x8 __attribute__((ext_vector_type(8)));
#define PRIO_MODE 1
#define NO_WSTEP 0
#define FUSED 1
#define CODE_PF 1
#define LAS __attribute__((address_space(3)))
#define DI __device__ __forceinline__

constexpr u32 WS_MODP = 0;
constexpr u32 WS_MODF = 0xC000;
constexpr u32 WS_ST1  = 0x10000;
constexpr u32 WS_FLG  = 0x20000;
constexpr u32 WS_QF   = 0x100000;
constexpr u32 WS_KV   = 0x300000;
constexpr u32 WS_OF   = 0x800000;
constexpr u32 WS_X1   = 0xA00000;
constexpr u32 WS_Y    = 0xC00000;
constexpr u32 WS_ST2  = 0xE00000;
constexpr u32 WS_ACT  = 0xF00000;
constexpr u32 WS_BYTES = 0x1600000;
constexpr int CODE_LINES = 300;
constexpr int KV_TILE = 2112, KV_HEAD = 64 * KV_TILE;

template <int I> struct IC { static constexpr int value = I; };
template <int B, int E, class F> DI void static_for(F&& f) { if constexpr (B < E) { f(IC<B>{}); static_for<B + 1, E>(f); } }
#define SFOR(n, B, E) static_for<B, E>([&](auto ic_) __attribute__((always_inline)) { constexpr int n = decltype(ic_)::value;
#define SEND });

DI float bf_lo(u32 u) { return __uint_as_float(u << 16); }
DI float bf_hi(u32 u) { return __uint_as_float(u & 0xffff0000u); }
DI u32 pack2(float a, float b) { bf16x2 r = {(__bf16)a, (__bf16)b}; return __builtin_bit_cast(u32, r); }
DI float bfround(float a) { return (float)(__bf16)a; }
DI float xor32_sum(float v) {
  const auto r = __builtin_amdgcn_permlane32_swap(__float_as_uint(v), __float_as_uint(v), false, false);
  return __uint_as_float(r[0]) + __uint_as_float(r[1]);
}
DI float dpp_xor1(float v) { return __int_as_float(__builtin_amdgcn_mov_dpp(__float_as_int(v), 0xB1, 0xF, 0xF, true)); }
DI float dpp_xor2(float v) { return __int_as_float(__builtin_amdgcn_mov_dpp(__float_as_int(v), 0x4E, 0xF, 0xF, true)); }
DI float shfl_idx(float v, int srcLane) { return __int_as_float(__builtin_amdgcn_ds_bpermute(srcLane << 2, __float_as_int(v))); }

DI u32x4 make_srd(const void* p, u32 bytes) {
  const size_t a = (size_t)p;
  u32x4 r = {(u32)a, (u32)(a >> 32) & 0xffffu, bytes, 0x20000u};
  return r;
}
#define GLDS(voff, srd, sb, GO, lb, LO)                                                                                   \
  do { u32 tmp_;                                                                                                          \
    asm volatile("s_add_u32 m0, %4, %6\n\ts_add_u32 %0, %3, %5\n\tbuffer_load_dwordx4 %1, %2, %0 offen lds"            \
                 : "=&s"(tmp_) : "v"(voff), "s"(srd), "s"(sb), "s"(lb), "i"(GO), "i"(LO) : "memory", "scc"); } while (0)
#define GLD(W, dst, voff, srd, sb, GO)                                                                                    \
  do { u32 tmp_;                                                                                                          \
    asm volatile("s_add_u32 %1, %4, %5\n\tbuffer_load_dword" W " %0, %2, %3, %1 offen"                                   \
                 : "=v"(dst), "=&s"(tmp_) : "v"(voff), "s"(srd), "s"(sb), "i"(GO) : "memory", "scc"); } while (0)
#define GLD128(dst, voff, srd, sb, GO) GLD("x4", dst, voff, srd, sb, GO)
#define GLD64(dst, voff, srd, sb, GO) GLD("x2", dst, voff, srd, sb, GO)
#define GLD32(dst, voff, srd, sb, GO) GLD("", dst, voff, srd, sb, GO)
#define WAITV(N) asm volatile("s_waitcnt vmcnt(" #N ")" ::: "memory")
#define PIN(x) asm volatile("" : "+v"(x))
#define BAR() do { asm volatile("s_waitcnt lgkmcnt(0)" ::: "memory"); __builtin_amdgcn_s_barrier(); asm volatile("" ::: "memory"); } while (0)

#define ST_DECL
#define STAMP(i)
#define ST_OUT(kid, wsp)

#define CODE_ADDR(sym) ({ unsigned long long pc_; int off_;                                                               \
    asm volatile("s_getpc_b64 %0\n.Lca%=:\n\ts_mov_b32 %1, " sym "-.Lca%=" : "=s"(pc_), "=s"(off_)); (const char*)(pc_ + (long long)off_); })

DI bf16x8 lds_frag(const char* smem, u32 off) { return *(const LAS bf16x8*)((const LAS char*)smem + off); }
DI bf16x8 lds_tr2(const char* smem, u32 off0, u32 off1) {
  s16x4 a = __builtin_amdgcn_ds_read_tr16_b64_v4i16((LAS s16x4*)((LAS char*)smem + off0));
  s16x4 b = __builtin_amdgcn_ds_read_tr16_b64_v4i16((LAS s16x4*)((LAS char*)smem + off1));
  s16x8 r = __builtin_shufflevector(a, b, 0, 1, 2, 3, 4, 5, 6, 7);
  return __builtin_bit_cast(bf16x8, r);
}
DI u32 lds_base(const char* smem) { return (u32)(size_t)(const LAS char*)smem; }
#define SB() __builtin_amdgcn_sched_barrier(0)
#define RAWBAR() do { asm volatile("" ::: "memory"); __builtin_amdgcn_s_barrier(); asm volatile("" ::: "memory"); } while (0)
#define MFMA32(a, b, c) __builtin_amdgcn_mfma_f32_32x32x16_bf16((a), (b), (c), 0, 0, 0)
#define MFMA16(a, b, c) __builtin_amdgcn_mfma_f32_16x16x32_bf16((a), (b), (c), 0, 0, 0)

extern "C" __global__ __launch_bounds__(256) void k0_mod(const u16* __restrict__ c, const u16* __restrict__ wmod, const u16* __restrict__ x,
                                              float* __restrict__ modp, f32x2* __restrict__ st1) {
  __shared__ float red[4][64];
  ST_DECL STAMP(0);
  const int b = blockIdx.x, t = threadIdx.x, w = t >> 6, l = t & 63;
  if (b < 192) {
    const int cb = b % 48, kq = b / 48, c8 = t & 7, r = t >> 3;
    u32x4 v[4]; float s[4];
#pragma unroll
    for (int i = 0; i < 4; ++i) {
      const int row = kq * 128 + i * 32 + r;
      v[i] = *(const u32x4*)(wmod + (size_t)row * 3072 + cb * 64 + c8 * 8);
      s[i] = __uint_as_float((u32)c[row] << 16);
    }
    float acc[8];
#pragma unroll
    for (int e = 0; e < 8; ++e) acc[e] = 0.f;
#pragma unroll
    for (int i = 0; i < 4; ++i) {
      const float sv = bfround(s[i] / (1.f + __expf(-s[i])));
#pragma unroll
      for (int e = 0; e < 4; ++e) { acc[2 * e] += sv * bf_lo(v[i][e]); acc[2 * e + 1] += sv * bf_hi(v[i][e]); }
    }
#pragma unroll
    for (int e = 0; e < 8; ++e) {
      acc[e] += __shfl_xor(acc[e], 8); acc[e] += __shfl_xor(acc[e], 16); acc[e] += __shfl_xor(acc[e], 32);
    }
    if (l < 8) {
#pragma unroll
      for (int e = 0; e < 8; ++e) red[w][l * 8 + e] = acc[e];
    }
    __syncthreads();
    if (t < 64) modp[(cb * 64 + t) * 4 + kq] = (red[0][t] + red[1][t]) + (red[2][t] + red[3][t]);
  } else {
    const int row0 = (b - 192) * 8 + w * 2;
    u32x4 v[2];
#pragma unroll
    for (int i = 0; i < 2; ++i) v[i] = *(const u32x4*)(x + (size_t)(row0 + i) * 512 + l * 8);
#pragma unroll
    for (int i = 0; i < 2; ++i) {
      float f[8];
#pragma unroll
      for (int e = 0; e < 4; ++e) { f[2 * e] = bf_lo(v[i][e]); f[2 * e + 1] = bf_hi(v[i][e]); }
      float sum = 0.f;
#pragma unroll
      for (int e = 0; e < 8; ++e) sum += f[e];
#pragma unroll
      for (int o = 1; o < 64; o <<= 1) sum += __shfl_xor(sum, o);
      const float mean = sum * (1.f / 512.f);
      float sq = 0.f;
#pragma unroll
      for (int e = 0; e < 8; ++e) { const float d = f[e] - mean; sq += d * d; }
#pragma unroll
      for (int o = 1; o < 64; o <<= 1) sq += __shfl_xor(sq, o);
      if (l == 0) { f32x2 o2 = {mean, __builtin_amdgcn_rsqf(sq * (1.f / 512.f) + 1e-5f)}; st1[row0 + i] = o2; }
    }
  }
  ST_OUT(0, modp);
  {
    const char* cp = CODE_ADDR("k1_qkv");
    const int line = (b >> 3) * 8 + (l & 7);
    u32 dummy;
    if (w == 3 && line < CODE_LINES) asm volatile("global_load_dword %0, %1, %2" : "=v"(dummy) : "v"(line * 128), "s"(cp) : "memory");
  }
}

DI u32x4 modulate8(const u32x4 v, float rs, float mr, const float* c1, const float* sh) {
  u32x4 o;
#pragma unroll
  for (int e = 0; e < 4; ++e) {
    const float a = __builtin_fmaf(__builtin_fmaf(bf_lo(v[e]), rs, mr), c1[2 * e], sh[2 * e]);
    const float b = __builtin_fmaf(__builtin_fmaf(bf_hi(v[e]), rs, mr), c1[2 * e + 1], sh[2 * e + 1]);
    o[e] = pack2(a, b);
  }
  return o;
}

extern "C" __global__ __launch_bounds__(512) void k1_qkv(const u16* __restrict__ x, const u16* __restrict__ wqkv, const float* __restrict__ st1,
                                              char* __restrict__ ws) {
  __shared__ __attribute__((aligned(16))) char smem[163840];
  constexpr u32 WOFF = 65536, WSLOT = 24576;
  ST_DECL STAMP(0);
  const int bid = blockIdx.x, xcd = bid & 7, loc = bid >> 3;
  const int pm = (xcd >> 1) * 8 + (loc >> 2), cg = (xcd & 1) * 4 + (loc & 3);
  const int t = threadIdx.x, l = t & 63;
  const int w = __builtin_amdgcn_readfirstlane(t >> 6);
  const u32 lb = lds_base(smem);
  const u32x4 sws = make_srd(ws, WS_BYTES), sx = make_srd(x, 2048 * 1024), sw = make_srd(wqkv, 512 * 3072);
  const u32 zs = 0;
  u32x4 fin, mp[2];
  const u32 t16 = (u32)t * 16;
  GLD128(mp[0], t16, sws, zs, WS_MODP);
  GLD128(mp[1], t16, sws, zs, WS_MODP + 8192);
  GLD128(fin, t16, sws, (u32)(bid < 6 ? bid : 0) * 8192, WS_MODP);
  u32x4 xa[8];
  const u32 lo16 = (u32)l * 16;
  const u32 xsb = (u32)(pm * 64 + w * 8) * 1024;
#pragma unroll
  for (int i = 0; i < 8; ++i) GLD128(xa[i], lo16, sx, xsb, i * 1024);
  const u32 wvo = (u32)((8 * (l >> 5) + (l >> 3 & 3)) * 3072 + ((((l >> 1) ^ (l >> 4)) & 3) * 16 + (l & 1) * 8) * 2);
  const u32 wsb = (u32)((32 * (w >> 2) + 16 * (w & 1) + 4 * (w >> 1 & 1)) * 3072 + cg * 128), wlb = lb + w * 1024;
#define K1_ISSUE(kc) _Pragma("unroll") for (int c = 0; c < 3; ++c) GLDS(wvo, sw, wsb, (kc) * 64 * 3072 + c * 1024, wlb, WOFF + ((kc) & 3) * WSLOT + c * 8192)
  K1_ISSUE(0);
  float rs[8], mr[8];
#pragma unroll
  for (int i = 0; i < 8; ++i) { rs[i] = st1[(pm * 64 + w * 8 + i) * 2 + 1]; mr[i] = -st1[(pm * 64 + w * 8 + i) * 2] * rs[i]; }
  STAMP(1);
  WAITV(11);
  PIN(fin); PIN(mp[0]); PIN(mp[1]);
#define SUM4(v) ((__uint_as_float(v[0]) + __uint_as_float(v[1])) + (__uint_as_float(v[2]) + __uint_as_float(v[3])))
  ((LAS float*)(LAS char*)smem)[t] = bfround(1.f + bfround(SUM4(mp[1])));
  ((LAS float*)(LAS char*)smem)[512 + t] = bfround(SUM4(mp[0]));
  {
    float f = bfround(SUM4(fin));
    if (bid == 4) f = bfround(1.f + f);
    if (bid < 6) ((float*)(ws + WS_MODF))[bid * 512 + t] = f;
    if (t < 384) ((u32*)(ws + WS_FLG))[bid * 384 + t] = 0u;
  }
  BAR();
  float c1[8], sh[8];
  {
    const LAS f32x4* tc = (const LAS f32x4*)((LAS char*)smem + l * 32);
    const f32x4 a0 = tc[0], a1 = tc[1], b0 = tc[128], b1 = tc[129];
#pragma unroll
    for (int e = 0; e < 4; ++e) { c1[e] = a0[e]; c1[4 + e] = a1[e]; sh[e] = b0[e]; sh[4 + e] = b1[e]; }
  }
  K1_ISSUE(1);
  WAITV(6);
  BAR();
  STAMP(2);
  const u32 awr = (u32)(((w >> 1) * 16 + (l >> 2)) * 1024 + (l & 3) * 256 + (w & 1) * 128);
#pragma unroll
  for (int i = 0; i < 8; ++i) {
    PIN(xa[i]);
    *(LAS u32x4*)((LAS char*)smem + awr + ((i ^ (l & 7)) << 4)) = modulate8(xa[i], rs[i], mr[i], c1, sh);
    if (i == 1) { K1_ISSUE(2); }
    if (i == 4) { K1_ISSUE(3); }
  }
  const int tm = w & 1, hd = w >> 1, g = l >> 4;
  u32 ard[2];
#pragma unroll
  for (int e = 0; e < 2; ++e) ard[e] = (u32)(tm * 32768 + e * 1024 + g * 256 + (((l & 15) ^ (e * 4 + g)) << 4));
  u32 brd = WOFF + (u32)((l >> 2) * 128 + ((hd ^ (l >> 3 & 3)) * 32) + (l & 3) * 8);
  PIN(brd);
  f32x4 acc[2][3];
#pragma unroll
  for (int a = 0; a < 2; ++a)
#pragma unroll
    for (int c = 0; c < 3; ++c) acc[a][c] = (f32x4){0.f, 0.f, 0.f, 0.f};
  WAITV(9);
  BAR();
  STAMP(3);
#pragma unroll
  for (int kc = 0; kc < 8; ++kc) {
    bf16x8 hf[2][2], wf[2][3];
#pragma unroll
    for (int e = 0; e < 2; ++e) {
#pragma unroll
      for (int a = 0; a < 2; ++a) hf[e][a] = lds_frag(smem, ard[e] + (a * 16 + kc * 2) * 1024);
#pragma unroll
      for (int c = 0; c < 3; ++c) {
        const u32 o = brd + (kc & 3) * WSLOT + c * 8192 + e * 4096;
        wf[e][c] = lds_tr2(smem, o, o + 2048);
      }
    }
#pragma unroll
    for (int e = 0; e < 2; ++e)
#pragma unroll
      for (int a = 0; a < 2; ++a) {
        acc[a][0] = MFMA16(wf[e][0], hf[e][a], acc[a][0]);
        acc[a][1] = MFMA16(wf[e][1], hf[e][a], acc[a][1]);
        acc[a][2] = MFMA16(hf[e][a], wf[e][2], acc[a][2]);
      }
    if (kc < 7) {
      if (kc < 5) WAITV(6); else if (kc == 5) WAITV(3); else WAITV(0);
      BAR();
      if (kc < 4) { K1_ISSUE(kc + 4); }
    }
    if (kc == 3) STAMP(4);
  }
  STAMP(5);
  const int head = cg * 4 + hd, tile = pm * 2 + tm;
  constexpr float QS = 0.25f * 1.4426950408889634f;
  char* kv = ws + WS_KV + (size_t)head * KV_HEAD + tile * KV_TILE;
#pragma unroll
  for (int a = 0; a < 2; ++a) {
    const u32 fo = (u32)((a * 16 + (l & 15) + 32 * (g >> 1)) * 16 + (g & 1) * 8);
    u32x2 q = {pack2(acc[a][0][0] * QS, acc[a][0][1] * QS), pack2(acc[a][0][2] * QS, acc[a][0][3] * QS)};
    u32x2 k = {pack2(acc[a][1][0], acc[a][1][1]), pack2(acc[a][1][2], acc[a][1][3])};
    u32x2 v = {pack2(acc[a][2][0], acc[a][2][1]), pack2(acc[a][2][2], acc[a][2][3])};
    *(u32x2*)(ws + WS_QF + (size_t)(head * 64 + tile) * 1024 + fo) = q;
    *(u32x2*)(kv + fo) = k;
    const u32 vo = (u32)(1024 + (a * 2 + (g >> 1)) * 272 + (g & 1) * 8);
    *(u32x2*)(kv + vo + (l & 15) * 16) = v;
    if ((l & 15) == 0) { u32x2 one = {0x3f803f80u, 0x3f803f80u}; *(u32x2*)(kv + vo + 256) = one; }
  }
  STAMP(6);
  ST_OUT(1, ws);
}

#define GLDS_SC1(voff, srd, sb, GO, lb, LO)                                                                               \
  do { u32 tmp_;                                                                                                          \
    asm volatile("s_add_u32 m0, %4, %6\n\ts_add_u32 %0, %3, %5\n\tbuffer_load_dwordx4 %1, %2, %0 offen sc1 lds"         \
                 : "=&s"(tmp_) : "v"(voff), "s"(srd), "s"(sb), "s"(lb), "i"(GO), "i"(LO) : "memory", "scc"); } while (0)
#define GLD128_SC1(dst, voff, srd, sb, GO)                                                                                \
  do { u32 tmp_;                                                                                                          \
    asm volatile("s_add_u32 %1, %4, %5\n\tbuffer_load_dwordx4 %0, %2, %3, %1 offen sc1"                                 \
                 : "=v"(dst), "=&s"(tmp_) : "v"(voff), "s"(srd), "s"(sb), "i"(GO) : "memory", "scc"); } while (0)
#define GST_SC1(W, val, voff, srd, sb, GO)                                                                                \
  do { u32 tmp_;                                                                                                          \
    asm volatile("s_add_u32 %0, %4, %5\n\tbuffer_store_dword" W " %1, %2, %3, %0 offen sc1\n\ts_nop 1"                  \
                 : "=&s"(tmp_) : "v"(val), "v"(voff), "s"(srd), "s"(sb), "i"(GO) : "memory", "scc"); } while (0)
#define GTOUCH(voff, srd, sb, GO, lb)                                                                                     \
  do { u32 tmp_;                                                                                                          \
    asm volatile("s_mov_b32 m0, %4\n\ts_add_u32 %0, %3, %5\n\tbuffer_load_dword %1, %2, %0 offen lds"                   \
                 : "=&s"(tmp_) : "v"(voff), "s"(srd), "s"(sb), "s"(lb), "i"(GO) : "memory", "scc"); } while (0)
#define GLDS64(vaddr, lb, LO)                                                                                             \
    asm volatile("s_add_u32 m0, %1, %2\n\ts_nop 0\n\tglobal_load_lds_dwordx4 %0, off" :: "v"(vaddr), "s"(lb), "i"(LO) : "memory", "scc")

DI void raise_flag(char* ws, int h, bool on, int cons, int idx) {
  if (on) __hip_atomic_store((u32*)(ws + WS_FLG) + (cons * 3 + h) * 128 + idx, 1u, __ATOMIC_RELAXED, __HIP_MEMORY_SCOPE_AGENT);
}
DI void wait_flags(char* ws, int h, int nlanes, int w) {
  if (w == 0) {
    const int l = threadIdx.x & 63;
    unsigned long long* c = (unsigned long long*)(ws + WS_FLG + (blockIdx.x * 3 + h) * 512) + (l < nlanes ? l : 0);
#pragma unroll 1
    for (u32 spins = 0; spins < (1u << 17); ++spins) {
      if (__all(__hip_atomic_load(c, __ATOMIC_RELAXED, __HIP_MEMORY_SCOPE_AGENT) == 0x100000001ull)) break;
      __builtin_amdgcn_s_sleep(1);
    }
    asm volatile("" ::: "memory");
  }
}

template <int PH>
DI void kb_body(const u16* __restrict__ x, const u16* __restrict__ wo, const u16* __restrict__ wg, const u16* __restrict__ wu,
                const u16* __restrict__ wd, char* __restrict__ ws, u16* __restrict__ out, char* smem) {
  ST_DECL STAMP(0);
  const int bid = blockIdx.x, xcd = bid & 7, loc = bid >> 3;
  const int t = threadIdx.x, l = t & 63;
  const int w = __builtin_amdgcn_readfirstlane(t >> 6);
  const u32 lb = lds_base(smem);
  const u32x4 sws = make_srd(ws, WS_BYTES);
  const u32 lo16 = (u32)l * 16, zs = 0;
  const int hb = l >> 5;
  const int pm3 = loc >> 3, pn3 = loc & 7;
  const int lq = loc >= 28 ? 27 : loc;
  const int pan4 = (xcd >> 1) * 4 + lq / 7, pn4 = (xcd & 1) * 7 + lq % 7;
  constexpr u32 STG = KV_HEAD;
  constexpr u32 TAB = 159744, UVP = TAB + 2048, SCR = UVP;
  constexpr u32 XT = KV_HEAD, EX3 = XT + 8192, RST3 = 131072, RST4 = 157696;
  f32x16 zero;
#pragma unroll
  for (int i = 0; i < 16; ++i) zero[i] = 0.f;

  bf16x8 wf[32];
  const char* wsrc = (const char*)((l & 2) ? wu : wg) + (size_t)(l >> 2) * 3584 + (pn4 * 128 + w * 16 + (l & 1) * 8) * 2;
  const u32 stg_lb = lb + w * 3072;
  u32 stg_rd0 = (u32)(w * 3072 + (hb * 4 + (l >> 2 & 3)) * 64 + (l >> 4 & 1) * 32 + (l & 3) * 8), stg_rd1 = stg_rd0 + STG;
  PIN(stg_rd1);
#define WSLOT_OFF(p) ((((p) % 6) < 3 ? STG : 0u) + ((p) % 3) * 1024u)
#define WSTEP(p)                                                                                        \
  do {                                                                                                  \
    if constexpr ((p) >= 5) {                                                                                     \
      if ((p) <= 32) asm volatile("s_waitcnt vmcnt(4) lgkmcnt(0)" ::: "memory");                        \
      else if ((p) == 33) asm volatile("s_waitcnt vmcnt(3)" ::: "memory");                              \
      else if ((p) == 34) asm volatile("s_waitcnt vmcnt(2)" ::: "memory");                              \
      else if ((p) == 35) asm volatile("s_waitcnt vmcnt(1)" ::: "memory");                              \
      else asm volatile("s_waitcnt vmcnt(0)" ::: "memory");                                             \
      const u32 o_ = ((((p) - 5) % 6) < 3 ? stg_rd1 : stg_rd0) + (((p) - 5) % 3) * 1024;                \
      wf[(p) - 5] = lds_tr2(smem, o_, o_ + 512);                                                        \
    }                                                                                                   \
    if ((p) < 32) { GLDS64(wsrc, stg_lb, WSLOT_OFF(p)); wsrc += 16 * 3584; }                            \
  } while (0)

  const int tm = w & 1;
  const u32 wvo = (u32)((l >> 3) * 1024 + ((((l >> 2) ^ (l >> 4)) & 1) * 32 + (l & 3) * 8) * 2);
  const u32 wrd0 = (u32)((hb * 4 + (l >> 2 & 3)) * 128 + (l >> 3 & 1) * 64 + (l >> 4 & 1) * 32 + (l & 3) * 8);
  const int ncol = pn3 * 64 + (w >> 1 & 1) * 32 + (w >> 2) * 16;
  const u32 mvo = (u32)((ncol + hb * 4) * 4);
  const int rt3 = xcd * 8 + pm3 * 2;

  float tsb, tc1;
#define TAB_WRITE()                                                                                          \
  do { PIN(tsb); PIN(tc1);                                                                                   \
    const u32 to = TAB + (u32)((t >> 4) * 32 + (t >> 2 & 1) * 16 + ((t & 3) + 4 * (t >> 3 & 1)) * 2);        \
    *(LAS u16*)((LAS char*)smem + to) = (u16)(__float_as_uint(tc1) >> 16);                                   \
    *(LAS u16*)((LAS char*)smem + to + 1024) = (u16)(__float_as_uint(tsb) >> 16); } while (0)
#define TAB_LOAD() do { GLD32(tsb, (u32)t * 4, sws, zs, WS_MODF + 1536 * 4); GLD32(tc1, (u32)t * 4, sws, zs, WS_MODF + 2048 * 4); } while (0)
  const u32x4 so = make_srd(wo, 512 * 1024), sx = make_srd(x, 2048 * 1024), sd = make_srd(wd, 1792 * 1024);
#define P3_PIECE(j_)                                                                                         \
  do { if constexpr ((j_) < 8) GLDS(wvo, so, (u32)(w * 8192 + pn3 * 128), (j_) * 65536, lb + w * 1024, (j_) * 8192); \
    else { const u32 xvo_ = (u32)((l >> 3) * 1024 + (((l & 7) ^ ((4 * w + (l >> 4)) & 7)) * 16));            \
      GLDS(xvo_, sx, (u32)((rt3 * 32 + w * 8) * 1024 + pn3 * 128), 0, lb + w * 1024, XT); } } while (0)

  if (PH == 0 || PH == 2) {
    const int head = xcd * 4 + (loc >> 3), qt = (loc & 7) * 8 + w;
    const u32 ksb = (u32)(head * KV_HEAD + w * 1024), wlb = lb + w * 1024;
#define KVR(r) GLDS(lo16, sws, ksb, WS_KV + (r) * 8192, wlb, (r) * 8192)
    u32x4 qraw;
    GLD128(qraw, lo16, sws, (u32)(head * 64 + qt) * 1024, WS_QF);
    KVR(0); KVR(1);
    if (w < 4) KVR(16);
    KVR(2); KVR(3);
    const int r = l & 31;
    u32 krd = lo16;
    u32 vrd = (u32)(1024 + hb * 272 + (r < 16 ? r * 16 : (r == 16 ? 256 : 0x40000)));
    STAMP(1);
    WAITV(2);
    PIN(qraw);
    BAR();
    STAMP(2);
    const bf16x8 qf = __builtin_bit_cast(bf16x8, qraw);
    float m;
    bf16x8 kf = lds_frag(smem, krd);
    {
      f32x16 s0 = MFMA32(kf, qf, zero);
      m = s0[0];
#pragma unroll
      for (int i = 1; i < 16; ++i) m = fmaxf(m, s0[i]);
      m = fmaxf(m, __shfl_xor(m, 32));
    }
    f32x16 negm;
#pragma unroll
    for (int i = 0; i < 16; ++i) negm[i] = -m;
    f32x16 o = zero;
    f32x16 sc = MFMA32(kf, qf, negm);
    kf = lds_frag(smem, krd + KV_TILE);
    SFOR(j, 0, 64)
      if constexpr ((j & 1) == 0) __builtin_amdgcn_s_setprio(3 - (j >> 1 & 3));
      if (j == 5) { WAITV(10); BAR(); }
      if (j == 13) { WAITV(0); BAR(); }
      if (j == 14) STAMP(3);
      if constexpr (j < 6) { KVR(4 + 2 * j); KVR(5 + 2 * j); }
      if (PH == 0) {
        if constexpr (j >= 14 && j < 51) WSTEP(j - 14);
        if (j == 51) STAMP(21);
        if (j == 52) { BAR(); TAB_LOAD(); }
        if constexpr (j >= 52 && j < 61) P3_PIECE(j - 52);
      }
      if (j == 30 || j == 60) { krd += 30 * KV_TILE; vrd += 30 * KV_TILE; PIN(krd); PIN(vrd); }
      const u32 jo = (u32)(j % 30) * KV_TILE;
      const bf16x8 v0 = lds_frag(smem, vrd + jo), v1 = lds_frag(smem, vrd + jo + 544);
      f32x16 sn;
      if (j < 63) sn = MFMA32(kf, qf, negm);
      if (j < 62) kf = lds_frag(smem, krd + jo + 2 * KV_TILE);
      __builtin_amdgcn_sched_barrier(0);
#pragma unroll
      for (int i = 0; i < 11; ++i) sc[i] = __builtin_amdgcn_exp2f(sc[i]);
      __builtin_amdgcn_sched_barrier(0);
      o = __builtin_amdgcn_smfmac_f32_32x32x32_bf16(v0, __builtin_bit_cast(bf16x16, __builtin_shufflevector(sc, sc, 0, 1, 2, 3, 4, 5, 6, 7)), o, 0xDDDD, 0, 0);
      __builtin_amdgcn_sched_barrier(0);
#pragma unroll
      for (int i = 11; i < 16; ++i) sc[i] = __builtin_amdgcn_exp2f(sc[i]);
      o = __builtin_amdgcn_smfmac_f32_32x32x32_bf16(v1, __builtin_bit_cast(bf16x16, __builtin_shufflevector(sc, sc, 8, 9, 10, 11, 12, 13, 14, 15)), o, 0xDDDD, 0, 0);
      __builtin_amdgcn_sched_barrier(0);
      if (j < 63) sc = sn;
    SEND
    float lsum = shfl_idx(o[8], r);
    bool ok = lsum < 1e18f;
#pragma unroll
    for (int i = 0; i < 8; ++i) ok = ok && (fabsf(o[i]) < 3e38f);
    if (!__all(ok)) {
      asm volatile("" ::: "memory");
      const char* kg = ws + WS_KV + (size_t)head * KV_HEAD + lo16;
      const char* vg = ws + WS_KV + (size_t)head * KV_HEAD + 1024 + hb * 272 + (r == 16 ? 256 : (r & 15) * 16);
#pragma unroll 1
      for (int j = 0; j < 64; ++j) {
        const f32x16 sv = MFMA32(*(const bf16x8*)(kg + j * KV_TILE), qf, zero);
#pragma unroll
        for (int i = 0; i < 16; ++i) m = fmaxf(m, sv[i]);
      }
      m = fmaxf(m, __shfl_xor(m, 32));
#pragma unroll
      for (int i = 0; i < 16; ++i) negm[i] = -m;
      o = zero;
#pragma unroll 1
      for (int j = 0; j < 64; ++j) {
        f32x16 sv = MFMA32(*(const bf16x8*)(kg + j * KV_TILE), qf, negm);
        const bf16x8 v0 = *(const bf16x8*)(vg + j * KV_TILE), v1 = *(const bf16x8*)(vg + j * KV_TILE + 544);
#pragma unroll
        for (int i = 0; i < 16; ++i) sv[i] = __builtin_amdgcn_exp2f(sv[i]);
        o = __builtin_amdgcn_smfmac_f32_32x32x32_bf16(v0, __builtin_bit_cast(bf16x16, __builtin_shufflevector(sv, sv, 0, 1, 2, 3, 4, 5, 6, 7)), o, 0xDDDD, 0, 0);
        o = __builtin_amdgcn_smfmac_f32_32x32x32_bf16(v1, __builtin_bit_cast(bf16x16, __builtin_shufflevector(sv, sv, 8, 9, 10, 11, 12, 13, 14, 15)), o, 0xDDDD, 0, 0);
      }
      lsum = shfl_idx(o[8], r);
    }
    const float inv = 1.f / lsum;
    u32x4 ov;
#pragma unroll
    for (int e = 0; e < 4; ++e) ov[e] = pack2(o[2 * e] * inv, o[2 * e + 1] * inv);
    GST_SC1("x4", ov, lo16, sws, (u32)(qt * 32 + head) * 1024, WS_OF);
    STAMP(4);
    WAITV(0);
    STAMP(5);
    if (PH == 0) {
      raise_flag(ws, 0, l < 8, (loc & 7) + 8 * ((w >> 1) * 8 + l), head * 2 + (w & 1));
      TAB_WRITE();
    }
  }

  if (PH == 4) {
    SFOR(p, 0, 37) WSTEP(p); SEND
    BAR();
  }

  f32x16 uv = zero;
  const u32 trd = TAB + (u32)((l >> 4 & 1) * 1024 + hb * 16);
#define UV_PART(k0)                                                                                          \
  do { bf16x8 tf_[16];                                                                                       \
    _Pragma("unroll") for (int i_ = 0; i_ < 4; ++i_) tf_[i_] = lds_frag(smem, trd + ((k0) + i_) * 32);       \
    _Pragma("unroll") for (int i_ = 0; i_ < 16; ++i_) {                                                      \
      if (i_ + 4 < 16) tf_[i_ + 4] = lds_frag(smem, trd + ((k0) + i_ + 4) * 32);                             \
      uv = MFMA32(wf[(k0) + i_], tf_[i_], uv); SB(); } } while (0)
  if (PH == 4) { TAB_LOAD(); WAITV(0); TAB_WRITE(); BAR(); UV_PART(0); }

  if (PH == 0 || PH == 3) {
    if (PH == 3) { SFOR(j, 0, 9) P3_PIECE(j); SEND }
    STAMP(7);
    if (PH == 0) { wait_flags(ws, 0, 32, w); BAR(); }
    STAMP(8);
    const u32 olo = (u32)((w >> 2) * 32768 + (w & 3) * 4096);
    const u32 osb = (u32)rt3 * 32768 + olo, olb = lb + olo;
#pragma unroll
    for (int s = 0; s < 2; ++s)
#pragma unroll
      for (int j = 0; j < 4; ++j) GLDS_SC1(lo16, sws, osb, WS_OF + (s * 16 + j) * 1024, olb, 65536 + (s * 16 + j) * 1024);
    u32x4 ga[2], cb[2];
#pragma unroll
    for (int e = 0; e < 2; ++e) { GLD128(ga[e], mvo, sws, zs, WS_MODF + 1024 * 4 + e * 32); GLD128(cb[e], mvo, sws, zs, WS_MODF + 2048 * 4 + e * 32); }
    if (PH == 0) UV_PART(0);
    const int tn = w >> 1 & 1, kh = w >> 2;
    u32 ord = 65536 + (u32)(tm * 32768 + kh * 8192) + lo16;
    PIN(ord);
    const u32 wrd = (wrd0 ^ (u32)(tn * 64)) + kh * 16384;
    f32x16 acc = zero;
#pragma unroll
    for (int s = 0; s < 2; ++s) {
      bf16x8 wq[8], oq[8];
      if (PH == 0) {
#pragma unroll
        for (int j = 0; j < 8; ++j) wq[j] = lds_tr2(smem, wrd + (s * 16 + j) * 2048, wrd + (s * 16 + j) * 2048 + 1024);
      }
      if (s == 0) WAITV(8); else WAITV(4);
      BAR();
      if (PH != 0) {
#pragma unroll
        for (int j = 0; j < 8; ++j) wq[j] = lds_tr2(smem, wrd + (s * 16 + j) * 2048, wrd + (s * 16 + j) * 2048 + 1024);
      }
#pragma unroll
      for (int j = 0; j < 4; ++j) oq[j] = lds_frag(smem, ord + (s * 16 + j) * 1024);
      SB();
#pragma unroll
      for (int j = 0; j < 8; ++j) {
        if (j + 4 < 8) oq[j + 4] = lds_frag(smem, ord + (s * 16 + j + 4) * 1024);
        acc = MFMA32(wq[j], oq[j], acc);
        SB();
      }
    }
    STAMP(9);
    LAS f32x4* ex = (LAS f32x4*)((LAS char*)smem + EX3);
    float mine[8];
#pragma unroll
    for (int i = 0; i < 8; ++i) mine[i] = kh ? acc[8 + i] : acc[i];
    {
      f32x4 a, b;
#pragma unroll
      for (int i = 0; i < 4; ++i) { a[i] = kh ? acc[i] : acc[8 + i]; b[i] = kh ? acc[4 + i] : acc[12 + i]; }
      ex[(w ^ 4) * 128 + l] = a; ex[(w ^ 4) * 128 + 64 + l] = b;
    }
    WAITV(0);
    BAR();
    {
      const f32x4 a = ex[w * 128 + l], b = ex[w * 128 + 64 + l];
#pragma unroll
      for (int i = 0; i < 4; ++i) { mine[i] += a[i]; mine[4 + i] += b[i]; }
    }
    const int row = tm * 32 + (l & 31);
    float xv[8];
    u32x4 x1p, yp;
#pragma unroll
    for (int e = 0; e < 2; ++e) {
      PIN(ga[e]); PIN(cb[e]);
      const u32x2 xr = *(const LAS u32x2*)((LAS char*)smem + XT + row * 128 + (((tn * 4 + kh * 2 + e) ^ (row >> 1 & 7)) * 16) + hb * 8);
#pragma unroll
      for (int h = 0; h < 2; ++h) {
        const u32 pk = pack2(bf_lo(xr[h]) + __uint_as_float(ga[e][2 * h]) * mine[4 * e + 2 * h], bf_hi(xr[h]) + __uint_as_float(ga[e][2 * h + 1]) * mine[4 * e + 2 * h + 1]);
        x1p[2 * e + h] = pk;
        xv[4 * e + 2 * h] = bf_lo(pk); xv[4 * e + 2 * h + 1] = bf_hi(pk);
        yp[2 * e + h] = pack2(bf_lo(pk) * __uint_as_float(cb[e][2 * h]), bf_hi(pk) * __uint_as_float(cb[e][2 * h + 1]));
      }
    }
    GST_SC1("x4", yp, lo16, sws, (u32)((rt3 + tm) * 32 + pn3 * 4 + tn * 2 + kh) * 1024, WS_Y);
    float sum = 0.f;
#pragma unroll
    for (int i = 0; i < 8; ++i) sum += xv[i];
    sum = xor32_sum(sum);
    const float mean = sum * (1.f / 16.f);
    float sq = 0.f;
#pragma unroll
    for (int i = 0; i < 8; ++i) { const float d = xv[i] - mean; sq += d * d; }
    sq = xor32_sum(sq);
    LAS f32x2* sp = (LAS f32x2*)((LAS char*)smem + RST3);
    if (l < 32) sp[(w >> 1) * 64 + row] = (f32x2){mean, sq};
    BAR();
    if (w == 0) {
      const f32x2 p0 = sp[l], p1 = sp[64 + l], p2 = sp[128 + l], p3 = sp[192 + l];
      const float mu = 0.25f * ((p0[0] + p1[0]) + (p2[0] + p3[0]));
      const float d0 = p0[0] - mu, d1 = p1[0] - mu, d2 = p2[0] - mu, d3 = p3[0] - mu;
      const f32x2 o2 = {mu, (p0[1] + p1[1]) + (p2[1] + p3[1]) + 16.f * ((d0 * d0 + d1 * d1) + (d2 * d2 + d3 * d3))};
      GST_SC1("x2", o2, (u32)(hb * 2048 + (l & 31) * 8), sws, (u32)(rt3 * 2048 + pn3 * 256), WS_ST2);
    }
    STAMP(10);
    WAITV(0);
    if (PH == 0) {
      const int pan = xcd * 2 + (pm3 >> 1);
      raise_flag(ws, 1, l < 14, (pan >> 2) * 2 + (l >= 7 ? 1 : 0) + 8 * ((pan & 3) * 7 + (l >= 7 ? l - 7 : l)), ((pm3 & 1) * 8 + pn3) * 8 + w);
    }
    STAMP(11);
    *(u32x4*)(ws + WS_X1 + (size_t)(bid * 512 + t) * 16) = x1p;
  }

  const u32 dsb = (u32)(w * 16384 + pn3 * 128), dlb = lb + w * 2048;
#define WDPIECE(c, slot, h) GLDS(wvo, sd, dsb, (c) * 131072 + (h) * 8192, dlb, (slot) * 16384 + (h) * 1024)
#define WDISSUE(c, slot) do { GLDS(wvo, sd, dsb, (c) * 131072, dlb, (slot) * 16384); GLDS(wvo, sd, dsb, (c) * 131072 + 8192, dlb, (slot) * 16384 + 1024); } while (0)
  if ((PH == 0 || PH == 4) && loc < 28) {
    if (PH == 0) { wait_flags(ws, 1, 64, w); BAR(); }
    STAMP(12);
    const int rt4 = pan4 * 4;
    const u32 ysb = (u32)(rt4 * 32768 + w * 1024), ylb = lb + w * 1024;
#define YISSUE(a) _Pragma("unroll") for (int j = 0; j < 4; ++j) GLDS_SC1(lo16, sws, ysb, WS_Y + (a) * 32768 + j * 8192, ylb, (a) * 32768 + j * 8192)
    YISSUE(0);
    GLDS_SC1(lo16, sws, (u32)(rt4 * 2048 + w * 1024), WS_ST2, ylb, 131072);
    YISSUE(1);
    UV_PART(16);
    float uu[16], vv[16];
#pragma unroll
    for (int i = 0; i < 16; ++i) {
      const auto r = __builtin_amdgcn_permlane16_swap(__float_as_uint(uv[i]), __float_as_uint(uv[i]), false, false);
      uu[i] = __uint_as_float(r[0]); vv[i] = __uint_as_float(r[1]);
    }
    u32 yrd0 = lo16, yrd1 = lo16 + 65536;
    PIN(yrd1);
    const u32 asb = (u32)(rt4 * 112 + pn4 * 8 + w) * 1024;
#define YFRAG(n) lds_frag(smem, ((n) < 64 ? yrd0 : yrd1) + ((n) & 63) * 1024)
    WAITV(4);
    BAR();
    STAMP(13);
    bf16x8 yf[128];
#pragma unroll
    for (int n = 0; n < 4; ++n) yf[n] = YFRAG(n);
    SB();
    f32x16 acc = zero, accp = zero;
    f32x2 stp = {0.f, 0.f};
    float eg[8], eu[8], et[8];
    u32x4 ov;
    SFOR(n, 0, 160)
      constexpr int a = n >> 5, ks = n & 31;
      if (n == 0) STAMP(22);
      if (n == 26) STAMP(23);
      if (n == 56) STAMP(25);
      if (n == 36) STAMP(27);
      if (n == 68) STAMP(29);
      if (n == 100) STAMP(31);
      if (n == 26) { WAITV(8); BAR(); }
      if (n == 56) { if (PH == 0) WAITV(4); else WAITV(0); RAWBAR(); }
      if constexpr (n < 24 && n % 3 == 0) GLDS_SC1(lo16, sws, ysb, WS_Y + 65536 + (n / 3) * 8192, ylb, 65536 + (n / 3) * 8192);
      if constexpr (PH == 0) {
        if (n == 36 || n == 68 || n == 100) RAWBAR();
        if constexpr (n == 60) {
          const int rr = pm3 * 224 + w * 28 + (l < 28 ? l : 27);
          GTOUCH((u32)(rr + (rr < 256 ? 768 : 896)) * 1024, sd, (u32)(pn3 * 128), 0, lb + SCR);
        }
        if constexpr (n >= 36 && n < 52 && n % 4 == 0) WDPIECE((n - 36) / 8, (n - 36) / 8, (n - 36) / 4 & 1);
        if constexpr (n >= 68 && n < 84 && n % 4 == 0) WDPIECE(2 + (n - 68) / 8, 2 + (n - 68) / 8, (n - 68) / 4 & 1);
        if constexpr (n == 84 || n == 88) WDPIECE(8, 8, (n - 84) / 4);
        if constexpr (n >= 100 && n < 116 && n % 4 == 0) WDPIECE(4 + (n - 100) / 8, 4 + (n - 100) / 8, (n - 100) / 4 & 1);
      }
      if (n == 26) STAMP(24);
      if (n == 56) STAMP(26);
      if (n == 36) STAMP(28);
      if (n == 68) STAMP(30);
      if (n == 100) STAMP(32);
      if constexpr (n == 4) {
      const LAS f32x2* sp = (const LAS f32x2*)((LAS char*)smem + 131072 + (t >> 7) * 2048 + (t & 3) * 512 + (t >> 2 & 31) * 8);
      const f32x2 p0 = sp[0], p1 = sp[32];
      float mu = p0[0] + p1[0];
      mu += dpp_xor1(mu); mu += dpp_xor2(mu);
      mu *= 0.125f;
      float q = p0[1] + p1[1] + 64.f * ((p0[0] - mu) * (p0[0] - mu) + (p1[0] - mu) * (p1[0] - mu));
      q += dpp_xor1(q); q += dpp_xor2(q);
      const float rs = __builtin_amdgcn_rsqf(q * (1.f / 512.f) + 1e-5f);
      ((LAS f32x2*)((LAS char*)smem + RST4))[t >> 2] = (f32x2){rs, -mu * rs};
    }
      if (ks == 28 && a < 4) stp = ((const LAS f32x2*)((LAS char*)smem + RST4))[a * 32 + (l & 31)];
      if (ks == 0 && a > 0) { accp = acc; acc = zero; }
      if (n == 128) STAMP(14);
      if constexpr (n + 4 < 128) yf[n + 4] = YFRAG(n + 4);
      if constexpr (n < 128) acc = MFMA32(wf[ks], yf[n], acc);
      if constexpr (a > 0 && ks >= 2 && ks < 26) {
        constexpr int i = (ks - 2) / 3, ph = (ks - 2) % 3;
        if (ph == 0) {
          eg[i] = __builtin_fmaf(accp[i], stp[0], __builtin_fmaf(uu[i], stp[1], vv[i]));
          eu[i] = __builtin_fmaf(accp[8 + i], stp[0], __builtin_fmaf(uu[8 + i], stp[1], vv[8 + i]));
          et[i] = -1.4426950408889634f * eg[i];
          PIN(eg[i]); PIN(eu[i]); PIN(et[i]);
        } else if (ph == 1) {
          et[i] = 1.f + __builtin_amdgcn_exp2f(et[i]);
          eg[i] *= eu[i];
          PIN(eg[i]); PIN(et[i]);
        } else {
          eg[i] *= __builtin_amdgcn_rcpf(et[i]);
          PIN(eg[i]);
          if constexpr (i & 1) ov[i >> 1] = pack2(eg[i - 1], eg[i]);
        }
      }
      if constexpr (a > 0 && ks == 26) GST_SC1("x4", ov, lo16, sws, asb, WS_ACT + (a - 1) * 112 * 1024);
      if (n < 128) SB();
    SEND
    STAMP(15);
    WAITV(0);
    if (PH == 0) raise_flag(ws, 2, l < 16, (pan4 >> 1) + 8 * ((pan4 & 1) * 16 + l), pn4 * 8 + w);
    STAMP(16);
  } else if (PH == 0) {
    BAR();
    WDISSUE(0, 0); WDISSUE(1, 1); WDISSUE(2, 2); WDISSUE(3, 3); WDISSUE(8, 8); WDISSUE(4, 4); WDISSUE(5, 5);
  }

  if (PH == 0 || PH == 5) {
    if (PH == 5) { WDISSUE(0, 0); WDISSUE(1, 1); WDISSUE(2, 2); WDISSUE(3, 3); WDISSUE(8, 8); WDISSUE(4, 4); WDISSUE(5, 5); }
    WAITV(0);
    BAR();
    if (PH == 5 || w != 0) { WDISSUE(6, 6); WDISSUE(7, 7); WDISSUE(9, 9); }
    const int kq = w >> 1;
    u32 wra[3][2];
#pragma unroll
    for (int i = 0; i < 3; ++i) { wra[i][0] = wrd0 + kq * 4096 + i * 65536; wra[i][1] = wra[i][0] ^ 64u; PIN(wra[i][0]); PIN(wra[i][1]); }
    bf16x8 dq[14][4];
#define DFRAG(c) _Pragma("unroll") for (int i_ = 0; i_ < 4; ++i_) {                                              \
      const u32 o_ = wra[((c) % 10) >> 2][i_ & 1] + (((c) % 10) & 3) * 16384 + (i_ >> 1) * 2048; dq[c][i_] = lds_tr2(smem, o_, o_ + 1024); }
    DFRAG(0); DFRAG(1); DFRAG(2); DFRAG(3);
    BAR();
    if (PH == 5 || w != 0) { WDISSUE(10, 0); WDISSUE(11, 1); WDISSUE(12, 2); WDISSUE(13, 3); }
    STAMP(17);
    if (PH == 0) {
      wait_flags(ws, 2, 56, w);
      RAWBAR();
    }
    STAMP(18);
    const u32 asb = (u32)((rt3 + tm) * 112 + kq * 2) * 1024;
    u32x4 af[14][2];
#define AISSUE(c) do { GLD128_SC1(af[c][0], lo16, sws, asb, WS_ACT + (c) * 8192); GLD128_SC1(af[c][1], lo16, sws, asb, WS_ACT + (c) * 8192 + 1024); } while (0)
    AISSUE(0); AISSUE(1);
    if (PH == 0 && w == 0) { WDISSUE(6, 6); WDISSUE(7, 7); WDISSUE(9, 9); WDISSUE(10, 0); WDISSUE(11, 1); WDISSUE(12, 2); WDISSUE(13, 3); }
    AISSUE(2); AISSUE(3); AISSUE(4); AISSUE(5);
    f32x16 acc[2] = {zero, zero};
    u32x4 x1p, gb[2];
#pragma unroll
    for (int c = 0; c < 14; ++c) {
      if (c < 8) WAITV(10); else if (c == 8) WAITV(13); else if (c == 9) WAITV(11);
      else if (c == 10) WAITV(9); else if (c == 11) WAITV(7); else if (c == 12) WAITV(5); else WAITV(0);
      if (c == 0) STAMP(19);
      if (c == 4 || c == 9) RAWBAR();
      if (c < 8) AISSUE(c + 6);
      if (c == 7) {
        GLD128(x1p, (u32)t * 16, sws, (u32)bid * 8192, WS_X1);
        GLD128(gb[0], mvo, sws, zs, WS_MODF + 2560 * 4); GLD128(gb[1], mvo, sws, zs, WS_MODF + 2560 * 4 + 32);
      }
      if (c >= 3 && c < 13) { DFRAG(c + 1); }
      PIN(af[c][0]); PIN(af[c][1]);
      SB();
#pragma unroll
      for (int i = 0; i < 4; ++i) acc[i & 1] = MFMA32(dq[c][i], __builtin_bit_cast(bf16x8, af[c][i >> 1]), acc[i & 1]);
      SB();
    }
    STAMP(20);
    LAS f32x4* ex = (LAS f32x4*)((LAS char*)smem + 65536);
#pragma unroll
    for (int qd = 0; qd < 4; ++qd)
#pragma unroll
      for (int h = 0; h < 2; ++h) {
        const int b = (qd >> 1) * 8 + h * 4;
        ex[(((tm + 2 * qd) * 4 + kq) * 2 + h) * 64 + l] = (f32x4){acc[qd & 1][b], acc[qd & 1][b + 1], acc[qd & 1][b + 2], acc[qd & 1][b + 3]};
      }
    BAR();
    u32 res[4];
    PIN(x1p);
#pragma unroll
    for (int h = 0; h < 2; ++h) {
      PIN(gb[h]);
      const f32x4 s = (ex[((w * 4 + 0) * 2 + h) * 64 + l] + ex[((w * 4 + 1) * 2 + h) * 64 + l]) + (ex[((w * 4 + 2) * 2 + h) * 64 + l] + ex[((w * 4 + 3) * 2 + h) * 64 + l]);
      res[2 * h] = pack2(bf_lo(x1p[2 * h]) + __uint_as_float(gb[h][0]) * s[0], bf_hi(x1p[2 * h]) + __uint_as_float(gb[h][1]) * s[1]);
      res[2 * h + 1] = pack2(bf_lo(x1p[2 * h + 1]) + __uint_as_float(gb[h][2]) * s[2], bf_hi(x1p[2 * h + 1]) + __uint_as_float(gb[h][3]) * s[3]);
    }
    const auto r0 = __builtin_amdgcn_permlane32_swap(res[0], res[2], false, false);
    const auto r1 = __builtin_amdgcn_permlane32_swap(res[1], res[3], false, false);
    const u32x4 ov = {r0[0], r1[0], r0[1], r1[1]};
    *(u32x4*)((char*)out + (size_t)((rt3 + tm) * 32 + (l & 31)) * 1024 + (ncol + hb * 8) * 2) = ov;
  }
  ST_OUT(PH == 0 ? 2 : PH, ws);
}

#define KB_KERNEL(name, PH)                                                                                                       \
  extern "C" __global__ __launch_bounds__(512) void name(const u16* __restrict__ x, const u16* __restrict__ wo, const u16* __restrict__ wg, \
      const u16* __restrict__ wu, const u16* __restrict__ wd, char* __restrict__ ws, u16* __restrict__ out) {                       \
    __shared__ __attribute__((aligned(16))) char smem[163840];                                                                      \
    kb_body<PH>(x, wo, wg, wu, wd, ws, out, smem);                                                                                  \
  }
KB_KERNEL(kb_block, 0)
extern "C" void kernel_launch(void* const* d_in, const int* in_sizes, int n_in, void* d_out, int out_size, void* d_ws, size_t ws_size,
                              hipStream_t stream) {
  const u16* x = (const u16*)d_in[0];
  const u16* wqkv = (const u16*)d_in[1];
  const u16* wo = (const u16*)d_in[2];
  const u16* wg = (const u16*)d_in[4];
  const u16* wu = (const u16*)d_in[5];
  const u16* wd = (const u16*)d_in[6];
  const u16* c = (const u16*)d_in[7];
  const u16* wmod = (const u16*)d_in[8];
  char* ws = (char*)d_ws;
  k0_mod<<<448, 256, 0, stream>>>(c, wmod, x, (float*)(ws + WS_MODP), (f32x2*)(ws + WS_ST1));
  k1_qkv<<<256, 512, 0, stream>>>(x, wqkv, (const float*)(ws + WS_ST1), ws);
  kb_block<<<256, 512, 0, stream>>>(x, wo, wg, wu, wd, ws, (u16*)d_out);
}
```

```cpp
#include <hip/hip_runtime.h>
#include <hip/hip_bf16.h>

typedef unsigned short u16;
typedef unsigned int u32;
typedef u32 u32x2 __attribute__((ext_vector_type(2)));
typedef u32 u32x4 __attribute__((ext_vector_type(4)));
typedef float f32x2 __attribute__((ext_vector_type(2)));
typedef float f32x4 __attribute__((ext_vector_type(4)));
typedef float f32x8 __attribute__((ext_vector_type(8)));
typedef float f32x16 __attribute__((ext_vector_type(16)));
typedef __bf16 bf16x2 __attribute__((ext_vector_type(2)));
typedef __bf16 bf16x8 __attribute__((ext_vector_type(8)));
typedef __bf16 bf16x16 __attribute__((ext_vector_type(16)));
typedef short s16x4 __attribute__((ext_vector_type(4)));
typedef short s16x8 __attribute__((ext_vector_type(8)));
#define PRIO_MODE 1
#define NO_WSTEP 0
#define FUSED 1
#define CODE_PF 0
#define LAS __attribute__((address_space(3)))
#define DI __device__ __forceinline__

constexpr u32 WS_MODP = 0;
constexpr u32 WS_MODF = 0xC000;
constexpr u32 WS_ST1  = 0x10000;
constexpr u32 WS_FLG  = 0x20000;
constexpr u32 WS_QF   = 0x100000;
constexpr u32 WS_KV   = 0x300000;
constexpr u32 WS_OF   = 0x800000;
constexpr u32 WS_X1   = 0xA00000;
constexpr u32 WS_Y    = 0xC00000;
constexpr u32 WS_ST2  = 0xE00000;
constexpr u32 WS_ACT  = 0xF00000;
constexpr u32 WS_BYTES = 0x1600000;
constexpr int CODE_LINES = 300;
constexpr int KV_TILE = 2112, KV_HEAD = 64 * KV_TILE;

template <int I> struct IC { static constexpr int value = I; };
template <int B, int E, class F> DI void static_for(F&& f) { if constexpr (B < E) { f(IC<B>{}); static_for<B + 1, E>(f); } }
#define SFOR(n, B, E) static_for<B, E>([&](auto ic_) __attribute__((always_inline)) { constexpr int n = decltype(ic_)::value;
#define SEND });

DI float bf_lo(u32 u) { return __uint_as_float(u << 16); }
DI float bf_hi(u32 u) { return __uint_as_float(u & 0xffff0000u); }
DI u32 pack2(float a, float b) { bf16x2 r = {(__bf16)a, (__bf16)b}; return __builtin_bit_cast(u32, r); }
DI float bfround(float a) { return (float)(__bf16)a; }
DI float xor32_sum(float v) {
  const auto r = __builtin_amdgcn_permlane32_swap(__float_as_uint(v), __float_as_uint(v), false, false);
  return __uint_as_float(r[0]) + __uint_as_float(r[1]);
}
DI float dpp_xor1(float v) { return __int_as_float(__builtin_amdgcn_mov_dpp(__float_as_int(v), 0xB1, 0xF, 0xF, true)); }
DI float dpp_xor2(float v) { return __int_as_float(__builtin_amdgcn_mov_dpp(__float_as_int(v), 0x4E, 0xF, 0xF, true)); }
DI float shfl_idx(float v, int srcLane) { return __int_as_float(__builtin_amdgcn_ds_bpermute(srcLane << 2, __float_as_int(v))); }

DI u32x4 make_srd(const void* p, u32 bytes) {
  const size_t a = (size_t)p;
  u32x4 r = {(u32)a, (u32)(a >> 32) & 0xffffu, bytes, 0x20000u};
  return r;
}
#define GLDS(voff, srd, sb, GO, lb, LO)                                                                                   \
  do { u32 tmp_;                                                                                                          \
    asm volatile("s_add_u32 m0, %4, %6\n\ts_add_u32 %0, %3, %5\n\tbuffer_load_dwordx4 %1, %2, %0 offen lds"            \
                 : "=&s"(tmp_) : "v"(voff), "s"(srd), "s"(sb), "s"(lb), "i"(GO), "i"(LO) : "memory", "scc"); } while (0)
#define GLD(W, dst, voff, srd, sb, GO)                                                                                    \
  do { u32 tmp_;                                                                                                          \
    asm volatile("s_add_u32 %1, %4, %5\n\tbuffer_load_dword" W " %0, %2, %3, %1 offen"                                   \
                 : "=v"(dst), "=&s"(tmp_) : "v"(voff), "s"(srd), "s"(sb), "i"(GO) : "memory", "scc"); } while (0)
#define GLD128(dst, voff, srd, sb, GO) GLD("x4", dst, voff, srd, sb, GO)
#define GLD64(dst, voff, srd, sb, GO) GLD("x2", dst, voff, srd, sb, GO)
#define GLD32(dst, voff, srd, sb, GO) GLD("", dst, voff, srd, sb, GO)
#define WAITV(N) asm volatile("s_waitcnt vmcnt(" #N ")" ::: "memory")
#define PIN(x) asm volatile("" : "+v"(x))
#define BAR() do { asm volatile("s_waitcnt lgkmcnt(0)" ::: "memory"); __builtin_amdgcn_s_barrier(); asm volatile("" ::: "memory"); } while (0)

#define ST_DECL
#define STAMP(i)
#define ST_OUT(kid, wsp)

#define CODE_ADDR(sym) ({ unsigned long long pc_; int off_;                                                               \
    asm volatile("s_getpc_b64 %0\n.Lca%=:\n\ts_mov_b32 %1, " sym "-.Lca%=" : "=s"(pc_), "=s"(off_)); (const char*)(pc_ + (long long)off_); })

DI bf16x8 lds_frag(const char* smem, u32 off) { return *(const LAS bf16x8*)((const LAS char*)smem + off); }
DI bf16x8 lds_tr2(const char* smem, u32 off0, u32 off1) {
  s16x4 a = __builtin_amdgcn_ds_read_tr16_b64_v4i16((LAS s16x4*)((LAS char*)smem + off0));
  s16x4 b = __builtin_amdgcn_ds_read_tr16_b64_v4i16((LAS s16x4*)((LAS char*)smem + off1));
  s16x8 r = __builtin_shufflevector(a, b, 0, 1, 2, 3, 4, 5, 6, 7);
  return __builtin_bit_cast(bf16x8, r);
}
DI u32 lds_base(const char* smem) { return (u32)(size_t)(const LAS char*)smem; }
#define SB() __builtin_amdgcn_sched_barrier(0)
#define RAWBAR() do { asm volatile("" ::: "memory"); __builtin_amdgcn_s_barrier(); asm volatile("" ::: "memory"); } while (0)
#define MFMA32(a, b, c) __builtin_amdgcn_mfma_f32_32x32x16_bf16((a), (b), (c), 0, 0, 0)
#define MFMA16(a, b, c) __builtin_amdgcn_mfma_f32_16x16x32_bf16((a), (b), (c), 0, 0, 0)

extern "C" __global__ __launch_bounds__(256) void k0_mod(const u16* __restrict__ c, const u16* __restrict__ wmod, const u16* __restrict__ x,
                                              float* __restrict__ modp, f32x2* __restrict__ st1) {
  __shared__ float red[4][64];
  ST_DECL STAMP(0);
  const int b = blockIdx.x, t = threadIdx.x, w = t >> 6, l = t & 63;
  if (b < 192) {
    const int cb = b % 48, kq = b / 48, c8 = t & 7, r = t >> 3;
    u32x4 v[4]; float s[4];
#pragma unroll
    for (int i = 0; i < 4; ++i) {
      const int row = kq * 128 + i * 32 + r;
      v[i] = *(const u32x4*)(wmod + (size_t)row * 3072 + cb * 64 + c8 * 8);
      s[i] = __uint_as_float((u32)c[row] << 16);
    }
    float acc[8];
#pragma unroll
    for (int e = 0; e < 8; ++e) acc[e] = 0.f;
#pragma unroll
    for (int i = 0; i < 4; ++i) {
      const float sv = bfround(s[i] / (1.f + __expf(-s[i])));
#pragma unroll
      for (int e = 0; e < 4; ++e) { acc[2 * e] += sv * bf_lo(v[i][e]); acc[2 * e + 1] += sv * bf_hi(v[i][e]); }
    }
#pragma unroll
    for (int e = 0; e < 8; ++e) {
      acc[e] += __shfl_xor(acc[e], 8); acc[e] += __shfl_xor(acc[e], 16); acc[e] += __shfl_xor(acc[e], 32);
    }
    if (l < 8) {
#pragma unroll
      for (int e = 0; e < 8; ++e) red[w][l * 8 + e] = acc[e];
    }
    __syncthreads();
    if (t < 64) modp[(cb * 64 + t) * 4 + kq] = (red[0][t] + red[1][t]) + (red[2][t] + red[3][t]);
  } else {
    const int row0 = (b - 192) * 8 + w * 2;
    u32x4 v[2];
#pragma unroll
    for (int i = 0; i < 2; ++i) v[i] = *(const u32x4*)(x + (size_t)(row0 + i) * 512 + l * 8);
#pragma unroll
    for (int i = 0; i < 2; ++i) {
      float f[8];
#pragma unroll
      for (int e = 0; e < 4; ++e) { f[2 * e] = bf_lo(v[i][e]); f[2 * e + 1] = bf_hi(v[i][e]); }
      float sum = 0.f;
#pragma unroll
      for (int e = 0; e < 8; ++e) sum += f[e];
#pragma unroll
      for (int o = 1; o < 64; o <<= 1) sum += __shfl_xor(sum, o);
      const float mean = sum * (1.f / 512.f);
      float sq = 0.f;
#pragma unroll
      for (int e = 0; e < 8; ++e) { const float d = f[e] - mean; sq += d * d; }
#pragma unroll
      for (int o = 1; o < 64; o <<= 1) sq += __shfl_xor(sq, o);
      if (l == 0) { f32x2 o2 = {mean, __builtin_amdgcn_rsqf(sq * (1.f / 512.f) + 1e-5f)}; st1[row0 + i] = o2; }
    }
  }
  ST_OUT(0, modp);
}

DI u32x4 modulate8(const u32x4 v, float rs, float mr, const float* c1, const float* sh) {
  u32x4 o;
#pragma unroll
  for (int e = 0; e < 4; ++e) {
    const float a = __builtin_fmaf(__builtin_fmaf(bf_lo(v[e]), rs, mr), c1[2 * e], sh[2 * e]);
    const float b = __builtin_fmaf(__builtin_fmaf(bf_hi(v[e]), rs, mr), c1[2 * e + 1], sh[2 * e + 1]);
    o[e] = pack2(a, b);
  }
  return o;
}

extern "C" __global__ __launch_bounds__(512) void k1_qkv(const u16* __restrict__ x, const u16* __restrict__ wqkv, const float* __restrict__ st1,
                                              char* __restrict__ ws) {
  __shared__ __attribute__((aligned(16))) char smem[163840];
  constexpr u32 WOFF = 65536, WSLOT = 24576;
  ST_DECL STAMP(0);
  const int bid = blockIdx.x, xcd = bid & 7, loc = bid >> 3;
  const int pm = (xcd >> 1) * 8 + (loc >> 2), cg = (xcd & 1) * 4 + (loc & 3);
  const int t = threadIdx.x, l = t & 63;
  const int w = __builtin_amdgcn_readfirstlane(t >> 6);
  const u32 lb = lds_base(smem);
  const u32x4 sws = make_srd(ws, WS_BYTES), sx = make_srd(x, 2048 * 1024), sw = make_srd(wqkv, 512 * 3072);
  const u32 zs = 0;
  u32x4 fin, mp[2];
  const u32 t16 = (u32)t * 16;
  GLD128(mp[0], t16, sws, zs, WS_MODP);
  GLD128(mp[1], t16, sws, zs, WS_MODP + 8192);
  GLD128(fin, t16, sws, (u32)(bid < 6 ? bid : 0) * 8192, WS_MODP);
  u32x4 xa[8];
  const u32 lo16 = (u32)l * 16;
  const u32 xsb = (u32)(pm * 64 + w * 8) * 1024;
#pragma unroll
  for (int i = 0; i < 8; ++i) GLD128(xa[i], lo16, sx, xsb, i * 1024);
  const u32 wvo = (u32)((8 * (l >> 5) + (l >> 3 & 3)) * 3072 + ((((l >> 1) ^ (l >> 4)) & 3) * 16 + (l & 1) * 8) * 2);
  const u32 wsb = (u32)((32 * (w >> 2) + 16 * (w & 1) + 4 * (w >> 1 & 1)) * 3072 + cg * 128), wlb = lb + w * 1024;
#define K1_ISSUE(kc) _Pragma("unroll") for (int c = 0; c < 3; ++c) GLDS(wvo, sw, wsb, (kc) * 64 * 3072 + c * 1024, wlb, WOFF + ((kc) & 3) * WSLOT + c * 8192)
  K1_ISSUE(0);
  float rs[8], mr[8];
#pragma unroll
  for (int i = 0; i < 8; ++i) { rs[i] = st1[(pm * 64 + w * 8 + i) * 2 + 1]; mr[i] = -st1[(pm * 64 + w * 8 + i) * 2] * rs[i]; }
  STAMP(1);
  WAITV(11);
  PIN(fin); PIN(mp[0]); PIN(mp[1]);
#define SUM4(v) ((__uint_as_float(v[0]) + __uint_as_float(v[1])) + (__uint_as_float(v[2]) + __uint_as_float(v[3])))
  ((LAS float*)(LAS char*)smem)[t] = bfround(1.f + bfround(SUM4(mp[1])));
  ((LAS float*)(LAS char*)smem)[512 + t] = bfround(SUM4(mp[0]));
  {
    float f = bfround(SUM4(fin));
    if (bid == 4) f = bfround(1.f + f);
    if (bid < 6) ((float*)(ws + WS_MODF))[bid * 512 + t] = f;
    if (t < 384) ((u32*)(ws + WS_FLG))[bid * 384 + t] = 0u;
  }
  BAR();
  float c1[8], sh[8];
  {
    const LAS f32x4* tc = (const LAS f32x4*)((LAS char*)smem + l * 32);
    const f32x4 a0 = tc[0], a1 = tc[1], b0 = tc[128], b1 = tc[129];
#pragma unroll
    for (int e = 0; e < 4; ++e) { c1[e] = a0[e]; c1[4 + e] = a1[e]; sh[e] = b0[e]; sh[4 + e] = b1[e]; }
  }
  K1_ISSUE(1);
  WAITV(6);
  BAR();
  STAMP(2);
  const u32 awr = (u32)(((w >> 1) * 16 + (l >> 2)) * 1024 + (l & 3) * 256 + (w & 1) * 128);
#pragma unroll
  for (int i = 0; i < 8; ++i) {
    PIN(xa[i]);
    *(LAS u32x4*)((LAS char*)smem + awr + ((i ^ (l & 7)) << 4)) = modulate8(xa[i], rs[i], mr[i], c1, sh);
    if (i == 1) { K1_ISSUE(2); }
    if (i == 4) { K1_ISSUE(3); }
  }
  const int tm = w & 1, hd = w >> 1, g = l >> 4;
  u32 ard[2];
#pragma unroll
  for (int e = 0; e < 2; ++e) ard[e] = (u32)(tm * 32768 + e * 1024 + g * 256 + (((l & 15) ^ (e * 4 + g)) << 4));
  u32 brd = WOFF + (u32)((l >> 2) * 128 + ((hd ^ (l >> 3 & 3)) * 32) + (l & 3) * 8);
  PIN(brd);
  f32x4 acc[2][3];
#pragma unroll
  for (int a = 0; a < 2; ++a)
#pragma unroll
    for (int c = 0; c < 3; ++c) acc[a][c] = (f32x4){0.f, 0.f, 0.f, 0.f};
  WAITV(9);
  BAR();
  STAMP(3);
#pragma unroll
  for (int kc = 0; kc < 8; ++kc) {
    bf16x8 hf[2][2], wf[2][3];
#pragma unroll
    for (int e = 0; e < 2; ++e) {
#pragma unroll
      for (int a = 0; a < 2; ++a) hf[e][a] = lds_frag(smem, ard[e] + (a * 16 + kc * 2) * 1024);
#pragma unroll
      for (int c = 0; c < 3; ++c) {
        const u32 o = brd + (kc & 3) * WSLOT + c * 8192 + e * 4096;
        wf[e][c] = lds_tr2(smem, o, o + 2048);
      }
    }
#pragma unroll
    for (int e = 0; e < 2; ++e)
#pragma unroll
      for (int a = 0; a < 2; ++a) {
        acc[a][0] = MFMA16(wf[e][0], hf[e][a], acc[a][0]);
        acc[a][1] = MFMA16(wf[e][1], hf[e][a], acc[a][1]);
        acc[a][2] = MFMA16(hf[e][a], wf[e][2], acc[a][2]);
      }
    if (kc < 7) {
      if (kc < 5) WAITV(6); else if (kc == 5) WAITV(3); else WAITV(0);
      BAR();
      if (kc < 4) { K1_ISSUE(kc + 4); }
    }
    if (kc == 3) STAMP(4);
  }
  STAMP(5);
  const int head = cg * 4 + hd, tile = pm * 2 + tm;
  constexpr float QS = 0.25f * 1.4426950408889634f;
  char* kv = ws + WS_KV + (size_t)head * KV_HEAD + tile * KV_TILE;
#pragma unroll
  for (int a = 0; a < 2; ++a) {
    const u32 fo = (u32)((a * 16 + (l & 15) + 32 * (g >> 1)) * 16 + (g & 1) * 8);
    u32x2 q = {pack2(acc[a][0][0] * QS, acc[a][0][1] * QS), pack2(acc[a][0][2] * QS, acc[a][0][3] * QS)};
    u32x2 k = {pack2(acc[a][1][0], acc[a][1][1]), pack2(acc[a][1][2], acc[a][1][3])};
    u32x2 v = {pack2(acc[a][2][0], acc[a][2][1]), pack2(acc[a][2][2], acc[a][2][3])};
    *(u32x2*)(ws + WS_QF + (size_t)(head * 64 + tile) * 1024 + fo) = q;
    *(u32x2*)(kv + fo) = k;
    const u32 vo = (u32)(1024 + (a * 2 + (g >> 1)) * 272 + (g & 1) * 8);
    *(u32x2*)(kv + vo + (l & 15) * 16) = v;
    if ((l & 15) == 0) { u32x2 one = {0x3f803f80u, 0x3f803f80u}; *(u32x2*)(kv + vo + 256) = one; }
  }
  STAMP(6);
  ST_OUT(1, ws);
}

#define GLDS_SC1(voff, srd, sb, GO, lb, LO)                                                                               \
  do { u32 tmp_;                                                                                                          \
    asm volatile("s_add_u32 m0, %4, %6\n\ts_add_u32 %0, %3, %5\n\tbuffer_load_dwordx4 %1, %2, %0 offen sc1 lds"         \
                 : "=&s"(tmp_) : "v"(voff), "s"(srd), "s"(sb), "s"(lb), "i"(GO), "i"(LO) : "memory", "scc"); } while (0)
#define GLD128_SC1(dst, voff, srd, sb, GO)                                                                                \
  do { u32 tmp_;                                                                                                          \
    asm volatile("s_add_u32 %1, %4, %5\n\tbuffer_load_dwordx4 %0, %2, %3, %1 offen sc1"                                 \
                 : "=v"(dst), "=&s"(tmp_) : "v"(voff), "s"(srd), "s"(sb), "i"(GO) : "memory", "scc"); } while (0)
#define GST_SC1(W, val, voff, srd, sb, GO)                                                                                \
  do { u32 tmp_;                                                                                                          \
    asm volatile("s_add_u32 %0, %4, %5\n\tbuffer_store_dword" W " %1, %2, %3, %0 offen sc1\n\ts_nop 1"                  \
                 : "=&s"(tmp_) : "v"(val), "v"(voff), "s"(srd), "s"(sb), "i"(GO) : "memory", "scc"); } while (0)
#define GTOUCH(voff, srd, sb, GO, lb)                                                                                     \
  do { u32 tmp_;                                                                                                          \
    asm volatile("s_mov_b32 m0, %4\n\ts_add_u32 %0, %3, %5\n\tbuffer_load_dword %1, %2, %0 offen lds"                   \
                 : "=&s"(tmp_) : "v"(voff), "s"(srd), "s"(sb), "s"(lb), "i"(GO) : "memory", "scc"); } while (0)
#define GLDS64(vaddr, lb, LO)                                                                                             \
    asm volatile("s_add_u32 m0, %1, %2\n\ts_nop 0\n\tglobal_load_lds_dwordx4 %0, off" :: "v"(vaddr), "s"(lb), "i"(LO) : "memory", "scc")

DI void raise_flag(char* ws, int h, bool on, int cons, int idx) {
  if (on) __hip_atomic_store((u32*)(ws + WS_FLG) + (cons * 3 + h) * 128 + idx, 1u, __ATOMIC_RELAXED, __HIP_MEMORY_SCOPE_AGENT);
}
DI void wait_flags(char* ws, int h, int nlanes, int w) {
  if (w == 0) {
    const int l = threadIdx.x & 63;
    unsigned long long* c = (unsigned long long*)(ws + WS_FLG + (blockIdx.x * 3 + h) * 512) + (l < nlanes ? l : 0);
#pragma unroll 1
    for (u32 spins = 0; spins < (1u << 17); ++spins) {
      if (__all(__hip_atomic_load(c, __ATOMIC_RELAXED, __HIP_MEMORY_SCOPE_AGENT) == 0x100000001ull)) break;
      __builtin_amdgcn_s_sleep(1);
    }
    asm volatile("" ::: "memory");
  }
}

template <int PH>
DI void kb_body(const u16* __restrict__ x, const u16* __restrict__ wo, const u16* __restrict__ wg, const u16* __restrict__ wu,
                const u16* __restrict__ wd, char* __restrict__ ws, u16* __restrict__ out, char* smem) {
  ST_DECL STAMP(0);
  const int bid = blockIdx.x, xcd = bid & 7, loc = bid >> 3;
  const int t = threadIdx.x, l = t & 63;
  const int w = __builtin_amdgcn_readfirstlane(t >> 6);
  const u32 lb = lds_base(smem);
  const u32x4 sws = make_srd(ws, WS_BYTES);
  const u32 lo16 = (u32)l * 16, zs = 0;
  const int hb = l >> 5;
  const int pm3 = loc >> 3, pn3 = loc & 7;
  const int lq = loc >= 28 ? 27 : loc;
  const int pan4 = (xcd >> 1) * 4 + lq / 7, pn4 = (xcd & 1) * 7 + lq % 7;
  constexpr u32 STG = KV_HEAD;
  constexpr u32 TAB = 159744, UVP = TAB + 2048, SCR = UVP;
  constexpr u32 XT = KV_HEAD, EX3 = XT + 8192, RST3 = 131072, RST4 = 157696;
  f32x16 zero;
#pragma unroll
  for (int i = 0; i < 16; ++i) zero[i] = 0.f;

  bf16x8 wf[32];
  const char* wsrc = (const char*)((l & 2) ? wu : wg) + (size_t)(l >> 2) * 3584 + (pn4 * 128 + w * 16 + (l & 1) * 8) * 2;
  const u32 stg_lb = lb + w * 3072;
  u32 stg_rd0 = (u32)(w * 3072 + (hb * 4 + (l >> 2 & 3)) * 64 + (l >> 4 & 1) * 32 + (l & 3) * 8), stg_rd1 = stg_rd0 + STG;
  PIN(stg_rd1);
#define WSLOT_OFF(p) ((((p) % 6) < 3 ? STG : 0u) + ((p) % 3) * 1024u)
#define WSTEP(p)                                                                                        \
  do {                                                                                                  \
    if constexpr ((p) >= 5) {                                                                                     \
      if ((p) <= 32) asm volatile("s_waitcnt vmcnt(4) lgkmcnt(0)" ::: "memory");                        \
      else if ((p) == 33) asm volatile("s_waitcnt vmcnt(3)" ::: "memory");                              \
      else if ((p) == 34) asm volatile("s_waitcnt vmcnt(2)" ::: "memory");                              \
      else if ((p) == 35) asm volatile("s_waitcnt vmcnt(1)" ::: "memory");                              \
      else asm volatile("s_waitcnt vmcnt(0)" ::: "memory");                                             \
      const u32 o_ = ((((p) - 5) % 6) < 3 ? stg_rd1 : stg_rd0) + (((p) - 5) % 3) * 1024;                \
      wf[(p) - 5] = lds_tr2(smem, o_, o_ + 512);                                                        \
    }                                                                                                   \
    if ((p) < 32) { GLDS64(wsrc, stg_lb, WSLOT_OFF(p)); wsrc += 16 * 3584; }                            \
  } while (0)

  const int tm = w & 1;
  const u32 wvo = (u32)((l >> 3) * 1024 + ((((l >> 2) ^ (l >> 4)) & 1) * 32 + (l & 3) * 8) * 2);
  const u32 wrd0 = (u32)((hb * 4 + (l >> 2 & 3)) * 128 + (l >> 3 & 1) * 64 + (l >> 4 & 1) * 32 + (l & 3) * 8);
  const int ncol = pn3 * 64 + (w >> 1 & 1) * 32 + (w >> 2) * 16;
  const u32 mvo = (u32)((ncol + hb * 4) * 4);
  const int rt3 = xcd * 8 + pm3 * 2;

  float tsb, tc1;
#define TAB_WRITE()                                                                                          \
  do { PIN(tsb); PIN(tc1);                                                                                   \
    const u32 to = TAB + (u32)((t >> 4) * 32 + (t >> 2 & 1) * 16 + ((t & 3) + 4 * (t >> 3 & 1)) * 2);        \
    *(LAS u16*)((LAS char*)smem + to) = (u16)(__float_as_uint(tc1) >> 16);                                   \
    *(LAS u16*)((LAS char*)smem + to + 1024) = (u16)(__float_as_uint(tsb) >> 16); } while (0)
#define TAB_LOAD() do { GLD32(tsb, (u32)t * 4, sws, zs, WS_MODF + 1536 * 4); GLD32(tc1, (u32)t * 4, sws, zs, WS_MODF + 2048 * 4); } while (0)
  const u32x4 so = make_srd(wo, 512 * 1024), sx = make_srd(x, 2048 * 1024), sd = make_srd(wd, 1792 * 1024);
#define P3_PIECE(j_)                                                                                         \
  do { if constexpr ((j_) < 8) GLDS(wvo, so, (u32)(w * 8192 + pn3 * 128), (j_) * 65536, lb + w * 1024, (j_) * 8192); \
    else { const u32 xvo_ = (u32)((l >> 3) * 1024 + (((l & 7) ^ ((4 * w + (l >> 4)) & 7)) * 16));            \
      GLDS(xvo_, sx, (u32)((rt3 * 32 + w * 8) * 1024 + pn3 * 128), 0, lb + w * 1024, XT); } } while (0)

  if (PH == 0 || PH == 2) {
    const int head = xcd * 4 + (loc >> 3), qt = (loc & 7) * 8 + w;
    const u32 ksb = (u32)(head * KV_HEAD + w * 1024), wlb = lb + w * 1024;
#define KVR(r) GLDS(lo16, sws, ksb, WS_KV + (r) * 8192, wlb, (r) * 8192)
    u32x4 qraw;
    GLD128(qraw, lo16, sws, (u32)(head * 64 + qt) * 1024, WS_QF);
    KVR(0); KVR(1);
    if (w < 4) KVR(16);
    KVR(2); KVR(3);
    const int r = l & 31;
    u32 krd = lo16;
    u32 vrd = (u32)(1024 + hb * 272 + (r < 16 ? r * 16 : (r == 16 ? 256 : 0x40000)));
    STAMP(1);
    WAITV(2);
    PIN(qraw);
    BAR();
    STAMP(2);
    const bf16x8 qf = __builtin_bit_cast(bf16x8, qraw);
    float m;
    bf16x8 kf = lds_frag(smem, krd);
    {
      f32x16 s0 = MFMA32(kf, qf, zero);
      m = s0[0];
#pragma unroll
      for (int i = 1; i < 16; ++i) m = fmaxf(m, s0[i]);
      m = fmaxf(m, __shfl_xor(m, 32));
    }
    f32x16 negm;
#pragma unroll
    for (int i = 0; i < 16; ++i) negm[i] = -m;
    f32x16 o = zero;
    f32x16 sc = MFMA32(kf, qf, negm);
    kf = lds_frag(smem, krd + KV_TILE);
    SFOR(j, 0, 64)
      if constexpr ((j & 1) == 0) __builtin_amdgcn_s_setprio(3 - (j >> 1 & 3));
      if (j == 5) { WAITV(10); BAR(); }
      if (j == 13) { WAITV(0); BAR(); }
      if (j == 14) STAMP(3);
      if constexpr (j < 6) { KVR(4 + 2 * j); KVR(5 + 2 * j); }
      if (PH == 0) {
        if constexpr (j >= 14 && j < 51) WSTEP(j - 14);
        if (j == 51) STAMP(21);
        if (j == 52) { BAR(); TAB_LOAD(); }
        if constexpr (j >= 52 && j < 61) P3_PIECE(j - 52);
      }
      if (j == 30 || j == 60) { krd += 30 * KV_TILE; vrd += 30 * KV_TILE; PIN(krd); PIN(vrd); }
      const u32 jo = (u32)(j % 30) * KV_TILE;
      const bf16x8 v0 = lds_frag(smem, vrd + jo), v1 = lds_frag(smem, vrd + jo + 544);
      f32x16 sn;
      if (j < 63) sn = MFMA32(kf, qf, negm);
      if (j < 62) kf = lds_frag(smem, krd + jo + 2 * KV_TILE);
      __builtin_amdgcn_sched_barrier(0);
#pragma unroll
      for (int i = 0; i < 11; ++i) sc[i] = __builtin_amdgcn_exp2f(sc[i]);
      __builtin_amdgcn_sched_barrier(0);
      o = __builtin_amdgcn_smfmac_f32_32x32x32_bf16(v0, __builtin_bit_cast(bf16x16, __builtin_shufflevector(sc, sc, 0, 1, 2, 3, 4, 5, 6, 7)), o, 0xDDDD, 0, 0);
      __builtin_amdgcn_sched_barrier(0);
#pragma unroll
      for (int i = 11; i < 16; ++i) sc[i] = __builtin_amdgcn_exp2f(sc[i]);
      o = __builtin_amdgcn_smfmac_f32_32x32x32_bf16(v1, __builtin_bit_cast(bf16x16, __builtin_shufflevector(sc, sc, 8, 9, 10, 11, 12, 13, 14, 15)), o, 0xDDDD, 0, 0);
      __builtin_amdgcn_sched_barrier(0);
      if (j < 63) sc = sn;
    SEND
    float lsum = shfl_idx(o[8], r);
    bool ok = lsum < 1e18f;
#pragma unroll
    for (int i = 0; i < 8; ++i) ok = ok && (fabsf(o[i]) < 3e38f);
    if (!__all(ok)) {
      asm volatile("" ::: "memory");
      const char* kg = ws + WS_KV + (size_t)head * KV_HEAD + lo16;
      const char* vg = ws + WS_KV + (size_t)head * KV_HEAD + 1024 + hb * 272 + (r == 16 ? 256 : (r & 15) * 16);
#pragma unroll 1
      for (int j = 0; j < 64; ++j) {
        const f32x16 sv = MFMA32(*(const bf16x8*)(kg + j * KV_TILE), qf, zero);
#pragma unroll
        for (int i = 0; i < 16; ++i) m = fmaxf(m, sv[i]);
      }
      m = fmaxf(m, __shfl_xor(m, 32));
#pragma unroll
      for (int i = 0; i < 16; ++i) negm[i] = -m;
      o = zero;
#pragma unroll 1
      for (int j = 0; j < 64; ++j) {
        f32x16 sv = MFMA32(*(const bf16x8*)(kg + j * KV_TILE), qf, negm);
        const bf16x8 v0 = *(const bf16x8*)(vg + j * KV_TILE), v1 = *(const bf16x8*)(vg + j * KV_TILE + 544);
#pragma unroll
        for (int i = 0; i < 16; ++i) sv[i] = __builtin_amdgcn_exp2f(sv[i]);
        o = __builtin_amdgcn_smfmac_f32_32x32x32_bf16(v0, __builtin_bit_cast(bf16x16, __builtin_shufflevector(sv, sv, 0, 1, 2, 3, 4, 5, 6, 7)), o, 0xDDDD, 0, 0);
        o = __builtin_amdgcn_smfmac_f32_32x32x32_bf16(v1, __builtin_bit_cast(bf16x16, __builtin_shufflevector(sv, sv, 8, 9, 10, 11, 12, 13, 14, 15)), o, 0xDDDD, 0, 0);
      }
      lsum = shfl_idx(o[8], r);
    }
    const float inv = 1.f / lsum;
    u32x4 ov;
#pragma unroll
    for (int e = 0; e < 4; ++e) ov[e] = pack2(o[2 * e] * inv, o[2 * e + 1] * inv);
    GST_SC1("x4", ov, lo16, sws, (u32)(qt * 32 + head) * 1024, WS_OF);
    STAMP(4);
    WAITV(0);
    STAMP(5);
    if (PH == 0) {
      raise_flag(ws, 0, l < 8, (loc & 7) + 8 * ((w >> 1) * 8 + l), head * 2 + (w & 1));
      TAB_WRITE();
    }
  }

  if (PH == 4) {
    SFOR(p, 0, 37) WSTEP(p); SEND
    BAR();
  }

  f32x16 uv = zero;
  const u32 trd = TAB + (u32)((l >> 4 & 1) * 1024 + hb * 16);
#define UV_PART(k0)                                                                                          \
  do { bf16x8 tf_[16];                                                                                       \
    _Pragma("unroll") for (int i_ = 0; i_ < 4; ++i_) tf_[i_] = lds_frag(smem, trd + ((k0) + i_) * 32);       \
    _Pragma("unroll") for (int i_ = 0; i_ < 16; ++i_) {                                                      \
      if (i_ + 4 < 16) tf_[i_ + 4] = lds_frag(smem, trd + ((k0) + i_ + 4) * 32);                             \
      uv = MFMA32(wf[(k0) + i_], tf_[i_], uv); SB(); } } while (0)
  if (PH == 4) { TAB_LOAD(); WAITV(0); TAB_WRITE(); BAR(); UV_PART(0); }

  if (PH == 0 || PH == 3) {
    if (PH == 3) { SFOR(j, 0, 9) P3_PIECE(j); SEND }
    STAMP(7);
    if (PH == 0) { wait_flags(ws, 0, 32, w); BAR(); }
    STAMP(8);
    const u32 olo = (u32)((w >> 2) * 32768 + (w & 3) * 4096);
    const u32 osb = (u32)rt3 * 32768 + olo, olb = lb + olo;
#pragma unroll
    for (int s = 0; s < 2; ++s)
#pragma unroll
      for (int j = 0; j < 4; ++j) GLDS_SC1(lo16, sws, osb, WS_OF + (s * 16 + j) * 1024, olb, 65536 + (s * 16 + j) * 1024);
    u32x4 ga[2], cb[2];
#pragma unroll
    for (int e = 0; e < 2; ++e) { GLD128(ga[e], mvo, sws, zs, WS_MODF + 1024 * 4 + e * 32); GLD128(cb[e], mvo, sws, zs, WS_MODF + 2048 * 4 + e * 32); }
    if (PH == 0) UV_PART(0);
    const int tn = w >> 1 & 1, kh = w >> 2;
    u32 ord = 65536 + (u32)(tm * 32768 + kh * 8192) + lo16;
    PIN(ord);
    const u32 wrd = (wrd0 ^ (u32)(tn * 64)) + kh * 16384;
    f32x16 acc = zero;
#pragma unroll
    for (int s = 0; s < 2; ++s) {
      bf16x8 wq[8], oq[8];
      if (PH == 0) {
#pragma unroll
        for (int j = 0; j < 8; ++j) wq[j] = lds_tr2(smem, wrd + (s * 16 + j) * 2048, wrd + (s * 16 + j) * 2048 + 1024);
      }
      if (s == 0) WAITV(8); else WAITV(4);
      BAR();
      if (PH != 0) {
#pragma unroll
        for (int j = 0; j < 8; ++j) wq[j] = lds_tr2(smem, wrd + (s * 16 + j) * 2048, wrd + (s * 16 + j) * 2048 + 1024);
      }
#pragma unroll
      for (int j = 0; j < 4; ++j) oq[j] = lds_frag(smem, ord + (s * 16 + j) * 1024);
      SB();
#pragma unroll
      for (int j = 0; j < 8; ++j) {
        if (j + 4 < 8) oq[j + 4] = lds_frag(smem, ord + (s * 16 + j + 4) * 1024);
        acc = MFMA32(wq[j], oq[j], acc);
        SB();
      }
    }
    STAMP(9);
    LAS f32x4* ex = (LAS f32x4*)((LAS char*)smem + EX3);
    float mine[8];
#pragma unroll
    for (int i = 0; i < 8; ++i) mine[i] = kh ? acc[8 + i] : acc[i];
    {
      f32x4 a, b;
#pragma unroll
      for (int i = 0; i < 4; ++i) { a[i] = kh ? acc[i] : acc[8 + i]; b[i] = kh ? acc[4 + i] : acc[12 + i]; }
      ex[(w ^ 4) * 128 + l] = a; ex[(w ^ 4) * 128 + 64 + l] = b;
    }
    WAITV(0);
    BAR();
    {
      const f32x4 a = ex[w * 128 + l], b = ex[w * 128 + 64 + l];
#pragma unroll
      for (int i = 0; i < 4; ++i) { mine[i] += a[i]; mine[4 + i] += b[i]; }
    }
    const int row = tm * 32 + (l & 31);
    float xv[8];
    u32x4 x1p, yp;
#pragma unroll
    for (int e = 0; e < 2; ++e) {
      PIN(ga[e]); PIN(cb[e]);
      const u32x2 xr = *(const LAS u32x2*)((LAS char*)smem + XT + row * 128 + (((tn * 4 + kh * 2 + e) ^ (row >> 1 & 7)) * 16) + hb * 8);
#pragma unroll
      for (int h = 0; h < 2; ++h) {
        const u32 pk = pack2(bf_lo(xr[h]) + __uint_as_float(ga[e][2 * h]) * mine[4 * e + 2 * h], bf_hi(xr[h]) + __uint_as_float(ga[e][2 * h + 1]) * mine[4 * e + 2 * h + 1]);
        x1p[2 * e + h] = pk;
        xv[4 * e + 2 * h] = bf_lo(pk); xv[4 * e + 2 * h + 1] = bf_hi(pk);
        yp[2 * e + h] = pack2(bf_lo(pk) * __uint_as_float(cb[e][2 * h]), bf_hi(pk) * __uint_as_float(cb[e][2 * h + 1]));
      }
    }
    GST_SC1("x4", yp, lo16, sws, (u32)((rt3 + tm) * 32 + pn3 * 4 + tn * 2 + kh) * 1024, WS_Y);
    float sum = 0.f;
#pragma unroll
    for (int i = 0; i < 8; ++i) sum += xv[i];
    sum = xor32_sum(sum);
    const float mean = sum * (1.f / 16.f);
    float sq = 0.f;
#pragma unroll
    for (int i = 0; i < 8; ++i) { const float d = xv[i] - mean; sq += d * d; }
    sq = xor32_sum(sq);
    LAS f32x2* sp = (LAS f32x2*)((LAS char*)smem + RST3);
    if (l < 32) sp[(w >> 1) * 64 + row] = (f32x2){mean, sq};
    BAR();
    if (w == 0) {
      const f32x2 p0 = sp[l], p1 = sp[64 + l], p2 = sp[128 + l], p3 = sp[192 + l];
      const float mu = 0.25f * ((p0[0] + p1[0]) + (p2[0] + p3[0]));
      const float d0 = p0[0] - mu, d1 = p1[0] - mu, d2 = p2[0] - mu, d3 = p3[0] - mu;
      const f32x2 o2 = {mu, (p0[1] + p1[1]) + (p2[1] + p3[1]) + 16.f * ((d0 * d0 + d1 * d1) + (d2 * d2 + d3 * d3))};
      GST_SC1("x2", o2, (u32)(hb * 2048 + (l & 31) * 8), sws, (u32)(rt3 * 2048 + pn3 * 256), WS_ST2);
    }
    STAMP(10);
    WAITV(0);
    if (PH == 0) {
      const int pan = xcd * 2 + (pm3 >> 1);
      raise_flag(ws, 1, l < 14, (pan >> 2) * 2 + (l >= 7 ? 1 : 0) + 8 * ((pan & 3) * 7 + (l >= 7 ? l - 7 : l)), ((pm3 & 1) * 8 + pn3) * 8 + w);
    }
    STAMP(11);
    *(u32x4*)(ws + WS_X1 + (size_t)(bid * 512 + t) * 16) = x1p;
  }

  const u32 dsb = (u32)(w * 16384 + pn3 * 128), dlb = lb + w * 2048;
#define WDPIECE(c, slot, h) GLDS(wvo, sd, dsb, (c) * 131072 + (h) * 8192, dlb, (slot) * 16384 + (h) * 1024)
#define WDISSUE(c, slot) do { GLDS(wvo, sd, dsb, (c) * 131072, dlb, (slot) * 16384); GLDS(wvo, sd, dsb, (c) * 131072 + 8192, dlb, (slot) * 16384 + 1024); } while (0)
  if ((PH == 0 || PH == 4) && loc < 28) {
    if (PH == 0) { wait_flags(ws, 1, 64, w); BAR(); }
    STAMP(12);
    const int rt4 = pan4 * 4;
    const u32 ysb = (u32)(rt4 * 32768 + w * 1024), ylb = lb + w * 1024;
#define YISSUE(a) _Pragma("unroll") for (int j = 0; j < 4; ++j) GLDS_SC1(lo16, sws, ysb, WS_Y + (a) * 32768 + j * 8192, ylb, (a) * 32768 + j * 8192)
    YISSUE(0);
    GLDS_SC1(lo16, sws, (u32)(rt4 * 2048 + w * 1024), WS_ST2, ylb, 131072);
    YISSUE(1);
    UV_PART(16);
    float uu[16], vv[16];
#pragma unroll
    for (int i = 0; i < 16; ++i) {
      const auto r = __builtin_amdgcn_permlane16_swap(__float_as_uint(uv[i]), __float_as_uint(uv[i]), false, false);
      uu[i] = __uint_as_float(r[0]); vv[i] = __uint_as_float(r[1]);
    }
    u32 yrd0 = lo16, yrd1 = lo16 + 65536;
    PIN(yrd1);
    const u32 asb = (u32)(rt4 * 112 + pn4 * 8 + w) * 1024;
#define YFRAG(n) lds_frag(smem, ((n) < 64 ? yrd0 : yrd1) + ((n) & 63) * 1024)
    WAITV(4);
    BAR();
    STAMP(13);
    bf16x8 yf[128];
#pragma unroll
    for (int n = 0; n < 4; ++n) yf[n] = YFRAG(n);
    SB();
    f32x16 acc = zero, accp = zero;
    f32x2 stp = {0.f, 0.f};
    float eg[8], eu[8], et[8];
    u32x4 ov;
    SFOR(n, 0, 160)
      constexpr int a = n >> 5, ks = n & 31;
      if (n == 0) STAMP(22);
      if (n == 26) STAMP(23);
      if (n == 56) STAMP(25);
      if (n == 36) STAMP(27);
      if (n == 68) STAMP(29);
      if (n == 100) STAMP(31);
      if (n == 26) { WAITV(8); BAR(); }
      if (n == 56) { if (PH == 0) WAITV(4); else WAITV(0); RAWBAR(); }
      if constexpr (n < 24 && n % 3 == 0) GLDS_SC1(lo16, sws, ysb, WS_Y + 65536 + (n / 3) * 8192, ylb, 65536 + (n / 3) * 8192);
      if constexpr (PH == 0) {
        if (n == 36 || n == 68 || n == 100) RAWBAR();
        if constexpr (n == 60) {
          const int rr = pm3 * 224 + w * 28 + (l < 28 ? l : 27);
          GTOUCH((u32)(rr + (rr < 256 ? 768 : 896)) * 1024, sd, (u32)(pn3 * 128), 0, lb + SCR);
        }
        if constexpr (n >= 36 && n < 52 && n % 4 == 0) WDPIECE((n - 36) / 8, (n - 36) / 8, (n - 36) / 4 & 1);
        if constexpr (n >= 68 && n < 84 && n % 4 == 0) WDPIECE(2 + (n - 68) / 8, 2 + (n - 68) / 8, (n - 68) / 4 & 1);
        if constexpr (n == 84 || n == 88) WDPIECE(8, 8, (n - 84) / 4);
        if constexpr (n >= 100 && n < 116 && n % 4 == 0) WDPIECE(4 + (n - 100) / 8, 4 + (n - 100) / 8, (n - 100) / 4 & 1);
      }
      if (n == 26) STAMP(24);
      if (n == 56) STAMP(26);
      if (n == 36) STAMP(28);
      if (n == 68) STAMP(30);
      if (n == 100) STAMP(32);
      if constexpr (n == 4) {
      const LAS f32x2* sp = (const LAS f32x2*)((LAS char*)smem + 131072 + (t >> 7) * 2048 + (t & 3) * 512 + (t >> 2 & 31) * 8);
      const f32x2 p0 = sp[0], p1 = sp[32];
      float mu = p0[0] + p1[0];
      mu += dpp_xor1(mu); mu += dpp_xor2(mu);
      mu *= 0.125f;
      float q = p0[1] + p1[1] + 64.f * ((p0[0] - mu) * (p0[0] - mu) + (p1[0] - mu) * (p1[0] - mu));
      q += dpp_xor1(q); q += dpp_xor2(q);
      const float rs = __builtin_amdgcn_rsqf(q * (1.f / 512.f) + 1e-5f);
      ((LAS f32x2*)((LAS char*)smem + RST4))[t >> 2] = (f32x2){rs, -mu * rs};
    }
      if (ks == 28 && a < 4) stp = ((const LAS f32x2*)((LAS char*)smem + RST4))[a * 32 + (l & 31)];
      if (ks == 0 && a > 0) { accp = acc; acc = zero; }
      if (n == 128) STAMP(14);
      if constexpr (n + 4 < 128) yf[n + 4] = YFRAG(n + 4);
      if constexpr (n < 128) acc = MFMA32(wf[ks], yf[n], acc);
      if constexpr (a > 0 && ks >= 2 && ks < 26) {
        constexpr int i = (ks - 2) / 3, ph = (ks - 2) % 3;
        if (ph == 0) {
          eg[i] = __builtin_fmaf(accp[i], stp[0], __builtin_fmaf(uu[i], stp[1], vv[i]));
          eu[i] = __builtin_fmaf(accp[8 + i], stp[0], __builtin_fmaf(uu[8 + i], stp[1], vv[8 + i]));
          et[i] = -1.4426950408889634f * eg[i];
          PIN(eg[i]); PIN(eu[i]); PIN(et[i]);
        } else if (ph == 1) {
          et[i] = 1.f + __builtin_amdgcn_exp2f(et[i]);
          eg[i] *= eu[i];
          PIN(eg[i]); PIN(et[i]);
        } else {
          eg[i] *= __builtin_amdgcn_rcpf(et[i]);
          PIN(eg[i]);
          if constexpr (i & 1) ov[i >> 1] = pack2(eg[i - 1], eg[i]);
        }
      }
      if constexpr (a > 0 && ks == 26) GST_SC1("x4", ov, lo16, sws, asb, WS_ACT + (a - 1) * 112 * 1024);
      if (n < 128) SB();
    SEND
    STAMP(15);
    WAITV(0);
    if (PH == 0) raise_flag(ws, 2, l < 16, (pan4 >> 1) + 8 * ((pan4 & 1) * 16 + l), pn4 * 8 + w);
    STAMP(16);
  } else if (PH == 0) {
    BAR();
    WDISSUE(0, 0); WDISSUE(1, 1); WDISSUE(2, 2); WDISSUE(3, 3); WDISSUE(8, 8); WDISSUE(4, 4); WDISSUE(5, 5);
  }

  if (PH == 0 || PH == 5) {
    if (PH == 5) { WDISSUE(0, 0); WDISSUE(1, 1); WDISSUE(2, 2); WDISSUE(3, 3); WDISSUE(8, 8); WDISSUE(4, 4); WDISSUE(5, 5); }
    WAITV(0);
    BAR();
    if (PH == 5 || w != 0) { WDISSUE(6, 6); WDISSUE(7, 7); WDISSUE(9, 9); }
    const int kq = w >> 1;
    u32 wra[3][2];
#pragma unroll
    for (int i = 0; i < 3; ++i) { wra[i][0] = wrd0 + kq * 4096 + i * 65536; wra[i][1] = wra[i][0] ^ 64u; PIN(wra[i][0]); PIN(wra[i][1]); }
    bf16x8 dq[14][4];
#define DFRAG(c) _Pragma("unroll") for (int i_ = 0; i_ < 4; ++i_) {                                              \
      const u32 o_ = wra[((c) % 10) >> 2][i_ & 1] + (((c) % 10) & 3) * 16384 + (i_ >> 1) * 2048; dq[c][i_] = lds_tr2(smem, o_, o_ + 1024); }
    DFRAG(0); DFRAG(1); DFRAG(2); DFRAG(3);
    BAR();
    if (PH == 5 || w != 0) { WDISSUE(10, 0); WDISSUE(11, 1); WDISSUE(12, 2); WDISSUE(13, 3); }
    STAMP(17);
    if (PH == 0) {
      wait_flags(ws, 2, 56, w);
      RAWBAR();
    }
    STAMP(18);
    const u32 asb = (u32)((rt3 + tm) * 112 + kq * 2) * 1024;
    u32x4 af[14][2];
#define AISSUE(c) do { GLD128_SC1(af[c][0], lo16, sws, asb, WS_ACT + (c) * 8192); GLD128_SC1(af[c][1], lo16, sws, asb, WS_ACT + (c) * 8192 + 1024); } while (0)
    AISSUE(0); AISSUE(1);
    if (PH == 0 && w == 0) { WDISSUE(6, 6); WDISSUE(7, 7); WDISSUE(9, 9); WDISSUE(10, 0); WDISSUE(11, 1); WDISSUE(12, 2); WDISSUE(13, 3); }
    AISSUE(2); AISSUE(3); AISSUE(4); AISSUE(5);
    f32x16 acc[2] = {zero, zero};
    u32x4 x1p, gb[2];
#pragma unroll
    for (int c = 0; c < 14; ++c) {
      if (c < 8) WAITV(10); else if (c == 8) WAITV(13); else if (c == 9) WAITV(11);
      else if (c == 10) WAITV(9); else if (c == 11) WAITV(7); else if (c == 12) WAITV(5); else WAITV(0);
      if (c == 0) STAMP(19);
      if (c == 4 || c == 9) RAWBAR();
      if (c < 8) AISSUE(c + 6);
      if (c == 7) {
        GLD128(x1p, (u32)t * 16, sws, (u32)bid * 8192, WS_X1);
        GLD128(gb[0], mvo, sws, zs, WS_MODF + 2560 * 4); GLD128(gb[1], mvo, sws, zs, WS_MODF + 2560 * 4 + 32);
      }
      if (c >= 3 && c < 13) { DFRAG(c + 1); }
      PIN(af[c][0]); PIN(af[c][1]);
      SB();
#pragma unroll
      for (int i = 0; i < 4; ++i) acc[i & 1] = MFMA32(dq[c][i], __builtin_bit_cast(bf16x8, af[c][i >> 1]), acc[i & 1]);
      SB();
    }
    STAMP(20);
    LAS f32x4* ex = (LAS f32x4*)((LAS char*)smem + 65536);
#pragma unroll
    for (int qd = 0; qd < 4; ++qd)
#pragma unroll
      for (int h = 0; h < 2; ++h) {
        const int b = (qd >> 1) * 8 + h * 4;
        ex[(((tm + 2 * qd) * 4 + kq) * 2 + h) * 64 + l] = (f32x4){acc[qd & 1][b], acc[qd & 1][b + 1], acc[qd & 1][b + 2], acc[qd & 1][b + 3]};
      }
    BAR();
    u32 res[4];
    PIN(x1p);
#pragma unroll
    for (int h = 0; h < 2; ++h) {
      PIN(gb[h]);
      const f32x4 s = (ex[((w * 4 + 0) * 2 + h) * 64 + l] + ex[((w * 4 + 1) * 2 + h) * 64 + l]) + (ex[((w * 4 + 2) * 2 + h) * 64 + l] + ex[((w * 4 + 3) * 2 + h) * 64 + l]);
      res[2 * h] = pack2(bf_lo(x1p[2 * h]) + __uint_as_float(gb[h][0]) * s[0], bf_hi(x1p[2 * h]) + __uint_as_float(gb[h][1]) * s[1]);
      res[2 * h + 1] = pack2(bf_lo(x1p[2 * h + 1]) + __uint_as_float(gb[h][2]) * s[2], bf_hi(x1p[2 * h + 1]) + __uint_as_float(gb[h][3]) * s[3]);
    }
    const auto r0 = __builtin_amdgcn_permlane32_swap(res[0], res[2], false, false);
    const auto r1 = __builtin_amdgcn_permlane32_swap(res[1], res[3], false, false);
    const u32x4 ov = {r0[0], r1[0], r0[1], r1[1]};
    *(u32x4*)((char*)out + (size_t)((rt3 + tm) * 32 + (l & 31)) * 1024 + (ncol + hb * 8) * 2) = ov;
  }
  ST_OUT(PH == 0 ? 2 : PH, ws);
}

#define KB_KERNEL(name, PH)                                                                                                       \
  extern "C" __global__ __launch_bounds__(512) void name(const u16* __restrict__ x, const u16* __restrict__ wo, const u16* __restrict__ wg, \
      const u16* __restrict__ wu, const u16* __restrict__ wd, char* __restrict__ ws, u16* __restrict__ out) {                       \
    __shared__ __attribute__((aligned(16))) char smem[163840];                                                                      \
    kb_body<PH>(x, wo, wg, wu, wd, ws, out, smem);                                                                                  \
  }
KB_KERNEL(kb_block, 0)
extern "C" void kernel_launch(void* const* d_in, const int* in_sizes, int n_in, void* d_out, int out_size, void* d_ws, size_t ws_size,
                              hipStream_t stream) {
  const u16* x = (const u16*)d_in[0];
  const u16* wqkv = (const u16*)d_in[1];
  const u16* wo = (const u16*)d_in[2];
  const u16* wg = (const u16*)d_in[4];
  const u16* wu = (const u16*)d_in[5];
  const u16* wd = (const u16*)d_in[6];
  const u16* c = (const u16*)d_in[7];
  const u16* wmod = (const u16*)d_in[8];
  char* ws = (char*)d_ws;
  k0_mod<<<448, 256, 0, stream>>>(c, wmod, x, (float*)(ws + WS_MODP), (f32x2*)(ws + WS_ST1));
  k1_qkv<<<256, 512, 0, stream>>>(x, wqkv, (const float*)(ws + WS_ST1), ws);
  kb_block<<<256, 512, 0, stream>>>(x, wo, wg, wu, wd, ws, (u16*)d_out);
}
```

```cpp
#include <hip/hip_runtime.h>
#include <hip/hip_bf16.h>

typedef unsigned short u16;
typedef unsigned int u32;
typedef u32 u32x2 __attribute__((ext_vector_type(2)));
typedef u32 u32x4 __attribute__((ext_vector_type(4)));
typedef float f32x2 __attribute__((ext_vector_type(2)));
typedef float f32x4 __attribute__((ext_vector_type(4)));
typedef float f32x8 __attribute__((ext_vector_type(8)));
typedef float f32x16 __attribute__((ext_vector_type(16)));
typedef __bf16 bf16x2 __attribute__((ext_vector_type(2)));
typedef __bf16 bf16x8 __attribute__((ext_vector_type(8)));
typedef __bf16 bf16x16 __attribute__((ext_vector_type(16)));
typedef short s16x4 __attribute__((ext_vector_type(4)));
typedef short s16x8 __attribute__((ext_vector_type(8)));
#define PRIO_MODE 1
#define GATHER_AT 5
#define LAS __attribute__((address_space(3)))
#define DI __device__ __forceinline__

constexpr u32 WS_GRAN = 0;
constexpr u32 WS_EPOCH = 0x18000;
constexpr u32 WS_CNT  = 0x20000;
constexpr u32 WS_QF   = 0x100000;
constexpr u32 WS_KV   = 0x300000;
constexpr u32 WS_OF   = 0x800000;
constexpr u32 WS_X1   = 0xA00000;
constexpr u32 WS_Y    = 0xC00000;
constexpr u32 WS_ST2  = 0xE00000;
constexpr u32 WS_ACT  = 0xF00000;
constexpr u32 WS_BYTES = 0x1600000;
constexpr int KV_TILE = 2112, KV_HEAD = 64 * KV_TILE;

template <int I> struct IC { static constexpr int value = I; };
template <int B, int E, class F> DI void static_for(F&& f) { if constexpr (B < E) { f(IC<B>{}); static_for<B + 1, E>(f); } }
#define SFOR(n, B, E) static_for<B, E>([&](auto ic_) __attribute__((always_inline)) { constexpr int n = decltype(ic_)::value;
#define SEND });

DI float bf_lo(u32 u) { return __uint_as_float(u << 16); }
DI float bf_hi(u32 u) { return __uint_as_float(u & 0xffff0000u); }
DI u32 pack2(float a, float b) { bf16x2 r = {(__bf16)a, (__bf16)b}; return __builtin_bit_cast(u32, r); }
DI float bfround(float a) { return (float)(__bf16)a; }
DI float xor32_sum(float v) {
  const auto r = __builtin_amdgcn_permlane32_swap(__float_as_uint(v), __float_as_uint(v), false, false);
  return __uint_as_float(r[0]) + __uint_as_float(r[1]);
}
DI float dpp_xor1(float v) { return __int_as_float(__builtin_amdgcn_mov_dpp(__float_as_int(v), 0xB1, 0xF, 0xF, true)); }
DI float dpp_xor2(float v) { return __int_as_float(__builtin_amdgcn_mov_dpp(__float_as_int(v), 0x4E, 0xF, 0xF, true)); }
DI float shfl_idx(float v, int srcLane) { return __int_as_float(__builtin_amdgcn_ds_bpermute(srcLane << 2, __float_as_int(v))); }

DI u32x4 make_srd(const void* p, u32 bytes) {
  const size_t a = (size_t)p;
  u32x4 r = {(u32)a, (u32)(a >> 32) & 0xffffu, bytes, 0x20000u};
  return r;
}
#define GLDS(voff, srd, sb, GO, lb, LO)                                                                                   \
  do { u32 tmp_;                                                                                                          \
    asm volatile("s_add_u32 m0, %4, %6\n\ts_add_u32 %0, %3, %5\n\tbuffer_load_dwordx4 %1, %2, %0 offen lds"            \
                 : "=&s"(tmp_) : "v"(voff), "s"(srd), "s"(sb), "s"(lb), "i"(GO), "i"(LO) : "memory", "scc"); } while (0)
#define GLD(W, dst, voff, srd, sb, GO)                                                                                    \
  do { u32 tmp_;                                                                                                          \
    asm volatile("s_add_u32 %1, %4, %5\n\tbuffer_load_dword" W " %0, %2, %3, %1 offen"                                   \
                 : "=v"(dst), "=&s"(tmp_) : "v"(voff), "s"(srd), "s"(sb), "i"(GO) : "memory", "scc"); } while (0)
#define GLD128(dst, voff, srd, sb, GO) GLD("x4", dst, voff, srd, sb, GO)
#define GLD64(dst, voff, srd, sb, GO) GLD("x2", dst, voff, srd, sb, GO)
#define GLD32(dst, voff, srd, sb, GO) GLD("", dst, voff, srd, sb, GO)
#define WAITV(N) asm volatile("s_waitcnt vmcnt(" #N ")" ::: "memory")
#define PIN(x) asm volatile("" : "+v"(x))
#define BAR() do { asm volatile("s_waitcnt lgkmcnt(0)" ::: "memory"); __builtin_amdgcn_s_barrier(); asm volatile("" ::: "memory"); } while (0)

#define ST_DECL
#define STAMP(i)
#define ST_OUT(kid, wsp)

DI bf16x8 lds_frag(const char* smem, u32 off) { return *(const LAS bf16x8*)((const LAS char*)smem + off); }
DI bf16x8 lds_tr2(const char* smem, u32 off0, u32 off1) {
  s16x4 a = __builtin_amdgcn_ds_read_tr16_b64_v4i16((LAS s16x4*)((LAS char*)smem + off0));
  s16x4 b = __builtin_amdgcn_ds_read_tr16_b64_v4i16((LAS s16x4*)((LAS char*)smem + off1));
  s16x8 r = __builtin_shufflevector(a, b, 0, 1, 2, 3, 4, 5, 6, 7);
  return __builtin_bit_cast(bf16x8, r);
}
DI u32 lds_base(const char* smem) { return (u32)(size_t)(const LAS char*)smem; }
#define SB() __builtin_amdgcn_sched_barrier(0)
#define RAWBAR() do { asm volatile("" ::: "memory"); __builtin_amdgcn_s_barrier(); asm volatile("" ::: "memory"); } while (0)
#define MFMA32(a, b, c) __builtin_amdgcn_mfma_f32_32x32x16_bf16((a), (b), (c), 0, 0, 0)
#define MFMA16(a, b, c) __builtin_amdgcn_mfma_f32_16x16x32_bf16((a), (b), (c), 0, 0, 0)

DI u32x4 modulate8(const u32x4 v, float rs, float mr, const float* c1, const float* sh) {
  u32x4 o;
#pragma unroll
  for (int e = 0; e < 4; ++e) {
    const float a = __builtin_fmaf(__builtin_fmaf(bf_lo(v[e]), rs, mr), c1[2 * e], sh[2 * e]);
    const float b = __builtin_fmaf(__builtin_fmaf(bf_hi(v[e]), rs, mr), c1[2 * e + 1], sh[2 * e + 1]);
    o[e] = pack2(a, b);
  }
  return o;
}

#define GLDS_SC1(voff, srd, sb, GO, lb, LO)                                                                               \
  do { u32 tmp_;                                                                                                          \
    asm volatile("s_add_u32 m0, %4, %6\n\ts_add_u32 %0, %3, %5\n\tbuffer_load_dwordx4 %1, %2, %0 offen sc1 lds"         \
                 : "=&s"(tmp_) : "v"(voff), "s"(srd), "s"(sb), "s"(lb), "i"(GO), "i"(LO) : "memory", "scc"); } while (0)
#define GLD128_SC1(dst, voff, srd, sb, GO)                                                                                \
  do { u32 tmp_;                                                                                                          \
    asm volatile("s_add_u32 %1, %4, %5\n\tbuffer_load_dwordx4 %0, %2, %3, %1 offen sc1"                                 \
                 : "=v"(dst), "=&s"(tmp_) : "v"(voff), "s"(srd), "s"(sb), "i"(GO) : "memory", "scc"); } while (0)
#define GST_SC1(W, val, voff, srd, sb, GO)                                                                                \
  do { u32 tmp_;                                                                                                          \
    asm volatile("s_add_u32 %0, %4, %5\n\tbuffer_store_dword" W " %1, %2, %3, %0 offen sc1\n\ts_nop 1"                  \
                 : "=&s"(tmp_) : "v"(val), "v"(voff), "s"(srd), "s"(sb), "i"(GO) : "memory", "scc"); } while (0)
#define GTOUCH(voff, srd, sb, GO, lb)                                                                                     \
  do { u32 tmp_;                                                                                                          \
    asm volatile("s_mov_b32 m0, %4\n\ts_add_u32 %0, %3, %5\n\tbuffer_load_dword %1, %2, %0 offen lds"                   \
                 : "=&s"(tmp_) : "v"(voff), "s"(srd), "s"(sb), "s"(lb), "i"(GO) : "memory", "scc"); } while (0)
#define GLDS64(vaddr, lb, LO)                                                                                             \
    asm volatile("s_add_u32 m0, %1, %2\n\ts_nop 0\n\tglobal_load_lds_dwordx4 %0, off" :: "v"(vaddr), "s"(lb), "i"(LO) : "memory", "scc")

DI void wave_sum8(const float* v, float* z) {
  float u[4];
#pragma unroll
  for (int i = 0; i < 4; ++i) {
    const auto a = __builtin_amdgcn_permlane32_swap(__float_as_uint(v[i]), __float_as_uint(v[i + 4]), false, false);
    u[i] = __uint_as_float(a[0]) + __uint_as_float(a[1]);
  }
#pragma unroll
  for (int i = 0; i < 2; ++i) {
    const auto a = __builtin_amdgcn_permlane16_swap(__float_as_uint(u[i]), __float_as_uint(u[i + 2]), false, false);
    float s = __uint_as_float(a[0]) + __uint_as_float(a[1]);
    s += dpp_xor1(s); s += dpp_xor2(s);
    s += __int_as_float(__builtin_amdgcn_mov_dpp(__float_as_int(s), 0x141, 0xF, 0xF, true));
    s += __int_as_float(__builtin_amdgcn_mov_dpp(__float_as_int(s), 0x140, 0xF, 0xF, true));
    z[i] = s;
  }
}
#define ROW_OF(z, r) __int_as_float(__builtin_amdgcn_readlane(__float_as_int(z[(r) & 1]), ((r) >> 1) * 16))
DI float col8_sum(float v) {
  v += __int_as_float(__builtin_amdgcn_mov_dpp(__float_as_int(v), 0x128, 0xF, 0xF, true));
  const auto a = __builtin_amdgcn_permlane16_swap(__float_as_uint(v), __float_as_uint(v), false, false);
  return xor32_sum(__uint_as_float(a[0]) + __uint_as_float(a[1]));
}

DI void signal_cnt(char* ws, int idx) {
  if (threadIdx.x == 0) __hip_atomic_fetch_add((u32*)(ws + WS_CNT) + idx * 64, 1u, __ATOMIC_RELAXED, __HIP_MEMORY_SCOPE_AGENT);
}
DI void wait_cnt(char* ws, int idx, u32 want, int w) {
  if (w == 0) {
    u32* c = (u32*)(ws + WS_CNT) + idx * 64;
#pragma unroll 1
    for (u32 spins = 0; spins < (1u << 17); ++spins) {
      if ((u32)__builtin_amdgcn_readfirstlane(__hip_atomic_load(c, __ATOMIC_RELAXED, __HIP_MEMORY_SCOPE_AGENT)) >= want) break;
      __builtin_amdgcn_s_sleep(1);
    }
    asm volatile("" ::: "memory");
  }
}
DI float gsum(const u32x4 a, const u32x4 b) { return (__uint_as_float(a[0]) + __uint_as_float(a[2])) + (__uint_as_float(b[0]) + __uint_as_float(b[2])); }
DI bool gok(const u32x4 a, u32 tag) { return a[1] == tag && a[3] == tag; }
#define SMF(v, p, o) __builtin_amdgcn_smfmac_f32_32x32x32_bf16((v), __builtin_bit_cast(bf16x16, (p)), (o), 0xDDDD, 0, 0)
#define LO8(s) __builtin_shufflevector(s, s, 0, 1, 2, 3, 4, 5, 6, 7)
#define HI8(s) __builtin_shufflevector(s, s, 8, 9, 10, 11, 12, 13, 14, 15)

extern "C" __global__ __launch_bounds__(512) void adaln_block(const u16* __restrict__ x, const u16* __restrict__ wqkv, const u16* __restrict__ wo,
                                                              const u16* __restrict__ wg, const u16* __restrict__ wu, const u16* __restrict__ wd,
                                                              const u16* __restrict__ c, const u16* __restrict__ wmod, char* __restrict__ ws,
                                                              u16* __restrict__ out) {
  __shared__ __attribute__((aligned(16))) char smem[163840];
  ST_DECL STAMP(0);
  const int bid = blockIdx.x, xcd = bid & 7, loc = bid >> 3;
  const int t = threadIdx.x, l = t & 63;
  const int w = __builtin_amdgcn_readfirstlane(t >> 6);
  const u32 lb = lds_base(smem);
  const u32x4 sws = make_srd(ws, WS_BYTES), sx = make_srd(x, 2048 * 1024);
  const u32 lo16 = (u32)l * 16, zs = 0;
  const int hb = l >> 5;
  const int pm3 = loc >> 3, pn3 = loc & 7;
  constexpr u32 TAB = 159744, MT = TAB + 2048, SCR = TAB;
  u32 tag;

  {
    constexpr u32 WSLOT = 24576, RED = 49152, MTAB = 53248, EXO = 114688;
    const int pm = (xcd >> 1) * 8 + (loc >> 2), cg = (xcd & 1) * 4 + (loc & 3);
    const u32x4 sw = make_srd(wqkv, 512 * 3072), sm = make_srd(wmod, 512 * 6144), sc = make_srd(c, 1024);
    const int task = bid < 192 ? bid : 191, mcb = task >> 2, mkq = task & 3;
    u32 ev, cv[2];
    u32x4 mv[2], xa[8];
    if (bid == 0 && w == 0) GST_SC1("", zs, (u32)l * 256, sws, zs, WS_CNT);
    asm volatile("buffer_load_dword %0, %1, %2, %3 offen sc1" : "=v"(ev) : "v"(zs), "s"(sws), "s"(WS_EPOCH) : "memory");
#pragma unroll
    for (int i = 0; i < 2; ++i) GLD128(mv[i], (u32)((t >> 3) * 6144 + (t & 7) * 16), sm, (u32)(mkq * 128 * 6144 + mcb * 128), i * 64 * 6144);
#pragma unroll
    for (int i = 0; i < 2; ++i) GLD32(cv[i], (u32)((t >> 4) * 4), sc, (u32)(mkq * 256), i * 128);
    RAWBAR();
    const int rx = loc & 3, rw = loc >> 2;
    const u32 xsb = (u32)(pm * 64 + w * 2) * 1024;
#define XROW(i) GLD128(xa[i], lo16, sx, xsb + (u32)((((i) >> 1) + rx) & 3) * 16384, ((i) & 1) * 1024)
    const u32 wvo = (u32)((8 * (l >> 5) + (l >> 3 & 3)) * 3072 + ((((l >> 1) ^ (l >> 4)) & 3) * 16 + (l & 1) * 8) * 2);
    const u32 wsb = (u32)((32 * (w >> 2) + 16 * (w & 1) + 4 * (w >> 1 & 1)) * 3072 + cg * 128), wlb = lb + w * 1024;
#define KA_SLOT(kc) ((kc) < 2 ? (kc) * WSLOT : 65536 + (((kc) - 2) & 3) * WSLOT)
#define KA_PIECE(n) GLDS(wvo, sw, wsb + (u32)(((n) / 3 + rw) & 7) * (64 * 3072), ((n) % 3) * 1024, wlb, KA_SLOT((n) / 3) + ((n) % 3) * 8192)
    STAMP(26);
    XROW(0); XROW(1); XROW(2); XROW(3);
    STAMP(27);
    WAITV(4);
    STAMP(28);
    PIN(ev); PIN(mv[0]); PIN(mv[1]); PIN(cv[0]); PIN(cv[1]);
    tag = (u32)__builtin_amdgcn_readfirstlane(ev) + 1u;
    {
      float acc[8];
#pragma unroll
      for (int e = 0; e < 8; ++e) acc[e] = 0.f;
#pragma unroll
      for (int i = 0; i < 2; ++i) {
        const float cc = (t & 8) ? bf_hi(cv[i]) : bf_lo(cv[i]);
        const float sv = bfround(cc * __builtin_amdgcn_rcpf(1.f + __builtin_amdgcn_exp2f(-1.4426950408889634f * cc)));
#pragma unroll
        for (int e = 0; e < 4; ++e) { acc[2 * e] += sv * bf_lo(mv[i][e]); acc[2 * e + 1] += sv * bf_hi(mv[i][e]); }
      }
      float u[4], z[2];
#pragma unroll
      for (int i = 0; i < 4; ++i) {
        const auto a = __builtin_amdgcn_permlane32_swap(__float_as_uint(acc[i]), __float_as_uint(acc[i + 4]), false, false);
        u[i] = __uint_as_float(a[0]) + __uint_as_float(a[1]);
      }
#pragma unroll
      for (int i = 0; i < 2; ++i) {
        const auto a = __builtin_amdgcn_permlane16_swap(__float_as_uint(u[i]), __float_as_uint(u[i + 2]), false, false);
        z[i] = __uint_as_float(a[0]) + __uint_as_float(a[1]);
        z[i] += __int_as_float(__builtin_amdgcn_mov_dpp(__float_as_int(z[i]), 0x128, 0xF, 0xF, true));
      }
      *(LAS f32x2*)((LAS char*)smem + RED + w * 256 + (l & 7) * 32 + (l >> 4) * 8) = (f32x2){z[0], z[1]};
    }
    XROW(4); XROW(5); XROW(6); XROW(7);
    STAMP(29);
    BAR();
    if (w == 0 && bid < 192) {
      const LAS float* rp = (const LAS float*)((LAS char*)smem + RED) + l;
      const float v = ((rp[0] + rp[64]) + (rp[128] + rp[192])) + ((rp[256] + rp[320]) + (rp[384] + rp[448]));
      const u32x2 gr = {__float_as_uint(v), tag};
      GST_SC1("x2", gr, (u32)l * 32, sws, (u32)(mcb * 2048 + mkq * 8), WS_GRAN);
    }
    STAMP(1);
    KA_PIECE(0); KA_PIECE(1); KA_PIECE(2); KA_PIECE(3); KA_PIECE(4); KA_PIECE(5);
    WAITV(6);
    STAMP(30);
    u32x4 gq[4];
#define GATHER(tail)                                                                                                            \
    asm volatile("buffer_load_dwordx4 %0, %4, %5, %6 offen sc1\n\tbuffer_load_dwordx4 %1, %4, %5, %6 offen offset:16 sc1\n\t"   \
                 "buffer_load_dwordx4 %2, %4, %5, %7 offen sc1\n\tbuffer_load_dwordx4 %3, %4, %5, %7 offen offset:16 sc1" tail   \
                 : "=&v"(gq[0]), "=&v"(gq[1]), "=&v"(gq[2]), "=&v"(gq[3]) : "v"((u32)t * 32), "s"(sws), "s"(WS_GRAN), "s"(WS_GRAN + 16384) : "memory")
    float rs[8], mr[8];
    {
      float v[8], z[2];
      SFOR(i, 0, 8)
        PIN(xa[i]);
        v[i] = ((bf_lo(xa[i][0]) + bf_hi(xa[i][0])) + (bf_lo(xa[i][1]) + bf_hi(xa[i][1]))) + ((bf_lo(xa[i][2]) + bf_hi(xa[i][2])) + (bf_lo(xa[i][3]) + bf_hi(xa[i][3])));
        PIN(v[i]);
        KA_PIECE(6 + i);
      SEND
      STAMP(31);
      wave_sum8(v, z);
      z[0] *= (1.f / 512.f); z[1] *= (1.f / 512.f);
#define VAR_ROWS(B, E)                                                                                                   \
      SFOR(i, B, E)                                                                                                      \
        PIN(xa[i]);                                                                                                      \
        const float mu = ROW_OF(z, i);                                                                                   \
        float sq = 0.f;                                                                                                  \
        _Pragma("unroll") for (int e = 0; e < 4; ++e) {                                                                  \
          const float a = bf_lo(xa[i][e]) - mu, b = bf_hi(xa[i][e]) - mu; sq = __builtin_fmaf(a, a, sq); sq = __builtin_fmaf(b, b, sq); } \
        v[i] = sq;                                                                                                       \
        PIN(v[i]);                                                                                                       \
        if (i < 4) KA_PIECE(14 + (i & 3));                                                                               \
      SEND
      VAR_ROWS(0, GATHER_AT)
      GATHER("");
      VAR_ROWS(GATHER_AT, 8)
      float q[2];
      wave_sum8(v, q);
#pragma unroll
      for (int j = 0; j < 2; ++j) { q[j] = __builtin_amdgcn_rsqf(q[j] * (1.f / 512.f) + 1e-5f); z[j] = -z[j] * q[j]; }
      SFOR(i, 0, 8) rs[i] = ROW_OF(q, i); mr[i] = ROW_OF(z, i); SEND
    }
    STAMP(2);
    const int hd = w >> 1, kh = w & 1, g = l >> 4;
    u32 brd0 = (u32)((l >> 2) * 128 + ((hd ^ (l >> 3 & 3)) * 32) + (l & 3) * 8 + kh * 4096), brd1 = brd0 + 65536, brd2 = brd0 + 65536 + 2 * WSLOT;
    PIN(brd1); PIN(brd2);
    bf16x8 wq[8][3];
#define KA_FRAG(kc) _Pragma("unroll") for (int c_ = 0; c_ < 3; ++c_) {                                                    \
      const u32 o_ = ((kc) < 2 ? brd0 : (((kc) & 2) ? brd1 : brd2)) + ((kc) & 1) * WSLOT + c_ * 8192; wq[kc][c_] = lds_tr2(smem, o_, o_ + 2048); }
    WAITV(10);
    RAWBAR();
    STAMP(32);
    KA_FRAG(2); KA_FRAG(3); KA_FRAG(0); KA_FRAG(1);
    BAR();
    SFOR(n, 18, 24) KA_PIECE(n); SEND
    WAITV(6);
    PIN(gq[0]); PIN(gq[1]); PIN(gq[2]); PIN(gq[3]);
    STAMP(3);
#pragma unroll 1
    for (u32 spins = 0; spins < (1u << 17); ++spins) {
      if (__all(gok(gq[0], tag) && gok(gq[1], tag) && gok(gq[2], tag) && gok(gq[3], tag))) break;
      __builtin_amdgcn_s_sleep(2);
      GATHER("\n\ts_waitcnt vmcnt(0)");
    }
    ((LAS float*)((LAS char*)smem + MTAB))[t] = bfround(1.f + bfround(gsum(gq[2], gq[3])));
    ((LAS float*)((LAS char*)smem + MTAB))[512 + t] = bfround(gsum(gq[0], gq[1]));
    BAR();
    STAMP(33);
    float c1[8], sh[8];
    {
      const LAS f32x4* tc = (const LAS f32x4*)((LAS char*)smem + MTAB + l * 32);
      const f32x4 a0 = tc[0], a1 = tc[1], b0 = tc[128], b1 = tc[129];
#pragma unroll
      for (int e = 0; e < 4; ++e) { c1[e] = a0[e]; c1[4 + e] = a1[e]; sh[e] = b0[e]; sh[4 + e] = b1[e]; }
    }
    KA_FRAG(4); KA_FRAG(5);
    u32 awr[2];
#pragma unroll
    for (int i = 0; i < 2; ++i) awr[i] = (u32)((((l >> 2) - 2 * rw) & 15) * 1024 + (l & 3) * 256 + (((w * 2 + i) ^ (l & 7)) << 4));
    const u32 ard = (u32)(kh * 1024 + g * 256 + (((l & 15) ^ (kh * 4 + g)) << 4));
#define KA_PUT(i, val) *(LAS u32x4*)((LAS char*)smem + awr[(i) & 1] + ((i) >> 1) * 16384) = (val)
    u32x4 hv[8];
#pragma unroll
    for (int i = 0; i < 4; ++i) KA_PUT(i, modulate8(xa[i], rs[i], mr[i], c1, sh));
#pragma unroll
    for (int i = 4; i < 8; ++i) { hv[i] = modulate8(xa[i], rs[i], mr[i], c1, sh); PIN(hv[i]); }
    BAR();
    STAMP(4);
    f32x4 acc[4][3], keep[2][3];
#pragma unroll
    for (int a = 0; a < 4; ++a) acc[a][0] = acc[a][1] = acc[a][2] = (f32x4){0.f, 0.f, 0.f, 0.f};
    LAS f32x4* ex = (LAS f32x4*)((LAS char*)smem + EXO);
#define KA_SEND(p) _Pragma("unroll") for (int c_ = 0; c_ < 3; ++c_) {                                                      \
      keep[p][c_] = kh ? acc[2 * (p) + 1][c_] : acc[2 * (p)][c_];                                                          \
      ex[(((p) * 8 + (w ^ 1)) * 3 + c_) * 64 + l] = kh ? acc[2 * (p)][c_] : acc[2 * (p) + 1][c_]; }
    constexpr float QS = 0.25f * 1.4426950408889634f;
    const int head = cg * 4 + hd;
    const u32 kvb = WS_KV + (u32)(head * KV_HEAD + pm * 2 * KV_TILE), qb0 = WS_QF + (u32)(head * 64 + pm * 2) * 1024;
    const int th = (kh + rx) & 1, tp0 = (kh + rx) >> 1 & 1;
    const u32 fo = (u32)((th * 16 + (l & 15) + 32 * (g >> 1)) * 16);
    const u32 vvo = (l & 31) <= 16 ? (u32)(1024 + (th * 2 + (g >> 1)) * 272 + (l & 31) * 16) : 0x40000000u;
#define KA_STORE(p)                                                                                                        \
    do { f32x4 r_[3];                                                                                                      \
      _Pragma("unroll") for (int c_ = 0; c_ < 3; ++c_) r_[c_] = keep[p][c_] + ex[(((p) * 8 + w) * 3 + c_) * 64 + l];       \
      const auto a0 = __builtin_amdgcn_permlane16_swap(pack2(r_[0][0] * QS, r_[0][1] * QS), pack2(r_[1][0], r_[1][1]), false, false); \
      const auto a1 = __builtin_amdgcn_permlane16_swap(pack2(r_[0][2] * QS, r_[0][3] * QS), pack2(r_[1][2], r_[1][3]), false, false); \
      const u32x4 qk = {a0[0], a1[0], a0[1], a1[1]};                               \
      GST_SC1("x4", qk, fo + ((g & 1) ? kvb + (tp0 ^ (p)) * KV_TILE : qb0 + (tp0 ^ (p)) * 1024), sws, zs, 0);                              \
      const auto b0 = __builtin_amdgcn_permlane16_swap(pack2(r_[2][0], r_[2][1]), 0x3f803f80u, false, false);              \
      const auto b1 = __builtin_amdgcn_permlane16_swap(pack2(r_[2][2], r_[2][3]), 0x3f803f80u, false, false);              \
      const u32x4 vo = {b0[0], b1[0], b0[1], b1[1]};                                       \
      GST_SC1("x4", vo, vvo + kvb + (tp0 ^ (p)) * KV_TILE, sws, zs, 0); } while (0)
#define KA_MT(n) ((n) < 12 ? (n) / 6 : ((n) < 16 ? ((n) - 12) / 2 : (n) / 8))
#define KA_KC(n) ((n) < 12 ? (n) % 6 : ((n) < 16 ? 6 + ((n) & 1) : (n) % 8))
#define HFRAG(n) lds_frag(smem, ard + KA_MT(n) * 16384 + KA_KC(n) * 2048)
    bf16x8 hf[32];
#pragma unroll
    for (int i = 0; i < 4; ++i) hf[i] = HFRAG(i);
    SB();
    SFOR(n, 0, 32)
      constexpr int mt = KA_MT(n), kc = KA_KC(n);
      if constexpr (n < 4) KA_PUT(4 + n, hv[4 + n]);
      if (n == 8) STAMP(34);
      if (n == 9) STAMP(35);
      if (n == 16) STAMP(36);
      if (n == 20) STAMP(37);
      if (n == 22) STAMP(38);
      if (n == 31) STAMP(39);
      if constexpr (n == 8) { WAITV(0); BAR(); KA_FRAG(6); KA_FRAG(7); }
      if constexpr (n == 20) BAR();
      if constexpr (n + 4 < 32) hf[n + 4] = HFRAG(n + 4);
      acc[mt][0] = MFMA16(wq[kc][0], hf[n], acc[mt][0]);
      acc[mt][1] = MFMA16(wq[kc][1], hf[n], acc[mt][1]);
      acc[mt][2] = MFMA16(hf[n], wq[kc][2], acc[mt][2]);
      if constexpr (n == 16) { KA_SEND(0); }
      if constexpr (n == 21) KA_STORE(0);
      if constexpr (n == 31) { KA_SEND(1); }
      SB();
    SEND
    BAR();
    STAMP(5);
    KA_STORE(1);
    u32x4 tq[6];
    const u32 msb = WS_GRAN + (u32)(((w == 0 ? 1024 : (w == 1 ? 2048 : 2560)) + pn3 * 64) * 32);
#define TGATHER(tail)                                                                                                           \
    asm volatile("buffer_load_dwordx4 %0, %6, %8, %9 offen sc1\n\tbuffer_load_dwordx4 %1, %6, %8, %9 offen offset:16 sc1\n\t"   \
                 "buffer_load_dwordx4 %2, %6, %8, %10 offen sc1\n\tbuffer_load_dwordx4 %3, %6, %8, %10 offen offset:16 sc1\n\t" \
                 "buffer_load_dwordx4 %4, %7, %8, %11 offen sc1\n\tbuffer_load_dwordx4 %5, %7, %8, %11 offen offset:16 sc1" tail \
                 : "=&v"(tq[0]), "=&v"(tq[1]), "=&v"(tq[2]), "=&v"(tq[3]), "=&v"(tq[4]), "=&v"(tq[5])                           \
                 : "v"((u32)t * 32), "v"((u32)l * 32), "s"(sws), "s"(WS_GRAN + 1536 * 32), "s"(WS_GRAN + 2048 * 32), "s"(msb) : "memory")
    TGATHER("");
    WAITV(6);
    STAMP(6);
    BAR();
    signal_cnt(ws, cg);
    WAITV(0);
#pragma unroll
    for (int i = 0; i < 6; ++i) PIN(tq[i]);
#pragma unroll 1
    for (u32 spins = 0; spins < (1u << 17); ++spins) {
      if (__all(gok(tq[0], tag) && gok(tq[1], tag) && gok(tq[2], tag) && gok(tq[3], tag) && gok(tq[4], tag) && gok(tq[5], tag))) break;
      __builtin_amdgcn_s_sleep(2);
      TGATHER("\n\ts_waitcnt vmcnt(0)");
    }
    *(LAS u16*)((LAS char*)smem + TAB + t * 2) = (u16)(__float_as_uint(bfround(1.f + bfround(gsum(tq[2], tq[3])))) >> 16);
    *(LAS u16*)((LAS char*)smem + TAB + 1024 + t * 2) = (u16)(__float_as_uint(bfround(gsum(tq[0], tq[1]))) >> 16);
    const float mv_ = bfround(gsum(tq[4], tq[5]));
    ((LAS float*)((LAS char*)smem + MT))[t] = (w == 1) ? bfround(1.f + mv_) : mv_;
  }

  const int lq = loc >= 28 ? 27 : loc;
  const int pan4 = (xcd >> 1) * 4 + lq / 7, pn4 = (xcd & 1) * 7 + lq % 7;
  constexpr u32 STG = KV_HEAD;
  f32x16 zero;
#pragma unroll
  for (int i = 0; i < 16; ++i) zero[i] = 0.f;
  bf16x8 wf[32];

  {
    const char* wsrc = (const char*)((l & 2) ? wu : wg) + (size_t)(l >> 2) * 3584 + (pn4 * 128 + w * 16 + (l & 1) * 8) * 2;
    const u32 stg_lb = lb + w * 3072;
    u32 stg_rd0 = (u32)(w * 3072 + (hb * 8 + (l >> 2 & 3)) * 64 + (l >> 4 & 1) * 32 + (l & 3) * 8), stg_rd1 = stg_rd0 + STG;
    PIN(stg_rd1);
#define WSTEP(p)                                                                                        \
    do {                                                                                                \
      if constexpr ((p) >= 5) {                                                                         \
        if ((p) <= 32) asm volatile("s_waitcnt vmcnt(4) lgkmcnt(0)" ::: "memory");                      \
        else if ((p) == 33) WAITV(3); else if ((p) == 34) WAITV(2); else if ((p) == 35) WAITV(1); else WAITV(0); \
        const u32 o_ = ((((p) - 5) % 6) < 3 ? stg_rd1 : stg_rd0) + (((p) - 5) % 3) * 1024;              \
        wf[(p) - 5] = lds_tr2(smem, o_, o_ + 256);                                                      \
      }                                                                                                 \
      if ((p) < 32) { GLDS64(wsrc, stg_lb, (((p) % 6) < 3 ? STG : 0u) + ((p) % 3) * 1024u); wsrc += 16 * 3584; } \
    } while (0)
    const int head = xcd * 4 + (loc >> 3), qt = (loc & 7) * 8 + w;
    const u32 ksb = (u32)(head * KV_HEAD + w * 1024), wlb = lb + w * 1024;
    const int r = l & 31;
    u32 krd = lo16;
    u32 vrd = (u32)(1024 + hb * 272 + (r < 16 ? r * 16 : (r == 16 ? 256 : 0x40000)));
    STAMP(7);
    wait_cnt(ws, xcd, 32, w);
    BAR();
    STAMP(8);
#define KVR(r) GLDS_SC1(lo16, sws, ksb, WS_KV + (r) * 8192, wlb, (r) * 8192)
    u32x4 qraw;
    GLD128_SC1(qraw, lo16, sws, (u32)(head * 64 + qt) * 1024, WS_QF);
    KVR(0); KVR(1);
    if (w < 4) KVR(16);
    KVR(2); KVR(3);
    WAITV(2);
    PIN(qraw);
    RAWBAR();
    STAMP(9);
    const bf16x8 qf = __builtin_bit_cast(bf16x8, qraw);
    float m;
    bf16x8 kf = lds_frag(smem, krd);
    {
      f32x16 s0 = MFMA32(kf, qf, zero);
      m = s0[0];
#pragma unroll
      for (int i = 1; i < 16; ++i) m = fmaxf(m, s0[i]);
      const auto a = __builtin_amdgcn_permlane32_swap(__float_as_uint(m), __float_as_uint(m), false, false);
      m = fmaxf(__uint_as_float(a[0]), __uint_as_float(a[1]));
    }
    f32x16 negm;
#pragma unroll
    for (int i = 0; i < 16; ++i) negm[i] = -m;
    f32x16 o = zero;
    f32x16 sc = MFMA32(kf, qf, negm);
    kf = lds_frag(smem, krd + KV_TILE);
    SFOR(j, 0, 64)
      if constexpr ((j & 1) == 0) __builtin_amdgcn_s_setprio(3 - (j >> 1 & 3));
      if (j == 5) { WAITV(10); RAWBAR(); }
      if (j == 13) { WAITV(0); RAWBAR(); }
      if (j == 14) STAMP(10);
      if constexpr (j < 6) { KVR(4 + 2 * j); KVR(5 + 2 * j); }
      if constexpr (j >= 14 && j < 63 && (j - 14) % 4 != 3) WSTEP((j - 14) - (j - 14) / 4);
      if (j == 30 || j == 60) { krd += 30 * KV_TILE; vrd += 30 * KV_TILE; PIN(krd); PIN(vrd); }
      const u32 jo = (u32)(j % 30) * KV_TILE;
      const bf16x8 v0 = lds_frag(smem, vrd + jo), v1 = lds_frag(smem, vrd + jo + 544);
      f32x16 sn;
      if (j < 63) sn = MFMA32(kf, qf, negm);
      if (j < 62) kf = lds_frag(smem, krd + jo + 2 * KV_TILE);
      SB();
#pragma unroll
      for (int i = 0; i < 11; ++i) sc[i] = __builtin_amdgcn_exp2f(sc[i]);
      SB();
      o = SMF(v0, LO8(sc), o);
      SB();
#pragma unroll
      for (int i = 11; i < 16; ++i) sc[i] = __builtin_amdgcn_exp2f(sc[i]);
      o = SMF(v1, HI8(sc), o);
      SB();
      if (j < 63) sc = sn;
    SEND
    float lsum = __uint_as_float(__builtin_amdgcn_permlane32_swap(__float_as_uint(o[8]), __float_as_uint(o[8]), false, false)[0]);
    bool ok = lsum < 1e18f;
#pragma unroll
    for (int i = 0; i < 8; ++i) ok = ok && (fabsf(o[i]) < 3e38f);
    if (!__all(ok)) {
      asm volatile("" ::: "memory");
      const __amdgpu_buffer_rsrc_t rk = __builtin_amdgcn_make_buffer_rsrc(ws + WS_KV + (size_t)head * KV_HEAD, 0, KV_HEAD, 0x20000);
      const int vg = 1024 + hb * 272 + (r == 16 ? 256 : (r & 15) * 16);
#define KVLD(off, j) __builtin_bit_cast(bf16x8, __builtin_amdgcn_raw_buffer_load_b128(rk, (off), (j) * KV_TILE, 16))
#pragma unroll 1
      for (int j = 0; j < 64; ++j) {
        const f32x16 sv = MFMA32(KVLD(lo16, j), qf, zero);
#pragma unroll
        for (int i = 0; i < 16; ++i) m = fmaxf(m, sv[i]);
      }
      m = fmaxf(m, __shfl_xor(m, 32));
#pragma unroll
      for (int i = 0; i < 16; ++i) negm[i] = -m;
      o = zero;
#pragma unroll 1
      for (int j = 0; j < 64; ++j) {
        f32x16 sv = MFMA32(KVLD(lo16, j), qf, negm);
        const bf16x8 v0 = KVLD(vg, j), v1 = KVLD(vg + 544, j);
#pragma unroll
        for (int i = 0; i < 16; ++i) sv[i] = __builtin_amdgcn_exp2f(sv[i]);
        o = SMF(v0, LO8(sv), o);
        o = SMF(v1, HI8(sv), o);
      }
      lsum = shfl_idx(o[8], r);
    }
    const float inv = 1.f / lsum;
    u32x4 ov;
#pragma unroll
    for (int e = 0; e < 4; ++e) ov[e] = pack2(o[2 * e] * inv, o[2 * e + 1] * inv);
    GST_SC1("x4", ov, lo16, sws, (u32)(qt * 32 + head) * 1024, WS_OF);
    STAMP(11);
    WAITV(0);
    BAR();
    signal_cnt(ws, 8 + (loc & 7));
    STAMP(12);
  }

  const int tm = w & 1;
  const u32 wvo = (u32)((l >> 3) * 1024 + ((((l >> 2) ^ (l >> 4)) & 1) * 32 + (l & 3) * 8) * 2);
  const u32 wrd0 = (u32)((hb * 4 + (l >> 2 & 3)) * 128 + (l >> 3 & 1) * 64 + (l >> 4 & 1) * 32 + (l & 3) * 8);
  const int rt3 = xcd * 8 + pm3 * 2;
  const int erow = w * 8 + (l & 7), ecol = l >> 3;
  f32x16 uv = zero;
  const u32 trd = TAB + (u32)((l >> 4 & 1) * 1024 + hb * 16);
#define UV_PART(k0)                                                                                          \
  do { bf16x8 tf_[16];                                                                                       \
    _Pragma("unroll") for (int i_ = 0; i_ < 4; ++i_) tf_[i_] = lds_frag(smem, trd + ((k0) + i_) * 32);       \
    _Pragma("unroll") for (int i_ = 0; i_ < 16; ++i_) {                                                      \
      if (i_ + 4 < 16) tf_[i_ + 4] = lds_frag(smem, trd + ((k0) + i_ + 4) * 32);                             \
      uv = MFMA32(wf[(k0) + i_], tf_[i_], uv); SB(); }                                                       \
    PIN(uv); } while (0)

  {
    const u32x4 so = make_srd(wo, 512 * 1024);
    const int ro = pn3 & 3;
    SFOR(j, 0, 8) GLDS(wvo, so, (u32)(w * 8192 + pn3 * 128 + ((j + 2 * ro) & 7) * 65536), 0, lb + w * 1024, j * 8192); SEND
    u32x4 xr;
    GLD128(xr, (u32)(erow * 1024 + ecol * 16), sx, (u32)(rt3 * 32768 + pn3 * 128), 0);
    UV_PART(0);
    WAITV(0);
    PIN(xr);
    STAMP(13);
    wait_cnt(ws, 8 + xcd, 32, w);
    RAWBAR();
    STAMP(14);
    if (bid == 0 && t == 0) __hip_atomic_store((u32*)(ws + WS_EPOCH), tag, __ATOMIC_RELAXED, __HIP_MEMORY_SCOPE_AGENT);
    const u32 olo = (u32)((w >> 2) * 32768 + (w & 3) * 2048);
    const u32 osb = (u32)rt3 * 32768 + olo, olb = lb + olo;
    SFOR(j, 0, 8) GLDS_SC1(lo16, sws, osb + (u32)(((j >> 1) + ro) & 3) * 8192, WS_OF + (j & 1) * 1024, olb, 65536 + (j >> 1) * 8192 + (j & 1) * 1024); SEND
    const int tn = w >> 1 & 1, kh = w >> 2;
    u32 ord = 65536 + (u32)(tm * 32768 + kh * 4096) + lo16;
    PIN(ord);
    const u32 wrd = (wrd0 ^ (u32)(tn * 64)) + kh * 8192;
    f32x16 acc = zero;
    bf16x8 wq[16];
#define WOFRAG(n) wq[n] = lds_tr2(smem, wrd + ((n) >> 2) * 16384 + ((n) & 3) * 2048, wrd + ((n) >> 2) * 16384 + ((n) & 3) * 2048 + 1024)
#pragma unroll
    for (int n = 0; n < 8; ++n) WOFRAG(n);
    SFOR(s, 0, 4)
      if (s == 0) WAITV(6); else if (s == 1) WAITV(4); else if (s == 2) WAITV(2); else WAITV(0);
      RAWBAR();
      bf16x8 oq[4];
#pragma unroll
      for (int j = 0; j < 4; ++j) oq[j] = lds_frag(smem, ord + s * 8192 + j * 1024);
      if constexpr (s < 2) {
#pragma unroll
        for (int j = 0; j < 4; ++j) WOFRAG(8 + s * 4 + j);
      }
      SB();
#pragma unroll
      for (int j = 0; j < 4; ++j) acc = MFMA32(wq[s * 4 + j], oq[j], acc);
      SB();
    SEND
    STAMP(15);
    {
      LAS char* eb = (LAS char*)smem + kh * 17408 + (tm * 32 + (l & 31)) * 272 + (tn * 32 + hb * 4) * 4;
#pragma unroll
      for (int e = 0; e < 4; ++e) *(LAS f32x4*)(eb + e * 32) = (f32x4){acc[4 * e], acc[4 * e + 1], acc[4 * e + 2], acc[4 * e + 3]};
    }
    f32x4 ga[2], cb[2];
#pragma unroll
    for (int e = 0; e < 2; ++e) { ga[e] = *(const LAS f32x4*)((LAS char*)smem + MT + ecol * 32 + e * 16); cb[e] = *(const LAS f32x4*)((LAS char*)smem + MT + 256 + ecol * 32 + e * 16); }
    BAR();
    float xv[8];
    u32x4 x1p, yp;
#pragma unroll
    for (int e = 0; e < 2; ++e) {
      const LAS char* eb = (const LAS char*)smem + erow * 272 + ecol * 32 + e * 16;
      const f32x4 a = *(const LAS f32x4*)eb + *(const LAS f32x4*)(eb + 17408);
#pragma unroll
      for (int h = 0; h < 2; ++h) {
        const u32 pk = pack2(bf_lo(xr[2 * e + h]) + ga[e][2 * h] * a[2 * h], bf_hi(xr[2 * e + h]) + ga[e][2 * h + 1] * a[2 * h + 1]);
        x1p[2 * e + h] = pk;
        xv[4 * e + 2 * h] = bf_lo(pk); xv[4 * e + 2 * h + 1] = bf_hi(pk);
        yp[2 * e + h] = pack2(bf_lo(pk) * cb[e][2 * h], bf_hi(pk) * cb[e][2 * h + 1]);
      }
    }
    GST_SC1("x4", yp, (u32)((ecol >> 1) * 1024 + ((ecol & 1) * 32 + (erow & 31)) * 16), sws, (u32)((rt3 + (w >> 2)) * 32 + pn3 * 4) * 1024, WS_Y);
    float sum = ((xv[0] + xv[1]) + (xv[2] + xv[3])) + ((xv[4] + xv[5]) + (xv[6] + xv[7]));
    const float mean = col8_sum(sum) * (1.f / 64.f);
    float sq = 0.f;
#pragma unroll
    for (int i = 0; i < 8; ++i) { const float d = xv[i] - mean; sq = __builtin_fmaf(d, d, sq); }
    const f32x2 o2 = {mean, col8_sum(sq)};
    GST_SC1("x2", o2, l < 8 ? (u32)((erow & 31) * 8) : 0x40000000u, sws, (u32)((rt3 + (w >> 2)) * 2048 + pn3 * 256), WS_ST2);
    STAMP(16);
    WAITV(0);
    RAWBAR();
    signal_cnt(ws, 16 + xcd * 2 + (pm3 >> 1));
    STAMP(17);
    *(u32x4*)(ws + WS_X1 + (size_t)(bid * 512 + t) * 16) = x1p;
  }

  const u32x4 sd = make_srd(wd, 1792 * 1024);
  const u32 dsb = (u32)(w * 16384 + pn3 * 128), dlb = lb + w * 2048;
#define WDPIECE(c, slot, h) GLDS(wvo, sd, dsb, (c) * 131072 + (h) * 8192, dlb, (slot) * 16384 + (h) * 1024)
#define WDISSUE(c, slot) do { WDPIECE(c, slot, 0); WDPIECE(c, slot, 1); } while (0)
  if (loc < 28) {
    constexpr u32 RST4 = 157696;
    UV_PART(16);
    float uu[16], vv[16];
#pragma unroll
    for (int i = 0; i < 16; ++i) {
      const auto r = __builtin_amdgcn_permlane16_swap(__float_as_uint(uv[i]), __float_as_uint(uv[i]), false, false);
      uu[i] = __uint_as_float(r[0]); vv[i] = __uint_as_float(r[1]);
      PIN(uu[i]); PIN(vv[i]);
    }
    wait_cnt(ws, 16 + pan4, 16, w);
    RAWBAR();
    STAMP(18);
    const int rt4 = pan4 * 4;
    const u32 ysb = (u32)(rt4 * 32768 + w * 1024), ylb = lb + w * 1024;
#define YPIECE(i) GLDS_SC1(lo16, sws, ysb, WS_Y + (i) * 8192, ylb, (i) * 8192)
    YPIECE(0); YPIECE(1); YPIECE(2); YPIECE(3);
    GLDS_SC1(lo16, sws, (u32)(rt4 * 2048 + w * 1024), WS_ST2, ylb, 131072);
    YPIECE(4); YPIECE(5);
    u32 yrd0 = lo16, yrd1 = lo16 + 65536;
    PIN(yrd1);
    const u32 asb = (u32)(rt4 * 112 + pn4 * 8 + w) * 1024;
#define YFRAG(n) lds_frag(smem, ((n) < 64 ? yrd0 : yrd1) + ((n) & 63) * 1024)
    WAITV(5);
    RAWBAR();
    STAMP(19);
    bf16x8 yf[128];
#pragma unroll
    for (int n = 0; n < 4; ++n) yf[n] = YFRAG(n);
    SB();
    f32x16 acc = zero, accp = zero;
    f32x2 stp = {0.f, 0.f};
    float eg[8], eu[8], et[8];
    u32x4 ov;
    SFOR(n, 0, 160)
      constexpr int a = n >> 5, ks = n & 31;
      if (n == 10) { WAITV(7); RAWBAR(); }
      if (n == 26) { WAITV(8); BAR(); }
      if (n == 56) { WAITV(4); RAWBAR(); }
      if (n == 100) WAITV(3);
      if constexpr (n < 20 && n % 2 == 0) YPIECE(6 + n / 2);
      if (n == 36 || n == 68 || n == 100) RAWBAR();
      if constexpr (n == 60) {
        const int rr = pm3 * 224 + w * 28 + (l < 28 ? l : 27);
        GTOUCH((u32)(rr + (rr < 256 ? 768 : 896)) * 1024, sd, (u32)(pn3 * 128), 0, lb + SCR);
      }
      if constexpr (n >= 36 && n < 52 && n % 4 == 0) WDPIECE((n - 36) / 8, (n - 36) / 8, (n - 36) / 4 & 1);
      if constexpr (n >= 68 && n < 84 && n % 4 == 0) WDPIECE(2 + (n - 68) / 8, 2 + (n - 68) / 8, (n - 68) / 4 & 1);
      if constexpr (n == 84 || n == 88) WDPIECE(8, 8, (n - 84) / 4);
      if constexpr (n >= 100 && n < 116 && n % 4 == 0) WDPIECE(4 + (n - 100) / 8, 4 + (n - 100) / 8, (n - 100) / 4 & 1);
      if constexpr (n == 12) {
        const LAS f32x2* sp = (const LAS f32x2*)((LAS char*)smem + 131072 + (t >> 7) * 2048 + (t & 3) * 512 + (t >> 2 & 31) * 8);
        const f32x2 p0 = sp[0], p1 = sp[32];
        float mu = p0[0] + p1[0];
        mu += dpp_xor1(mu); mu += dpp_xor2(mu);
        mu *= 0.125f;
        float q = p0[1] + p1[1] + 64.f * ((p0[0] - mu) * (p0[0] - mu) + (p1[0] - mu) * (p1[0] - mu));
        q += dpp_xor1(q); q += dpp_xor2(q);
        const float rs = __builtin_amdgcn_rsqf(q * (1.f / 512.f) + 1e-5f);
        ((LAS f32x2*)((LAS char*)smem + RST4))[t >> 2] = (f32x2){rs, -mu * rs};
      }
      if (ks == 28 && a < 4) stp = ((const LAS f32x2*)((LAS char*)smem + RST4))[a * 32 + (l & 31)];
      if (ks == 0 && a > 0) { accp = acc; acc = zero; }
      if (n == 128) STAMP(20);
      if constexpr (n + 4 < 128) yf[n + 4] = YFRAG(n + 4);
      if constexpr (n < 128) acc = MFMA32(wf[ks], yf[n], acc);
      if constexpr (a > 0 && ks >= 2 && ks < 26) {
        constexpr int i = (ks - 2) / 3, ph = (ks - 2) % 3;
        if (ph == 0) {
          eg[i] = __builtin_fmaf(accp[i], stp[0], __builtin_fmaf(uu[i], stp[1], vv[i]));
          eu[i] = __builtin_fmaf(accp[8 + i], stp[0], __builtin_fmaf(uu[8 + i], stp[1], vv[8 + i]));
          et[i] = -1.4426950408889634f * eg[i];
          if (a < 4) { PIN(eg[i]); PIN(eu[i]); PIN(et[i]); }
        } else if (ph == 1) {
          et[i] = 1.f + __builtin_amdgcn_exp2f(et[i]);
          eg[i] *= eu[i];
          if (a < 4) { PIN(eg[i]); PIN(et[i]); }
        } else {
          eg[i] *= __builtin_amdgcn_rcpf(et[i]);
          if (a < 4) PIN(eg[i]);
          if constexpr (i & 1) ov[i >> 1] = pack2(eg[i - 1], eg[i]);
        }
      }
      if constexpr (a > 0 && ks == 26) GST_SC1("x4", ov, lo16, sws, asb, WS_ACT + (a - 1) * 112 * 1024);
      if (n < 128) SB();
    SEND
    STAMP(21);
  } else {
    WDISSUE(0, 0); WDISSUE(1, 1); WDISSUE(2, 2); WDISSUE(3, 3); WDISSUE(8, 8); WDISSUE(4, 4); WDISSUE(5, 5);
    WAITV(0);
    RAWBAR();
  }

  {
    const int kq = w >> 1;
    u32 wra[3][2];
#pragma unroll
    for (int i = 0; i < 3; ++i) { wra[i][0] = wrd0 + kq * 4096 + i * 65536; wra[i][1] = wra[i][0] ^ 64u; PIN(wra[i][0]); PIN(wra[i][1]); }
    bf16x8 dq[14][4];
#define DFRAG(c) _Pragma("unroll") for (int i_ = 0; i_ < 4; ++i_) {                                              \
      const u32 o_ = wra[((c) % 10) >> 2][i_ & 1] + (((c) % 10) & 3) * 16384 + (i_ >> 1) * 2048; dq[c][i_] = lds_tr2(smem, o_, o_ + 1024); }
    DFRAG(0); DFRAG(1); DFRAG(2); DFRAG(3);
    f32x4 gb[2];
#pragma unroll
    for (int e = 0; e < 2; ++e) gb[e] = *(const LAS f32x4*)((LAS char*)smem + MT + 512 + ecol * 32 + e * 16);
    WAITV(0);
    BAR();
    if (loc < 28) signal_cnt(ws, 32 + pan4);
    STAMP(22);
#define WDREST() do { WDISSUE(6, 6); WDISSUE(7, 7); WDISSUE(9, 9); WDISSUE(10, 0); WDISSUE(11, 1); WDISSUE(12, 2); WDISSUE(13, 3); } while (0)
    if (w != 0) WDREST();
    wait_cnt(ws, 32 + xcd * 2 + (pm3 >> 1), 14, w);
    RAWBAR();
    STAMP(23);
    const u32 asb = (u32)((rt3 + tm) * 112 + kq * 2) * 1024;
    u32x4 af[14][2];
#define AISSUE(c) do { GLD128_SC1(af[c][0], lo16, sws, asb, WS_ACT + (c) * 8192); GLD128_SC1(af[c][1], lo16, sws, asb, WS_ACT + (c) * 8192 + 1024); } while (0)
    AISSUE(0); AISSUE(1);
    if (w == 0) WDREST();
    AISSUE(2); AISSUE(3); AISSUE(4); AISSUE(5);
    f32x16 acc[2] = {zero, zero};
    u32x4 x1p;
    SFOR(c, 0, 14)
      if (c < 8) WAITV(10); else if (c == 8) WAITV(11); else if (c == 9) WAITV(9);
      else if (c == 10) WAITV(7); else if (c == 11) WAITV(5); else if (c == 12) WAITV(3); else WAITV(0);
      if (c == 0) STAMP(24);
      if (c == 4 || c == 10) RAWBAR();
      if constexpr (c < 8) AISSUE(c + 6);
      if (c == 7) GLD128(x1p, (u32)t * 16, sws, (u32)bid * 8192, WS_X1);
      if constexpr (c >= 3 && c < 13) { DFRAG(c + 1); }
      PIN(af[c][0]); PIN(af[c][1]);
      SB();
#pragma unroll
      for (int i = 0; i < 4; ++i) acc[i & 1] = MFMA32(dq[c][i], __builtin_bit_cast(bf16x8, af[c][i >> 1]), acc[i & 1]);
      SB();
    SEND
    STAMP(25);
    {
      LAS char* eb = (LAS char*)smem + 65536 + kq * 17408 + (tm * 32 + (l & 31)) * 272 + hb * 16;
#pragma unroll
      for (int tn = 0; tn < 2; ++tn)
#pragma unroll
        for (int e = 0; e < 4; ++e)
          *(LAS f32x4*)(eb + tn * 128 + e * 32) = (f32x4){acc[tn][4 * e], acc[tn][4 * e + 1], acc[tn][4 * e + 2], acc[tn][4 * e + 3]};
    }
    BAR();
    PIN(x1p);
    u32x4 ov;
#pragma unroll
    for (int e = 0; e < 2; ++e) {
      const LAS char* eb = (const LAS char*)smem + 65536 + erow * 272 + ecol * 32 + e * 16;
      const f32x4 s = (*(const LAS f32x4*)eb + *(const LAS f32x4*)(eb + 17408)) + (*(const LAS f32x4*)(eb + 2 * 17408) + *(const LAS f32x4*)(eb + 3 * 17408));
      ov[2 * e] = pack2(bf_lo(x1p[2 * e]) + gb[e][0] * s[0], bf_hi(x1p[2 * e]) + gb[e][1] * s[1]);
      ov[2 * e + 1] = pack2(bf_lo(x1p[2 * e + 1]) + gb[e][2] * s[2], bf_hi(x1p[2 * e + 1]) + gb[e][3] * s[3]);
    }
    *(u32x4*)((char*)out + (size_t)(rt3 * 32 + erow) * 1024 + pn3 * 128 + ecol * 16) = ov;
  }
  ST_OUT(2, ws);
}

extern "C" void kernel_launch(void* const* d_in, const int* in_sizes, int n_in, void* d_out, int out_size, void* d_ws, size_t ws_size,
                              hipStream_t stream) {
  adaln_block<<<256, 512, 0, stream>>>((const u16*)d_in[0], (const u16*)d_in[1], (const u16*)d_in[2], (const u16*)d_in[4], (const u16*)d_in[5],
                                       (const u16*)d_in[6], (const u16*)d_in[7], (const u16*)d_in[8], (char*)d_ws, (u16*)d_out);
}
```

```cpp
#include <hip/hip_runtime.h>
#include <hip/hip_bf16.h>

typedef unsigned short u16;
typedef unsigned int u32;
typedef u32 u32x2 __attribute__((ext_vector_type(2)));
typedef u32 u32x4 __attribute__((ext_vector_type(4)));
typedef float f32x2 __attribute__((ext_vector_type(2)));
typedef float f32x4 __attribute__((ext_vector_type(4)));
typedef float f32x8 __attribute__((ext_vector_type(8)));
typedef float f32x16 __attribute__((ext_vector_type(16)));
typedef __bf16 bf16x2 __attribute__((ext_vector_type(2)));
typedef __bf16 bf16x8 __attribute__((ext_vector_type(8)));
typedef __bf16 bf16x16 __attribute__((ext_vector_type(16)));
typedef short s16x4 __attribute__((ext_vector_type(4)));
typedef short s16x8 __attribute__((ext_vector_type(8)));
#define PRIO_MODE 1
#define GATHER_AT 5
#define TLB_WARM 1
#define LAS __attribute__((address_space(3)))
#define DI __device__ __forceinline__

constexpr u32 WS_GRAN = 0;
constexpr u32 WS_EPOCH = 0x18000;
constexpr u32 WS_CNT  = 0x20000;
constexpr u32 WS_QF   = 0x100000;
constexpr u32 WS_KV   = 0x300000;
constexpr u32 WS_OF   = 0x800000;
constexpr u32 WS_X1   = 0xA00000;
constexpr u32 WS_Y    = 0xC00000;
constexpr u32 WS_ST2  = 0xE00000;
constexpr u32 WS_ACT  = 0xF00000;
constexpr u32 WS_BYTES = 0x1600000;
constexpr int KV_TILE = 2112, KV_HEAD = 64 * KV_TILE;

template <int I> struct IC { static constexpr int value = I; };
template <int B, int E, class F> DI void static_for(F&& f) { if constexpr (B < E) { f(IC<B>{}); static_for<B + 1, E>(f); } }
#define SFOR(n, B, E) static_for<B, E>([&](auto ic_) __attribute__((always_inline)) { constexpr int n = decltype(ic_)::value;
#define SEND });

DI float bf_lo(u32 u) { return __uint_as_float(u << 16); }
DI float bf_hi(u32 u) { return __uint_as_float(u & 0xffff0000u); }
DI u32 pack2(float a, float b) { bf16x2 r = {(__bf16)a, (__bf16)b}; return __builtin_bit_cast(u32, r); }
DI float bfround(float a) { return (float)(__bf16)a; }
DI float xor32_sum(float v) {
  const auto r = __builtin_amdgcn_permlane32_swap(__float_as_uint(v), __float_as_uint(v), false, false);
  return __uint_as_float(r[0]) + __uint_as_float(r[1]);
}
DI float dpp_xor1(float v) { return __int_as_float(__builtin_amdgcn_mov_dpp(__float_as_int(v), 0xB1, 0xF, 0xF, true)); }
DI float dpp_xor2(float v) { return __int_as_float(__builtin_amdgcn_mov_dpp(__float_as_int(v), 0x4E, 0xF, 0xF, true)); }
DI float shfl_idx(float v, int srcLane) { return __int_as_float(__builtin_amdgcn_ds_bpermute(srcLane << 2, __float_as_int(v))); }

DI u32x4 make_srd(const void* p, u32 bytes) {
  const size_t a = (size_t)p;
  u32x4 r = {(u32)a, (u32)(a >> 32) & 0xffffu, bytes, 0x20000u};
  return r;
}
#define GLDS(voff, srd, sb, GO, lb, LO)                                                                                   \
  do { u32 tmp_;                                                                                                          \
    asm volatile("s_add_u32 m0, %4, %6\n\ts_add_u32 %0, %3, %5\n\tbuffer_load_dwordx4 %1, %2, %0 offen lds"            \
                 : "=&s"(tmp_) : "v"(voff), "s"(srd), "s"(sb), "s"(lb), "i"(GO), "i"(LO) : "memory", "scc"); } while (0)
#define GLD(W, dst, voff, srd, sb, GO)                                                                                    \
  do { u32 tmp_;                                                                                                          \
    asm volatile("s_add_u32 %1, %4, %5\n\tbuffer_load_dword" W " %0, %2, %3, %1 offen"                                   \
                 : "=v"(dst), "=&s"(tmp_) : "v"(voff), "s"(srd), "s"(sb), "i"(GO) : "memory", "scc"); } while (0)
#define GLD128(dst, voff, srd, sb, GO) GLD("x4", dst, voff, srd, sb, GO)
#define GLD64(dst, voff, srd, sb, GO) GLD("x2", dst, voff, srd, sb, GO)
#define GLD32(dst, voff, srd, sb, GO) GLD("", dst, voff, srd, sb, GO)
#define WAITV(N) asm volatile("s_waitcnt vmcnt(" #N ")" ::: "memory")
#define PIN(x) asm volatile("" : "+v"(x))
#define BAR() do { asm volatile("s_waitcnt lgkmcnt(0)" ::: "memory"); __builtin_amdgcn_s_barrier(); asm volatile("" ::: "memory"); } while (0)

#define ST_DECL
#define STAMP(i)
#define ST_OUT(kid, wsp)

DI bf16x8 lds_frag(const char* smem, u32 off) { return *(const LAS bf16x8*)((const LAS char*)smem + off); }
DI bf16x8 lds_tr2(const char* smem, u32 off0, u32 off1) {
  s16x4 a = __builtin_amdgcn_ds_read_tr16_b64_v4i16((LAS s16x4*)((LAS char*)smem + off0));
  s16x4 b = __builtin_amdgcn_ds_read_tr16_b64_v4i16((LAS s16x4*)((LAS char*)smem + off1));
  s16x8 r = __builtin_shufflevector(a, b, 0, 1, 2, 3, 4, 5, 6, 7);
  return __builtin_bit_cast(bf16x8, r);
}
DI u32 lds_base(const char* smem) { return (u32)(size_t)(const LAS char*)smem; }
#define SB() __builtin_amdgcn_sched_barrier(0)
#define RAWBAR() do { asm volatile("" ::: "memory"); __builtin_amdgcn_s_barrier(); asm volatile("" ::: "memory"); } while (0)
#define MFMA32(a, b, c) __builtin_amdgcn_mfma_f32_32x32x16_bf16((a), (b), (c), 0, 0, 0)
#define MFMA16(a, b, c) __builtin_amdgcn_mfma_f32_16x16x32_bf16((a), (b), (c), 0, 0, 0)

DI u32x4 modulate8(const u32x4 v, float rs, float mr, const float* c1, const float* sh) {
  u32x4 o;
#pragma unroll
  for (int e = 0; e < 4; ++e) {
    const float a = __builtin_fmaf(__builtin_fmaf(bf_lo(v[e]), rs, mr), c1[2 * e], sh[2 * e]);
    const float b = __builtin_fmaf(__builtin_fmaf(bf_hi(v[e]), rs, mr), c1[2 * e + 1], sh[2 * e + 1]);
    o[e] = pack2(a, b);
  }
  return o;
}

#define GLDS_SC1(voff, srd, sb, GO, lb, LO)                                                                               \
  do { u32 tmp_;                                                                                                          \
    asm volatile("s_add_u32 m0, %4, %6\n\ts_add_u32 %0, %3, %5\n\tbuffer_load_dwordx4 %1, %2, %0 offen sc1 lds"         \
                 : "=&s"(tmp_) : "v"(voff), "s"(srd), "s"(sb), "s"(lb), "i"(GO), "i"(LO) : "memory", "scc"); } while (0)
#define GLD128_SC1(dst, voff, srd, sb, GO)                                                                                \
  do { u32 tmp_;                                                                                                          \
    asm volatile("s_add_u32 %1, %4, %5\n\tbuffer_load_dwordx4 %0, %2, %3, %1 offen sc1"                                 \
                 : "=v"(dst), "=&s"(tmp_) : "v"(voff), "s"(srd), "s"(sb), "i"(GO) : "memory", "scc"); } while (0)
#define GST_SC1(W, val, voff, srd, sb, GO)                                                                                \
  do { u32 tmp_;                                                                                                          \
    asm volatile("s_add_u32 %0, %4, %5\n\tbuffer_store_dword" W " %1, %2, %3, %0 offen sc1\n\ts_nop 1"                  \
                 : "=&s"(tmp_) : "v"(val), "v"(voff), "s"(srd), "s"(sb), "i"(GO) : "memory", "scc"); } while (0)
#define GTOUCH(voff, srd, sb, GO, lb)                                                                                     \
  do { u32 tmp_;                                                                                                          \
    asm volatile("s_mov_b32 m0, %4\n\ts_add_u32 %0, %3, %5\n\tbuffer_load_dword %1, %2, %0 offen sc1 lds"               \
                 : "=&s"(tmp_) : "v"(voff), "s"(srd), "s"(sb), "s"(lb), "i"(GO) : "memory", "scc"); } while (0)
#define GLDS64(vaddr, lb, LO)                                                                                             \
    asm volatile("s_add_u32 m0, %1, %2\n\ts_nop 0\n\tglobal_load_lds_dwordx4 %0, off" :: "v"(vaddr), "s"(lb), "i"(LO) : "memory", "scc")

DI void wave_sum8(const float* v, float* z) {
  float u[4];
#pragma unroll
  for (int i = 0; i < 4; ++i) {
    const auto a = __builtin_amdgcn_permlane32_swap(__float_as_uint(v[i]), __float_as_uint(v[i + 4]), false, false);
    u[i] = __uint_as_float(a[0]) + __uint_as_float(a[1]);
  }
#pragma unroll
  for (int i = 0; i < 2; ++i) {
    const auto a = __builtin_amdgcn_permlane16_swap(__float_as_uint(u[i]), __float_as_uint(u[i + 2]), false, false);
    float s = __uint_as_float(a[0]) + __uint_as_float(a[1]);
    s += dpp_xor1(s); s += dpp_xor2(s);
    s += __int_as_float(__builtin_amdgcn_mov_dpp(__float_as_int(s), 0x141, 0xF, 0xF, true));
    s += __int_as_float(__builtin_amdgcn_mov_dpp(__float_as_int(s), 0x140, 0xF, 0xF, true));
    z[i] = s;
  }
}
#define ROW_OF(z, r) __int_as_float(__builtin_amdgcn_readlane(__float_as_int(z[(r) & 1]), ((r) >> 1) * 16))
DI float col8_sum(float v) {
  v += __int_as_float(__builtin_amdgcn_mov_dpp(__float_as_int(v), 0x128, 0xF, 0xF, true));
  const auto a = __builtin_amdgcn_permlane16_swap(__float_as_uint(v), __float_as_uint(v), false, false);
  return xor32_sum(__uint_as_float(a[0]) + __uint_as_float(a[1]));
}

DI void signal_cnt(char* ws, int idx) {
  if (threadIdx.x == 0) __hip_atomic_fetch_add((u32*)(ws + WS_CNT) + idx * 64, 1u, __ATOMIC_RELAXED, __HIP_MEMORY_SCOPE_AGENT);
}
DI void wait_cnt(char* ws, int idx, u32 want, int w) {
  if (w == 0) {
    u32* c = (u32*)(ws + WS_CNT) + idx * 64;
#pragma unroll 1
    for (u32 spins = 0; spins < (1u << 17); ++spins) {
      if ((u32)__builtin_amdgcn_readfirstlane(__hip_atomic_load(c, __ATOMIC_RELAXED, __HIP_MEMORY_SCOPE_AGENT)) >= want) break;
      __builtin_amdgcn_s_sleep(1);
    }
    asm volatile("" ::: "memory");
  }
}
DI float gsum(const u32x4 a, const u32x4 b) { return (__uint_as_float(a[0]) + __uint_as_float(a[2])) + (__uint_as_float(b[0]) + __uint_as_float(b[2])); }
DI bool gok(const u32x4 a, u32 tag) { return a[1] == tag && a[3] == tag; }
#define SMF(v, p, o) __builtin_amdgcn_smfmac_f32_32x32x32_bf16((v), __builtin_bit_cast(bf16x16, (p)), (o), 0xDDDD, 0, 0)
#define LO8(s) __builtin_shufflevector(s, s, 0, 1, 2, 3, 4, 5, 6, 7)
#define HI8(s) __builtin_shufflevector(s, s, 8, 9, 10, 11, 12, 13, 14, 15)

extern "C" __global__ __launch_bounds__(512) void adaln_block(const u16* __restrict__ x, const u16* __restrict__ wqkv, const u16* __restrict__ wo,
                                                              const u16* __restrict__ wg, const u16* __restrict__ wu, const u16* __restrict__ wd,
                                                              const u16* __restrict__ c, const u16* __restrict__ wmod, char* __restrict__ ws,
                                                              u16* __restrict__ out) {
  __shared__ __attribute__((aligned(16))) char smem[163840];
  ST_DECL STAMP(0);
  const int bid = blockIdx.x, xcd = bid & 7, loc = bid >> 3;
  const int t = threadIdx.x, l = t & 63;
  const int w = __builtin_amdgcn_readfirstlane(t >> 6);
  const u32 lb = lds_base(smem);
  const u32x4 sws = make_srd(ws, WS_BYTES), sx = make_srd(x, 2048 * 1024);
  const u32 lo16 = (u32)l * 16, zs = 0;
  const int hb = l >> 5;
  const int pm3 = loc >> 3, pn3 = loc & 7;
  constexpr u32 TAB = 159744, MT = TAB + 2048, SCR = TAB;
  u32 tag;

  {
    constexpr u32 WSLOT = 24576, RED = 49152, MTAB = 53248, EXO = 114688;
    const int pm = (xcd >> 1) * 8 + (loc >> 2), cg = (xcd & 1) * 4 + (loc & 3);
    const u32x4 sw = make_srd(wqkv, 512 * 3072), sm = make_srd(wmod, 512 * 6144), sc = make_srd(c, 1024);
    const int task = bid < 192 ? bid : 191, mcb = task >> 2, mkq = task & 3;
    u32 ev, cv[2];
    u32x4 mv[2], xa[8];
    if (bid == 0 && w == 0) GST_SC1("", zs, (u32)l * 256, sws, zs, WS_CNT);
    asm volatile("buffer_load_dword %0, %1, %2, %3 offen sc1" : "=v"(ev) : "v"(zs), "s"(sws), "s"(WS_EPOCH) : "memory");
#pragma unroll
    for (int i = 0; i < 2; ++i) GLD128(mv[i], (u32)((t >> 3) * 6144 + (t & 7) * 16), sm, (u32)(mkq * 128 * 6144 + mcb * 128), i * 64 * 6144);
#pragma unroll
    for (int i = 0; i < 2; ++i) GLD32(cv[i], (u32)((t >> 4) * 4), sc, (u32)(mkq * 256), i * 128);
    RAWBAR();
    const int rx = loc & 3, rw = loc >> 2;
    const u32 xsb = (u32)(pm * 64 + w * 2) * 1024;
#define XROW(i) GLD128(xa[i], lo16, sx, xsb + (u32)((((i) >> 1) + rx) & 3) * 16384, ((i) & 1) * 1024)
    const u32 wvo = (u32)((8 * (l >> 5) + (l >> 3 & 3)) * 3072 + ((((l >> 1) ^ (l >> 4)) & 3) * 16 + (l & 1) * 8) * 2);
    const u32 wsb = (u32)((32 * (w >> 2) + 16 * (w & 1) + 4 * (w >> 1 & 1)) * 3072 + cg * 128), wlb = lb + w * 1024;
#define KA_SLOT(kc) ((kc) < 2 ? (kc) * WSLOT : 65536 + (((kc) - 2) & 3) * WSLOT)
#define KA_PIECE(n) GLDS(wvo, sw, wsb + (u32)(((n) / 3 + rw) & 7) * (64 * 3072), ((n) % 3) * 1024, wlb, KA_SLOT((n) / 3) + ((n) % 3) * 8192)
    STAMP(26);
    XROW(0); XROW(1); XROW(2); XROW(3);
    STAMP(27);
    WAITV(4);
    STAMP(28);
    PIN(ev); PIN(mv[0]); PIN(mv[1]); PIN(cv[0]); PIN(cv[1]);
    tag = (u32)__builtin_amdgcn_readfirstlane(ev) + 1u;
    {
      float acc[8];
#pragma unroll
      for (int e = 0; e < 8; ++e) acc[e] = 0.f;
#pragma unroll
      for (int i = 0; i < 2; ++i) {
        const float cc = (t & 8) ? bf_hi(cv[i]) : bf_lo(cv[i]);
        const float sv = bfround(cc * __builtin_amdgcn_rcpf(1.f + __builtin_amdgcn_exp2f(-1.4426950408889634f * cc)));
#pragma unroll
        for (int e = 0; e < 4; ++e) { acc[2 * e] += sv * bf_lo(mv[i][e]); acc[2 * e + 1] += sv * bf_hi(mv[i][e]); }
      }
      float u[4], z[2];
#pragma unroll
      for (int i = 0; i < 4; ++i) {
        const auto a = __builtin_amdgcn_permlane32_swap(__float_as_uint(acc[i]), __float_as_uint(acc[i + 4]), false, false);
        u[i] = __uint_as_float(a[0]) + __uint_as_float(a[1]);
      }
#pragma unroll
      for (int i = 0; i < 2; ++i) {
        const auto a = __builtin_amdgcn_permlane16_swap(__float_as_uint(u[i]), __float_as_uint(u[i + 2]), false, false);
        z[i] = __uint_as_float(a[0]) + __uint_as_float(a[1]);
        z[i] += __int_as_float(__builtin_amdgcn_mov_dpp(__float_as_int(z[i]), 0x128, 0xF, 0xF, true));
      }
      *(LAS f32x2*)((LAS char*)smem + RED + w * 256 + (l & 7) * 32 + (l >> 4) * 8) = (f32x2){z[0], z[1]};
    }
    XROW(4); XROW(5); XROW(6); XROW(7);
    STAMP(29);
    BAR();
    if (w == 0 && bid < 192) {
      const LAS float* rp = (const LAS float*)((LAS char*)smem + RED) + l;
      const float v = ((rp[0] + rp[64]) + (rp[128] + rp[192])) + ((rp[256] + rp[320]) + (rp[384] + rp[448]));
      const u32x2 gr = {__float_as_uint(v), tag};
      GST_SC1("x2", gr, (u32)l * 32, sws, (u32)(mcb * 2048 + mkq * 8), WS_GRAN);
    }
    STAMP(1);
    {
      const char* tp = w == 0 ? (const char*)wo : (w == 1 ? (const char*)wg : (w == 2 ? (const char*)wu : (w == 3 ? (const char*)wd : (w == 4 ? (const char*)out : ws))));
      const u32 tb = w == 0 ? 512 * 1024 : (w < 4 ? 512 * 3584 : (w == 4 ? 2048 * 1024 : WS_BYTES));
      const u32x4 st = make_srd(tp, tb);
      GTOUCH((u32)l * (tb >> 6) + (w > 5 ? (u32)(w - 5) * 0x1D400 : 0u), st, zs, 0, lb + 57344);
    }
    KA_PIECE(0); KA_PIECE(1); KA_PIECE(2); KA_PIECE(3); KA_PIECE(4); KA_PIECE(5);
    if (TLB_WARM) WAITV(7); else WAITV(6);
    STAMP(30);
    u32x4 gq[4];
#define GATHER(tail)                                                                                                            \
    asm volatile("buffer_load_dwordx4 %0, %4, %5, %6 offen sc1\n\tbuffer_load_dwordx4 %1, %4, %5, %6 offen offset:16 sc1\n\t"   \
                 "buffer_load_dwordx4 %2, %4, %5, %7 offen sc1\n\tbuffer_load_dwordx4 %3, %4, %5, %7 offen offset:16 sc1" tail   \
                 : "=&v"(gq[0]), "=&v"(gq[1]), "=&v"(gq[2]), "=&v"(gq[3]) : "v"((u32)t * 32), "s"(sws), "s"(WS_GRAN), "s"(WS_GRAN + 16384) : "memory")
    float rs[8], mr[8];
    {
      float v[8], z[2];
      SFOR(i, 0, 8)
        PIN(xa[i]);
        v[i] = ((bf_lo(xa[i][0]) + bf_hi(xa[i][0])) + (bf_lo(xa[i][1]) + bf_hi(xa[i][1]))) + ((bf_lo(xa[i][2]) + bf_hi(xa[i][2])) + (bf_lo(xa[i][3]) + bf_hi(xa[i][3])));
        PIN(v[i]);
        KA_PIECE(6 + i);
      SEND
      STAMP(31);
      wave_sum8(v, z);
      z[0] *= (1.f / 512.f); z[1] *= (1.f / 512.f);
#define VAR_ROWS(B, E)                                                                                                   \
      SFOR(i, B, E)                                                                                                      \
        PIN(xa[i]);                                                                                                      \
        const float mu = ROW_OF(z, i);                                                                                   \
        float sq = 0.f;                                                                                                  \
        _Pragma("unroll") for (int e = 0; e < 4; ++e) {                                                                  \
          const float a = bf_lo(xa[i][e]) - mu, b = bf_hi(xa[i][e]) - mu; sq = __builtin_fmaf(a, a, sq); sq = __builtin_fmaf(b, b, sq); } \
        v[i] = sq;                                                                                                       \
        PIN(v[i]);                                                                                                       \
        if (i < 4) KA_PIECE(14 + (i & 3));                                                                               \
      SEND
      VAR_ROWS(0, GATHER_AT)
      GATHER("");
      VAR_ROWS(GATHER_AT, 8)
      float q[2];
      wave_sum8(v, q);
#pragma unroll
      for (int j = 0; j < 2; ++j) { q[j] = __builtin_amdgcn_rsqf(q[j] * (1.f / 512.f) + 1e-5f); z[j] = -z[j] * q[j]; }
      SFOR(i, 0, 8) rs[i] = ROW_OF(q, i); mr[i] = ROW_OF(z, i); SEND
    }
    STAMP(2);
    const int hd = w >> 1, kh = w & 1, g = l >> 4;
    u32 brd0 = (u32)((l >> 2) * 128 + ((hd ^ (l >> 3 & 3)) * 32) + (l & 3) * 8 + kh * 4096), brd1 = brd0 + 65536, brd2 = brd0 + 65536 + 2 * WSLOT;
    PIN(brd1); PIN(brd2);
    bf16x8 wq[8][3];
#define KA_FRAG(kc) _Pragma("unroll") for (int c_ = 0; c_ < 3; ++c_) {                                                    \
      const u32 o_ = ((kc) < 2 ? brd0 : (((kc) & 2) ? brd1 : brd2)) + ((kc) & 1) * WSLOT + c_ * 8192; wq[kc][c_] = lds_tr2(smem, o_, o_ + 2048); }
    WAITV(10);
    RAWBAR();
    STAMP(32);
    KA_FRAG(2); KA_FRAG(3); KA_FRAG(0); KA_FRAG(1);
    BAR();
    SFOR(n, 18, 24) KA_PIECE(n); SEND
    WAITV(6);
    PIN(gq[0]); PIN(gq[1]); PIN(gq[2]); PIN(gq[3]);
    STAMP(3);
#pragma unroll 1
    for (u32 spins = 0; spins < (1u << 17); ++spins) {
      if (__all(gok(gq[0], tag) && gok(gq[1], tag) && gok(gq[2], tag) && gok(gq[3], tag))) break;
      __builtin_amdgcn_s_sleep(2);
      GATHER("\n\ts_waitcnt vmcnt(0)");
    }
    ((LAS float*)((LAS char*)smem + MTAB))[t] = bfround(1.f + bfround(gsum(gq[2], gq[3])));
    ((LAS float*)((LAS char*)smem + MTAB))[512 + t] = bfround(gsum(gq[0], gq[1]));
    BAR();
    STAMP(33);
    float c1[8], sh[8];
    {
      const LAS f32x4* tc = (const LAS f32x4*)((LAS char*)smem + MTAB + l * 32);
      const f32x4 a0 = tc[0], a1 = tc[1], b0 = tc[128], b1 = tc[129];
#pragma unroll
      for (int e = 0; e < 4; ++e) { c1[e] = a0[e]; c1[4 + e] = a1[e]; sh[e] = b0[e]; sh[4 + e] = b1[e]; }
    }
    KA_FRAG(4); KA_FRAG(5);
    u32 awr[2];
#pragma unroll
    for (int i = 0; i < 2; ++i) awr[i] = (u32)((((l >> 2) - 2 * rw) & 15) * 1024 + (l & 3) * 256 + (((w * 2 + i) ^ (l & 7)) << 4));
    const u32 ard = (u32)(kh * 1024 + g * 256 + (((l & 15) ^ (kh * 4 + g)) << 4));
#define KA_PUT(i, val) *(LAS u32x4*)((LAS char*)smem + awr[(i) & 1] + ((i) >> 1) * 16384) = (val)
    u32x4 hv[8];
#pragma unroll
    for (int i = 0; i < 4; ++i) KA_PUT(i, modulate8(xa[i], rs[i], mr[i], c1, sh));
#pragma unroll
    for (int i = 4; i < 8; ++i) { hv[i] = modulate8(xa[i], rs[i], mr[i], c1, sh); PIN(hv[i]); }
    BAR();
    STAMP(4);
    f32x4 acc[4][3], keep[2][3];
#pragma unroll
    for (int a = 0; a < 4; ++a) acc[a][0] = acc[a][1] = acc[a][2] = (f32x4){0.f, 0.f, 0.f, 0.f};
    LAS f32x4* ex = (LAS f32x4*)((LAS char*)smem + EXO);
#define KA_SEND(p) _Pragma("unroll") for (int c_ = 0; c_ < 3; ++c_) {                                                      \
      keep[p][c_] = kh ? acc[2 * (p) + 1][c_] : acc[2 * (p)][c_];                                                          \
      ex[(((p) * 8 + (w ^ 1)) * 3 + c_) * 64 + l] = kh ? acc[2 * (p)][c_] : acc[2 * (p) + 1][c_]; }
    constexpr float QS = 0.25f * 1.4426950408889634f;
    const int head = cg * 4 + hd;
    const u32 kvb = WS_KV + (u32)(head * KV_HEAD + pm * 2 * KV_TILE), qb0 = WS_QF + (u32)(head * 64 + pm * 2) * 1024;
    const int th = (kh + rx) & 1, tp0 = (kh + rx) >> 1 & 1;
    const u32 fo = (u32)((th * 16 + (l & 15) + 32 * (g >> 1)) * 16);
    const u32 vvo = (l & 31) <= 16 ? (u32)(1024 + (th * 2 + (g >> 1)) * 272 + (l & 31) * 16) : 0x40000000u;
#define KA_STORE(p)                                                                                                        \
    do { f32x4 r_[3];                                                                                                      \
      _Pragma("unroll") for (int c_ = 0; c_ < 3; ++c_) r_[c_] = keep[p][c_] + ex[(((p) * 8 + w) * 3 + c_) * 64 + l];       \
      const auto a0 = __builtin_amdgcn_permlane16_swap(pack2(r_[0][0] * QS, r_[0][1] * QS), pack2(r_[1][0], r_[1][1]), false, false); \
      const auto a1 = __builtin_amdgcn_permlane16_swap(pack2(r_[0][2] * QS, r_[0][3] * QS), pack2(r_[1][2], r_[1][3]), false, false); \
      const u32x4 qk = {a0[0], a1[0], a0[1], a1[1]};                               \
      GST_SC1("x4", qk, fo + ((g & 1) ? kvb + (tp0 ^ (p)) * KV_TILE : qb0 + (tp0 ^ (p)) * 1024), sws, zs, 0);                              \
      const auto b0 = __builtin_amdgcn_permlane16_swap(pack2(r_[2][0], r_[2][1]), 0x3f803f80u, false, false);              \
      const auto b1 = __builtin_amdgcn_permlane16_swap(pack2(r_[2][2], r_[2][3]), 0x3f803f80u, false, false);              \
      const u32x4 vo = {b0[0], b1[0], b0[1], b1[1]};                                       \
      GST_SC1("x4", vo, vvo + kvb + (tp0 ^ (p)) * KV_TILE, sws, zs, 0); } while (0)
#define KA_MT(n) ((n) < 12 ? (n) / 6 : ((n) < 16 ? ((n) - 12) / 2 : (n) / 8))
#define KA_KC(n) ((n) < 12 ? (n) % 6 : ((n) < 16 ? 6 + ((n) & 1) : (n) % 8))
#define HFRAG(n) lds_frag(smem, ard + KA_MT(n) * 16384 + KA_KC(n) * 2048)
    bf16x8 hf[32];
#pragma unroll
    for (int i = 0; i < 4; ++i) hf[i] = HFRAG(i);
    SB();
    SFOR(n, 0, 32)
      constexpr int mt = KA_MT(n), kc = KA_KC(n);
      if constexpr (n < 4) KA_PUT(4 + n, hv[4 + n]);
      if (n == 8) STAMP(34);
      if (n == 9) STAMP(35);
      if (n == 16) STAMP(36);
      if (n == 20) STAMP(37);
      if (n == 22) STAMP(38);
      if (n == 31) STAMP(39);
      if constexpr (n == 8) { WAITV(0); BAR(); KA_FRAG(6); KA_FRAG(7); }
      if constexpr (n == 20) BAR();
      if constexpr (n + 4 < 32) hf[n + 4] = HFRAG(n + 4);
      acc[mt][0] = MFMA16(wq[kc][0], hf[n], acc[mt][0]);
      acc[mt][1] = MFMA16(wq[kc][1], hf[n], acc[mt][1]);
      acc[mt][2] = MFMA16(hf[n], wq[kc][2], acc[mt][2]);
      if constexpr (n == 16) { KA_SEND(0); }
      if constexpr (n == 21) KA_STORE(0);
      if constexpr (n == 31) { KA_SEND(1); }
      SB();
    SEND
    BAR();
    STAMP(5);
    KA_STORE(1);
    u32x4 tq[6];
    const u32 msb = WS_GRAN + (u32)(((w == 0 ? 1024 : (w == 1 ? 2048 : 2560)) + pn3 * 64) * 32);
#define TGATHER(tail)                                                                                                           \
    asm volatile("buffer_load_dwordx4 %0, %6, %8, %9 offen sc1\n\tbuffer_load_dwordx4 %1, %6, %8, %9 offen offset:16 sc1\n\t"   \
                 "buffer_load_dwordx4 %2, %6, %8, %10 offen sc1\n\tbuffer_load_dwordx4 %3, %6, %8, %10 offen offset:16 sc1\n\t" \
                 "buffer_load_dwordx4 %4, %7, %8, %11 offen sc1\n\tbuffer_load_dwordx4 %5, %7, %8, %11 offen offset:16 sc1" tail \
                 : "=&v"(tq[0]), "=&v"(tq[1]), "=&v"(tq[2]), "=&v"(tq[3]), "=&v"(tq[4]), "=&v"(tq[5])                           \
                 : "v"((u32)t * 32), "v"((u32)l * 32), "s"(sws), "s"(WS_GRAN + 1536 * 32), "s"(WS_GRAN + 2048 * 32), "s"(msb) : "memory")
    TGATHER("");
    WAITV(6);
    STAMP(6);
    BAR();
    signal_cnt(ws, cg);
    WAITV(0);
#pragma unroll
    for (int i = 0; i < 6; ++i) PIN(tq[i]);
#pragma unroll 1
    for (u32 spins = 0; spins < (1u << 17); ++spins) {
      if (__all(gok(tq[0], tag) && gok(tq[1], tag) && gok(tq[2], tag) && gok(tq[3], tag) && gok(tq[4], tag) && gok(tq[5], tag))) break;
      __builtin_amdgcn_s_sleep(2);
      TGATHER("\n\ts_waitcnt vmcnt(0)");
    }
    *(LAS u16*)((LAS char*)smem + TAB + t * 2) = (u16)(__float_as_uint(bfround(1.f + bfround(gsum(tq[2], tq[3])))) >> 16);
    *(LAS u16*)((LAS char*)smem + TAB + 1024 + t * 2) = (u16)(__float_as_uint(bfround(gsum(tq[0], tq[1]))) >> 16);
    const float mv_ = bfround(gsum(tq[4], tq[5]));
    ((LAS float*)((LAS char*)smem + MT))[t] = (w == 1) ? bfround(1.f + mv_) : mv_;
  }

  const int lq = loc >= 28 ? 27 : loc;
  const int pan4 = (xcd >> 1) * 4 + lq / 7, pn4 = (xcd & 1) * 7 + lq % 7;
  constexpr u32 STG = KV_HEAD;
  f32x16 zero;
#pragma unroll
  for (int i = 0; i < 16; ++i) zero[i] = 0.f;
  bf16x8 wf[32];

  {
    const char* wsrc = (const char*)((l & 2) ? wu : wg) + (size_t)(l >> 2) * 3584 + (pn4 * 128 + w * 16 + (l & 1) * 8) * 2;
    const u32 stg_lb = lb + w * 3072;
    u32 stg_rd0 = (u32)(w * 3072 + (hb * 8 + (l >> 2 & 3)) * 64 + (l >> 4 & 1) * 32 + (l & 3) * 8), stg_rd1 = stg_rd0 + STG;
    PIN(stg_rd1);
#define WSTEP(p)                                                                                        \
    do {                                                                                                \
      if constexpr ((p) >= 5) {                                                                         \
        if ((p) <= 32) asm volatile("s_waitcnt vmcnt(4) lgkmcnt(0)" ::: "memory");                      \
        else if ((p) == 33) WAITV(3); else if ((p) == 34) WAITV(2); else if ((p) == 35) WAITV(1); else WAITV(0); \
        const u32 o_ = ((((p) - 5) % 6) < 3 ? stg_rd1 : stg_rd0) + (((p) - 5) % 3) * 1024;              \
        wf[(p) - 5] = lds_tr2(smem, o_, o_ + 256);                                                      \
      }                                                                                                 \
      if ((p) < 32) { GLDS64(wsrc, stg_lb, (((p) % 6) < 3 ? STG : 0u) + ((p) % 3) * 1024u); wsrc += 16 * 3584; } \
    } while (0)
    const int head = xcd * 4 + (loc >> 3), qt = (loc & 7) * 8 + w;
    const u32 ksb = (u32)(head * KV_HEAD + w * 1024), wlb = lb + w * 1024;
    const int r = l & 31;
    u32 krd = lo16;
    u32 vrd = (u32)(1024 + hb * 272 + (r < 16 ? r * 16 : (r == 16 ? 256 : 0x40000)));
    STAMP(7);
    wait_cnt(ws, xcd, 32, w);
    BAR();
    STAMP(8);
#define KVR(r) GLDS_SC1(lo16, sws, ksb, WS_KV + (r) * 8192, wlb, (r) * 8192)
    u32x4 qraw;
    GLD128_SC1(qraw, lo16, sws, (u32)(head * 64 + qt) * 1024, WS_QF);
    KVR(0); KVR(1);
    if (w < 4) KVR(16);
    KVR(2); KVR(3);
    WAITV(2);
    PIN(qraw);
    RAWBAR();
    STAMP(9);
    const bf16x8 qf = __builtin_bit_cast(bf16x8, qraw);
    float m;
    bf16x8 kf = lds_frag(smem, krd);
    {
      f32x16 s0 = MFMA32(kf, qf, zero);
      m = s0[0];
#pragma unroll
      for (int i = 1; i < 16; ++i) m = fmaxf(m, s0[i]);
      const auto a = __builtin_amdgcn_permlane32_swap(__float_as_uint(m), __float_as_uint(m), false, false);
      m = fmaxf(__uint_as_float(a[0]), __uint_as_float(a[1]));
    }
    f32x16 negm;
#pragma unroll
    for (int i = 0; i < 16; ++i) negm[i] = -m;
    f32x16 o = zero;
    f32x16 sc = MFMA32(kf, qf, negm);
    kf = lds_frag(smem, krd + KV_TILE);
    SFOR(j, 0, 64)
      if constexpr ((j & 1) == 0) __builtin_amdgcn_s_setprio(3 - (j >> 1 & 3));
      if (j == 5) { WAITV(10); RAWBAR(); }
      if (j == 13) { WAITV(0); RAWBAR(); }
      if (j == 14) STAMP(10);
      if constexpr (j < 6) { KVR(4 + 2 * j); KVR(5 + 2 * j); }
      if constexpr (j >= 14 && j < 63 && (j - 14) % 4 != 3) WSTEP((j - 14) - (j - 14) / 4);
      if (j == 30 || j == 60) { krd += 30 * KV_TILE; vrd += 30 * KV_TILE; PIN(krd); PIN(vrd); }
      const u32 jo = (u32)(j % 30) * KV_TILE;
      const bf16x8 v0 = lds_frag(smem, vrd + jo), v1 = lds_frag(smem, vrd + jo + 544);
      f32x16 sn;
      if (j < 63) sn = MFMA32(kf, qf, negm);
      if (j < 62) kf = lds_frag(smem, krd + jo + 2 * KV_TILE);
      SB();
#pragma unroll
      for (int i = 0; i < 11; ++i) sc[i] = __builtin_amdgcn_exp2f(sc[i]);
      SB();
      o = SMF(v0, LO8(sc), o);
      SB();
#pragma unroll
      for (int i = 11; i < 16; ++i) sc[i] = __builtin_amdgcn_exp2f(sc[i]);
      o = SMF(v1, HI8(sc), o);
      SB();
      if (j < 63) sc = sn;
    SEND
    float lsum = __uint_as_float(__builtin_amdgcn_permlane32_swap(__float_as_uint(o[8]), __float_as_uint(o[8]), false, false)[0]);
    bool ok = lsum < 1e18f;
#pragma unroll
    for (int i = 0; i < 8; ++i) ok = ok && (fabsf(o[i]) < 3e38f);
    if (!__all(ok)) {
      asm volatile("" ::: "memory");
      const __amdgpu_buffer_rsrc_t rk = __builtin_amdgcn_make_buffer_rsrc(ws + WS_KV + (size_t)head * KV_HEAD, 0, KV_HEAD, 0x20000);
      const int vg = 1024 + hb * 272 + (r == 16 ? 256 : (r & 15) * 16);
#define KVLD(off, j) __builtin_bit_cast(bf16x8, __builtin_amdgcn_raw_buffer_load_b128(rk, (off), (j) * KV_TILE, 16))
#pragma unroll 1
      for (int j = 0; j < 64; ++j) {
        const f32x16 sv = MFMA32(KVLD(lo16, j), qf, zero);
#pragma unroll
        for (int i = 0; i < 16; ++i) m = fmaxf(m, sv[i]);
      }
      m = fmaxf(m, __shfl_xor(m, 32));
#pragma unroll
      for (int i = 0; i < 16; ++i) negm[i] = -m;
      o = zero;
#pragma unroll 1
      for (int j = 0; j < 64; ++j) {
        f32x16 sv = MFMA32(KVLD(lo16, j), qf, negm);
        const bf16x8 v0 = KVLD(vg, j), v1 = KVLD(vg + 544, j);
#pragma unroll
        for (int i = 0; i < 16; ++i) sv[i] = __builtin_amdgcn_exp2f(sv[i]);
        o = SMF(v0, LO8(sv), o);
        o = SMF(v1, HI8(sv), o);
      }
      lsum = shfl_idx(o[8], r);
    }
    const float inv = 1.f / lsum;
    u32x4 ov;
#pragma unroll
    for (int e = 0; e < 4; ++e) ov[e] = pack2(o[2 * e] * inv, o[2 * e + 1] * inv);
    GST_SC1("x4", ov, lo16, sws, (u32)(qt * 32 + head) * 1024, WS_OF);
    STAMP(11);
    WAITV(0);
    BAR();
    signal_cnt(ws, 8 + (loc & 7));
    STAMP(12);
  }

  const int tm = w & 1;
  const u32 wvo = (u32)((l >> 3) * 1024 + ((((l >> 2) ^ (l >> 4)) & 1) * 32 + (l & 3) * 8) * 2);
  const u32 wrd0 = (u32)((hb * 4 + (l >> 2 & 3)) * 128 + (l >> 3 & 1) * 64 + (l >> 4 & 1) * 32 + (l & 3) * 8);
  const int rt3 = xcd * 8 + pm3 * 2;
  const int erow = w * 8 + (l & 7), ecol = l >> 3;
  f32x16 uv = zero;
  const u32 trd = TAB + (u32)((l >> 4 & 1) * 1024 + hb * 16);
#define UV_PART(k0)                                                                                          \
  do { bf16x8 tf_[16];                                                                                       \
    _Pragma("unroll") for (int i_ = 0; i_ < 4; ++i_) tf_[i_] = lds_frag(smem, trd + ((k0) + i_) * 32);       \
    _Pragma("unroll") for (int i_ = 0; i_ < 16; ++i_) {                                                      \
      if (i_ + 4 < 16) tf_[i_ + 4] = lds_frag(smem, trd + ((k0) + i_ + 4) * 32);                             \
      uv = MFMA32(wf[(k0) + i_], tf_[i_], uv); SB(); }                                                       \
    PIN(uv); } while (0)

  {
    const u32x4 so = make_srd(wo, 512 * 1024);
    const int ro = pn3 & 3;
    SFOR(j, 0, 8) GLDS(wvo, so, (u32)(w * 8192 + pn3 * 128 + ((j + 2 * ro) & 7) * 65536), 0, lb + w * 1024, j * 8192); SEND
    u32x4 xr;
    GLD128(xr, (u32)(erow * 1024 + ecol * 16), sx, (u32)(rt3 * 32768 + pn3 * 128), 0);
    UV_PART(0);
    WAITV(0);
    PIN(xr);
    STAMP(13);
    wait_cnt(ws, 8 + xcd, 32, w);
    RAWBAR();
    STAMP(14);
    if (bid == 0 && t == 0) __hip_atomic_store((u32*)(ws + WS_EPOCH), tag, __ATOMIC_RELAXED, __HIP_MEMORY_SCOPE_AGENT);
    const u32 olo = (u32)((w >> 2) * 32768 + (w & 3) * 2048);
    const u32 osb = (u32)rt3 * 32768 + olo, olb = lb + olo;
    SFOR(j, 0, 8) GLDS_SC1(lo16, sws, osb + (u32)(((j >> 1) + ro) & 3) * 8192, WS_OF + (j & 1) * 1024, olb, 65536 + (j >> 1) * 8192 + (j & 1) * 1024); SEND
    const int tn = w >> 1 & 1, kh = w >> 2;
    u32 ord = 65536 + (u32)(tm * 32768 + kh * 4096) + lo16;
    PIN(ord);
    const u32 wrd = (wrd0 ^ (u32)(tn * 64)) + kh * 8192;
    f32x16 acc = zero;
    bf16x8 wq[16];
#define WOFRAG(n) wq[n] = lds_tr2(smem, wrd + ((n) >> 2) * 16384 + ((n) & 3) * 2048, wrd + ((n) >> 2) * 16384 + ((n) & 3) * 2048 + 1024)
#pragma unroll
    for (int n = 0; n < 8; ++n) WOFRAG(n);
    SFOR(s, 0, 4)
      if (s == 0) WAITV(6); else if (s == 1) WAITV(4); else if (s == 2) WAITV(2); else WAITV(0);
      RAWBAR();
      bf16x8 oq[4];
#pragma unroll
      for (int j = 0; j < 4; ++j) oq[j] = lds_frag(smem, ord + s * 8192 + j * 1024);
      if constexpr (s < 2) {
#pragma unroll
        for (int j = 0; j < 4; ++j) WOFRAG(8 + s * 4 + j);
      }
      SB();
#pragma unroll
      for (int j = 0; j < 4; ++j) acc = MFMA32(wq[s * 4 + j], oq[j], acc);
      SB();
    SEND
    STAMP(15);
    {
      LAS char* eb = (LAS char*)smem + kh * 17408 + (tm * 32 + (l & 31)) * 272 + (tn * 32 + hb * 4) * 4;
#pragma unroll
      for (int e = 0; e < 4; ++e) *(LAS f32x4*)(eb + e * 32) = (f32x4){acc[4 * e], acc[4 * e + 1], acc[4 * e + 2], acc[4 * e + 3]};
    }
    f32x4 ga[2], cb[2];
#pragma unroll
    for (int e = 0; e < 2; ++e) { ga[e] = *(const LAS f32x4*)((LAS char*)smem + MT + ecol * 32 + e * 16); cb[e] = *(const LAS f32x4*)((LAS char*)smem + MT + 256 + ecol * 32 + e * 16); }
    BAR();
    float xv[8];
    u32x4 x1p, yp;
#pragma unroll
    for (int e = 0; e < 2; ++e) {
      const LAS char* eb = (const LAS char*)smem + erow * 272 + ecol * 32 + e * 16;
      const f32x4 a = *(const LAS f32x4*)eb + *(const LAS f32x4*)(eb + 17408);
#pragma unroll
      for (int h = 0; h < 2; ++h) {
        const u32 pk = pack2(bf_lo(xr[2 * e + h]) + ga[e][2 * h] * a[2 * h], bf_hi(xr[2 * e + h]) + ga[e][2 * h + 1] * a[2 * h + 1]);
        x1p[2 * e + h] = pk;
        xv[4 * e + 2 * h] = bf_lo(pk); xv[4 * e + 2 * h + 1] = bf_hi(pk);
        yp[2 * e + h] = pack2(bf_lo(pk) * cb[e][2 * h], bf_hi(pk) * cb[e][2 * h + 1]);
      }
    }
    GST_SC1("x4", yp, (u32)((ecol >> 1) * 1024 + ((ecol & 1) * 32 + (erow & 31)) * 16), sws, (u32)((rt3 + (w >> 2)) * 32 + pn3 * 4) * 1024, WS_Y);
    float sum = ((xv[0] + xv[1]) + (xv[2] + xv[3])) + ((xv[4] + xv[5]) + (xv[6] + xv[7]));
    const float mean = col8_sum(sum) * (1.f / 64.f);
    float sq = 0.f;
#pragma unroll
    for (int i = 0; i < 8; ++i) { const float d = xv[i] - mean; sq = __builtin_fmaf(d, d, sq); }
    const f32x2 o2 = {mean, col8_sum(sq)};
    GST_SC1("x2", o2, l < 8 ? (u32)((erow & 31) * 8) : 0x40000000u, sws, (u32)((rt3 + (w >> 2)) * 2048 + pn3 * 256), WS_ST2);
    STAMP(16);
    WAITV(0);
    RAWBAR();
    signal_cnt(ws, 16 + xcd * 2 + (pm3 >> 1));
    STAMP(17);
    *(u32x4*)(ws + WS_X1 + (size_t)(bid * 512 + t) * 16) = x1p;
  }

  const u32x4 sd = make_srd(wd, 1792 * 1024);
  const u32 dsb = (u32)(w * 16384 + pn3 * 128), dlb = lb + w * 2048;
#define WDPIECE(c, slot, h) GLDS(wvo, sd, dsb, (c) * 131072 + (h) * 8192, dlb, (slot) * 16384 + (h) * 1024)
#define WDISSUE(c, slot) do { WDPIECE(c, slot, 0); WDPIECE(c, slot, 1); } while (0)
  if (loc < 28) {
    constexpr u32 RST4 = 157696;
    UV_PART(16);
    float uu[16], vv[16];
#pragma unroll
    for (int i = 0; i < 16; ++i) {
      const auto r = __builtin_amdgcn_permlane16_swap(__float_as_uint(uv[i]), __float_as_uint(uv[i]), false, false);
      uu[i] = __uint_as_float(r[0]); vv[i] = __uint_as_float(r[1]);
      PIN(uu[i]); PIN(vv[i]);
    }
    wait_cnt(ws, 16 + pan4, 16, w);
    RAWBAR();
    STAMP(18);
    const int rt4 = pan4 * 4;
    const u32 ysb = (u32)(rt4 * 32768 + w * 1024), ylb = lb + w * 1024;
#define YPIECE(i) GLDS_SC1(lo16, sws, ysb, WS_Y + (i) * 8192, ylb, (i) * 8192)
    YPIECE(0); YPIECE(1); YPIECE(2); YPIECE(3);
    GLDS_SC1(lo16, sws, (u32)(rt4 * 2048 + w * 1024), WS_ST2, ylb, 131072);
    YPIECE(4); YPIECE(5);
    u32 yrd0 = lo16, yrd1 = lo16 + 65536;
    PIN(yrd1);
    const u32 asb = (u32)(rt4 * 112 + pn4 * 8 + w) * 1024;
#define YFRAG(n) lds_frag(smem, ((n) < 64 ? yrd0 : yrd1) + ((n) & 63) * 1024)
    WAITV(5);
    RAWBAR();
    STAMP(19);
    bf16x8 yf[128];
#pragma unroll
    for (int n = 0; n < 4; ++n) yf[n] = YFRAG(n);
    SB();
    f32x16 acc = zero, accp = zero;
    f32x2 stp = {0.f, 0.f};
    float eg[8], eu[8], et[8];
    u32x4 ov;
    SFOR(n, 0, 160)
      constexpr int a = n >> 5, ks = n & 31;
      if (n == 10) { WAITV(7); RAWBAR(); }
      if (n == 26) { WAITV(8); BAR(); }
      if (n == 56) { WAITV(4); RAWBAR(); }
      if (n == 100) WAITV(3);
      if constexpr (n < 20 && n % 2 == 0) YPIECE(6 + n / 2);
      if (n == 36 || n == 68 || n == 100) RAWBAR();
      if constexpr (n == 60) {
        const int rr = pm3 * 224 + w * 28 + (l < 28 ? l : 27);
        GTOUCH((u32)(rr + (rr < 256 ? 768 : 896)) * 1024, sd, (u32)(pn3 * 128), 0, lb + SCR);
      }
      if constexpr (n >= 36 && n < 52 && n % 4 == 0) WDPIECE((n - 36) / 8, (n - 36) / 8, (n - 36) / 4 & 1);
      if constexpr (n >= 68 && n < 84 && n % 4 == 0) WDPIECE(2 + (n - 68) / 8, 2 + (n - 68) / 8, (n - 68) / 4 & 1);
      if constexpr (n == 84 || n == 88) WDPIECE(8, 8, (n - 84) / 4);
      if constexpr (n >= 100 && n < 116 && n % 4 == 0) WDPIECE(4 + (n - 100) / 8, 4 + (n - 100) / 8, (n - 100) / 4 & 1);
      if constexpr (n == 12) {
        const LAS f32x2* sp = (const LAS f32x2*)((LAS char*)smem + 131072 + (t >> 7) * 2048 + (t & 3) * 512 + (t >> 2 & 31) * 8);
        const f32x2 p0 = sp[0], p1 = sp[32];
        float mu = p0[0] + p1[0];
        mu += dpp_xor1(mu); mu += dpp_xor2(mu);
        mu *= 0.125f;
        float q = p0[1] + p1[1] + 64.f * ((p0[0] - mu) * (p0[0] - mu) + (p1[0] - mu) * (p1[0] - mu));
        q += dpp_xor1(q); q += dpp_xor2(q);
        const float rs = __builtin_amdgcn_rsqf(q * (1.f / 512.f) + 1e-5f);
        ((LAS f32x2*)((LAS char*)smem + RST4))[t >> 2] = (f32x2){rs, -mu * rs};
      }
      if (ks == 28 && a < 4) stp = ((const LAS f32x2*)((LAS char*)smem + RST4))[a * 32 + (l & 31)];
      if (ks == 0 && a > 0) { accp = acc; acc = zero; }
      if (n == 128) STAMP(20);
      if constexpr (n + 4 < 128) yf[n + 4] = YFRAG(n + 4);
      if constexpr (n < 128) acc = MFMA32(wf[ks], yf[n], acc);
      if constexpr (a > 0 && ks >= 2 && ks < 26) {
        constexpr int i = (ks - 2) / 3, ph = (ks - 2) % 3;
        if (ph == 0) {
          eg[i] = __builtin_fmaf(accp[i], stp[0], __builtin_fmaf(uu[i], stp[1], vv[i]));
          eu[i] = __builtin_fmaf(accp[8 + i], stp[0], __builtin_fmaf(uu[8 + i], stp[1], vv[8 + i]));
          et[i] = -1.4426950408889634f * eg[i];
          if (a < 4) { PIN(eg[i]); PIN(eu[i]); PIN(et[i]); }
        } else if (ph == 1) {
          et[i] = 1.f + __builtin_amdgcn_exp2f(et[i]);
          eg[i] *= eu[i];
          if (a < 4) { PIN(eg[i]); PIN(et[i]); }
        } else {
          eg[i] *= __builtin_amdgcn_rcpf(et[i]);
          if (a < 4) PIN(eg[i]);
          if constexpr (i & 1) ov[i >> 1] = pack2(eg[i - 1], eg[i]);
        }
      }
      if constexpr (a > 0 && ks == 26) GST_SC1("x4", ov, lo16, sws, asb, WS_ACT + (a - 1) * 112 * 1024);
      if (n < 128) SB();
    SEND
    STAMP(21);
  } else {
    WDISSUE(0, 0); WDISSUE(1, 1); WDISSUE(2, 2); WDISSUE(3, 3); WDISSUE(8, 8); WDISSUE(4, 4); WDISSUE(5, 5);
    WAITV(0);
    RAWBAR();
  }

  {
    const int kq = w >> 1;
    u32 wra[3][2];
#pragma unroll
    for (int i = 0; i < 3; ++i) { wra[i][0] = wrd0 + kq * 4096 + i * 65536; wra[i][1] = wra[i][0] ^ 64u; PIN(wra[i][0]); PIN(wra[i][1]); }
    bf16x8 dq[14][4];
#define DFRAG(c) _Pragma("unroll") for (int i_ = 0; i_ < 4; ++i_) {                                              \
      const u32 o_ = wra[((c) % 10) >> 2][i_ & 1] + (((c) % 10) & 3) * 16384 + (i_ >> 1) * 2048; dq[c][i_] = lds_tr2(smem, o_, o_ + 1024); }
    DFRAG(0); DFRAG(1); DFRAG(2); DFRAG(3);
    f32x4 gb[2];
#pragma unroll
    for (int e = 0; e < 2; ++e) gb[e] = *(const LAS f32x4*)((LAS char*)smem + MT + 512 + ecol * 32 + e * 16);
    WAITV(0);
    BAR();
    if (loc < 28) signal_cnt(ws, 32 + pan4);
    STAMP(22);
#define WDREST() do { WDISSUE(6, 6); WDISSUE(7, 7); WDISSUE(9, 9); WDISSUE(10, 0); WDISSUE(11, 1); WDISSUE(12, 2); WDISSUE(13, 3); } while (0)
    if (w != 0) WDREST();
    wait_cnt(ws, 32 + xcd * 2 + (pm3 >> 1), 14, w);
    RAWBAR();
    STAMP(23);
    const u32 asb = (u32)((rt3 + tm) * 112 + kq * 2) * 1024;
    u32x4 af[14][2];
#define AISSUE(c) do { GLD128_SC1(af[c][0], lo16, sws, asb, WS_ACT + (c) * 8192); GLD128_SC1(af[c][1], lo16, sws, asb, WS_ACT + (c) * 8192 + 1024); } while (0)
    AISSUE(0); AISSUE(1);
    if (w == 0) WDREST();
    AISSUE(2); AISSUE(3); AISSUE(4); AISSUE(5);
    f32x16 acc[2] = {zero, zero};
    u32x4 x1p;
    SFOR(c, 0, 14)
      if (c < 8) WAITV(10); else if (c == 8) WAITV(11); else if (c == 9) WAITV(9);
      else if (c == 10) WAITV(7); else if (c == 11) WAITV(5); else if (c == 12) WAITV(3); else WAITV(0);
      if (c == 0) STAMP(24);
      if (c == 4 || c == 10) RAWBAR();
      if constexpr (c < 8) AISSUE(c + 6);
      if (c == 7) GLD128(x1p, (u32)t * 16, sws, (u32)bid * 8192, WS_X1);
      if constexpr (c >= 3 && c < 13) { DFRAG(c + 1); }
      PIN(af[c][0]); PIN(af[c][1]);
      SB();
#pragma unroll
      for (int i = 0; i < 4; ++i) acc[i & 1] = MFMA32(dq[c][i], __builtin_bit_cast(bf16x8, af[c][i >> 1]), acc[i & 1]);
      SB();
    SEND
    STAMP(25);
    {
      LAS char* eb = (LAS char*)smem + 65536 + kq * 17408 + (tm * 32 + (l & 31)) * 272 + hb * 16;
#pragma unroll
      for (int tn = 0; tn < 2; ++tn)
#pragma unroll
        for (int e = 0; e < 4; ++e)
          *(LAS f32x4*)(eb + tn * 128 + e * 32) = (f32x4){acc[tn][4 * e], acc[tn][4 * e + 1], acc[tn][4 * e + 2], acc[tn][4 * e + 3]};
    }
    BAR();
    PIN(x1p);
    u32x4 ov;
#pragma unroll
    for (int e = 0; e < 2; ++e) {
      const LAS char* eb = (const LAS char*)smem + 65536 + erow * 272 + ecol * 32 + e * 16;
      const f32x4 s = (*(const LAS f32x4*)eb + *(const LAS f32x4*)(eb + 17408)) + (*(const LAS f32x4*)(eb + 2 * 17408) + *(const LAS f32x4*)(eb + 3 * 17408));
      ov[2 * e] = pack2(bf_lo(x1p[2 * e]) + gb[e][0] * s[0], bf_hi(x1p[2 * e]) + gb[e][1] * s[1]);
      ov[2 * e + 1] = pack2(bf_lo(x1p[2 * e + 1]) + gb[e][2] * s[2], bf_hi(x1p[2 * e + 1]) + gb[e][3] * s[3]);
    }
    *(u32x4*)((char*)out + (size_t)(rt3 * 32 + erow) * 1024 + pn3 * 128 + ecol * 16) = ov;
  }
  ST_OUT(2, ws);
}

extern "C" void kernel_launch(void* const* d_in, const int* in_sizes, int n_in, void* d_out, int out_size, void* d_ws, size_t ws_size,
                              hipStream_t stream) {
  adaln_block<<<256, 512, 0, stream>>>((const u16*)d_in[0], (const u16*)d_in[1], (const u16*)d_in[2], (const u16*)d_in[4], (const u16*)d_in[5],
                                       (const u16*)d_in[6], (const u16*)d_in[7], (const u16*)d_in[8], (char*)d_ws, (u16*)d_out);
}
```

```cpp
#include <hip/hip_runtime.h>
#include <hip/hip_bf16.h>

typedef unsigned short u16;
typedef unsigned int u32;
typedef u32 u32x2 __attribute__((ext_vector_type(2)));
typedef u32 u32x4 __attribute__((ext_vector_type(4)));
typedef float f32x2 __attribute__((ext_vector_type(2)));
typedef float f32x4 __attribute__((ext_vector_type(4)));
typedef float f32x8 __attribute__((ext_vector_type(8)));
typedef float f32x16 __attribute__((ext_vector_type(16)));
typedef __bf16 bf16x2 __attribute__((ext_vector_type(2)));
typedef __bf16 bf16x8 __attribute__((ext_vector_type(8)));
typedef __bf16 bf16x16 __attribute__((ext_vector_type(16)));
typedef short s16x4 __attribute__((ext_vector_type(4)));
typedef short s16x8 __attribute__((ext_vector_type(8)));
#define PRIO_MODE 1
#define GATHER_AT 5
#define AORD 0
#define E1 9
#define AWIN 8
#define OUT_WT 0
#define CODE_PF 1
#define LAS __attribute__((address_space(3)))
#define DI __device__ __forceinline__

constexpr u32 WS_GRAN = 0;
constexpr u32 WS_EPOCH = 0x18000;
constexpr u32 WS_CNT  = 0x20000;
constexpr u32 WS_QF   = 0x100000;
constexpr u32 WS_KV   = 0x300000;
constexpr u32 WS_OF   = 0x800000;
constexpr u32 WS_X1   = 0xA00000;
constexpr u32 WS_Y    = 0xC00000;
constexpr u32 WS_ST2  = 0xE00000;
constexpr u32 WS_ACT  = 0xF00000;
constexpr u32 WS_BYTES = 0x1600000;
constexpr int KV_TILE = 2112, KV_HEAD = 64 * KV_TILE;

template <int I> struct IC { static constexpr int value = I; };
template <int B, int E, class F> DI void static_for(F&& f) { if constexpr (B < E) { f(IC<B>{}); static_for<B + 1, E>(f); } }
#define SFOR(n, B, E) static_for<B, E>([&](auto ic_) __attribute__((always_inline)) { constexpr int n = decltype(ic_)::value;
#define SEND });

DI float bf_lo(u32 u) { return __uint_as_float(u << 16); }
DI float bf_hi(u32 u) { return __uint_as_float(u & 0xffff0000u); }
DI u32 pack2(float a, float b) { bf16x2 r = {(__bf16)a, (__bf16)b}; return __builtin_bit_cast(u32, r); }
DI float bfround(float a) { return (float)(__bf16)a; }
DI float ln_rstd(float var) { return __builtin_amdgcn_rcpf(bfround(__builtin_amdgcn_sqrtf(bfround(bfround(var) + 1e-5f)))); }
DI float xor32_sum(float v) {
  const auto r = __builtin_amdgcn_permlane32_swap(__float_as_uint(v), __float_as_uint(v), false, false);
  return __uint_as_float(r[0]) + __uint_as_float(r[1]);
}
DI float dpp_xor1(float v) { return __int_as_float(__builtin_amdgcn_mov_dpp(__float_as_int(v), 0xB1, 0xF, 0xF, true)); }
DI float dpp_xor2(float v) { return __int_as_float(__builtin_amdgcn_mov_dpp(__float_as_int(v), 0x4E, 0xF, 0xF, true)); }
DI float shfl_idx(float v, int srcLane) { return __int_as_float(__builtin_amdgcn_ds_bpermute(srcLane << 2, __float_as_int(v))); }

DI u32x4 make_srd(const void* p, u32 bytes) {
  const size_t a = (size_t)p;
  u32x4 r = {(u32)a, (u32)(a >> 32) & 0xffffu, bytes, 0x20000u};
  return r;
}
#define GLDS(voff, srd, sb, GO, lb, LO)                                                                                   \
  do { u32 tmp_;                                                                                                          \
    asm volatile("s_add_u32 m0, %4, %6\n\ts_add_u32 %0, %3, %5\n\tbuffer_load_dwordx4 %1, %2, %0 offen lds"            \
                 : "=&s"(tmp_) : "v"(voff), "s"(srd), "s"(sb), "s"(lb), "i"(GO), "i"(LO) : "memory", "scc"); } while (0)
#define GLD(W, dst, voff, srd, sb, GO)                                                                                    \
  do { u32 tmp_;                                                                                                          \
    asm volatile("s_add_u32 %1, %4, %5\n\tbuffer_load_dword" W " %0, %2, %3, %1 offen"                                   \
                 : "=v"(dst), "=&s"(tmp_) : "v"(voff), "s"(srd), "s"(sb), "i"(GO) : "memory", "scc"); } while (0)
#define GLD128(dst, voff, srd, sb, GO) GLD("x4", dst, voff, srd, sb, GO)
#define GLD64(dst, voff, srd, sb, GO) GLD("x2", dst, voff, srd, sb, GO)
#define GLD32(dst, voff, srd, sb, GO) GLD("", dst, voff, srd, sb, GO)
#define WAITV(N) asm volatile("s_waitcnt vmcnt(" #N ")" ::: "memory")
#define PIN(x) asm volatile("" : "+v"(x))
#define BAR() do { asm volatile("s_waitcnt lgkmcnt(0)" ::: "memory"); __builtin_amdgcn_s_barrier(); asm volatile("" ::: "memory"); } while (0)

#define ST_DECL
#define STAMP(i)
#define ST_OUT(kid, wsp)

DI bf16x8 lds_frag(const char* smem, u32 off) { return *(const LAS bf16x8*)((const LAS char*)smem + off); }
DI bf16x8 lds_tr2(const char* smem, u32 off0, u32 off1) {
  s16x4 a = __builtin_amdgcn_ds_read_tr16_b64_v4i16((LAS s16x4*)((LAS char*)smem + off0));
  s16x4 b = __builtin_amdgcn_ds_read_tr16_b64_v4i16((LAS s16x4*)((LAS char*)smem + off1));
  s16x8 r = __builtin_shufflevector(a, b, 0, 1, 2, 3, 4, 5, 6, 7);
  return __builtin_bit_cast(bf16x8, r);
}
DI u32 lds_base(const char* smem) { return (u32)(size_t)(const LAS char*)smem; }
#define SB() __builtin_amdgcn_sched_barrier(0)
#define RAWBAR() do { asm volatile("" ::: "memory"); __builtin_amdgcn_s_barrier(); asm volatile("" ::: "memory"); } while (0)
#define MFMA32(a, b, c) __builtin_amdgcn_mfma_f32_32x32x16_bf16((a), (b), (c), 0, 0, 0)
#define MFMA16(a, b, c) __builtin_amdgcn_mfma_f32_16x16x32_bf16((a), (b), (c), 0, 0, 0)

DI u32x4 modulate8(const f32x8 n, const float* c1, const float* sh) {
  u32x4 o;
#pragma unroll
  for (int e = 0; e < 4; ++e) o[e] = pack2(__builtin_fmaf(n[2 * e], c1[2 * e], sh[2 * e]), __builtin_fmaf(n[2 * e + 1], c1[2 * e + 1], sh[2 * e + 1]));
  return o;
}

#define GLDS_SC1(voff, srd, sb, GO, lb, LO)                                                                               \
  do { u32 tmp_;                                                                                                          \
    asm volatile("s_add_u32 m0, %4, %6\n\ts_add_u32 %0, %3, %5\n\tbuffer_load_dwordx4 %1, %2, %0 offen sc1 lds"         \
                 : "=&s"(tmp_) : "v"(voff), "s"(srd), "s"(sb), "s"(lb), "i"(GO), "i"(LO) : "memory", "scc"); } while (0)
#define GLD128_SC1(dst, voff, srd, sb, GO)                                                                                \
  do { u32 tmp_;                                                                                                          \
    asm volatile("s_add_u32 %1, %4, %5\n\tbuffer_load_dwordx4 %0, %2, %3, %1 offen sc1"                                 \
                 : "=v"(dst), "=&s"(tmp_) : "v"(voff), "s"(srd), "s"(sb), "i"(GO) : "memory", "scc"); } while (0)
#define GST_SC1(W, val, voff, srd, sb, GO)                                                                                \
  do { u32 tmp_;                                                                                                          \
    asm volatile("s_add_u32 %0, %4, %5\n\tbuffer_store_dword" W " %1, %2, %3, %0 offen sc1\n\ts_nop 1"                  \
                 : "=&s"(tmp_) : "v"(val), "v"(voff), "s"(srd), "s"(sb), "i"(GO) : "memory", "scc"); } while (0)
#define GTOUCH(voff, srd, sb, GO, lb)                                                                                     \
  do { u32 tmp_;                                                                                                          \
    asm volatile("s_mov_b32 m0, %4\n\ts_add_u32 %0, %3, %5\n\tbuffer_load_dword %1, %2, %0 offen lds"                   \
                 : "=&s"(tmp_) : "v"(voff), "s"(srd), "s"(sb), "s"(lb), "i"(GO) : "memory", "scc"); } while (0)
#define GLDS64(vaddr, lb, LO)                                                                                             \
    asm volatile("s_add_u32 m0, %1, %2\n\ts_nop 0\n\tglobal_load_lds_dwordx4 %0, off" :: "v"(vaddr), "s"(lb), "i"(LO) : "memory", "scc")

DI void wave_sum8(const float* v, float* z) {
  float u[4];
#pragma unroll
  for (int i = 0; i < 4; ++i) {
    const auto a = __builtin_amdgcn_permlane32_swap(__float_as_uint(v[i]), __float_as_uint(v[i + 4]), false, false);
    u[i] = __uint_as_float(a[0]) + __uint_as_float(a[1]);
  }
#pragma unroll
  for (int i = 0; i < 2; ++i) {
    const auto a = __builtin_amdgcn_permlane16_swap(__float_as_uint(u[i]), __float_as_uint(u[i + 2]), false, false);
    float s = __uint_as_float(a[0]) + __uint_as_float(a[1]);
    s += dpp_xor1(s); s += dpp_xor2(s);
    s += __int_as_float(__builtin_amdgcn_mov_dpp(__float_as_int(s), 0x141, 0xF, 0xF, true));
    s += __int_as_float(__builtin_amdgcn_mov_dpp(__float_as_int(s), 0x140, 0xF, 0xF, true));
    z[i] = s;
  }
}
#define ROW_OF(z, r) __int_as_float(__builtin_amdgcn_readlane(__float_as_int(z[(r) & 1]), ((r) >> 1) * 16))
DI float col8_sum(float v) {
  v += __int_as_float(__builtin_amdgcn_mov_dpp(__float_as_int(v), 0x128, 0xF, 0xF, true));
  const auto a = __builtin_amdgcn_permlane16_swap(__float_as_uint(v), __float_as_uint(v), false, false);
  return xor32_sum(__uint_as_float(a[0]) + __uint_as_float(a[1]));
}

DI void signal_cnt(char* ws, int idx) {
  if (threadIdx.x == 0) __hip_atomic_fetch_add((u32*)(ws + WS_CNT) + idx * 64, 1u, __ATOMIC_RELAXED, __HIP_MEMORY_SCOPE_AGENT);
}
DI void wait_cnt(char* ws, int idx, u32 want, int w) {
  if (w == 0) {
    u32* c = (u32*)(ws + WS_CNT) + idx * 64;
#pragma unroll 1
    for (u32 spins = 0; spins < (1u << 17); ++spins) {
      if ((u32)__builtin_amdgcn_readfirstlane(__hip_atomic_load(c, __ATOMIC_RELAXED, __HIP_MEMORY_SCOPE_AGENT)) >= want) break;
      __builtin_amdgcn_s_sleep(1);
    }
    asm volatile("" ::: "memory");
  }
}
DI float gsum(const u32x4 a) { return __uint_as_float(a[0]) + __uint_as_float(a[2]); }
DI bool gok(const u32x4 a, u32 tag) { return a[1] == tag && a[3] == tag; }
#define SMF(v, p, o) __builtin_amdgcn_smfmac_f32_32x32x32_bf16((v), __builtin_bit_cast(bf16x16, (p)), (o), 0xDDDD, 0, 0)
#define LO8(s) __builtin_shufflevector(s, s, 0, 1, 2, 3, 4, 5, 6, 7)
#define HI8(s) __builtin_shufflevector(s, s, 8, 9, 10, 11, 12, 13, 14, 15)

extern "C" __global__ __launch_bounds__(512) void adaln_block(const u16* __restrict__ x, const u16* __restrict__ wqkv, const u16* __restrict__ wo,
                                                              const u16* __restrict__ wg, const u16* __restrict__ wu, const u16* __restrict__ wd,
                                                              const u16* __restrict__ c, const u16* __restrict__ wmod, char* __restrict__ ws,
                                                              u16* __restrict__ out) {
  __shared__ __attribute__((aligned(16))) char smem[163840];
  ST_DECL STAMP(0);
  const int bid = blockIdx.x, xcd = bid & 7, loc = bid >> 3;
  const int t = threadIdx.x, l = t & 63;
  const int w = __builtin_amdgcn_readfirstlane(t >> 6);
  const u32 lb = lds_base(smem);
  const u32x4 sws = make_srd(ws, WS_BYTES), sx = make_srd(x, 2048 * 1024);
  const u32 lo16 = (u32)l * 16, zs = 0;
  const int hb = l >> 5;
  const int pm3 = loc >> 3, pn3 = loc & 7;
  constexpr u32 TAB = 159744, MT = TAB + 2048, SCR = TAB;
  u32 tag;
  asm volatile("" :: "s"(x), "s"(wqkv), "s"(wo), "s"(wg), "s"(wu), "s"(wd), "s"(c), "s"(wmod), "s"(ws), "s"(out));

  {
    constexpr u32 WSLOT = 24576, RED = 49152, MTAB = 53248;
    const int pm = (xcd >> 1) * 8 + (loc >> 2), cg = (xcd & 1) * 4 + (loc & 3);
    const u32x4 sw = make_srd(wqkv, 512 * 3072), sm = make_srd(wmod, bid < 96 ? 512 * 6144 : 0), sc = make_srd(c, bid < 96 ? 1024 : 0);
    const int mcb = bid >> 1, mkq = bid & 1;
    u32 ev;
    u32x2 cv;
    u32x4 mv[4], xa[8];
    if (bid == 0 && w == 7) GST_SC1("", zs, (u32)l * 256, sws, zs, WS_CNT);
    asm volatile("buffer_load_dword %0, %1, %2, %3 offen sc1" : "=v"(ev) : "v"(zs), "s"(sws), "s"(WS_EPOCH) : "memory");
#pragma unroll
    for (int i = 0; i < 4; ++i) GLD128(mv[i], (u32)((t >> 3) * 4 * 6144 + (t & 7) * 16), sm, (u32)(mkq * 256 * 6144 + mcb * 128), i * 6144);
    GLD64(cv, (u32)((t >> 3) * 8), sc, (u32)(mkq * 512), 0);
    RAWBAR();
    const int rx = loc & 3, rw = loc >> 2;
    const u32 xsb = (u32)(pm * 64 + w * 2) * 1024;
#define XROW(i) GLD128(xa[i], lo16, sx, xsb + (u32)(((i) >> 1) ^ rx) * 16384, ((i) & 1) * 1024)
    const u32 wvo = (u32)((l >> 3) * 3072 + ((((l >> 2) ^ (l >> 4)) & 1) * 32 + (l & 3) * 8) * 2);
    const u32 wsb = (u32)(w * 8 * 3072 + cg * 128), wlb = lb + w * 1024;
#define KA_SLOT(kc) (((kc) >> 1 == 1 ? 0 : ((kc) >> 1 == 2 ? 65536 + 2 * WSLOT : 65536)) + ((kc) & 1) * WSLOT)
#define KA_PIECE(n) GLDS(wvo, sw, wsb + (u32)(((n) / 3 + rw) & 7) * (64 * 3072), ((n) % 3) * 1024, wlb, KA_SLOT((n) / 3) + ((n) % 3) * 8192)
    STAMP(26);
    XROW(0); XROW(1); XROW(2);
    STAMP(27);
    WAITV(3);
    STAMP(28);
    PIN(ev); PIN(cv);
#pragma unroll
    for (int i = 0; i < 4; ++i) PIN(mv[i]);
    tag = (u32)__builtin_amdgcn_readfirstlane(ev) + 1u;
    XROW(3); XROW(4); XROW(5);
    if (bid < 96) {
      float acc[8];
#pragma unroll
      for (int e = 0; e < 8; ++e) acc[e] = 0.f;
#pragma unroll
      for (int i = 0; i < 4; ++i) {
        const float cc = (i & 1) ? bf_hi(cv[i >> 1]) : bf_lo(cv[i >> 1]);
        const float sv = bfround(cc * __builtin_amdgcn_rcpf(1.f + __builtin_amdgcn_exp2f(-1.4426950408889634f * cc)));
#pragma unroll
        for (int e = 0; e < 4; ++e) { acc[2 * e] += sv * bf_lo(mv[i][e]); acc[2 * e + 1] += sv * bf_hi(mv[i][e]); }
      }
      float u[4], z[2];
#pragma unroll
      for (int i = 0; i < 4; ++i) {
        const auto a = __builtin_amdgcn_permlane32_swap(__float_as_uint(acc[i]), __float_as_uint(acc[i + 4]), false, false);
        u[i] = __uint_as_float(a[0]) + __uint_as_float(a[1]);
      }
#pragma unroll
      for (int i = 0; i < 2; ++i) {
        const auto a = __builtin_amdgcn_permlane16_swap(__float_as_uint(u[i]), __float_as_uint(u[i + 2]), false, false);
        z[i] = __uint_as_float(a[0]) + __uint_as_float(a[1]);
        z[i] += __int_as_float(__builtin_amdgcn_mov_dpp(__float_as_int(z[i]), 0x128, 0xF, 0xF, true));
      }
      *(LAS f32x2*)((LAS char*)smem + RED + w * 256 + (l & 7) * 32 + (l >> 4) * 8) = (f32x2){z[0], z[1]};
      BAR();
      if (w == 7) {
        const LAS float* rp = (const LAS float*)((LAS char*)smem + RED) + l;
        const float v = ((rp[0] + rp[64]) + (rp[128] + rp[192])) + ((rp[256] + rp[320]) + (rp[384] + rp[448]));
        const u32x2 gr = {__float_as_uint(v), tag};
        GST_SC1("x2", gr, lo16, sws, (u32)(mcb * 1024 + mkq * 8), WS_GRAN);
      }
    }
    STAMP(1);
    XROW(6); XROW(7);
    if (w == 6) {
      const u32x4 scode = make_srd((const void*)(__builtin_amdgcn_s_getpc() & ~127ull), 36000);
      GTOUCH((u32)(loc * 10 + (l < 10 ? l : 9)) * 128, scode, zs, 0, lb + 57344);
    }
    SFOR(n, 0, 6) KA_PIECE(n); SEND
    u32x4 gq[2];
#define GATHER(tail)                                                                                                            \
    asm volatile("buffer_load_dwordx4 %0, %2, %3, %4 offen sc1\n\tbuffer_load_dwordx4 %1, %2, %3, %5 offen sc1" tail            \
                 : "=&v"(gq[0]), "=&v"(gq[1]) : "v"((u32)t * 16), "s"(sws), "s"(WS_GRAN), "s"(WS_GRAN + 8192) : "memory")
    const int kind = w < 4 ? (w >> 1) : 2, hp = w < 4 ? (w & 1) : (w >> 1 & 1);
    u32 brd0 = (u32)(kind * 8192 + (hb * 8 + (l >> 2 & 3)) * 128 + ((hp ^ (l >> 3)) & 1) * 64 + (l >> 4 & 1) * 32 + (l & 3) * 8);
    u32 brd1 = brd0 + 65536, brd2 = brd0 + 65536 + 2 * WSLOT;
    PIN(brd1); PIN(brd2);
    bf16x8 wq[32];
#define KA_FRAG(kc) _Pragma("unroll") for (int c_ = 0; c_ < 4; ++c_) {                                                    \
      const u32 o_ = ((kc) >> 1 == 1 ? brd0 : ((kc) >> 1 == 2 ? brd2 : brd1)) + ((kc) & 1) * WSLOT + c_ * 2048; wq[(kc) * 4 + c_] = lds_tr2(smem, o_, o_ + 512); }
    if (w < 4) { SFOR(n, 6, 18) KA_PIECE(n); SEND WAITV(12); RAWBAR(); KA_FRAG(0); KA_FRAG(1); BAR(); } else WAITV(6);
    STAMP(30);
    float rs[8], mr[8];
    {
      float v[8], z[2];
      SFOR(i, 0, 8)
        PIN(xa[i]);
        v[i] = ((bf_lo(xa[i][0]) + bf_hi(xa[i][0])) + (bf_lo(xa[i][1]) + bf_hi(xa[i][1]))) + ((bf_lo(xa[i][2]) + bf_hi(xa[i][2])) + (bf_lo(xa[i][3]) + bf_hi(xa[i][3])));
      SEND
      STAMP(31);
      wave_sum8(v, z);
      z[0] *= (1.f / 512.f); z[1] *= (1.f / 512.f);
#define VAR_ROWS(B, E)                                                                                                   \
      SFOR(i, B, E)                                                                                                      \
        const float mu = ROW_OF(z, i);                                                                                   \
        float sq = 0.f;                                                                                                  \
        _Pragma("unroll") for (int e = 0; e < 4; ++e) {                                                                  \
          const float a = bf_lo(xa[i][e]) - mu, b = bf_hi(xa[i][e]) - mu; sq = __builtin_fmaf(a, a, sq); sq = __builtin_fmaf(b, b, sq); } \
        v[i] = sq;                                                                                                       \
      SEND
      VAR_ROWS(0, 8)
      float q[2];
      wave_sum8(v, q);
#pragma unroll
      for (int j = 0; j < 2; ++j) { q[j] = ln_rstd(q[j] * (1.f / 512.f)); z[j] = -bfround(z[j]) * q[j]; }
      SFOR(i, 0, 8) rs[i] = ROW_OF(q, i); mr[i] = ROW_OF(z, i); SEND
    }
    f32x8 nv[8];
#pragma unroll
    for (int i = 0; i < 8; ++i) {
#pragma unroll
      for (int e = 0; e < 4; ++e) { nv[i][2 * e] = __builtin_fmaf(bf_lo(xa[i][e]), rs[i], mr[i]); nv[i][2 * e + 1] = __builtin_fmaf(bf_hi(xa[i][e]), rs[i], mr[i]); }
      PIN(nv[i]);
    }
    if (w >= 4) { WAITV(0); RAWBAR(); KA_FRAG(0); KA_FRAG(1); BAR(); SFOR(n, 6, 18) KA_PIECE(n); SEND }
    GATHER("");
#define KB_PIECE(n, d) GLDS(wvo, sw, wsb + (u32)(((n) / 3 + rw) & 7) * (64 * 3072), ((n) % 3) * 1024 - (d) * 32 * 3072, wlb, KA_SLOT((n) / 3) + ((n) % 3) * 8192 - (d) * 4096)
    if (w >= 4) { SFOR(n, 18, 24) KB_PIECE(n, 0); KB_PIECE(n, 1); SEND WAITV(20); } else WAITV(8);
    STAMP(2);
    RAWBAR();
    KA_FRAG(2); KA_FRAG(3);
    if (w >= 4) WAITV(12); else WAITV(0);
    PIN(gq[0]); PIN(gq[1]);
    STAMP(3);
#pragma unroll 1
    for (u32 spins = 0; spins < (1u << 17); ++spins) {
      if (__all(gok(gq[0], tag) && gok(gq[1], tag))) break;
      __builtin_amdgcn_s_sleep(2);
      GATHER("\n\ts_waitcnt vmcnt(0)");
    }
    ((LAS float*)((LAS char*)smem + MTAB))[t] = bfround(1.f + bfround(gsum(gq[1])));
    ((LAS float*)((LAS char*)smem + MTAB))[512 + t] = bfround(gsum(gq[0]));
    BAR();
    STAMP(33);
    float c1[8], sh[8];
    {
      const LAS f32x4* tc = (const LAS f32x4*)((LAS char*)smem + MTAB + l * 32);
      const f32x4 a0 = tc[0], a1 = tc[1], b0 = tc[128], b1 = tc[129];
#pragma unroll
      for (int e = 0; e < 4; ++e) { c1[e] = a0[e]; c1[4 + e] = a1[e]; sh[e] = b0[e]; sh[4 + e] = b1[e]; }
    }
    u32 awr[4];
#pragma unroll
    for (int i = 0; i < 4; ++i) awr[i] = (u32)((((l >> 1) - 4 * rw) & 31) * 1024 + (l & 1) * 512 + (((i >> 1) ^ rx) & 1) * 256 + (((w * 2 + (i & 1)) ^ (l & 7)) << 4));
#define KA_PUT(i) *(LAS u32x4*)((LAS char*)smem + awr[(i) & 3] + ((i) >> 2) * 32768) = hv[i]
    u32x4 hv[8];
#pragma unroll
    for (int i = 0; i < 4; ++i) { hv[i] = modulate8(nv[i], c1, sh); KA_PUT(i); }
    const int ta = w < 4 ? 0 : (w & 1), tx = rx >> 1;
    u32 ard[4];
#pragma unroll
    for (int i = 0; i < 4; ++i) ard[i] = (u32)(ta * 32768 + i * 1024 + hb * 512 + (((l & 31) ^ (i * 2 + hb)) << 4));
#define HFRAG(n) lds_frag(smem, ard[(n) & 3] + ((n) >> 2) * 4096)
    const int head = cg * 4 + hp * 2;
    const u32 kvb = WS_KV + (u32)(head * KV_HEAD + pm * 2 * KV_TILE);
    f32x16 z16;
#pragma unroll
    for (int i = 0; i < 16; ++i) z16[i] = 0.f;
    BAR();
    STAMP(4);
    if (w < 4) {
      const float qs = kind == 0 ? 0.25f * 1.4426950408889634f : 1.f;
      const u32 so = kind == 0 ? WS_QF + (u32)(head * 64 + pm * 2) * 1024 + lo16 : kvb + (u32)((l & 51) | (l & 4) << 1 | (l & 8) >> 1) * 16;
      const u32 sh_ = kind == 0 ? 65536u : (u32)KV_HEAD, st_ = kind == 0 ? 1024u : (u32)KV_TILE;
      bf16x8 hf[64];
      f32x16 acc = z16, accp = z16;
      u32x4 ov[2];
#pragma unroll
      for (int i = 0; i < 4; ++i) hf[i] = HFRAG(i);
      SB();
      SFOR(n, 0, 76)
        if constexpr (n < 4) { hv[4 + n] = modulate8(nv[4 + n], c1, sh); KA_PUT(4 + n); }
        if constexpr (n == 5) { KA_FRAG(4); }
        if constexpr (n == 9) RAWBAR();
        if constexpr (n == 11) { KA_FRAG(5); }
        if constexpr (n == 15) { KA_FRAG(6); }
        if constexpr (n == 19) { KA_FRAG(7); }
        if constexpr (n == 32 || n == 64) { accp = acc; acc = z16; }
        if constexpr (n + 4 < 64) hf[n + 4] = HFRAG(n + 4);
        if constexpr (n < 64) acc = MFMA32(wq[n & 31], hf[n], acc);
        if constexpr (n >= 32 && (n & 31) >= 2 && (n & 31) < 10) {
          constexpr int e = (n & 31) - 2;
          ov[e >> 2][e & 3] = pack2(accp[2 * e] * qs, accp[2 * e + 1] * qs);
        }
        if constexpr (n == 42 || n == 74) GST_SC1("x4", ov[0], so, sws, st_ * ((n / 32 - 1) ^ tx), 0);
        if constexpr (n == 43 || n == 75) GST_SC1("x4", ov[1], so, sws, st_ * ((n / 32 - 1) ^ tx) + sh_, 0);
        if (n < 64) SB();
      SEND
    } else {
      bf16x8 hf[32];
      f32x16 acc = z16;
#pragma unroll
      for (int i = 4; i < 8; ++i) { hv[i] = modulate8(nv[i], c1, sh); KA_PUT(i); }
      WAITV(0);
      BAR();
#pragma unroll
      for (int i = 0; i < 4; ++i) hf[i] = HFRAG(i);
      SB();
      SFOR(n, 0, 32)
        if constexpr (n >= 2 && n < 18 && (n & 3) == 2) { KA_FRAG(4 + n / 4); }
        if constexpr (n + 4 < 32) hf[n + 4] = HFRAG(n + 4);
        acc = MFMA32(hf[n], wq[n], acc);
        SB();
      SEND
      const u32 kvt = kvb + (u32)((ta ^ tx) * KV_TILE);
      const u32 vo = kvt + (u32)((l >> 4 & 1) * KV_HEAD + 1024 + hb * 272 + (l & 15) * 16);
#pragma unroll
      for (int e = 0; e < 2; ++e) {
        const u32x4 ov = {pack2(acc[8 * e], acc[8 * e + 1]), pack2(acc[8 * e + 2], acc[8 * e + 3]), pack2(acc[8 * e + 4], acc[8 * e + 5]), pack2(acc[8 * e + 6], acc[8 * e + 7])};
        GST_SC1("x4", ov, vo, sws, zs, e * 544);
      }
      const u32x4 one = {0x3f803f80u, 0x3f803f80u, 0x3f803f80u, 0x3f803f80u};
      GST_SC1("x4", one, l < 8 ? kvt + (u32)((l >> 2) * KV_HEAD + 1024 + (l & 3) * 272 + 256) : 0x40000000u, sws, zs, 0);
    }
    STAMP(5);
    u32x4 tq[3];
    const u32 msb = WS_GRAN + (u32)(((w == 0 ? 1024 : (w == 1 ? 2048 : 2560)) + pn3 * 64) * 16);
#define TGATHER(tail)                                                                                                           \
    asm volatile("buffer_load_dwordx4 %0, %3, %5, %6 offen sc1\n\tbuffer_load_dwordx4 %1, %3, %5, %7 offen sc1\n\t"             \
                 "buffer_load_dwordx4 %2, %4, %5, %8 offen sc1" tail                                                            \
                 : "=&v"(tq[0]), "=&v"(tq[1]), "=&v"(tq[2])                                                                     \
                 : "v"((u32)t * 16), "v"(lo16), "s"(sws), "s"(WS_GRAN + 1536 * 16), "s"(WS_GRAN + 2048 * 16), "s"(msb) : "memory")
    TGATHER("");
    WAITV(3);
    STAMP(6);
    BAR();
    signal_cnt(ws, cg);
    WAITV(0);
#pragma unroll
    for (int i = 0; i < 3; ++i) PIN(tq[i]);
#pragma unroll 1
    for (u32 spins = 0; spins < (1u << 17); ++spins) {
      if (__all(gok(tq[0], tag) && gok(tq[1], tag) && gok(tq[2], tag))) break;
      __builtin_amdgcn_s_sleep(2);
      TGATHER("\n\ts_waitcnt vmcnt(0)");
    }
    *(LAS u16*)((LAS char*)smem + TAB + t * 2) = (u16)(__float_as_uint(bfround(1.f + bfround(gsum(tq[1])))) >> 16);
    *(LAS u16*)((LAS char*)smem + TAB + 1024 + t * 2) = (u16)(__float_as_uint(bfround(gsum(tq[0]))) >> 16);
    const float mv_ = bfround(gsum(tq[2]));
    ((LAS float*)((LAS char*)smem + MT))[t] = (w == 1) ? bfround(1.f + mv_) : mv_;
  }

  const int lq = loc >= 28 ? 27 : loc;
  const int pan4 = (xcd >> 1) * 4 + lq / 7, pn4 = (xcd & 1) * 7 + lq % 7;
  constexpr u32 STG = KV_HEAD;
  f32x16 zero;
#pragma unroll
  for (int i = 0; i < 16; ++i) zero[i] = 0.f;
  bf16x8 wf[32];

  {
    const char* wsrc = (const char*)((l & 2) ? wu : wg) + (size_t)(l >> 2) * 3584 + (pn4 * 128 + w * 16 + (l & 1) * 8) * 2;
    const u32 stg_lb = lb + w * 3072;
    u32 stg_rd0 = (u32)(w * 3072 + (hb * 8 + (l >> 2 & 3)) * 64 + (l >> 4 & 1) * 32 + (l & 3) * 8), stg_rd1 = stg_rd0 + STG;
    PIN(stg_rd1);
#define WREAD(p)                                                                                        \
    do {                                                                                                \
      if constexpr ((p) >= 5) {                                                                         \
        if ((p) <= 32) WAITV(4); else if ((p) == 33) WAITV(3); else if ((p) == 34) WAITV(2); else if ((p) == 35) WAITV(1); else WAITV(0); \
        const u32 o_ = ((((p) - 5) % 6) < 3 ? stg_rd1 : stg_rd0) + (((p) - 5) % 3) * 1024;              \
        wf[(p) - 5] = lds_tr2(smem, o_, o_ + 256);                                                      \
      }                                                                                                 \
    } while (0)
#define WLOAD(p) do { if ((p) < 32) { GLDS64(wsrc, stg_lb, (((p) % 6) < 3 ? STG : 0u) + ((p) % 3) * 1024u); wsrc += 16 * 3584; } } while (0)
    const int head = xcd * 4 + (loc >> 3), qt = (loc & 7) * 8 + w;
    const u32 ksb = (u32)(head * KV_HEAD + w * 1024), wlb = lb + w * 1024;
    const int r = l & 31;
    u32 krd = lo16;
    u32 vrd = (u32)(1024 + hb * 272 + (r < 16 ? r * 16 : (r == 16 ? 256 : 0x40000)));
    STAMP(7);
    wait_cnt(ws, xcd, 32, w);
    BAR();
    STAMP(8);
#define KVR(r) GLDS_SC1(lo16, sws, ksb, WS_KV + (r) * 8192, wlb, (r) * 8192)
    u32x4 qraw;
    GLD128_SC1(qraw, lo16, sws, (u32)(head * 64 + qt) * 1024, WS_QF);
    KVR(0); KVR(1);
    if (w < 4) KVR(16);
    KVR(2); KVR(3);
    WAITV(2);
    PIN(qraw);
    RAWBAR();
    STAMP(9);
    const bf16x8 qf = __builtin_bit_cast(bf16x8, qraw);
    float m;
    bf16x8 kf = lds_frag(smem, krd);
    {
      f32x16 s0 = MFMA32(kf, qf, zero);
      m = s0[0];
#pragma unroll
      for (int i = 1; i < 16; ++i) m = fmaxf(m, s0[i]);
      const auto a = __builtin_amdgcn_permlane32_swap(__float_as_uint(m), __float_as_uint(m), false, false);
      m = fmaxf(__uint_as_float(a[0]), __uint_as_float(a[1]));
    }
    f32x16 negm;
#pragma unroll
    for (int i = 0; i < 16; ++i) negm[i] = -m;
    f32x16 o = zero;
    f32x16 sc = MFMA32(kf, qf, negm);
    kf = lds_frag(smem, krd + KV_TILE);
    bf16x8 v0 = lds_frag(smem, vrd), v1;
    SFOR(j, 0, 64)
      if constexpr ((j & 1) == 0) __builtin_amdgcn_s_setprio(3 - (j >> 1 & 3));
      if (j == 5) { WAITV(10); RAWBAR(); }
      if (j == 13) { WAITV(0); RAWBAR(); }
      if (j == 14) STAMP(10);
      constexpr bool wst = j >= 14 && j < 63 && (j - 14) % 4 != 3;
      constexpr int wp = (j - 14) - (j - 14) / 4;
      if (j == 28 || j == 56) { krd += 28 * KV_TILE; vrd += 28 * KV_TILE; PIN(krd); PIN(vrd); }
      const u32 jo = (u32)(j % 28) * KV_TILE;
#define EXPS(a, b) _Pragma("unroll") for (int i = (a); i < (b); ++i) sc[i] = __builtin_amdgcn_exp2f(sc[i])
      f32x16 sn;
      v0 = lds_frag(smem, vrd + jo); v1 = lds_frag(smem, vrd + jo + 544);
      if (j < 63) sn = MFMA32(kf, qf, negm);
      if (j < 62) kf = lds_frag(smem, krd + jo + 2 * KV_TILE);
      SB();
      EXPS(0, E1);
      SB();
      o = SMF(v0, LO8(sc), o);
      if constexpr (wst) { WREAD(wp); WLOAD(wp); }
      if constexpr (j < 6) { KVR(4 + 2 * j); KVR(5 + 2 * j); }
      SB();
      EXPS(E1, 16);
      o = SMF(v1, HI8(sc), o);
      SB();
      if (j < 63) sc = sn;
    SEND
    float lsum = __uint_as_float(__builtin_amdgcn_permlane32_swap(__float_as_uint(o[8]), __float_as_uint(o[8]), false, false)[0]);
    bool ok = lsum < 1e18f;
#pragma unroll
    for (int i = 0; i < 8; ++i) ok = ok && (fabsf(o[i]) < 3e38f);
    if (!__all(ok)) {
      asm volatile("" ::: "memory");
      const __amdgpu_buffer_rsrc_t rk = __builtin_amdgcn_make_buffer_rsrc(ws + WS_KV + (size_t)head * KV_HEAD, 0, KV_HEAD, 0x20000);
      const int vg = 1024 + hb * 272 + (r == 16 ? 256 : (r & 15) * 16);
#define KVLD(off, j) __builtin_bit_cast(bf16x8, __builtin_amdgcn_raw_buffer_load_b128(rk, (off), (j) * KV_TILE, 16))
#pragma unroll 1
      for (int j = 0; j < 64; ++j) {
        const f32x16 sv = MFMA32(KVLD(lo16, j), qf, zero);
#pragma unroll
        for (int i = 0; i < 16; ++i) m = fmaxf(m, sv[i]);
      }
      m = fmaxf(m, __shfl_xor(m, 32));
#pragma unroll
      for (int i = 0; i < 16; ++i) negm[i] = -m;
      o = zero;
#pragma unroll 1
      for (int j = 0; j < 64; ++j) {
        f32x16 sv = MFMA32(KVLD(lo16, j), qf, negm);
        const bf16x8 v0 = KVLD(vg, j), v1 = KVLD(vg + 544, j);
#pragma unroll
        for (int i = 0; i < 16; ++i) sv[i] = __builtin_amdgcn_exp2f(sv[i]);
        o = SMF(v0, LO8(sv), o);
        o = SMF(v1, HI8(sv), o);
      }
      lsum = shfl_idx(o[8], r);
    }
    const float inv = 1.f / lsum;
    u32x4 ov;
#pragma unroll
    for (int e = 0; e < 4; ++e) ov[e] = pack2(o[2 * e] * inv, o[2 * e + 1] * inv);
    GST_SC1("x4", ov, lo16, sws, (u32)(qt * 32 + head) * 1024, WS_OF);
    STAMP(11);
    WAITV(0);
    BAR();
    signal_cnt(ws, 8 + (loc & 7));
    STAMP(12);
  }

  const int tm = w & 1;
  const u32 wvo = (u32)((l >> 3) * 1024 + ((((l >> 2) ^ (l >> 4)) & 1) * 32 + (l & 3) * 8) * 2);
  const u32 wrd0 = (u32)((hb * 4 + (l >> 2 & 3)) * 128 + (l >> 3 & 1) * 64 + (l >> 4 & 1) * 32 + (l & 3) * 8);
  const int rte = xcd * 8 + (pm3 >> 1) * 4 + (pm3 & 1) + (w >> 2) * 2;
  const int erow = w * 8 + (l & 7), ecol = l >> 3;
  f32x16 uv = zero;
  const u32 trd = TAB + (u32)((l >> 4 & 1) * 1024 + hb * 16);
#define UV_PART(k0)                                                                                          \
  do { bf16x8 tf_[16];                                                                                       \
    _Pragma("unroll") for (int i_ = 0; i_ < 4; ++i_) tf_[i_] = lds_frag(smem, trd + ((k0) + i_) * 32);       \
    _Pragma("unroll") for (int i_ = 0; i_ < 16; ++i_) {                                                      \
      if (i_ + 4 < 16) tf_[i_ + 4] = lds_frag(smem, trd + ((k0) + i_ + 4) * 32);                             \
      uv = MFMA32(wf[(k0) + i_], tf_[i_], uv); SB(); }                                                       \
    PIN(uv); } while (0)

  {
    const u32x4 so = make_srd(wo, 512 * 1024);
    const int ro = pn3 & 3;
    SFOR(j, 0, 8) GLDS(wvo, so, (u32)(w * 8192 + pn3 * 128 + ((j + 2 * ro) & 7) * 65536), 0, lb + w * 1024, j * 8192); SEND
    u32x4 xr;
    GLD128(xr, (u32)((erow & 31) * 1024 + ecol * 16), sx, (u32)(rte * 32768 + pn3 * 128), 0);
    UV_PART(0);
    WAITV(0);
    PIN(xr);
    STAMP(13);
    wait_cnt(ws, 8 + xcd, 32, w);
    RAWBAR();
    STAMP(14);
    if (bid == 0 && t == 0) __hip_atomic_store((u32*)(ws + WS_EPOCH), tag, __ATOMIC_RELAXED, __HIP_MEMORY_SCOPE_AGENT);
    const u32 olo = (u32)((w >> 2) * 32768 + (w & 3) * 2048);
    const u32 osb = (u32)(rte * 32768 + (w & 3) * 2048), olb = lb + olo;
    SFOR(j, 0, 8) GLDS_SC1(lo16, sws, osb + (u32)(((j >> 1) + ro) & 3) * 8192, WS_OF + (j & 1) * 1024, olb, 65536 + (j >> 1) * 8192 + (j & 1) * 1024); SEND
    const int tn = w >> 1 & 1, kh = w >> 2;
    u32 ord = 65536 + (u32)(tm * 32768 + kh * 4096) + lo16;
    PIN(ord);
    const u32 wrd = (wrd0 ^ (u32)(tn * 64)) + kh * 8192;
    f32x16 acc = zero;
    bf16x8 wq[16];
#define WOFRAG(n) wq[n] = lds_tr2(smem, wrd + ((n) >> 2) * 16384 + ((n) & 3) * 2048, wrd + ((n) >> 2) * 16384 + ((n) & 3) * 2048 + 1024)
#pragma unroll
    for (int n = 0; n < 8; ++n) WOFRAG(n);
    SFOR(s, 0, 4)
      if (s == 0) WAITV(6); else if (s == 1) WAITV(4); else if (s == 2) WAITV(2); else WAITV(0);
      RAWBAR();
      bf16x8 oq[4];
#pragma unroll
      for (int j = 0; j < 4; ++j) oq[j] = lds_frag(smem, ord + s * 8192 + j * 1024);
      if constexpr (s < 2) {
#pragma unroll
        for (int j = 0; j < 4; ++j) WOFRAG(8 + s * 4 + j);
      }
      SB();
#pragma unroll
      for (int j = 0; j < 4; ++j) acc = MFMA32(wq[s * 4 + j], oq[j], acc);
      SB();
    SEND
    STAMP(15);
    {
      LAS char* eb = (LAS char*)smem + kh * 17408 + (tm * 32 + (l & 31)) * 272 + (tn * 32 + hb * 4) * 4;
#pragma unroll
      for (int e = 0; e < 4; ++e) *(LAS f32x4*)(eb + e * 32) = (f32x4){acc[4 * e], acc[4 * e + 1], acc[4 * e + 2], acc[4 * e + 3]};
    }
    f32x4 ga[2], cb[2];
#pragma unroll
    for (int e = 0; e < 2; ++e) { ga[e] = *(const LAS f32x4*)((LAS char*)smem + MT + ecol * 32 + e * 16); cb[e] = *(const LAS f32x4*)((LAS char*)smem + MT + 256 + ecol * 32 + e * 16); }
    BAR();
    float xv[8];
    u32x4 x1p, yp;
#pragma unroll
    for (int e = 0; e < 2; ++e) {
      const LAS char* eb = (const LAS char*)smem + erow * 272 + ecol * 32 + e * 16;
      const f32x4 a = *(const LAS f32x4*)eb + *(const LAS f32x4*)(eb + 17408);
#pragma unroll
      for (int h = 0; h < 2; ++h) {
        const u32 pk = pack2(bf_lo(xr[2 * e + h]) + ga[e][2 * h] * a[2 * h], bf_hi(xr[2 * e + h]) + ga[e][2 * h + 1] * a[2 * h + 1]);
        x1p[2 * e + h] = pk;
        xv[4 * e + 2 * h] = bf_lo(pk); xv[4 * e + 2 * h + 1] = bf_hi(pk);
        yp[2 * e + h] = pack2(bf_lo(pk) * cb[e][2 * h], bf_hi(pk) * cb[e][2 * h + 1]);
      }
    }
    GST_SC1("x4", yp, (u32)((ecol >> 1) * 1024 + ((ecol & 1) * 32 + (erow & 31)) * 16), sws, (u32)(rte * 32 + pn3 * 4) * 1024, WS_Y);
    float sum = ((xv[0] + xv[1]) + (xv[2] + xv[3])) + ((xv[4] + xv[5]) + (xv[6] + xv[7]));
    const float mean = col8_sum(sum) * (1.f / 64.f);
    float sq = 0.f;
#pragma unroll
    for (int i = 0; i < 8; ++i) { const float d = xv[i] - mean; sq = __builtin_fmaf(d, d, sq); }
    const f32x2 o2 = {mean, col8_sum(sq)};
    GST_SC1("x2", o2, l < 8 ? (u32)((erow & 31) * 8) : 0x40000000u, sws, (u32)(rte * 2048 + pn3 * 256), WS_ST2);
    STAMP(16);
    WAITV(0);
    RAWBAR();
    signal_cnt(ws, 16 + xcd * 2 + (pm3 >> 1));
    STAMP(17);
    GST_SC1("x4", x1p, (u32)(erow * 8 + ecol) * 16, sws, (u32)bid * 8192, WS_X1);
  }

  const u32x4 sd = make_srd(wd, 1792 * 1024);
  const u32 dsb = (u32)(w * 16384 + pn3 * 128), dlb = lb + w * 2048;
  const int r5 = (pn3 * 7) >> 2;
#define CH5(c) (u32)((c) + r5 >= 14 ? (c) + r5 - 14 : (c) + r5)
#define WDPIECE(c, slot, h) GLDS(wvo, sd, dsb + CH5(c) * 131072, (h) * 8192, dlb, (slot) * 16384 + (h) * 1024)
#define WDISSUE(c, slot) do { WDPIECE(c, slot, 0); WDPIECE(c, slot, 1); } while (0)
  const int kq = w & 3;
  const u32 wr5 = wrd0 + kq * 4096;
  bf16x8 dq[14][4];
#define DFRAG1(c, i) dq[c][i] = lds_tr2(smem, (wr5 ^ (((i) & 1) * 64u)) + (c) * 16384 + ((i) >> 1) * 2048, (wr5 ^ (((i) & 1) * 64u)) + (c) * 16384 + ((i) >> 1) * 2048 + 1024)
  if (loc < 28) {
    constexpr u32 RST4 = 157696;
    UV_PART(16);
    float uu[16], vv[16];
#pragma unroll
    for (int i = 0; i < 16; ++i) {
      const auto r = __builtin_amdgcn_permlane16_swap(__float_as_uint(uv[i]), __float_as_uint(uv[i]), false, false);
      uu[i] = __uint_as_float(r[0]); vv[i] = __uint_as_float(r[1]);
      PIN(uu[i]); PIN(vv[i]);
    }
    wait_cnt(ws, 16 + pan4, 16, w);
    RAWBAR();
    STAMP(18);
    const int rt4 = pan4 * 4;
    const u32 ysb = (u32)(rt4 * 32768 + w * 1024), ylb = lb + w * 1024;
#define YPIECE(i) GLDS_SC1(lo16, sws, ysb, WS_Y + (i) * 8192, ylb, (i) * 8192)
    YPIECE(0); YPIECE(1); YPIECE(2); YPIECE(3);
    GLDS_SC1(lo16, sws, (u32)(rt4 * 2048 + w * 1024), WS_ST2, ylb, 131072);
    YPIECE(4); YPIECE(5);
    u32 yrd0 = lo16, yrd1 = lo16 + 65536;
    PIN(yrd1);
    const u32 asb = (u32)(rt4 * 112 + pn4 * 8 + w) * 1024;
#define YFRAG(n) lds_frag(smem, ((n) < 64 ? yrd0 : yrd1) + ((n) & 63) * 1024)
    WAITV(5);
    RAWBAR();
    STAMP(19);
    bf16x8 yf[128];
#pragma unroll
    for (int n = 0; n < 4; ++n) yf[n] = YFRAG(n);
    SB();
#define signal_early()                                                                                               \
    do { unsigned long long ex_;                                                                                     \
      asm volatile("s_cmp_eq_u32 %5, 0\n\ts_cbranch_scc0 1f\n\ts_mov_b64 %0, exec\n\ts_mov_b64 exec, 1\n\t"          \
                   "buffer_atomic_add %1, %2, %3, %4 offen\n\ts_mov_b64 exec, %0\n1:"                                \
                   : "=&s"(ex_) : "v"(1u), "v"(0u), "s"(sws), "s"(WS_CNT + (u32)(48 + pan4) * 256), "s"(w) : "memory", "scc"); } while (0)
    f32x16 acc = zero, accp = zero;
    f32x2 stp = {0.f, 0.f};
    float eg[8], eu[8], et[8];
    u32x4 ov;
    SFOR(n, 0, 160)
      constexpr int a = n >> 5, ks = n & 31;
      if (n == 10) { WAITV(7); RAWBAR(); }
      if (n == 26) { WAITV(8); BAR(); }
      if (n == 56) { WAITV(4); RAWBAR(); }
      if (n == 100) WAITV(3);
      if (n == 121) { WAITV(4); RAWBAR(); signal_early(); }
      if constexpr (n < 20 && n % 2 == 0) YPIECE(6 + n / 2);
      if (n == 36 || n == 68 || n == 100) RAWBAR();
      if constexpr (n == 60) {
        const int rr = pm3 * 224 + w * 28 + (l < 28 ? l : 27);
        const int r2 = rr + (rr < 256 ? 768 : 896) + r5 * 128;
        GTOUCH((u32)(r2 >= 1792 ? r2 - 1792 : r2) * 1024, sd, (u32)(pn3 * 128), 0, lb + SCR);
      }
      if constexpr (n >= 36 && n < 52 && n % 4 == 0) WDPIECE((n - 36) / 8, (n - 36) / 8, (n - 36) / 4 & 1);
      if constexpr (n >= 68 && n < 84 && n % 4 == 0) WDPIECE(2 + (n - 68) / 8, 2 + (n - 68) / 8, (n - 68) / 4 & 1);
      if constexpr (n == 84 || n == 88) WDPIECE(8, 8, (n - 84) / 4);
      if constexpr (n >= 100 && n < 116 && n % 4 == 0) WDPIECE(4 + (n - 100) / 8, 4 + (n - 100) / 8, (n - 100) / 4 & 1);
      if constexpr (n >= 100 && n < 116) DFRAG1((n - 100) >> 2, (n - 100) & 3);
      if constexpr (n >= 121 && n < 129) WDPIECE(10 + (n - 121) / 2, (n - 121) / 2, n & 1);
      if constexpr (n == 12) {
        const LAS f32x2* sp = (const LAS f32x2*)((LAS char*)smem + 131072 + (t >> 7) * 2048 + (t & 3) * 512 + (t >> 2 & 31) * 8);
        const f32x2 p0 = sp[0], p1 = sp[32];
        float mu = p0[0] + p1[0];
        mu += dpp_xor1(mu); mu += dpp_xor2(mu);
        mu *= 0.125f;
        float q = p0[1] + p1[1] + 64.f * ((p0[0] - mu) * (p0[0] - mu) + (p1[0] - mu) * (p1[0] - mu));
        q += dpp_xor1(q); q += dpp_xor2(q);
        const float rs = __builtin_amdgcn_rsqf(q * (1.f / 512.f) + 1e-5f);
        ((LAS f32x2*)((LAS char*)smem + RST4))[t >> 2] = (f32x2){rs, -mu * rs};
      }
      if (ks == 28 && a < 4) stp = ((const LAS f32x2*)((LAS char*)smem + RST4))[a * 32 + (l & 31)];
      if (ks == 0 && a > 0) { accp = acc; acc = zero; }
      if (n == 128) STAMP(20);
      if constexpr (n + 4 < 128) yf[n + 4] = YFRAG(n + 4);
      if constexpr (n < 128) acc = MFMA32(wf[ks], yf[n], acc);
      if constexpr (a > 0 && ks >= 2 && ks < 26) {
        constexpr int i = (ks - 2) / 3, ph = (ks - 2) % 3;
        if (ph == 0) {
          eg[i] = __builtin_fmaf(accp[i], stp[0], __builtin_fmaf(uu[i], stp[1], vv[i]));
          eu[i] = __builtin_fmaf(accp[8 + i], stp[0], __builtin_fmaf(uu[8 + i], stp[1], vv[8 + i]));
          et[i] = -1.4426950408889634f * eg[i];
          if (a < 4) { PIN(eg[i]); PIN(eu[i]); PIN(et[i]); }
        } else if (ph == 1) {
          et[i] = 1.f + __builtin_amdgcn_exp2f(et[i]);
          eg[i] *= eu[i];
          if (a < 4) { PIN(eg[i]); PIN(et[i]); }
        } else {
          eg[i] *= __builtin_amdgcn_rcpf(et[i]);
          if (a < 4) PIN(eg[i]);
          if constexpr (i & 1) ov[i >> 1] = pack2(eg[i - 1], eg[i]);
        }
      }
      if constexpr (a > 0 && ks == 26) GST_SC1("x4", ov, lo16, sws, asb, WS_ACT + (a - 1) * 112 * 1024);
      if (n < 128) SB();
    SEND
    STAMP(21);
  } else {
    WDISSUE(0, 0); WDISSUE(1, 1); WDISSUE(2, 2); WDISSUE(3, 3); WDISSUE(8, 8); WDISSUE(4, 4); WDISSUE(5, 5);
    WAITV(0);
    RAWBAR();
    SFOR(n, 0, 16) DFRAG1(n >> 2, n & 3); SEND
    BAR();
    WDISSUE(10, 0); WDISSUE(11, 1); WDISSUE(12, 2); WDISSUE(13, 3);
  }

  {
    u32 wra[3][2];
#pragma unroll
    for (int i = 0; i < 3; ++i) { wra[i][0] = wr5 + i * 65536; wra[i][1] = wra[i][0] ^ 64u; PIN(wra[i][0]); PIN(wra[i][1]); }
#define DFRAG(c) _Pragma("unroll") for (int i_ = 0; i_ < 4; ++i_) {                                              \
      const u32 o_ = wra[((c) % 10) >> 2][i_ & 1] + (((c) % 10) & 3) * 16384 + (i_ >> 1) * 2048; dq[c][i_] = lds_tr2(smem, o_, o_ + 1024); }
    f32x4 gb[2];
#pragma unroll
    for (int e = 0; e < 2; ++e) gb[e] = *(const LAS f32x4*)((LAS char*)smem + MT + 512 + (l & 7) * 32 + e * 16);
    const int pan5 = xcd * 2 + (pm3 >> 1);
    wait_cnt(ws, 48 + pan5, 14, w);
    WAITV(0);
    BAR();
    if (loc < 28) signal_cnt(ws, 32 + pan4);
    STAMP(22);
#define WD2(c, slot) do { WDISSUE(c, slot); WDPIECE(c, slot, -8); WDPIECE(c, slot, -7); } while (0)
#define BAR_IF(lo) asm volatile("s_cmp_lt_u32 %0, 4\n\ts_cbranch_scc" lo " 1f\n\ts_barrier\n1:" :: "s"(w) : "memory", "scc")
    if (w >= 4) {
      WD2(6, 6); WD2(7, 7); WD2(9, 9);
      WAITV(0);
      RAWBAR();
      wait_cnt(ws, 32 + pan5, 14, 0);
    }
    STAMP(23);
    const u32 asb = (u32)(rte * 112 + kq * 2) * 1024;
    u32x4 af[14][2];
#define AISSUE(c) do { GLD128_SC1(af[c][0], lo16, sws, asb + CH5(c) * 8192, WS_ACT); GLD128_SC1(af[c][1], lo16, sws, asb + CH5(c) * 8192, WS_ACT + 1024); } while (0)
    SFOR(c, 0, AWIN) AISSUE(c); SEND
    f32x16 acc[2] = {zero, zero};
    u32x4 x1p;
    SFOR(c, 0, 14)
      constexpr int young = c + AWIN <= 14 ? 2 * (AWIN - 1) : (c < 13 ? 2 * (13 - c) + 1 : 0);
      asm volatile("s_waitcnt vmcnt(%0)" :: "i"(young) : "memory");
      if (c == 0) STAMP(24);
      if (c == 5) STAMP(37);
      if (c == 10) STAMP(38);
      if (c == 2) BAR_IF("0");
      if (c == 10) BAR_IF("1");
      if constexpr (c + AWIN < 14) AISSUE(c + AWIN);
      if (c + AWIN == 14) GLD128_SC1(x1p, (u32)t * 16, sws, (u32)bid * 8192, WS_X1);
      if constexpr (c >= 3 && c < 13) { DFRAG(c + 1); }
      PIN(af[c][0]); PIN(af[c][1]);
      SB();
#pragma unroll
      for (int i = 0; i < 4; ++i) acc[i & 1] = MFMA32(dq[c][i], __builtin_bit_cast(bf16x8, af[c][i >> 1]), acc[i & 1]);
      SB();
    SEND
    STAMP(25);
    BAR_IF("0");
    {
      LAS char* eb = (LAS char*)smem + 65536 + kq * 17408 + ((w >> 2) * 32 + (l & 31)) * 272 + hb * 16;
#pragma unroll
      for (int tn = 0; tn < 2; ++tn)
#pragma unroll
        for (int e = 0; e < 4; ++e)
          *(LAS f32x4*)(eb + tn * 128 + e * 32) = (f32x4){acc[tn][4 * e], acc[tn][4 * e + 1], acc[tn][4 * e + 2], acc[tn][4 * e + 3]};
    }
    STAMP(40);
    BAR();
    STAMP(29);
    PIN(x1p);
    u32x4 ov;
#pragma unroll
    for (int e = 0; e < 2; ++e) {
      const LAS char* eb = (const LAS char*)smem + 65536 + (t >> 3) * 272 + (l & 7) * 32 + e * 16;
      const f32x4 s = (*(const LAS f32x4*)eb + *(const LAS f32x4*)(eb + 17408)) + (*(const LAS f32x4*)(eb + 2 * 17408) + *(const LAS f32x4*)(eb + 3 * 17408));
      ov[2 * e] = pack2(bf_lo(x1p[2 * e]) + gb[e][0] * s[0], bf_hi(x1p[2 * e]) + gb[e][1] * s[1]);
      ov[2 * e + 1] = pack2(bf_lo(x1p[2 * e + 1]) + gb[e][2] * s[2], bf_hi(x1p[2 * e + 1]) + gb[e][3] * s[3]);
    }
    *(u32x4*)((char*)out + (size_t)(rte * 32 + (t >> 3 & 31)) * 1024 + pn3 * 128 + (l & 7) * 16) = ov;
  }
  ST_OUT(2, ws);
}

extern "C" void kernel_launch(void* const* d_in, const int* in_sizes, int n_in, void* d_out, int out_size, void* d_ws, size_t ws_size,
                              hipStream_t stream) {
  adaln_block<<<256, 512, 0, stream>>>((const u16*)d_in[0], (const u16*)d_in[1], (const u16*)d_in[2], (const u16*)d_in[4], (const u16*)d_in[5],
                                       (const u16*)d_in[6], (const u16*)d_in[7], (const u16*)d_in[8], (char*)d_ws, (u16*)d_out);
}
```

```cpp
#define DIAG_AT 99
#include <hip/hip_runtime.h>
#include <hip/hip_bf16.h>

typedef unsigned short u16;
typedef unsigned int u32;
typedef u32 u32x2 __attribute__((ext_vector_type(2)));
typedef u32 u32x4 __attribute__((ext_vector_type(4)));
typedef float f32x2 __attribute__((ext_vector_type(2)));
typedef float f32x4 __attribute__((ext_vector_type(4)));
typedef float f32x8 __attribute__((ext_vector_type(8)));
typedef float f32x16 __attribute__((ext_vector_type(16)));
typedef __bf16 bf16x2 __attribute__((ext_vector_type(2)));
typedef __bf16 bf16x8 __attribute__((ext_vector_type(8)));
typedef __bf16 bf16x16 __attribute__((ext_vector_type(16)));
typedef short s16x4 __attribute__((ext_vector_type(4)));
typedef short s16x8 __attribute__((ext_vector_type(8)));
#define PRIO_MODE 1
#define GATHER_AT 5
#define AORD 0
#define E1 9
#define AWIN 8
#define OUT_WT 0
#define CODE_PF 8
#define LAS __attribute__((address_space(3)))
#define DI __device__ __forceinline__

constexpr u32 WS_GRAN = 0;
constexpr u32 WS_EPOCH = 0x18000;
constexpr u32 WS_CNT  = 0x20000;
constexpr u32 WS_QF   = 0x100000;
constexpr u32 WS_KV   = 0x300000;
constexpr u32 WS_OF   = 0x800000;
constexpr u32 WS_X1   = 0xA00000;
constexpr u32 WS_Y    = 0xC00000;
constexpr u32 WS_ST2  = 0xE00000;
constexpr u32 WS_ACT  = 0xF00000;
constexpr u32 WS_BYTES = 0x1600000;
constexpr int KV_TILE = 2112, KV_HEAD = 64 * KV_TILE;

template <int I> struct IC { static constexpr int value = I; };
template <int B, int E, class F> DI void static_for(F&& f) { if constexpr (B < E) { f(IC<B>{}); static_for<B + 1, E>(f); } }
#define SFOR(n, B, E) static_for<B, E>([&](auto ic_) __attribute__((always_inline)) { constexpr int n = decltype(ic_)::value;
#define SEND });

DI float bf_lo(u32 u) { return __uint_as_float(u << 16); }
DI float bf_hi(u32 u) { return __uint_as_float(u & 0xffff0000u); }
DI u32 pack2(float a, float b) { bf16x2 r = {(__bf16)a, (__bf16)b}; return __builtin_bit_cast(u32, r); }
DI float bfround(float a) { return (float)(__bf16)a; }
DI float ln_rstd(float var) { return __builtin_amdgcn_rcpf(bfround(__builtin_amdgcn_sqrtf(bfround(bfround(var) + 1e-5f)))); }
DI float xor32_sum(float v) {
  const auto r = __builtin_amdgcn_permlane32_swap(__float_as_uint(v), __float_as_uint(v), false, false);
  return __uint_as_float(r[0]) + __uint_as_float(r[1]);
}
DI float dpp_xor1(float v) { return __int_as_float(__builtin_amdgcn_mov_dpp(__float_as_int(v), 0xB1, 0xF, 0xF, true)); }
DI float dpp_xor2(float v) { return __int_as_float(__builtin_amdgcn_mov_dpp(__float_as_int(v), 0x4E, 0xF, 0xF, true)); }
DI float shfl_idx(float v, int srcLane) { return __int_as_float(__builtin_amdgcn_ds_bpermute(srcLane << 2, __float_as_int(v))); }

DI u32x4 make_srd(const void* p, u32 bytes) {
  const size_t a = (size_t)p;
  u32x4 r = {(u32)a, (u32)(a >> 32) & 0xffffu, bytes, 0x20000u};
  return r;
}
#define GLDS(voff, srd, sb, GO, lb, LO)                                                                                   \
  do { u32 tmp_;                                                                                                          \
    asm volatile("s_add_u32 m0, %4, %6\n\ts_add_u32 %0, %3, %5\n\tbuffer_load_dwordx4 %1, %2, %0 offen lds"            \
                 : "=&s"(tmp_) : "v"(voff), "s"(srd), "s"(sb), "s"(lb), "i"(GO), "i"(LO) : "memory", "scc"); } while (0)
#define GLD(W, dst, voff, srd, sb, GO)                                                                                    \
  do { u32 tmp_;                                                                                                          \
    asm volatile("s_add_u32 %1, %4, %5\n\tbuffer_load_dword" W " %0, %2, %3, %1 offen"                                   \
                 : "=v"(dst), "=&s"(tmp_) : "v"(voff), "s"(srd), "s"(sb), "i"(GO) : "memory", "scc"); } while (0)
#define GLD128(dst, voff, srd, sb, GO) GLD("x4", dst, voff, srd, sb, GO)
#define GLD64(dst, voff, srd, sb, GO) GLD("x2", dst, voff, srd, sb, GO)
#define GLD32(dst, voff, srd, sb, GO) GLD("", dst, voff, srd, sb, GO)
#define WAITV(N) asm volatile("s_waitcnt vmcnt(" #N ")" ::: "memory")
#define PIN(x) asm volatile("" : "+v"(x))
#define BAR() do { asm volatile("s_waitcnt lgkmcnt(0)" ::: "memory"); __builtin_amdgcn_s_barrier(); asm volatile("" ::: "memory"); } while (0)

#define ST_DECL const unsigned long long dg0_ = __builtin_amdgcn_s_memrealtime();
#define STAMP(i) do { if ((i) == DIAG_AT) { while (__builtin_amdgcn_s_memrealtime() - dg0_ < 6000) __builtin_amdgcn_s_sleep(1); } } while (0)
#define ST_OUT(kid, wsp) STAMP(99)

DI bf16x8 lds_frag(const char* smem, u32 off) { return *(const LAS bf16x8*)((const LAS char*)smem + off); }
DI bf16x8 lds_tr2(const char* smem, u32 off0, u32 off1) {
  s16x4 a = __builtin_amdgcn_ds_read_tr16_b64_v4i16((LAS s16x4*)((LAS char*)smem + off0));
  s16x4 b = __builtin_amdgcn_ds_read_tr16_b64_v4i16((LAS s16x4*)((LAS char*)smem + off1));
  s16x8 r = __builtin_shufflevector(a, b, 0, 1, 2, 3, 4, 5, 6, 7);
  return __builtin_bit_cast(bf16x8, r);
}
DI u32 lds_base(const char* smem) { return (u32)(size_t)(const LAS char*)smem; }
#define SB() __builtin_amdgcn_sched_barrier(0)
#define RAWBAR() do { asm volatile("" ::: "memory"); __builtin_amdgcn_s_barrier(); asm volatile("" ::: "memory"); } while (0)
#define MFMA32(a, b, c) __builtin_amdgcn_mfma_f32_32x32x16_bf16((a), (b), (c), 0, 0, 0)
#define MFMA16(a, b, c) __builtin_amdgcn_mfma_f32_16x16x32_bf16((a), (b), (c), 0, 0, 0)

DI u32x4 modulate8(const f32x8 n, const float* c1, const float* sh) {
  u32x4 o;
#pragma unroll
  for (int e = 0; e < 4; ++e) o[e] = pack2(__builtin_fmaf(n[2 * e], c1[2 * e], sh[2 * e]), __builtin_fmaf(n[2 * e + 1], c1[2 * e + 1], sh[2 * e + 1]));
  return o;
}

#define GLDS_SC1(voff, srd, sb, GO, lb, LO)                                                                               \
  do { u32 tmp_;                                                                                                          \
    asm volatile("s_add_u32 m0, %4, %6\n\ts_add_u32 %0, %3, %5\n\tbuffer_load_dwordx4 %1, %2, %0 offen sc1 lds"         \
                 : "=&s"(tmp_) : "v"(voff), "s"(srd), "s"(sb), "s"(lb), "i"(GO), "i"(LO) : "memory", "scc"); } while (0)
#define GLD128_SC1(dst, voff, srd, sb, GO)                                                                                \
  do { u32 tmp_;                                                                                                          \
    asm volatile("s_add_u32 %1, %4, %5\n\tbuffer_load_dwordx4 %0, %2, %3, %1 offen sc1"                                 \
                 : "=v"(dst), "=&s"(tmp_) : "v"(voff), "s"(srd), "s"(sb), "i"(GO) : "memory", "scc"); } while (0)
#define GST_SC1(W, val, voff, srd, sb, GO)                                                                                \
  do { u32 tmp_;                                                                                                          \
    asm volatile("s_add_u32 %0, %4, %5\n\tbuffer_store_dword" W " %1, %2, %3, %0 offen sc1\n\ts_nop 1"                  \
                 : "=&s"(tmp_) : "v"(val), "v"(voff), "s"(srd), "s"(sb), "i"(GO) : "memory", "scc"); } while (0)
#define GTOUCH(voff, srd, sb, GO, lb)                                                                                     \
  do { u32 tmp_;                                                                                                          \
    asm volatile("s_mov_b32 m0, %4\n\ts_add_u32 %0, %3, %5\n\tbuffer_load_dword %1, %2, %0 offen lds"                   \
                 : "=&s"(tmp_) : "v"(voff), "s"(srd), "s"(sb), "s"(lb), "i"(GO) : "memory", "scc"); } while (0)
#define GLDS64(vaddr, lb, LO)                                                                                             \
    asm volatile("s_add_u32 m0, %1, %2\n\ts_nop 0\n\tglobal_load_lds_dwordx4 %0, off" :: "v"(vaddr), "s"(lb), "i"(LO) : "memory", "scc")

DI void wave_sum8(const float* v, float* z) {
  float u[4];
#pragma unroll
  for (int i = 0; i < 4; ++i) {
    const auto a = __builtin_amdgcn_permlane32_swap(__float_as_uint(v[i]), __float_as_uint(v[i + 4]), false, false);
    u[i] = __uint_as_float(a[0]) + __uint_as_float(a[1]);
  }
#pragma unroll
  for (int i = 0; i < 2; ++i) {
    const auto a = __builtin_amdgcn_permlane16_swap(__float_as_uint(u[i]), __float_as_uint(u[i + 2]), false, false);
    float s = __uint_as_float(a[0]) + __uint_as_float(a[1]);
    s += dpp_xor1(s); s += dpp_xor2(s);
    s += __int_as_float(__builtin_amdgcn_mov_dpp(__float_as_int(s), 0x141, 0xF, 0xF, true));
    s += __int_as_float(__builtin_amdgcn_mov_dpp(__float_as_int(s), 0x140, 0xF, 0xF, true));
    z[i] = s;
  }
}
#define ROW_OF(z, r) __int_as_float(__builtin_amdgcn_readlane(__float_as_int(z[(r) & 1]), ((r) >> 1) * 16))
DI float col8_sum(float v) {
  v += __int_as_float(__builtin_amdgcn_mov_dpp(__float_as_int(v), 0x128, 0xF, 0xF, true));
  const auto a = __builtin_amdgcn_permlane16_swap(__float_as_uint(v), __float_as_uint(v), false, false);
  return xor32_sum(__uint_as_float(a[0]) + __uint_as_float(a[1]));
}

DI void signal_cnt(char* ws, int idx) {
  if (threadIdx.x == 0) __hip_atomic_fetch_add((u32*)(ws + WS_CNT) + idx * 64, 1u, __ATOMIC_RELAXED, __HIP_MEMORY_SCOPE_AGENT);
}
DI void wait_cnt(char* ws, int idx, u32 want, int w) {
  if (w == 0) {
    u32* c = (u32*)(ws + WS_CNT) + idx * 64;
#pragma unroll 1
    for (u32 spins = 0; spins < (1u << 17); ++spins) {
      if ((u32)__builtin_amdgcn_readfirstlane(__hip_atomic_load(c, __ATOMIC_RELAXED, __HIP_MEMORY_SCOPE_AGENT)) >= want) break;
      __builtin_amdgcn_s_sleep(1);
    }
    asm volatile("" ::: "memory");
  }
}
DI float gsum(const u32x4 a) { return __uint_as_float(a[0]) + __uint_as_float(a[2]); }
DI bool gok(const u32x4 a, u32 tag) { return a[1] == tag && a[3] == tag; }
#define SMF(v, p, o) __builtin_amdgcn_smfmac_f32_32x32x32_bf16((v), __builtin_bit_cast(bf16x16, (p)), (o), 0xDDDD, 0, 0)
#define LO8(s) __builtin_shufflevector(s, s, 0, 1, 2, 3, 4, 5, 6, 7)
#define HI8(s) __builtin_shufflevector(s, s, 8, 9, 10, 11, 12, 13, 14, 15)

extern "C" __global__ __launch_bounds__(512) void adaln_block(const u16* __restrict__ x, const u16* __restrict__ wqkv, const u16* __restrict__ wo,
                                                              const u16* __restrict__ wg, const u16* __restrict__ wu, const u16* __restrict__ wd,
                                                              const u16* __restrict__ c, const u16* __restrict__ wmod, char* __restrict__ ws,
                                                              u16* __restrict__ out) {
  __shared__ __attribute__((aligned(16))) char smem[163840];
  ST_DECL STAMP(0);
  const int bid = blockIdx.x, xcd = bid & 7, loc = bid >> 3;
  const int t = threadIdx.x, l = t & 63;
  const int w = __builtin_amdgcn_readfirstlane(t >> 6);
  const u32 lb = lds_base(smem);
  {
    unsigned long long pc;
    u32 len;
    asm volatile("s_getpc_b64 %0\n.Lcode_mark:\n\ts_mov_b32 %1, .Lcode_end-.Lcode_mark" : "=s"(pc), "=s"(len));
    const u32x4 scode = make_srd((const void*)pc, loc < CODE_PF ? len : 0u);
    GTOUCH((u32)(loc * 512 + t) * 128 / CODE_PF, scode, 0u, 0, lb + 57344);
    SB();
  }
  const u32x4 sws = make_srd(ws, WS_BYTES), sx = make_srd(x, 2048 * 1024);
  const u32 lo16 = (u32)l * 16, zs = 0;
  const int hb = l >> 5;
  const int pm3 = loc >> 3, pn3 = loc & 7;
  constexpr u32 TAB = 159744, MT = TAB + 2048, SCR = TAB;
  u32 tag;
  asm volatile("" :: "s"(x), "s"(wqkv), "s"(wo), "s"(wg), "s"(wu), "s"(wd), "s"(c), "s"(wmod), "s"(ws), "s"(out));

  {
    constexpr u32 WSLOT = 24576, RED = 49152, MTAB = 53248;
    const int pm = (xcd >> 1) * 8 + (loc >> 2), cg = (xcd & 1) * 4 + (loc & 3);
    const u32x4 sw = make_srd(wqkv, 512 * 3072), sm = make_srd(wmod, bid < 96 ? 512 * 6144 : 0), sc = make_srd(c, bid < 96 ? 1024 : 0);
    const int mcb = bid >> 1, mkq = bid & 1;
    u32 ev;
    u32x2 cv;
    u32x4 mv[4], xa[8];
    if (bid == 0 && w == 7) GST_SC1("", zs, (u32)l * 256, sws, zs, WS_CNT);
    asm volatile("buffer_load_dword %0, %1, %2, %3 offen sc1" : "=v"(ev) : "v"(zs), "s"(sws), "s"(WS_EPOCH) : "memory");
#pragma unroll
    for (int i = 0; i < 4; ++i) GLD128(mv[i], (u32)((t >> 3) * 4 * 6144 + (t & 7) * 16), sm, (u32)(mkq * 256 * 6144 + mcb * 128), i * 6144);
    GLD64(cv, (u32)((t >> 3) * 8), sc, (u32)(mkq * 512), 0);
    RAWBAR();
    const int rx = loc & 3, rw = loc >> 2;
    const u32 xsb = (u32)(pm * 64 + w * 2) * 1024;
#define XROW(i) GLD128(xa[i], lo16, sx, xsb + (u32)(((i) >> 1) ^ rx) * 16384, ((i) & 1) * 1024)
    const u32 wvo = (u32)((l >> 3) * 3072 + ((((l >> 2) ^ (l >> 4)) & 1) * 32 + (l & 3) * 8) * 2);
    const u32 wsb = (u32)(w * 8 * 3072 + cg * 128), wlb = lb + w * 1024;
#define KA_SLOT(kc) (((kc) >> 1 == 1 ? 0 : ((kc) >> 1 == 2 ? 65536 + 2 * WSLOT : 65536)) + ((kc) & 1) * WSLOT)
#define KA_PIECE(n) GLDS(wvo, sw, wsb + (u32)(((n) / 3 + rw) & 7) * (64 * 3072), ((n) % 3) * 1024, wlb, KA_SLOT((n) / 3) + ((n) % 3) * 8192)
    STAMP(26);
    XROW(0); XROW(1); XROW(2);
    STAMP(27);
    WAITV(3);
    STAMP(28);
    PIN(ev); PIN(cv);
#pragma unroll
    for (int i = 0; i < 4; ++i) PIN(mv[i]);
    tag = (u32)__builtin_amdgcn_readfirstlane(ev) + 1u;
    XROW(3); XROW(4); XROW(5);
    if (bid < 96) {
      float acc[8];
#pragma unroll
      for (int e = 0; e < 8; ++e) acc[e] = 0.f;
#pragma unroll
      for (int i = 0; i < 4; ++i) {
        const float cc = (i & 1) ? bf_hi(cv[i >> 1]) : bf_lo(cv[i >> 1]);
        const float sv = bfround(cc * __builtin_amdgcn_rcpf(1.f + __builtin_amdgcn_exp2f(-1.4426950408889634f * cc)));
#pragma unroll
        for (int e = 0; e < 4; ++e) { acc[2 * e] += sv * bf_lo(mv[i][e]); acc[2 * e + 1] += sv * bf_hi(mv[i][e]); }
      }
      float u[4], z[2];
#pragma unroll
      for (int i = 0; i < 4; ++i) {
        const auto a = __builtin_amdgcn_permlane32_swap(__float_as_uint(acc[i]), __float_as_uint(acc[i + 4]), false, false);
        u[i] = __uint_as_float(a[0]) + __uint_as_float(a[1]);
      }
#pragma unroll
      for (int i = 0; i < 2; ++i) {
        const auto a = __builtin_amdgcn_permlane16_swap(__float_as_uint(u[i]), __float_as_uint(u[i + 2]), false, false);
        z[i] = __uint_as_float(a[0]) + __uint_as_float(a[1]);
        z[i] += __int_as_float(__builtin_amdgcn_mov_dpp(__float_as_int(z[i]), 0x128, 0xF, 0xF, true));
      }
      *(LAS f32x2*)((LAS char*)smem + RED + w * 256 + (l & 7) * 32 + (l >> 4) * 8) = (f32x2){z[0], z[1]};
      BAR();
      if (w == 7) {
        const LAS float* rp = (const LAS float*)((LAS char*)smem + RED) + l;
        const float v = ((rp[0] + rp[64]) + (rp[128] + rp[192])) + ((rp[256] + rp[320]) + (rp[384] + rp[448]));
        const u32x2 gr = {__float_as_uint(v), tag};
        GST_SC1("x2", gr, lo16, sws, (u32)(mcb * 1024 + mkq * 8), WS_GRAN);
      }
    }
    STAMP(1);
    XROW(6); XROW(7);
    SFOR(n, 0, 6) KA_PIECE(n); SEND
    u32x4 gq[2];
#define GATHER(tail)                                                                                                            \
    asm volatile("buffer_load_dwordx4 %0, %2, %3, %4 offen sc1\n\tbuffer_load_dwordx4 %1, %2, %3, %5 offen sc1" tail            \
                 : "=&v"(gq[0]), "=&v"(gq[1]) : "v"((u32)t * 16), "s"(sws), "s"(WS_GRAN), "s"(WS_GRAN + 8192) : "memory")
    const int kind = w < 4 ? (w >> 1) : 2, hp = w < 4 ? (w & 1) : (w >> 1 & 1);
    u32 brd0 = (u32)(kind * 8192 + (hb * 8 + (l >> 2 & 3)) * 128 + ((hp ^ (l >> 3)) & 1) * 64 + (l >> 4 & 1) * 32 + (l & 3) * 8);
    u32 brd1 = brd0 + 65536, brd2 = brd0 + 65536 + 2 * WSLOT;
    PIN(brd1); PIN(brd2);
    bf16x8 wq[32];
#define KA_FRAG(kc) _Pragma("unroll") for (int c_ = 0; c_ < 4; ++c_) {                                                    \
      const u32 o_ = ((kc) >> 1 == 1 ? brd0 : ((kc) >> 1 == 2 ? brd2 : brd1)) + ((kc) & 1) * WSLOT + c_ * 2048; wq[(kc) * 4 + c_] = lds_tr2(smem, o_, o_ + 512); }
    if (w < 4) { SFOR(n, 6, 18) KA_PIECE(n); SEND WAITV(12); RAWBAR(); KA_FRAG(0); KA_FRAG(1); BAR(); } else WAITV(6);
    STAMP(30);
    float rs[8], mr[8];
    {
      float v[8], z[2];
      SFOR(i, 0, 8)
        PIN(xa[i]);
        v[i] = ((bf_lo(xa[i][0]) + bf_hi(xa[i][0])) + (bf_lo(xa[i][1]) + bf_hi(xa[i][1]))) + ((bf_lo(xa[i][2]) + bf_hi(xa[i][2])) + (bf_lo(xa[i][3]) + bf_hi(xa[i][3])));
      SEND
      STAMP(31);
      wave_sum8(v, z);
      z[0] *= (1.f / 512.f); z[1] *= (1.f / 512.f);
#define VAR_ROWS(B, E)                                                                                                   \
      SFOR(i, B, E)                                                                                                      \
        const float mu = ROW_OF(z, i);                                                                                   \
        float sq = 0.f;                                                                                                  \
        _Pragma("unroll") for (int e = 0; e < 4; ++e) {                                                                  \
          const float a = bf_lo(xa[i][e]) - mu, b = bf_hi(xa[i][e]) - mu; sq = __builtin_fmaf(a, a, sq); sq = __builtin_fmaf(b, b, sq); } \
        v[i] = sq;                                                                                                       \
      SEND
      VAR_ROWS(0, 8)
      float q[2];
      wave_sum8(v, q);
#pragma unroll
      for (int j = 0; j < 2; ++j) { q[j] = ln_rstd(q[j] * (1.f / 512.f)); z[j] = -bfround(z[j]) * q[j]; }
      SFOR(i, 0, 8) rs[i] = ROW_OF(q, i); mr[i] = ROW_OF(z, i); SEND
    }
    f32x8 nv[8];
#pragma unroll
    for (int i = 0; i < 8; ++i) {
#pragma unroll
      for (int e = 0; e < 4; ++e) { nv[i][2 * e] = __builtin_fmaf(bf_lo(xa[i][e]), rs[i], mr[i]); nv[i][2 * e + 1] = __builtin_fmaf(bf_hi(xa[i][e]), rs[i], mr[i]); }
      PIN(nv[i]);
    }
    if (w >= 4) { WAITV(0); RAWBAR(); KA_FRAG(0); KA_FRAG(1); BAR(); SFOR(n, 6, 18) KA_PIECE(n); SEND }
    GATHER("");
#define KB_PIECE(n, d) GLDS(wvo, sw, wsb + (u32)(((n) / 3 + rw) & 7) * (64 * 3072), ((n) % 3) * 1024 - (d) * 32 * 3072, wlb, KA_SLOT((n) / 3) + ((n) % 3) * 8192 - (d) * 4096)
    if (w >= 4) { SFOR(n, 18, 24) KB_PIECE(n, 0); KB_PIECE(n, 1); SEND WAITV(20); } else WAITV(8);
    STAMP(2);
    RAWBAR();
    KA_FRAG(2); KA_FRAG(3);
    if (w >= 4) WAITV(12); else WAITV(0);
    PIN(gq[0]); PIN(gq[1]);
    STAMP(3);
#pragma unroll 1
    for (u32 spins = 0; spins < (1u << 17); ++spins) {
      if (__all(gok(gq[0], tag) && gok(gq[1], tag))) break;
      __builtin_amdgcn_s_sleep(2);
      GATHER("\n\ts_waitcnt vmcnt(0)");
    }
    ((LAS float*)((LAS char*)smem + MTAB))[t] = bfround(1.f + bfround(gsum(gq[1])));
    ((LAS float*)((LAS char*)smem + MTAB))[512 + t] = bfround(gsum(gq[0]));
    BAR();
    STAMP(33);
    float c1[8], sh[8];
    {
      const LAS f32x4* tc = (const LAS f32x4*)((LAS char*)smem + MTAB + l * 32);
      const f32x4 a0 = tc[0], a1 = tc[1], b0 = tc[128], b1 = tc[129];
#pragma unroll
      for (int e = 0; e < 4; ++e) { c1[e] = a0[e]; c1[4 + e] = a1[e]; sh[e] = b0[e]; sh[4 + e] = b1[e]; }
    }
    u32 awr[4];
#pragma unroll
    for (int i = 0; i < 4; ++i) awr[i] = (u32)((((l >> 1) - 4 * rw) & 31) * 1024 + (l & 1) * 512 + (((i >> 1) ^ rx) & 1) * 256 + (((w * 2 + (i & 1)) ^ (l & 7)) << 4));
#define KA_PUT(i) *(LAS u32x4*)((LAS char*)smem + awr[(i) & 3] + ((i) >> 2) * 32768) = hv[i]
    u32x4 hv[8];
#pragma unroll
    for (int i = 0; i < 4; ++i) { hv[i] = modulate8(nv[i], c1, sh); KA_PUT(i); }
    const int ta = w < 4 ? 0 : (w & 1), tx = rx >> 1;
    u32 ard[4];
#pragma unroll
    for (int i = 0; i < 4; ++i) ard[i] = (u32)(ta * 32768 + i * 1024 + hb * 512 + (((l & 31) ^ (i * 2 + hb)) << 4));
#define HFRAG(n) lds_frag(smem, ard[(n) & 3] + ((n) >> 2) * 4096)
    const int head = cg * 4 + hp * 2;
    const u32 kvb = WS_KV + (u32)(head * KV_HEAD + pm * 2 * KV_TILE);
    f32x16 z16;
#pragma unroll
    for (int i = 0; i < 16; ++i) z16[i] = 0.f;
    BAR();
    STAMP(4);
    if (w < 4) {
      const float qs = kind == 0 ? 0.25f * 1.4426950408889634f : 1.f;
      const u32 so = kind == 0 ? WS_QF + (u32)(head * 64 + pm * 2) * 1024 + lo16 : kvb + (u32)((l & 51) | (l & 4) << 1 | (l & 8) >> 1) * 16;
      const u32 sh_ = kind == 0 ? 65536u : (u32)KV_HEAD, st_ = kind == 0 ? 1024u : (u32)KV_TILE;
      bf16x8 hf[64];
      f32x16 acc = z16, accp = z16;
      u32x4 ov[2];
#pragma unroll
      for (int i = 0; i < 4; ++i) hf[i] = HFRAG(i);
      SB();
      SFOR(n, 0, 76)
        if constexpr (n < 4) { hv[4 + n] = modulate8(nv[4 + n], c1, sh); KA_PUT(4 + n); }
        if constexpr (n == 5) { KA_FRAG(4); }
        if constexpr (n == 9) RAWBAR();
        if constexpr (n == 11) { KA_FRAG(5); }
        if constexpr (n == 15) { KA_FRAG(6); }
        if constexpr (n == 19) { KA_FRAG(7); }
        if constexpr (n == 32 || n == 64) { accp = acc; acc = z16; }
        if constexpr (n + 4 < 64) hf[n + 4] = HFRAG(n + 4);
        if constexpr (n < 64) acc = MFMA32(wq[n & 31], hf[n], acc);
        if constexpr (n >= 32 && (n & 31) >= 2 && (n & 31) < 10) {
          constexpr int e = (n & 31) - 2;
          ov[e >> 2][e & 3] = pack2(accp[2 * e] * qs, accp[2 * e + 1] * qs);
        }
        if constexpr (n == 42 || n == 74) GST_SC1("x4", ov[0], so, sws, st_ * ((n / 32 - 1) ^ tx), 0);
        if constexpr (n == 43 || n == 75) GST_SC1("x4", ov[1], so, sws, st_ * ((n / 32 - 1) ^ tx) + sh_, 0);
        if (n < 64) SB();
      SEND
    } else {
      bf16x8 hf[32];
      f32x16 acc = z16;
#pragma unroll
      for (int i = 4; i < 8; ++i) { hv[i] = modulate8(nv[i], c1, sh); KA_PUT(i); }
      WAITV(0);
      BAR();
#pragma unroll
      for (int i = 0; i < 4; ++i) hf[i] = HFRAG(i);
      SB();
      SFOR(n, 0, 32)
        if constexpr (n >= 2 && n < 18 && (n & 3) == 2) { KA_FRAG(4 + n / 4); }
        if constexpr (n + 4 < 32) hf[n + 4] = HFRAG(n + 4);
        acc = MFMA32(hf[n], wq[n], acc);
        SB();
      SEND
      const u32 kvt = kvb + (u32)((ta ^ tx) * KV_TILE);
      const u32 vo = kvt + (u32)((l >> 4 & 1) * KV_HEAD + 1024 + hb * 272 + (l & 15) * 16);
#pragma unroll
      for (int e = 0; e < 2; ++e) {
        const u32x4 ov = {pack2(acc[8 * e], acc[8 * e + 1]), pack2(acc[8 * e + 2], acc[8 * e + 3]), pack2(acc[8 * e + 4], acc[8 * e + 5]), pack2(acc[8 * e + 6], acc[8 * e + 7])};
        GST_SC1("x4", ov, vo, sws, zs, e * 544);
      }
      const u32x4 one = {0x3f803f80u, 0x3f803f80u, 0x3f803f80u, 0x3f803f80u};
      GST_SC1("x4", one, l < 8 ? kvt + (u32)((l >> 2) * KV_HEAD + 1024 + (l & 3) * 272 + 256) : 0x40000000u, sws, zs, 0);
    }
    STAMP(5);
    u32x4 tq[3];
    const u32 msb = WS_GRAN + (u32)(((w == 0 ? 1024 : (w == 1 ? 2048 : 2560)) + pn3 * 64) * 16);
#define TGATHER(tail)                                                                                                           \
    asm volatile("buffer_load_dwordx4 %0, %3, %5, %6 offen sc1\n\tbuffer_load_dwordx4 %1, %3, %5, %7 offen sc1\n\t"             \
                 "buffer_load_dwordx4 %2, %4, %5, %8 offen sc1" tail                                                            \
                 : "=&v"(tq[0]), "=&v"(tq[1]), "=&v"(tq[2])                                                                     \
                 : "v"((u32)t * 16), "v"(lo16), "s"(sws), "s"(WS_GRAN + 1536 * 16), "s"(WS_GRAN + 2048 * 16), "s"(msb) : "memory")
    TGATHER("");
    WAITV(3);
    STAMP(6);
    BAR();
    signal_cnt(ws, cg);
    WAITV(0);
#pragma unroll
    for (int i = 0; i < 3; ++i) PIN(tq[i]);
#pragma unroll 1
    for (u32 spins = 0; spins < (1u << 17); ++spins) {
      if (__all(gok(tq[0], tag) && gok(tq[1], tag) && gok(tq[2], tag))) break;
      __builtin_amdgcn_s_sleep(2);
      TGATHER("\n\ts_waitcnt vmcnt(0)");
    }
    *(LAS u16*)((LAS char*)smem + TAB + t * 2) = (u16)(__float_as_uint(bfround(1.f + bfround(gsum(tq[1])))) >> 16);
    *(LAS u16*)((LAS char*)smem + TAB + 1024 + t * 2) = (u16)(__float_as_uint(bfround(gsum(tq[0]))) >> 16);
    const float mv_ = bfround(gsum(tq[2]));
    ((LAS float*)((LAS char*)smem + MT))[t] = (w == 1) ? bfround(1.f + mv_) : mv_;
  }

  const int lq = loc >= 28 ? 27 : loc;
  const int pan4 = (xcd >> 1) * 4 + lq / 7, pn4 = (xcd & 1) * 7 + lq % 7;
  constexpr u32 STG = KV_HEAD;
  f32x16 zero;
#pragma unroll
  for (int i = 0; i < 16; ++i) zero[i] = 0.f;
  bf16x8 wf[32];

  {
    const char* wsrc = (const char*)((l & 2) ? wu : wg) + (size_t)(l >> 2) * 3584 + (pn4 * 128 + w * 16 + (l & 1) * 8) * 2;
    const u32 stg_lb = lb + w * 3072;
    u32 stg_rd0 = (u32)(w * 3072 + (hb * 8 + (l >> 2 & 3)) * 64 + (l >> 4 & 1) * 32 + (l & 3) * 8), stg_rd1 = stg_rd0 + STG;
    PIN(stg_rd1);
#define WREAD(p)                                                                                        \
    do {                                                                                                \
      if constexpr ((p) >= 5) {                                                                         \
        if ((p) <= 32) WAITV(4); else if ((p) == 33) WAITV(3); else if ((p) == 34) WAITV(2); else if ((p) == 35) WAITV(1); else WAITV(0); \
        const u32 o_ = ((((p) - 5) % 6) < 3 ? stg_rd1 : stg_rd0) + (((p) - 5) % 3) * 1024;              \
        wf[(p) - 5] = lds_tr2(smem, o_, o_ + 256);                                                      \
      }                                                                                                 \
    } while (0)
#define WLOAD(p) do { if ((p) < 32) { GLDS64(wsrc, stg_lb, (((p) % 6) < 3 ? STG : 0u) + ((p) % 3) * 1024u); wsrc += 16 * 3584; } } while (0)
    const int head = xcd * 4 + (loc >> 3), qt = (loc & 7) * 8 + w;
    const u32 ksb = (u32)(head * KV_HEAD + w * 1024), wlb = lb + w * 1024;
    const int r = l & 31;
    u32 krd = lo16;
    u32 vrd = (u32)(1024 + hb * 272 + (r < 16 ? r * 16 : (r == 16 ? 256 : 0x40000)));
    STAMP(7);
    wait_cnt(ws, xcd, 32, w);
    BAR();
    STAMP(8);
#define KVR(r) GLDS_SC1(lo16, sws, ksb, WS_KV + (r) * 8192, wlb, (r) * 8192)
    u32x4 qraw;
    GLD128_SC1(qraw, lo16, sws, (u32)(head * 64 + qt) * 1024, WS_QF);
    KVR(0); KVR(1);
    if (w < 4) KVR(16);
    KVR(2); KVR(3);
    WAITV(2);
    PIN(qraw);
    RAWBAR();
    STAMP(9);
    const bf16x8 qf = __builtin_bit_cast(bf16x8, qraw);
    float m;
    bf16x8 kf = lds_frag(smem, krd);
    {
      f32x16 s0 = MFMA32(kf, qf, zero);
      m = s0[0];
#pragma unroll
      for (int i = 1; i < 16; ++i) m = fmaxf(m, s0[i]);
      const auto a = __builtin_amdgcn_permlane32_swap(__float_as_uint(m), __float_as_uint(m), false, false);
      m = fmaxf(__uint_as_float(a[0]), __uint_as_float(a[1]));
    }
    f32x16 negm;
#pragma unroll
    for (int i = 0; i < 16; ++i) negm[i] = -m;
    f32x16 o = zero;
    f32x16 sc = MFMA32(kf, qf, negm);
    kf = lds_frag(smem, krd + KV_TILE);
    bf16x8 v0 = lds_frag(smem, vrd), v1;
    SFOR(j, 0, 64)
      if constexpr ((j & 1) == 0) __builtin_amdgcn_s_setprio(3 - (j >> 1 & 3));
      if (j == 5) { WAITV(10); RAWBAR(); }
      if (j == 13) { WAITV(0); RAWBAR(); }
      if (j == 14) STAMP(10);
      constexpr bool wst = j >= 14 && j < 63 && (j - 14) % 4 != 3;
      constexpr int wp = (j - 14) - (j - 14) / 4;
      if (j == 28 || j == 56) { krd += 28 * KV_TILE; vrd += 28 * KV_TILE; PIN(krd); PIN(vrd); }
      const u32 jo = (u32)(j % 28) * KV_TILE;
#define EXPS(a, b) _Pragma("unroll") for (int i = (a); i < (b); ++i) sc[i] = __builtin_amdgcn_exp2f(sc[i])
      f32x16 sn;
      v0 = lds_frag(smem, vrd + jo); v1 = lds_frag(smem, vrd + jo + 544);
      if (j < 63) sn = MFMA32(kf, qf, negm);
      if (j < 62) kf = lds_frag(smem, krd + jo + 2 * KV_TILE);
      SB();
      EXPS(0, E1);
      SB();
      o = SMF(v0, LO8(sc), o);
      if constexpr (wst) { WREAD(wp); WLOAD(wp); }
      if constexpr (j < 6) { KVR(4 + 2 * j); KVR(5 + 2 * j); }
      SB();
      EXPS(E1, 16);
      o = SMF(v1, HI8(sc), o);
      SB();
      if (j < 63) sc = sn;
    SEND
    float lsum = __uint_as_float(__builtin_amdgcn_permlane32_swap(__float_as_uint(o[8]), __float_as_uint(o[8]), false, false)[0]);
    bool ok = lsum < 1e18f;
#pragma unroll
    for (int i = 0; i < 8; ++i) ok = ok && (fabsf(o[i]) < 3e38f);
    if (!__all(ok)) {
      asm volatile("" ::: "memory");
      const __amdgpu_buffer_rsrc_t rk = __builtin_amdgcn_make_buffer_rsrc(ws + WS_KV + (size_t)head * KV_HEAD, 0, KV_HEAD, 0x20000);
      const int vg = 1024 + hb * 272 + (r == 16 ? 256 : (r & 15) * 16);
#define KVLD(off, j) __builtin_bit_cast(bf16x8, __builtin_amdgcn_raw_buffer_load_b128(rk, (off), (j) * KV_TILE, 16))
#pragma unroll 1
      for (int j = 0; j < 64; ++j) {
        const f32x16 sv = MFMA32(KVLD(lo16, j), qf, zero);
#pragma unroll
        for (int i = 0; i < 16; ++i) m = fmaxf(m, sv[i]);
      }
      m = fmaxf(m, __shfl_xor(m, 32));
#pragma unroll
      for (int i = 0; i < 16; ++i) negm[i] = -m;
      o = zero;
#pragma unroll 1
      for (int j = 0; j < 64; ++j) {
        f32x16 sv = MFMA32(KVLD(lo16, j), qf, negm);
        const bf16x8 v0 = KVLD(vg, j), v1 = KVLD(vg + 544, j);
#pragma unroll
        for (int i = 0; i < 16; ++i) sv[i] = __builtin_amdgcn_exp2f(sv[i]);
        o = SMF(v0, LO8(sv), o);
        o = SMF(v1, HI8(sv), o);
      }
      lsum = shfl_idx(o[8], r);
    }
    const float inv = 1.f / lsum;
    u32x4 ov;
#pragma unroll
    for (int e = 0; e < 4; ++e) ov[e] = pack2(o[2 * e] * inv, o[2 * e + 1] * inv);
    GST_SC1("x4", ov, lo16, sws, (u32)(qt * 32 + head) * 1024, WS_OF);
    STAMP(11);
    WAITV(0);
    BAR();
    signal_cnt(ws, 8 + (loc & 7));
    STAMP(12);
  }

  const int tm = w & 1;
  const u32 wvo = (u32)((l >> 3) * 1024 + ((((l >> 2) ^ (l >> 4)) & 1) * 32 + (l & 3) * 8) * 2);
  const u32 wrd0 = (u32)((hb * 4 + (l >> 2 & 3)) * 128 + (l >> 3 & 1) * 64 + (l >> 4 & 1) * 32 + (l & 3) * 8);
  const int rte = xcd * 8 + (pm3 >> 1) * 4 + (pm3 & 1) + (w >> 2) * 2;
  const int erow = w * 8 + (l & 7), ecol = l >> 3;
  f32x16 uv = zero;
  const u32 trd = TAB + (u32)((l >> 4 & 1) * 1024 + hb * 16);
#define UV_PART(k0)                                                                                          \
  do { bf16x8 tf_[16];                                                                                       \
    _Pragma("unroll") for (int i_ = 0; i_ < 4; ++i_) tf_[i_] = lds_frag(smem, trd + ((k0) + i_) * 32);       \
    _Pragma("unroll") for (int i_ = 0; i_ < 16; ++i_) {                                                      \
      if (i_ + 4 < 16) tf_[i_ + 4] = lds_frag(smem, trd + ((k0) + i_ + 4) * 32);                             \
      uv = MFMA32(wf[(k0) + i_], tf_[i_], uv); SB(); }                                                       \
    PIN(uv); } while (0)

  {
    const u32x4 so = make_srd(wo, 512 * 1024);
    const int ro = pn3 & 3;
    SFOR(j, 0, 8) GLDS(wvo, so, (u32)(w * 8192 + pn3 * 128 + ((j + 2 * ro) & 7) * 65536), 0, lb + w * 1024, j * 8192); SEND
    u32x4 xr;
    GLD128(xr, (u32)((erow & 31) * 1024 + ecol * 16), sx, (u32)(rte * 32768 + pn3 * 128), 0);
    UV_PART(0);
    WAITV(0);
    PIN(xr);
    STAMP(13);
    wait_cnt(ws, 8 + xcd, 32, w);
    RAWBAR();
    STAMP(14);
    if (bid == 0 && t == 0) __hip_atomic_store((u32*)(ws + WS_EPOCH), tag, __ATOMIC_RELAXED, __HIP_MEMORY_SCOPE_AGENT);
    const u32 olo = (u32)((w >> 2) * 32768 + (w & 3) * 2048);
    const u32 osb = (u32)(rte * 32768 + (w & 3) * 2048), olb = lb + olo;
    SFOR(j, 0, 8) GLDS_SC1(lo16, sws, osb + (u32)(((j >> 1) + ro) & 3) * 8192, WS_OF + (j & 1) * 1024, olb, 65536 + (j >> 1) * 8192 + (j & 1) * 1024); SEND
    const int tn = w >> 1 & 1, kh = w >> 2;
    u32 ord = 65536 + (u32)(tm * 32768 + kh * 4096) + lo16;
    PIN(ord);
    const u32 wrd = (wrd0 ^ (u32)(tn * 64)) + kh * 8192;
    f32x16 acc = zero;
    bf16x8 wq[16];
#define WOFRAG(n) wq[n] = lds_tr2(smem, wrd + ((n) >> 2) * 16384 + ((n) & 3) * 2048, wrd + ((n) >> 2) * 16384 + ((n) & 3) * 2048 + 1024)
#pragma unroll
    for (int n = 0; n < 8; ++n) WOFRAG(n);
    SFOR(s, 0, 4)
      if (s == 0) WAITV(6); else if (s == 1) WAITV(4); else if (s == 2) WAITV(2); else WAITV(0);
      RAWBAR();
      bf16x8 oq[4];
#pragma unroll
      for (int j = 0; j < 4; ++j) oq[j] = lds_frag(smem, ord + s * 8192 + j * 1024);
      if constexpr (s < 2) {
#pragma unroll
        for (int j = 0; j < 4; ++j) WOFRAG(8 + s * 4 + j);
      }
      SB();
#pragma unroll
      for (int j = 0; j < 4; ++j) acc = MFMA32(wq[s * 4 + j], oq[j], acc);
      SB();
    SEND
    STAMP(15);
    {
      LAS char* eb = (LAS char*)smem + kh * 17408 + (tm * 32 + (l & 31)) * 272 + (tn * 32 + hb * 4) * 4;
#pragma unroll
      for (int e = 0; e < 4; ++e) *(LAS f32x4*)(eb + e * 32) = (f32x4){acc[4 * e], acc[4 * e + 1], acc[4 * e + 2], acc[4 * e + 3]};
    }
    f32x4 ga[2], cb[2];
#pragma unroll
    for (int e = 0; e < 2; ++e) { ga[e] = *(const LAS f32x4*)((LAS char*)smem + MT + ecol * 32 + e * 16); cb[e] = *(const LAS f32x4*)((LAS char*)smem + MT + 256 + ecol * 32 + e * 16); }
    BAR();
    float xv[8];
    u32x4 x1p, yp;
#pragma unroll
    for (int e = 0; e < 2; ++e) {
      const LAS char* eb = (const LAS char*)smem + erow * 272 + ecol * 32 + e * 16;
      const f32x4 a = *(const LAS f32x4*)eb + *(const LAS f32x4*)(eb + 17408);
#pragma unroll
      for (int h = 0; h < 2; ++h) {
        const u32 pk = pack2(bf_lo(xr[2 * e + h]) + ga[e][2 * h] * a[2 * h], bf_hi(xr[2 * e + h]) + ga[e][2 * h + 1] * a[2 * h + 1]);
        x1p[2 * e + h] = pk;
        xv[4 * e + 2 * h] = bf_lo(pk); xv[4 * e + 2 * h + 1] = bf_hi(pk);
        yp[2 * e + h] = pack2(bf_lo(pk) * cb[e][2 * h], bf_hi(pk) * cb[e][2 * h + 1]);
      }
    }
    GST_SC1("x4", yp, (u32)((ecol >> 1) * 1024 + ((ecol & 1) * 32 + (erow & 31)) * 16), sws, (u32)(rte * 32 + pn3 * 4) * 1024, WS_Y);
    float sum = ((xv[0] + xv[1]) + (xv[2] + xv[3])) + ((xv[4] + xv[5]) + (xv[6] + xv[7]));
    const float mean = col8_sum(sum) * (1.f / 64.f);
    float sq = 0.f;
#pragma unroll
    for (int i = 0; i < 8; ++i) { const float d = xv[i] - mean; sq = __builtin_fmaf(d, d, sq); }
    const f32x2 o2 = {mean, col8_sum(sq)};
    GST_SC1("x2", o2, l < 8 ? (u32)((erow & 31) * 8) : 0x40000000u, sws, (u32)(rte * 2048 + pn3 * 256), WS_ST2);
    STAMP(16);
    WAITV(0);
    RAWBAR();
    signal_cnt(ws, 16 + xcd * 2 + (pm3 >> 1));
    STAMP(17);
    GST_SC1("x4", x1p, (u32)(erow * 8 + ecol) * 16, sws, (u32)bid * 8192, WS_X1);
  }

  const u32x4 sd = make_srd(wd, 1792 * 1024);
  const u32 dsb = (u32)(w * 16384 + pn3 * 128), dlb = lb + w * 2048;
  const int r5 = (pn3 * 7) >> 2;
#define CH5(c) (u32)((c) + r5 >= 14 ? (c) + r5 - 14 : (c) + r5)
#define WDPIECE(c, slot, h) GLDS(wvo, sd, dsb + CH5(c) * 131072, (h) * 8192, dlb, (slot) * 16384 + (h) * 1024)
#define WDISSUE(c, slot) do { WDPIECE(c, slot, 0); WDPIECE(c, slot, 1); } while (0)
  const int kq = w & 3;
  const u32 wr5 = wrd0 + kq * 4096;
  bf16x8 dq[14][4];
#define DFRAG1(c, i) dq[c][i] = lds_tr2(smem, (wr5 ^ (((i) & 1) * 64u)) + (c) * 16384 + ((i) >> 1) * 2048, (wr5 ^ (((i) & 1) * 64u)) + (c) * 16384 + ((i) >> 1) * 2048 + 1024)
  if (loc < 28) {
    constexpr u32 RST4 = 157696;
    UV_PART(16);
    float uu[16], vv[16];
#pragma unroll
    for (int i = 0; i < 16; ++i) {
      const auto r = __builtin_amdgcn_permlane16_swap(__float_as_uint(uv[i]), __float_as_uint(uv[i]), false, false);
      uu[i] = __uint_as_float(r[0]); vv[i] = __uint_as_float(r[1]);
      PIN(uu[i]); PIN(vv[i]);
    }
    wait_cnt(ws, 16 + pan4, 16, w);
    RAWBAR();
    STAMP(18);
    const int rt4 = pan4 * 4;
    const u32 ysb = (u32)(rt4 * 32768 + w * 1024), ylb = lb + w * 1024;
#define YPIECE(i) GLDS_SC1(lo16, sws, ysb, WS_Y + (i) * 8192, ylb, (i) * 8192)
    YPIECE(0); YPIECE(1); YPIECE(2); YPIECE(3);
    GLDS_SC1(lo16, sws, (u32)(rt4 * 2048 + w * 1024), WS_ST2, ylb, 131072);
    YPIECE(4); YPIECE(5);
    u32 yrd0 = lo16, yrd1 = lo16 + 65536;
    PIN(yrd1);
    const u32 asb = (u32)(rt4 * 112 + pn4 * 8 + w) * 1024;
#define YFRAG(n) lds_frag(smem, ((n) < 64 ? yrd0 : yrd1) + ((n) & 63) * 1024)
    WAITV(5);
    RAWBAR();
    STAMP(19);
    bf16x8 yf[128];
#pragma unroll
    for (int n = 0; n < 4; ++n) yf[n] = YFRAG(n);
    SB();
#define signal_early()                                                                                               \
    do { unsigned long long ex_;                                                                                     \
      asm volatile("s_cmp_eq_u32 %5, 0\n\ts_cbranch_scc0 1f\n\ts_mov_b64 %0, exec\n\ts_mov_b64 exec, 1\n\t"          \
                   "buffer_atomic_add %1, %2, %3, %4 offen\n\ts_mov_b64 exec, %0\n1:"                                \
                   : "=&s"(ex_) : "v"(1u), "v"(0u), "s"(sws), "s"(WS_CNT + (u32)(48 + pan4) * 256), "s"(w) : "memory", "scc"); } while (0)
    f32x16 acc = zero, accp = zero;
    f32x2 stp = {0.f, 0.f};
    float eg[8], eu[8], et[8];
    u32x4 ov;
    SFOR(n, 0, 160)
      constexpr int a = n >> 5, ks = n & 31;
      if (n == 10) { WAITV(7); RAWBAR(); }
      if (n == 26) { WAITV(8); BAR(); }
      if (n == 56) { WAITV(4); RAWBAR(); }
      if (n == 100) WAITV(3);
      if (n == 121) { WAITV(4); RAWBAR(); signal_early(); }
      if constexpr (n < 20 && n % 2 == 0) YPIECE(6 + n / 2);
      if (n == 36 || n == 68 || n == 100) RAWBAR();
      if constexpr (n == 60) {
        const int rr = pm3 * 224 + w * 28 + (l < 28 ? l : 27);
        const int r2 = rr + (rr < 256 ? 768 : 896) + r5 * 128;
        GTOUCH((u32)(r2 >= 1792 ? r2 - 1792 : r2) * 1024, sd, (u32)(pn3 * 128), 0, lb + SCR);
      }
      if constexpr (n >= 36 && n < 52 && n % 4 == 0) WDPIECE((n - 36) / 8, (n - 36) / 8, (n - 36) / 4 & 1);
      if constexpr (n >= 68 && n < 84 && n % 4 == 0) WDPIECE(2 + (n - 68) / 8, 2 + (n - 68) / 8, (n - 68) / 4 & 1);
      if constexpr (n == 84 || n == 88) WDPIECE(8, 8, (n - 84) / 4);
      if constexpr (n >= 100 && n < 116 && n % 4 == 0) WDPIECE(4 + (n - 100) / 8, 4 + (n - 100) / 8, (n - 100) / 4 & 1);
      if constexpr (n >= 100 && n < 116) DFRAG1((n - 100) >> 2, (n - 100) & 3);
      if constexpr (n >= 121 && n < 129) WDPIECE(10 + (n - 121) / 2, (n - 121) / 2, n & 1);
      if constexpr (n == 12) {
        const LAS f32x2* sp = (const LAS f32x2*)((LAS char*)smem + 131072 + (t >> 7) * 2048 + (t & 3) * 512 + (t >> 2 & 31) * 8);
        const f32x2 p0 = sp[0], p1 = sp[32];
        float mu = p0[0] + p1[0];
        mu += dpp_xor1(mu); mu += dpp_xor2(mu);
        mu *= 0.125f;
        float q = p0[1] + p1[1] + 64.f * ((p0[0] - mu) * (p0[0] - mu) + (p1[0] - mu) * (p1[0] - mu));
        q += dpp_xor1(q); q += dpp_xor2(q);
        const float rs = __builtin_amdgcn_rsqf(q * (1.f / 512.f) + 1e-5f);
        ((LAS f32x2*)((LAS char*)smem + RST4))[t >> 2] = (f32x2){rs, -mu * rs};
      }
      if (ks == 28 && a < 4) stp = ((const LAS f32x2*)((LAS char*)smem + RST4))[a * 32 + (l & 31)];
      if (ks == 0 && a > 0) { accp = acc; acc = zero; }
      if (n == 128) STAMP(20);
      if constexpr (n + 4 < 128) yf[n + 4] = YFRAG(n + 4);
      if constexpr (n < 128) acc = MFMA32(wf[ks], yf[n], acc);
      if constexpr (a > 0 && ks >= 2 && ks < 26) {
        constexpr int i = (ks - 2) / 3, ph = (ks - 2) % 3;
        if (ph == 0) {
          eg[i] = __builtin_fmaf(accp[i], stp[0], __builtin_fmaf(uu[i], stp[1], vv[i]));
          eu[i] = __builtin_fmaf(accp[8 + i], stp[0], __builtin_fmaf(uu[8 + i], stp[1], vv[8 + i]));
          et[i] = -1.4426950408889634f * eg[i];
          if (a < 4) { PIN(eg[i]); PIN(eu[i]); PIN(et[i]); }
        } else if (ph == 1) {
          et[i] = 1.f + __builtin_amdgcn_exp2f(et[i]);
          eg[i] *= eu[i];
          if (a < 4) { PIN(eg[i]); PIN(et[i]); }
        } else {
          eg[i] *= __builtin_amdgcn_rcpf(et[i]);
          if (a < 4) PIN(eg[i]);
          if constexpr (i & 1) ov[i >> 1] = pack2(eg[i - 1], eg[i]);
        }
      }
      if constexpr (a > 0 && ks == 26) GST_SC1("x4", ov, lo16, sws, asb, WS_ACT + (a - 1) * 112 * 1024);
      if (n < 128) SB();
    SEND
    STAMP(21);
  } else {
    WDISSUE(0, 0); WDISSUE(1, 1); WDISSUE(2, 2); WDISSUE(3, 3); WDISSUE(8, 8); WDISSUE(4, 4); WDISSUE(5, 5);
    WAITV(0);
    RAWBAR();
    SFOR(n, 0, 16) DFRAG1(n >> 2, n & 3); SEND
    BAR();
    WDISSUE(10, 0); WDISSUE(11, 1); WDISSUE(12, 2); WDISSUE(13, 3);
  }

  {
    u32 wra[3][2];
#pragma unroll
    for (int i = 0; i < 3; ++i) { wra[i][0] = wr5 + i * 65536; wra[i][1] = wra[i][0] ^ 64u; PIN(wra[i][0]); PIN(wra[i][1]); }
#define DFRAG(c) _Pragma("unroll") for (int i_ = 0; i_ < 4; ++i_) {                                              \
      const u32 o_ = wra[((c) % 10) >> 2][i_ & 1] + (((c) % 10) & 3) * 16384 + (i_ >> 1) * 2048; dq[c][i_] = lds_tr2(smem, o_, o_ + 1024); }
    f32x4 gb[2];
#pragma unroll
    for (int e = 0; e < 2; ++e) gb[e] = *(const LAS f32x4*)((LAS char*)smem + MT + 512 + (l & 7) * 32 + e * 16);
    const int pan5 = xcd * 2 + (pm3 >> 1);
    wait_cnt(ws, 48 + pan5, 14, w);
    WAITV(0);
    BAR();
    if (loc < 28) signal_cnt(ws, 32 + pan4);
    STAMP(22);
#define WD2(c, slot) do { WDISSUE(c, slot); WDPIECE(c, slot, -8); WDPIECE(c, slot, -7); } while (0)
#define BAR_IF(lo) asm volatile("s_cmp_lt_u32 %0, 4\n\ts_cbranch_scc" lo " 1f\n\ts_barrier\n1:" :: "s"(w) : "memory", "scc")
    if (w >= 4) {
      WD2(6, 6); WD2(7, 7); WD2(9, 9);
      WAITV(0);
      RAWBAR();
      wait_cnt(ws, 32 + pan5, 14, 0);
    }
    STAMP(23);
    const u32 asb = (u32)(rte * 112 + kq * 2) * 1024;
    u32x4 af[14][2];
#define AISSUE(c) do { GLD128_SC1(af[c][0], lo16, sws, asb + CH5(c) * 8192, WS_ACT); GLD128_SC1(af[c][1], lo16, sws, asb + CH5(c) * 8192, WS_ACT + 1024); } while (0)
    SFOR(c, 0, AWIN) AISSUE(c); SEND
    f32x16 acc[2] = {zero, zero};
    u32x4 x1p;
    SFOR(c, 0, 14)
      constexpr int young = c + AWIN <= 14 ? 2 * (AWIN - 1) : (c < 13 ? 2 * (13 - c) + 1 : 0);
      asm volatile("s_waitcnt vmcnt(%0)" :: "i"(young) : "memory");
      if (c == 0) STAMP(24);
      if (c == 5) STAMP(37);
      if (c == 10) STAMP(38);
      if (c == 2) BAR_IF("0");
      if (c == 10) BAR_IF("1");
      if constexpr (c + AWIN < 14) AISSUE(c + AWIN);
      if (c + AWIN == 14) GLD128_SC1(x1p, (u32)t * 16, sws, (u32)bid * 8192, WS_X1);
      if constexpr (c >= 3 && c < 13) { DFRAG(c + 1); }
      PIN(af[c][0]); PIN(af[c][1]);
      SB();
#pragma unroll
      for (int i = 0; i < 4; ++i) acc[i & 1] = MFMA32(dq[c][i], __builtin_bit_cast(bf16x8, af[c][i >> 1]), acc[i & 1]);
      SB();
    SEND
    STAMP(25);
    BAR_IF("0");
    {
      LAS char* eb = (LAS char*)smem + 65536 + kq * 17408 + ((w >> 2) * 32 + (l & 31)) * 272 + hb * 16;
#pragma unroll
      for (int tn = 0; tn < 2; ++tn)
#pragma unroll
        for (int e = 0; e < 4; ++e)
          *(LAS f32x4*)(eb + tn * 128 + e * 32) = (f32x4){acc[tn][4 * e], acc[tn][4 * e + 1], acc[tn][4 * e + 2], acc[tn][4 * e + 3]};
    }
    STAMP(40);
    BAR();
    STAMP(29);
    PIN(x1p);
    u32x4 ov;
#pragma unroll
    for (int e = 0; e < 2; ++e) {
      const LAS char* eb = (const LAS char*)smem + 65536 + (t >> 3) * 272 + (l & 7) * 32 + e * 16;
      const f32x4 s = (*(const LAS f32x4*)eb + *(const LAS f32x4*)(eb + 17408)) + (*(const LAS f32x4*)(eb + 2 * 17408) + *(const LAS f32x4*)(eb + 3 * 17408));
      ov[2 * e] = pack2(bf_lo(x1p[2 * e]) + gb[e][0] * s[0], bf_hi(x1p[2 * e]) + gb[e][1] * s[1]);
      ov[2 * e + 1] = pack2(bf_lo(x1p[2 * e + 1]) + gb[e][2] * s[2], bf_hi(x1p[2 * e + 1]) + gb[e][3] * s[3]);
    }
    *(u32x4*)((char*)out + (size_t)(rte * 32 + (t >> 3 & 31)) * 1024 + pn3 * 128 + (l & 7) * 16) = ov;
  }
  ST_OUT(2, ws);
  asm volatile(".Lcode_end:");
}

extern "C" void kernel_launch(void* const* d_in, const int* in_sizes, int n_in, void* d_out, int out_size, void* d_ws, size_t ws_size,
                              hipStream_t stream) {
  adaln_block<<<256, 512, 0, stream>>>((const u16*)d_in[0], (const u16*)d_in[1], (const u16*)d_in[2], (const u16*)d_in[4], (const u16*)d_in[5],
                                       (const u16*)d_in[6], (const u16*)d_in[7], (const u16*)d_in[8], (char*)d_ws, (u16*)d_out);
}
```

```cpp
#define DIAG_AT 12
#include <hip/hip_runtime.h>
#include <hip/hip_bf16.h>

typedef unsigned short u16;
typedef unsigned int u32;
typedef u32 u32x2 __attribute__((ext_vector_type(2)));
typedef u32 u32x4 __attribute__((ext_vector_type(4)));
typedef float f32x2 __attribute__((ext_vector_type(2)));
typedef float f32x4 __attribute__((ext_vector_type(4)));
typedef float f32x8 __attribute__((ext_vector_type(8)));
typedef float f32x16 __attribute__((ext_vector_type(16)));
typedef __bf16 bf16x2 __attribute__((ext_vector_type(2)));
typedef __bf16 bf16x8 __attribute__((ext_vector_type(8)));
typedef __bf16 bf16x16 __attribute__((ext_vector_type(16)));
typedef short s16x4 __attribute__((ext_vector_type(4)));
typedef short s16x8 __attribute__((ext_vector_type(8)));
#define PRIO_MODE 1
#define GATHER_AT 5
#define AORD 0
#define E1 9
#define AWIN 8
#define OUT_WT 0
#define CODE_PF 8
#define LAS __attribute__((address_space(3)))
#define DI __device__ __forceinline__

constexpr u32 WS_GRAN = 0;
constexpr u32 WS_EPOCH = 0x18000;
constexpr u32 WS_CNT  = 0x20000;
constexpr u32 WS_QF   = 0x100000;
constexpr u32 WS_KV   = 0x300000;
constexpr u32 WS_OF   = 0x800000;
constexpr u32 WS_X1   = 0xA00000;
constexpr u32 WS_Y    = 0xC00000;
constexpr u32 WS_ST2  = 0xE00000;
constexpr u32 WS_ACT  = 0xF00000;
constexpr u32 WS_BYTES = 0x1600000;
constexpr int KV_TILE = 2112, KV_HEAD = 64 * KV_TILE;

template <int I> struct IC { static constexpr int value = I; };
template <int B, int E, class F> DI void static_for(F&& f) { if constexpr (B < E) { f(IC<B>{}); static_for<B + 1, E>(f); } }
#define SFOR(n, B, E) static_for<B, E>([&](auto ic_) __attribute__((always_inline)) { constexpr int n = decltype(ic_)::value;
#define SEND });

DI float bf_lo(u32 u) { return __uint_as_float(u << 16); }
DI float bf_hi(u32 u) { return __uint_as_float(u & 0xffff0000u); }
DI u32 pack2(float a, float b) { bf16x2 r = {(__bf16)a, (__bf16)b}; return __builtin_bit_cast(u32, r); }
DI float bfround(float a) { return (float)(__bf16)a; }
DI float ln_rstd(float var) { return __builtin_amdgcn_rcpf(bfround(__builtin_amdgcn_sqrtf(bfround(bfround(var) + 1e-5f)))); }
DI float xor32_sum(float v) {
  const auto r = __builtin_amdgcn_permlane32_swap(__float_as_uint(v), __float_as_uint(v), false, false);
  return __uint_as_float(r[0]) + __uint_as_float(r[1]);
}
DI float dpp_xor1(float v) { return __int_as_float(__builtin_amdgcn_mov_dpp(__float_as_int(v), 0xB1, 0xF, 0xF, true)); }
DI float dpp_xor2(float v) { return __int_as_float(__builtin_amdgcn_mov_dpp(__float_as_int(v), 0x4E, 0xF, 0xF, true)); }
DI float shfl_idx(float v, int srcLane) { return __int_as_float(__builtin_amdgcn_ds_bpermute(srcLane << 2, __float_as_int(v))); }

DI u32x4 make_srd(const void* p, u32 bytes) {
  const size_t a = (size_t)p;
  u32x4 r = {(u32)a, (u32)(a >> 32) & 0xffffu, bytes, 0x20000u};
  return r;
}
#define GLDS(voff, srd, sb, GO, lb, LO)                                                                                   \
  do { u32 tmp_;                                                                                                          \
    asm volatile("s_add_u32 m0, %4, %6\n\ts_add_u32 %0, %3, %5\n\tbuffer_load_dwordx4 %1, %2, %0 offen lds"            \
                 : "=&s"(tmp_) : "v"(voff), "s"(srd), "s"(sb), "s"(lb), "i"(GO), "i"(LO) : "memory", "scc"); } while (0)
#define GLD(W, dst, voff, srd, sb, GO)                                                                                    \
  do { u32 tmp_;                                                                                                          \
    asm volatile("s_add_u32 %1, %4, %5\n\tbuffer_load_dword" W " %0, %2, %3, %1 offen"                                   \
                 : "=v"(dst), "=&s"(tmp_) : "v"(voff), "s"(srd), "s"(sb), "i"(GO) : "memory", "scc"); } while (0)
#define GLD128(dst, voff, srd, sb, GO) GLD("x4", dst, voff, srd, sb, GO)
#define GLD64(dst, voff, srd, sb, GO) GLD("x2", dst, voff, srd, sb, GO)
#define GLD32(dst, voff, srd, sb, GO) GLD("", dst, voff, srd, sb, GO)
#define WAITV(N) asm volatile("s_waitcnt vmcnt(" #N ")" ::: "memory")
#define PIN(x) asm volatile("" : "+v"(x))
#define BAR() do { asm volatile("s_waitcnt lgkmcnt(0)" ::: "memory"); __builtin_amdgcn_s_barrier(); asm volatile("" ::: "memory"); } while (0)

#define ST_DECL const unsigned long long dg0_ = __builtin_amdgcn_s_memrealtime();
#define STAMP(i) do { if ((i) == DIAG_AT) { while (__builtin_amdgcn_s_memrealtime() - dg0_ < 6000) __builtin_amdgcn_s_sleep(1); } } while (0)
#define ST_OUT(kid, wsp) STAMP(99)

DI bf16x8 lds_frag(const char* smem, u32 off) { return *(const LAS bf16x8*)((const LAS char*)smem + off); }
DI bf16x8 lds_tr2(const char* smem, u32 off0, u32 off1) {
  s16x4 a = __builtin_amdgcn_ds_read_tr16_b64_v4i16((LAS s16x4*)((LAS char*)smem + off0));
  s16x4 b = __builtin_amdgcn_ds_read_tr16_b64_v4i16((LAS s16x4*)((LAS char*)smem + off1));
  s16x8 r = __builtin_shufflevector(a, b, 0, 1, 2, 3, 4, 5, 6, 7);
  return __builtin_bit_cast(bf16x8, r);
}
DI u32 lds_base(const char* smem) { return (u32)(size_t)(const LAS char*)smem; }
#define SB() __builtin_amdgcn_sched_barrier(0)
#define RAWBAR() do { asm volatile("" ::: "memory"); __builtin_amdgcn_s_barrier(); asm volatile("" ::: "memory"); } while (0)
#define MFMA32(a, b, c) __builtin_amdgcn_mfma_f32_32x32x16_bf16((a), (b), (c), 0, 0, 0)
#define MFMA16(a, b, c) __builtin_amdgcn_mfma_f32_16x16x32_bf16((a), (b), (c), 0, 0, 0)

DI u32x4 modulate8(const f32x8 n, const float* c1, const float* sh) {
  u32x4 o;
#pragma unroll
  for (int e = 0; e < 4; ++e) o[e] = pack2(__builtin_fmaf(n[2 * e], c1[2 * e], sh[2 * e]), __builtin_fmaf(n[2 * e + 1], c1[2 * e + 1], sh[2 * e + 1]));
  return o;
}

#define GLDS_SC1(voff, srd, sb, GO, lb, LO)                                                                               \
  do { u32 tmp_;                                                                                                          \
    asm volatile("s_add_u32 m0, %4, %6\n\ts_add_u32 %0, %3, %5\n\tbuffer_load_dwordx4 %1, %2, %0 offen sc1 lds"         \
                 : "=&s"(tmp_) : "v"(voff), "s"(srd), "s"(sb), "s"(lb), "i"(GO), "i"(LO) : "memory", "scc"); } while (0)
#define GLD128_SC1(dst, voff, srd, sb, GO)                                                                                \
  do { u32 tmp_;                                                                                                          \
    asm volatile("s_add_u32 %1, %4, %5\n\tbuffer_load_dwordx4 %0, %2, %3, %1 offen sc1"                                 \
                 : "=v"(dst), "=&s"(tmp_) : "v"(voff), "s"(srd), "s"(sb), "i"(GO) : "memory", "scc"); } while (0)
#define GST_SC1(W, val, voff, srd, sb, GO)                                                                                \
  do { u32 tmp_;                                                                                                          \
    asm volatile("s_add_u32 %0, %4, %5\n\tbuffer_store_dword" W " %1, %2, %3, %0 offen sc1\n\ts_nop 1"                  \
                 : "=&s"(tmp_) : "v"(val), "v"(voff), "s"(srd), "s"(sb), "i"(GO) : "memory", "scc"); } while (0)
#define GTOUCH(voff, srd, sb, GO, lb)                                                                                     \
  do { u32 tmp_;                                                                                                          \
    asm volatile("s_mov_b32 m0, %4\n\ts_add_u32 %0, %3, %5\n\tbuffer_load_dword %1, %2, %0 offen lds"                   \
                 : "=&s"(tmp_) : "v"(voff), "s"(srd), "s"(sb), "s"(lb), "i"(GO) : "memory", "scc"); } while (0)
#define GLDS64(vaddr, lb, LO)                                                                                             \
    asm volatile("s_add_u32 m0, %1, %2\n\ts_nop 0\n\tglobal_load_lds_dwordx4 %0, off" :: "v"(vaddr), "s"(lb), "i"(LO) : "memory", "scc")

DI void wave_sum8(const float* v, float* z) {
  float u[4];
#pragma unroll
  for (int i = 0; i < 4; ++i) {
    const auto a = __builtin_amdgcn_permlane32_swap(__float_as_uint(v[i]), __float_as_uint(v[i + 4]), false, false);
    u[i] = __uint_as_float(a[0]) + __uint_as_float(a[1]);
  }
#pragma unroll
  for (int i = 0; i < 2; ++i) {
    const auto a = __builtin_amdgcn_permlane16_swap(__float_as_uint(u[i]), __float_as_uint(u[i + 2]), false, false);
    float s = __uint_as_float(a[0]) + __uint_as_float(a[1]);
    s += dpp_xor1(s); s += dpp_xor2(s);
    s += __int_as_float(__builtin_amdgcn_mov_dpp(__float_as_int(s), 0x141, 0xF, 0xF, true));
    s += __int_as_float(__builtin_amdgcn_mov_dpp(__float_as_int(s), 0x140, 0xF, 0xF, true));
    z[i] = s;
  }
}
#define ROW_OF(z, r) __int_as_float(__builtin_amdgcn_readlane(__float_as_int(z[(r) & 1]), ((r) >> 1) * 16))
DI float col8_sum(float v) {
  v += __int_as_float(__builtin_amdgcn_mov_dpp(__float_as_int(v), 0x128, 0xF, 0xF, true));
  const auto a = __builtin_amdgcn_permlane16_swap(__float_as_uint(v), __float_as_uint(v), false, false);
  return xor32_sum(__uint_as_float(a[0]) + __uint_as_float(a[1]));
}

DI void signal_cnt(char* ws, int idx) {
  if (threadIdx.x == 0) __hip_atomic_fetch_add((u32*)(ws + WS_CNT) + idx * 64, 1u, __ATOMIC_RELAXED, __HIP_MEMORY_SCOPE_AGENT);
}
DI void wait_cnt(char* ws, int idx, u32 want, int w) {
  if (w == 0) {
    u32* c = (u32*)(ws + WS_CNT) + idx * 64;
#pragma unroll 1
    for (u32 spins = 0; spins < (1u << 17); ++spins) {
      if ((u32)__builtin_amdgcn_readfirstlane(__hip_atomic_load(c, __ATOMIC_RELAXED, __HIP_MEMORY_SCOPE_AGENT)) >= want) break;
      __builtin_amdgcn_s_sleep(1);
    }
    asm volatile("" ::: "memory");
  }
}
DI float gsum(const u32x4 a) { return __uint_as_float(a[0]) + __uint_as_float(a[2]); }
DI bool gok(const u32x4 a, u32 tag) { return a[1] == tag && a[3] == tag; }
#define SMF(v, p, o) __builtin_amdgcn_smfmac_f32_32x32x32_bf16((v), __builtin_bit_cast(bf16x16, (p)), (o), 0xDDDD, 0, 0)
#define LO8(s) __builtin_shufflevector(s, s, 0, 1, 2, 3, 4, 5, 6, 7)
#define HI8(s) __builtin_shufflevector(s, s, 8, 9, 10, 11, 12, 13, 14, 15)

extern "C" __global__ __launch_bounds__(512) void adaln_block(const u16* __restrict__ x, const u16* __restrict__ wqkv, const u16* __restrict__ wo,
                                                              const u16* __restrict__ wg, const u16* __restrict__ wu, const u16* __restrict__ wd,
                                                              const u16* __restrict__ c, const u16* __restrict__ wmod, char* __restrict__ ws,
                                                              u16* __restrict__ out) {
  __shared__ __attribute__((aligned(16))) char smem[163840];
  ST_DECL STAMP(0);
  const int bid = blockIdx.x, xcd = bid & 7, loc = bid >> 3;
  const int t = threadIdx.x, l = t & 63;
  const int w = __builtin_amdgcn_readfirstlane(t >> 6);
  const u32 lb = lds_base(smem);
  {
    unsigned long long pc;
    u32 len;
    asm volatile("s_getpc_b64 %0\n.Lcode_mark:\n\ts_mov_b32 %1, .Lcode_end-.Lcode_mark" : "=s"(pc), "=s"(len));
    const u32x4 scode = make_srd((const void*)pc, loc < CODE_PF ? len : 0u);
    GTOUCH((u32)(loc * 512 + t) * 128 / CODE_PF, scode, 0u, 0, lb + 57344);
    SB();
  }
  const u32x4 sws = make_srd(ws, WS_BYTES), sx = make_srd(x, 2048 * 1024);
  const u32 lo16 = (u32)l * 16, zs = 0;
  const int hb = l >> 5;
  const int pm3 = loc >> 3, pn3 = loc & 7;
  constexpr u32 TAB = 159744, MT = TAB + 2048, SCR = TAB;
  u32 tag;
  asm volatile("" :: "s"(x), "s"(wqkv), "s"(wo), "s"(wg), "s"(wu), "s"(wd), "s"(c), "s"(wmod), "s"(ws), "s"(out));

  {
    constexpr u32 WSLOT = 24576, RED = 49152, MTAB = 53248;
    const int pm = (xcd >> 1) * 8 + (loc >> 2), cg = (xcd & 1) * 4 + (loc & 3);
    const u32x4 sw = make_srd(wqkv, 512 * 3072), sm = make_srd(wmod, bid < 96 ? 512 * 6144 : 0), sc = make_srd(c, bid < 96 ? 1024 : 0);
    const int mcb = bid >> 1, mkq = bid & 1;
    u32 ev;
    u32x2 cv;
    u32x4 mv[4], xa[8];
    if (bid == 0 && w == 7) GST_SC1("", zs, (u32)l * 256, sws, zs, WS_CNT);
    asm volatile("buffer_load_dword %0, %1, %2, %3 offen sc1" : "=v"(ev) : "v"(zs), "s"(sws), "s"(WS_EPOCH) : "memory");
#pragma unroll
    for (int i = 0; i < 4; ++i) GLD128(mv[i], (u32)((t >> 3) * 4 * 6144 + (t & 7) * 16), sm, (u32)(mkq * 256 * 6144 + mcb * 128), i * 6144);
    GLD64(cv, (u32)((t >> 3) * 8), sc, (u32)(mkq * 512), 0);
    RAWBAR();
    const int rx = loc & 3, rw = loc >> 2;
    const u32 xsb = (u32)(pm * 64 + w * 2) * 1024;
#define XROW(i) GLD128(xa[i], lo16, sx, xsb + (u32)(((i) >> 1) ^ rx) * 16384, ((i) & 1) * 1024)
    const u32 wvo = (u32)((l >> 3) * 3072 + ((((l >> 2) ^ (l >> 4)) & 1) * 32 + (l & 3) * 8) * 2);
    const u32 wsb = (u32)(w * 8 * 3072 + cg * 128), wlb = lb + w * 1024;
#define KA_SLOT(kc) (((kc) >> 1 == 1 ? 0 : ((kc) >> 1 == 2 ? 65536 + 2 * WSLOT : 65536)) + ((kc) & 1) * WSLOT)
#define KA_PIECE(n) GLDS(wvo, sw, wsb + (u32)(((n) / 3 + rw) & 7) * (64 * 3072), ((n) % 3) * 1024, wlb, KA_SLOT((n) / 3) + ((n) % 3) * 8192)
    STAMP(26);
    XROW(0); XROW(1); XROW(2);
    STAMP(27);
    WAITV(3);
    STAMP(28);
    PIN(ev); PIN(cv);
#pragma unroll
    for (int i = 0; i < 4; ++i) PIN(mv[i]);
    tag = (u32)__builtin_amdgcn_readfirstlane(ev) + 1u;
    XROW(3); XROW(4); XROW(5);
    if (bid < 96) {
      float acc[8];
#pragma unroll
      for (int e = 0; e < 8; ++e) acc[e] = 0.f;
#pragma unroll
      for (int i = 0; i < 4; ++i) {
        const float cc = (i & 1) ? bf_hi(cv[i >> 1]) : bf_lo(cv[i >> 1]);
        const float sv = bfround(cc * __builtin_amdgcn_rcpf(1.f + __builtin_amdgcn_exp2f(-1.4426950408889634f * cc)));
#pragma unroll
        for (int e = 0; e < 4; ++e) { acc[2 * e] += sv * bf_lo(mv[i][e]); acc[2 * e + 1] += sv * bf_hi(mv[i][e]); }
      }
      float u[4], z[2];
#pragma unroll
      for (int i = 0; i < 4; ++i) {
        const auto a = __builtin_amdgcn_permlane32_swap(__float_as_uint(acc[i]), __float_as_uint(acc[i + 4]), false, false);
        u[i] = __uint_as_float(a[0]) + __uint_as_float(a[1]);
      }
#pragma unroll
      for (int i = 0; i < 2; ++i) {
        const auto a = __builtin_amdgcn_permlane16_swap(__float_as_uint(u[i]), __float_as_uint(u[i + 2]), false, false);
        z[i] = __uint_as_float(a[0]) + __uint_as_float(a[1]);
        z[i] += __int_as_float(__builtin_amdgcn_mov_dpp(__float_as_int(z[i]), 0x128, 0xF, 0xF, true));
      }
      *(LAS f32x2*)((LAS char*)smem + RED + w * 256 + (l & 7) * 32 + (l >> 4) * 8) = (f32x2){z[0], z[1]};
      BAR();
      if (w == 7) {
        const LAS float* rp = (const LAS float*)((LAS char*)smem + RED) + l;
        const float v = ((rp[0] + rp[64]) + (rp[128] + rp[192])) + ((rp[256] + rp[320]) + (rp[384] + rp[448]));
        const u32x2 gr = {__float_as_uint(v), tag};
        GST_SC1("x2", gr, lo16, sws, (u32)(mcb * 1024 + mkq * 8), WS_GRAN);
      }
    }
    STAMP(1);
    XROW(6); XROW(7);
    SFOR(n, 0, 6) KA_PIECE(n); SEND
    u32x4 gq[2];
#define GATHER(tail)                                                                                                            \
    asm volatile("buffer_load_dwordx4 %0, %2, %3, %4 offen sc1\n\tbuffer_load_dwordx4 %1, %2, %3, %5 offen sc1" tail            \
                 : "=&v"(gq[0]), "=&v"(gq[1]) : "v"((u32)t * 16), "s"(sws), "s"(WS_GRAN), "s"(WS_GRAN + 8192) : "memory")
    const int kind = w < 4 ? (w >> 1) : 2, hp = w < 4 ? (w & 1) : (w >> 1 & 1);
    u32 brd0 = (u32)(kind * 8192 + (hb * 8 + (l >> 2 & 3)) * 128 + ((hp ^ (l >> 3)) & 1) * 64 + (l >> 4 & 1) * 32 + (l & 3) * 8);
    u32 brd1 = brd0 + 65536, brd2 = brd0 + 65536 + 2 * WSLOT;
    PIN(brd1); PIN(brd2);
    bf16x8 wq[32];
#define KA_FRAG(kc) _Pragma("unroll") for (int c_ = 0; c_ < 4; ++c_) {                                                    \
      const u32 o_ = ((kc) >> 1 == 1 ? brd0 : ((kc) >> 1 == 2 ? brd2 : brd1)) + ((kc) & 1) * WSLOT + c_ * 2048; wq[(kc) * 4 + c_] = lds_tr2(smem, o_, o_ + 512); }
    if (w < 4) { SFOR(n, 6, 18) KA_PIECE(n); SEND WAITV(12); RAWBAR(); KA_FRAG(0); KA_FRAG(1); BAR(); } else WAITV(6);
    STAMP(30);
    float rs[8], mr[8];
    {
      float v[8], z[2];
      SFOR(i, 0, 8)
        PIN(xa[i]);
        v[i] = ((bf_lo(xa[i][0]) + bf_hi(xa[i][0])) + (bf_lo(xa[i][1]) + bf_hi(xa[i][1]))) + ((bf_lo(xa[i][2]) + bf_hi(xa[i][2])) + (bf_lo(xa[i][3]) + bf_hi(xa[i][3])));
      SEND
      STAMP(31);
      wave_sum8(v, z);
      z[0] *= (1.f / 512.f); z[1] *= (1.f / 512.f);
#define VAR_ROWS(B, E)                                                                                                   \
      SFOR(i, B, E)                                                                                                      \
        const float mu = ROW_OF(z, i);                                                                                   \
        float sq = 0.f;                                                                                                  \
        _Pragma("unroll") for (int e = 0; e < 4; ++e) {                                                                  \
          const float a = bf_lo(xa[i][e]) - mu, b = bf_hi(xa[i][e]) - mu; sq = __builtin_fmaf(a, a, sq); sq = __builtin_fmaf(b, b, sq); } \
        v[i] = sq;                                                                                                       \
      SEND
      VAR_ROWS(0, 8)
      float q[2];
      wave_sum8(v, q);
#pragma unroll
      for (int j = 0; j < 2; ++j) { q[j] = ln_rstd(q[j] * (1.f / 512.f)); z[j] = -bfround(z[j]) * q[j]; }
      SFOR(i, 0, 8) rs[i] = ROW_OF(q, i); mr[i] = ROW_OF(z, i); SEND
    }
    f32x8 nv[8];
#pragma unroll
    for (int i = 0; i < 8; ++i) {
#pragma unroll
      for (int e = 0; e < 4; ++e) { nv[i][2 * e] = __builtin_fmaf(bf_lo(xa[i][e]), rs[i], mr[i]); nv[i][2 * e + 1] = __builtin_fmaf(bf_hi(xa[i][e]), rs[i], mr[i]); }
      PIN(nv[i]);
    }
    if (w >= 4) { WAITV(0); RAWBAR(); KA_FRAG(0); KA_FRAG(1); BAR(); SFOR(n, 6, 18) KA_PIECE(n); SEND }
    GATHER("");
#define KB_PIECE(n, d) GLDS(wvo, sw, wsb + (u32)(((n) / 3 + rw) & 7) * (64 * 3072), ((n) % 3) * 1024 - (d) * 32 * 3072, wlb, KA_SLOT((n) / 3) + ((n) % 3) * 8192 - (d) * 4096)
    if (w >= 4) { SFOR(n, 18, 24) KB_PIECE(n, 0); KB_PIECE(n, 1); SEND WAITV(20); } else WAITV(8);
    STAMP(2);
    RAWBAR();
    KA_FRAG(2); KA_FRAG(3);
    if (w >= 4) WAITV(12); else WAITV(0);
    PIN(gq[0]); PIN(gq[1]);
    STAMP(3);
#pragma unroll 1
    for (u32 spins = 0; spins < (1u << 17); ++spins) {
      if (__all(gok(gq[0], tag) && gok(gq[1], tag))) break;
      __builtin_amdgcn_s_sleep(2);
      GATHER("\n\ts_waitcnt vmcnt(0)");
    }
    ((LAS float*)((LAS char*)smem + MTAB))[t] = bfround(1.f + bfround(gsum(gq[1])));
    ((LAS float*)((LAS char*)smem + MTAB))[512 + t] = bfround(gsum(gq[0]));
    BAR();
    STAMP(33);
    float c1[8], sh[8];
    {
      const LAS f32x4* tc = (const LAS f32x4*)((LAS char*)smem + MTAB + l * 32);
      const f32x4 a0 = tc[0], a1 = tc[1], b0 = tc[128], b1 = tc[129];
#pragma unroll
      for (int e = 0; e < 4; ++e) { c1[e] = a0[e]; c1[4 + e] = a1[e]; sh[e] = b0[e]; sh[4 + e] = b1[e]; }
    }
    u32 awr[4];
#pragma unroll
    for (int i = 0; i < 4; ++i) awr[i] = (u32)((((l >> 1) - 4 * rw) & 31) * 1024 + (l & 1) * 512 + (((i >> 1) ^ rx) & 1) * 256 + (((w * 2 + (i & 1)) ^ (l & 7)) << 4));
#define KA_PUT(i) *(LAS u32x4*)((LAS char*)smem + awr[(i) & 3] + ((i) >> 2) * 32768) = hv[i]
    u32x4 hv[8];
#pragma unroll
    for (int i = 0; i < 4; ++i) { hv[i] = modulate8(nv[i], c1, sh); KA_PUT(i); }
    const int ta = w < 4 ? 0 : (w & 1), tx = rx >> 1;
    u32 ard[4];
#pragma unroll
    for (int i = 0; i < 4; ++i) ard[i] = (u32)(ta * 32768 + i * 1024 + hb * 512 + (((l & 31) ^ (i * 2 + hb)) << 4));
#define HFRAG(n) lds_frag(smem, ard[(n) & 3] + ((n) >> 2) * 4096)
    const int head = cg * 4 + hp * 2;
    const u32 kvb = WS_KV + (u32)(head * KV_HEAD + pm * 2 * KV_TILE);
    f32x16 z16;
#pragma unroll
    for (int i = 0; i < 16; ++i) z16[i] = 0.f;
    BAR();
    STAMP(4);
    if (w < 4) {
      const float qs = kind == 0 ? 0.25f * 1.4426950408889634f : 1.f;
      const u32 so = kind == 0 ? WS_QF + (u32)(head * 64 + pm * 2) * 1024 + lo16 : kvb + (u32)((l & 51) | (l & 4) << 1 | (l & 8) >> 1) * 16;
      const u32 sh_ = kind == 0 ? 65536u : (u32)KV_HEAD, st_ = kind == 0 ? 1024u : (u32)KV_TILE;
      bf16x8 hf[64];
      f32x16 acc = z16, accp = z16;
      u32x4 ov[2];
#pragma unroll
      for (int i = 0; i < 4; ++i) hf[i] = HFRAG(i);
      SB();
      SFOR(n, 0, 76)
        if constexpr (n < 4) { hv[4 + n] = modulate8(nv[4 + n], c1, sh); KA_PUT(4 + n); }
        if constexpr (n == 5) { KA_FRAG(4); }
        if constexpr (n == 9) RAWBAR();
        if constexpr (n == 11) { KA_FRAG(5); }
        if constexpr (n == 15) { KA_FRAG(6); }
        if constexpr (n == 19) { KA_FRAG(7); }
        if constexpr (n == 32 || n == 64) { accp = acc; acc = z16; }
        if constexpr (n + 4 < 64) hf[n + 4] = HFRAG(n + 4);
        if constexpr (n < 64) acc = MFMA32(wq[n & 31], hf[n], acc);
        if constexpr (n >= 32 && (n & 31) >= 2 && (n & 31) < 10) {
          constexpr int e = (n & 31) - 2;
          ov[e >> 2][e & 3] = pack2(accp[2 * e] * qs, accp[2 * e + 1] * qs);
        }
        if constexpr (n == 42 || n == 74) GST_SC1("x4", ov[0], so, sws, st_ * ((n / 32 - 1) ^ tx), 0);
        if constexpr (n == 43 || n == 75) GST_SC1("x4", ov[1], so, sws, st_ * ((n / 32 - 1) ^ tx) + sh_, 0);
        if (n < 64) SB();
      SEND
    } else {
      bf16x8 hf[32];
      f32x16 acc = z16;
#pragma unroll
      for (int i = 4; i < 8; ++i) { hv[i] = modulate8(nv[i], c1, sh); KA_PUT(i); }
      WAITV(0);
      BAR();
#pragma unroll
      for (int i = 0; i < 4; ++i) hf[i] = HFRAG(i);
      SB();
      SFOR(n, 0, 32)
        if constexpr (n >= 2 && n < 18 && (n & 3) == 2) { KA_FRAG(4 + n / 4); }
        if constexpr (n + 4 < 32) hf[n + 4] = HFRAG(n + 4);
        acc = MFMA32(hf[n], wq[n], acc);
        SB();
      SEND
      const u32 kvt = kvb + (u32)((ta ^ tx) * KV_TILE);
      const u32 vo = kvt + (u32)((l >> 4 & 1) * KV_HEAD + 1024 + hb * 272 + (l & 15) * 16);
#pragma unroll
      for (int e = 0; e < 2; ++e) {
        const u32x4 ov = {pack2(acc[8 * e], acc[8 * e + 1]), pack2(acc[8 * e + 2], acc[8 * e + 3]), pack2(acc[8 * e + 4], acc[8 * e + 5]), pack2(acc[8 * e + 6], acc[8 * e + 7])};
        GST_SC1("x4", ov, vo, sws, zs, e * 544);
      }
      const u32x4 one = {0x3f803f80u, 0x3f803f80u, 0x3f803f80u, 0x3f803f80u};
      GST_SC1("x4", one, l < 8 ? kvt + (u32)((l >> 2) * KV_HEAD + 1024 + (l & 3) * 272 + 256) : 0x40000000u, sws, zs, 0);
    }
    STAMP(5);
    u32x4 tq[3];
    const u32 msb = WS_GRAN + (u32)(((w == 0 ? 1024 : (w == 1 ? 2048 : 2560)) + pn3 * 64) * 16);
#define TGATHER(tail)                                                                                                           \
    asm volatile("buffer_load_dwordx4 %0, %3, %5, %6 offen sc1\n\tbuffer_load_dwordx4 %1, %3, %5, %7 offen sc1\n\t"             \
                 "buffer_load_dwordx4 %2, %4, %5, %8 offen sc1" tail                                                            \
                 : "=&v"(tq[0]), "=&v"(tq[1]), "=&v"(tq[2])                                                                     \
                 : "v"((u32)t * 16), "v"(lo16), "s"(sws), "s"(WS_GRAN + 1536 * 16), "s"(WS_GRAN + 2048 * 16), "s"(msb) : "memory")
    TGATHER("");
    WAITV(3);
    STAMP(6);
    BAR();
    signal_cnt(ws, cg);
    WAITV(0);
#pragma unroll
    for (int i = 0; i < 3; ++i) PIN(tq[i]);
#pragma unroll 1
    for (u32 spins = 0; spins < (1u << 17); ++spins) {
      if (__all(gok(tq[0], tag) && gok(tq[1], tag) && gok(tq[2], tag))) break;
      __builtin_amdgcn_s_sleep(2);
      TGATHER("\n\ts_waitcnt vmcnt(0)");
    }
    *(LAS u16*)((LAS char*)smem + TAB + t * 2) = (u16)(__float_as_uint(bfround(1.f + bfround(gsum(tq[1])))) >> 16);
    *(LAS u16*)((LAS char*)smem + TAB + 1024 + t * 2) = (u16)(__float_as_uint(bfround(gsum(tq[0]))) >> 16);
    const float mv_ = bfround(gsum(tq[2]));
    ((LAS float*)((LAS char*)smem + MT))[t] = (w == 1) ? bfround(1.f + mv_) : mv_;
  }

  const int lq = loc >= 28 ? 27 : loc;
  const int pan4 = (xcd >> 1) * 4 + lq / 7, pn4 = (xcd & 1) * 7 + lq % 7;
  constexpr u32 STG = KV_HEAD;
  f32x16 zero;
#pragma unroll
  for (int i = 0; i < 16; ++i) zero[i] = 0.f;
  bf16x8 wf[32];

  {
    const char* wsrc = (const char*)((l & 2) ? wu : wg) + (size_t)(l >> 2) * 3584 + (pn4 * 128 + w * 16 + (l & 1) * 8) * 2;
    const u32 stg_lb = lb + w * 3072;
    u32 stg_rd0 = (u32)(w * 3072 + (hb * 8 + (l >> 2 & 3)) * 64 + (l >> 4 & 1) * 32 + (l & 3) * 8), stg_rd1 = stg_rd0 + STG;
    PIN(stg_rd1);
#define WREAD(p)                                                                                        \
    do {                                                                                                \
      if constexpr ((p) >= 5) {                                                                         \
        if ((p) <= 32) WAITV(4); else if ((p) == 33) WAITV(3); else if ((p) == 34) WAITV(2); else if ((p) == 35) WAITV(1); else WAITV(0); \
        const u32 o_ = ((((p) - 5) % 6) < 3 ? stg_rd1 : stg_rd0) + (((p) - 5) % 3) * 1024;              \
        wf[(p) - 5] = lds_tr2(smem, o_, o_ + 256);                                                      \
      }                                                                                                 \
    } while (0)
#define WLOAD(p) do { if ((p) < 32) { GLDS64(wsrc, stg_lb, (((p) % 6) < 3 ? STG : 0u) + ((p) % 3) * 1024u); wsrc += 16 * 3584; } } while (0)
    const int head = xcd * 4 + (loc >> 3), qt = (loc & 7) * 8 + w;
    const u32 ksb = (u32)(head * KV_HEAD + w * 1024), wlb = lb + w * 1024;
    const int r = l & 31;
    u32 krd = lo16;
    u32 vrd = (u32)(1024 + hb * 272 + (r < 16 ? r * 16 : (r == 16 ? 256 : 0x40000)));
    BAR();
    SFOR(p, 0, 37) WREAD(p); asm volatile("s_waitcnt lgkmcnt(0)" ::: "memory"); WLOAD(p); SEND
    STAMP(7);
    wait_cnt(ws, xcd, 32, w);
    BAR();
    STAMP(8);
#define KVR(r) GLDS_SC1(lo16, sws, ksb, WS_KV + (r) * 8192, wlb, (r) * 8192)
    u32x4 qraw;
    GLD128_SC1(qraw, lo16, sws, (u32)(head * 64 + qt) * 1024, WS_QF);
    KVR(0); KVR(1);
    if (w < 4) KVR(16);
    KVR(2); KVR(3);
    WAITV(2);
    PIN(qraw);
    RAWBAR();
    STAMP(9);
    const bf16x8 qf = __builtin_bit_cast(bf16x8, qraw);
    float m;
    bf16x8 kf = lds_frag(smem, krd);
    {
      f32x16 s0 = MFMA32(kf, qf, zero);
      m = s0[0];
#pragma unroll
      for (int i = 1; i < 16; ++i) m = fmaxf(m, s0[i]);
      const auto a = __builtin_amdgcn_permlane32_swap(__float_as_uint(m), __float_as_uint(m), false, false);
      m = fmaxf(__uint_as_float(a[0]), __uint_as_float(a[1]));
    }
    f32x16 negm;
#pragma unroll
    for (int i = 0; i < 16; ++i) negm[i] = -m;
    f32x16 o = zero;
    f32x16 sc = MFMA32(kf, qf, negm);
    kf = lds_frag(smem, krd + KV_TILE);
    bf16x8 v0 = lds_frag(smem, vrd), v1;
    SFOR(j, 0, 14)
      if constexpr ((j & 1) == 0) __builtin_amdgcn_s_setprio(3 - (j >> 1 & 3));
      if (j == 5) { WAITV(10); RAWBAR(); }
      if (j == 13) { WAITV(0); RAWBAR(); }
      if (j == 14) STAMP(10);
      constexpr bool wst = j >= 14 && j < 63 && (j - 14) % 4 != 3;
      constexpr int wp = (j - 14) - (j - 14) / 4;
      if (j == 28 || j == 56) { krd += 28 * KV_TILE; vrd += 28 * KV_TILE; PIN(krd); PIN(vrd); }
      const u32 jo = (u32)(j % 28) * KV_TILE;
#define EXPS(a, b) _Pragma("unroll") for (int i = (a); i < (b); ++i) sc[i] = __builtin_amdgcn_exp2f(sc[i])
      f32x16 sn;
      v0 = lds_frag(smem, vrd + jo); v1 = lds_frag(smem, vrd + jo + 544);
      if (j < 63) sn = MFMA32(kf, qf, negm);
      if (j < 62) kf = lds_frag(smem, krd + jo + 2 * KV_TILE);
      SB();
      EXPS(0, E1);
      SB();
      o = SMF(v0, LO8(sc), o);
      if constexpr (wst) { WREAD(wp); WLOAD(wp); }
      if constexpr (j < 6) { KVR(4 + 2 * j); KVR(5 + 2 * j); }
      SB();
      EXPS(E1, 16);
      o = SMF(v1, HI8(sc), o);
      SB();
      if (j < 63) sc = sn;
    SEND
    krd += 14 * KV_TILE; vrd += 14 * KV_TILE;
#pragma unroll 1
    for (int it = 0; it < 25; ++it) {
      if (it & 1) __builtin_amdgcn_s_setprio(0); else __builtin_amdgcn_s_setprio(1);
      SFOR(j, 0, 2)
        f32x16 sn;
        v0 = lds_frag(smem, vrd + j * KV_TILE); v1 = lds_frag(smem, vrd + j * KV_TILE + 544);
        sn = MFMA32(kf, qf, negm);
        kf = lds_frag(smem, krd + j * KV_TILE + 2 * KV_TILE);
        SB();
        EXPS(0, E1);
        SB();
        o = SMF(v0, LO8(sc), o);
        SB();
        EXPS(E1, 16);
        o = SMF(v1, HI8(sc), o);
        SB();
        sc = sn;
      SEND
      krd += 2 * KV_TILE; vrd += 2 * KV_TILE;
    }
    __builtin_amdgcn_s_setprio(0);
    float lsum = __uint_as_float(__builtin_amdgcn_permlane32_swap(__float_as_uint(o[8]), __float_as_uint(o[8]), false, false)[0]);
    bool ok = lsum < 1e18f;
#pragma unroll
    for (int i = 0; i < 8; ++i) ok = ok && (fabsf(o[i]) < 3e38f);
    if (!__all(ok)) {
      asm volatile("" ::: "memory");
      const __amdgpu_buffer_rsrc_t rk = __builtin_amdgcn_make_buffer_rsrc(ws + WS_KV + (size_t)head * KV_HEAD, 0, KV_HEAD, 0x20000);
      const int vg = 1024 + hb * 272 + (r == 16 ? 256 : (r & 15) * 16);
#define KVLD(off, j) __builtin_bit_cast(bf16x8, __builtin_amdgcn_raw_buffer_load_b128(rk, (off), (j) * KV_TILE, 16))
#pragma unroll 1
      for (int j = 0; j < 64; ++j) {
        const f32x16 sv = MFMA32(KVLD(lo16, j), qf, zero);
#pragma unroll
        for (int i = 0; i < 16; ++i) m = fmaxf(m, sv[i]);
      }
      m = fmaxf(m, __shfl_xor(m, 32));
#pragma unroll
      for (int i = 0; i < 16; ++i) negm[i] = -m;
      o = zero;
#pragma unroll 1
      for (int j = 0; j < 64; ++j) {
        f32x16 sv = MFMA32(KVLD(lo16, j), qf, negm);
        const bf16x8 v0 = KVLD(vg, j), v1 = KVLD(vg + 544, j);
#pragma unroll
        for (int i = 0; i < 16; ++i) sv[i] = __builtin_amdgcn_exp2f(sv[i]);
        o = SMF(v0, LO8(sv), o);
        o = SMF(v1, HI8(sv), o);
      }
      lsum = shfl_idx(o[8], r);
    }
    const float inv = 1.f / lsum;
    u32x4 ov;
#pragma unroll
    for (int e = 0; e < 4; ++e) ov[e] = pack2(o[2 * e] * inv, o[2 * e + 1] * inv);
    GST_SC1("x4", ov, lo16, sws, (u32)(qt * 32 + head) * 1024, WS_OF);
    STAMP(11);
    WAITV(0);
    BAR();
    signal_cnt(ws, 8 + (loc & 7));
    STAMP(12);
  }

  const int tm = w & 1;
  const u32 wvo = (u32)((l >> 3) * 1024 + ((((l >> 2) ^ (l >> 4)) & 1) * 32 + (l & 3) * 8) * 2);
  const u32 wrd0 = (u32)((hb * 4 + (l >> 2 & 3)) * 128 + (l >> 3 & 1) * 64 + (l >> 4 & 1) * 32 + (l & 3) * 8);
  const int rte = xcd * 8 + (pm3 >> 1) * 4 + (pm3 & 1) + (w >> 2) * 2;
  const int erow = w * 8 + (l & 7), ecol = l >> 3;
  f32x16 uv = zero;
  const u32 trd = TAB + (u32)((l >> 4 & 1) * 1024 + hb * 16);
#define UV_PART(k0)                                                                                          \
  do { bf16x8 tf_[16];                                                                                       \
    _Pragma("unroll") for (int i_ = 0; i_ < 4; ++i_) tf_[i_] = lds_frag(smem, trd + ((k0) + i_) * 32);       \
    _Pragma("unroll") for (int i_ = 0; i_ < 16; ++i_) {                                                      \
      if (i_ + 4 < 16) tf_[i_ + 4] = lds_frag(smem, trd + ((k0) + i_ + 4) * 32);                             \
      uv = MFMA32(wf[(k0) + i_], tf_[i_], uv); SB(); }                                                       \
    PIN(uv); } while (0)

  {
    const u32x4 so = make_srd(wo, 512 * 1024);
    const int ro = pn3 & 3;
    SFOR(j, 0, 8) GLDS(wvo, so, (u32)(w * 8192 + pn3 * 128 + ((j + 2 * ro) & 7) * 65536), 0, lb + w * 1024, j * 8192); SEND
    u32x4 xr;
    GLD128(xr, (u32)((erow & 31) * 1024 + ecol * 16), sx, (u32)(rte * 32768 + pn3 * 128), 0);
    UV_PART(0);
    WAITV(0);
    PIN(xr);
    STAMP(13);
    wait_cnt(ws, 8 + xcd, 32, w);
    RAWBAR();
    STAMP(14);
    if (bid == 0 && t == 0) __hip_atomic_store((u32*)(ws + WS_EPOCH), tag, __ATOMIC_RELAXED, __HIP_MEMORY_SCOPE_AGENT);
    const u32 olo = (u32)((w >> 2) * 32768 + (w & 3) * 2048);
    const u32 osb = (u32)(rte * 32768 + (w & 3) * 2048), olb = lb + olo;
    SFOR(j, 0, 8) GLDS_SC1(lo16, sws, osb + (u32)(((j >> 1) + ro) & 3) * 8192, WS_OF + (j & 1) * 1024, olb, 65536 + (j >> 1) * 8192 + (j & 1) * 1024); SEND
    const int tn = w >> 1 & 1, kh = w >> 2;
    u32 ord = 65536 + (u32)(tm * 32768 + kh * 4096) + lo16;
    PIN(ord);
    const u32 wrd = (wrd0 ^ (u32)(tn * 64)) + kh * 8192;
    f32x16 acc = zero;
    bf16x8 wq[16];
#define WOFRAG(n) wq[n] = lds_tr2(smem, wrd + ((n) >> 2) * 16384 + ((n) & 3) * 2048, wrd + ((n) >> 2) * 16384 + ((n) & 3) * 2048 + 1024)
#pragma unroll
    for (int n = 0; n < 8; ++n) WOFRAG(n);
    SFOR(s, 0, 4)
      if (s == 0) WAITV(6); else if (s == 1) WAITV(4); else if (s == 2) WAITV(2); else WAITV(0);
      RAWBAR();
      bf16x8 oq[4];
#pragma unroll
      for (int j = 0; j < 4; ++j) oq[j] = lds_frag(smem, ord + s * 8192 + j * 1024);
      if constexpr (s < 2) {
#pragma unroll
        for (int j = 0; j < 4; ++j) WOFRAG(8 + s * 4 + j);
      }
      SB();
#pragma unroll
      for (int j = 0; j < 4; ++j) acc = MFMA32(wq[s * 4 + j], oq[j], acc);
      SB();
    SEND
    STAMP(15);
    {
      LAS char* eb = (LAS char*)smem + kh * 17408 + (tm * 32 + (l & 31)) * 272 + (tn * 32 + hb * 4) * 4;
#pragma unroll
      for (int e = 0; e < 4; ++e) *(LAS f32x4*)(eb + e * 32) = (f32x4){acc[4 * e], acc[4 * e + 1], acc[4 * e + 2], acc[4 * e + 3]};
    }
    f32x4 ga[2], cb[2];
#pragma unroll
    for (int e = 0; e < 2; ++e) { ga[e] = *(const LAS f32x4*)((LAS char*)smem + MT + ecol * 32 + e * 16); cb[e] = *(const LAS f32x4*)((LAS char*)smem + MT + 256 + ecol * 32 + e * 16); }
    BAR();
    float xv[8];
    u32x4 x1p, yp;
#pragma unroll
    for (int e = 0; e < 2; ++e) {
      const LAS char* eb = (const LAS char*)smem + erow * 272 + ecol * 32 + e * 16;
      const f32x4 a = *(const LAS f32x4*)eb + *(const LAS f32x4*)(eb + 17408);
#pragma unroll
      for (int h = 0; h < 2; ++h) {
        const u32 pk = pack2(bf_lo(xr[2 * e + h]) + ga[e][2 * h] * a[2 * h], bf_hi(xr[2 * e + h]) + ga[e][2 * h + 1] * a[2 * h + 1]);
        x1p[2 * e + h] = pk;
        xv[4 * e + 2 * h] = bf_lo(pk); xv[4 * e + 2 * h + 1] = bf_hi(pk);
        yp[2 * e + h] = pack2(bf_lo(pk) * cb[e][2 * h], bf_hi(pk) * cb[e][2 * h + 1]);
      }
    }
    GST_SC1("x4", yp, (u32)((ecol >> 1) * 1024 + ((ecol & 1) * 32 + (erow & 31)) * 16), sws, (u32)(rte * 32 + pn3 * 4) * 1024, WS_Y);
    float sum = ((xv[0] + xv[1]) + (xv[2] + xv[3])) + ((xv[4] + xv[5]) + (xv[6] + xv[7]));
    const float mean = col8_sum(sum) * (1.f / 64.f);
    float sq = 0.f;
#pragma unroll
    for (int i = 0; i < 8; ++i) { const float d = xv[i] - mean; sq = __builtin_fmaf(d, d, sq); }
    const f32x2 o2 = {mean, col8_sum(sq)};
    GST_SC1("x2", o2, l < 8 ? (u32)((erow & 31) * 8) : 0x40000000u, sws, (u32)(rte * 2048 + pn3 * 256), WS_ST2);
    STAMP(16);
    WAITV(0);
    RAWBAR();
    signal_cnt(ws, 16 + xcd * 2 + (pm3 >> 1));
    STAMP(17);
    GST_SC1("x4", x1p, (u32)(erow * 8 + ecol) * 16, sws, (u32)bid * 8192, WS_X1);
  }

  const u32x4 sd = make_srd(wd, 1792 * 1024);
  const u32 dsb = (u32)(w * 16384 + pn3 * 128), dlb = lb + w * 2048;
  const int r5 = (pn3 * 7) >> 2;
#define CH5(c) (u32)((c) + r5 >= 14 ? (c) + r5 - 14 : (c) + r5)
#define WDPIECE(c, slot, h) GLDS(wvo, sd, dsb + CH5(c) * 131072, (h) * 8192, dlb, (slot) * 16384 + (h) * 1024)
#define WDISSUE(c, slot) do { WDPIECE(c, slot, 0); WDPIECE(c, slot, 1); } while (0)
  const int kq = w & 3;
  const u32 wr5 = wrd0 + kq * 4096;
  bf16x8 dq[14][4];
#define DFRAG1(c, i) dq[c][i] = lds_tr2(smem, (wr5 ^ (((i) & 1) * 64u)) + (c) * 16384 + ((i) >> 1) * 2048, (wr5 ^ (((i) & 1) * 64u)) + (c) * 16384 + ((i) >> 1) * 2048 + 1024)
  if (loc < 28) {
    constexpr u32 RST4 = 157696;
    UV_PART(16);
    float uu[16], vv[16];
#pragma unroll
    for (int i = 0; i < 16; ++i) {
      const auto r = __builtin_amdgcn_permlane16_swap(__float_as_uint(uv[i]), __float_as_uint(uv[i]), false, false);
      uu[i] = __uint_as_float(r[0]); vv[i] = __uint_as_float(r[1]);
      PIN(uu[i]); PIN(vv[i]);
    }
    wait_cnt(ws, 16 + pan4, 16, w);
    RAWBAR();
    STAMP(18);
    const int rt4 = pan4 * 4;
    const u32 ysb = (u32)(rt4 * 32768 + w * 1024), ylb = lb + w * 1024;
#define YPIECE(i) GLDS_SC1(lo16, sws, ysb, WS_Y + (i) * 8192, ylb, (i) * 8192)
    YPIECE(0); YPIECE(1); YPIECE(2); YPIECE(3);
    GLDS_SC1(lo16, sws, (u32)(rt4 * 2048 + w * 1024), WS_ST2, ylb, 131072);
    YPIECE(4); YPIECE(5);
    u32 yrd0 = lo16, yrd1 = lo16 + 65536;
    PIN(yrd1);
    const u32 asb = (u32)(rt4 * 112 + pn4 * 8 + w) * 1024;
#define YFRAG(n) lds_frag(smem, ((n) < 64 ? yrd0 : yrd1) + ((n) & 63) * 1024)
    WAITV(5);
    RAWBAR();
    STAMP(19);
    bf16x8 yf[128];
#pragma unroll
    for (int n = 0; n < 4; ++n) yf[n] = YFRAG(n);
    SB();
#define signal_early()                                                                                               \
    do { unsigned long long ex_;                                                                                     \
      asm volatile("s_cmp_eq_u32 %5, 0\n\ts_cbranch_scc0 1f\n\ts_mov_b64 %0, exec\n\ts_mov_b64 exec, 1\n\t"          \
                   "buffer_atomic_add %1, %2, %3, %4 offen\n\ts_mov_b64 exec, %0\n1:"                                \
                   : "=&s"(ex_) : "v"(1u), "v"(0u), "s"(sws), "s"(WS_CNT + (u32)(48 + pan4) * 256), "s"(w) : "memory", "scc"); } while (0)
    f32x16 acc = zero, accp = zero;
    f32x2 stp = {0.f, 0.f};
    float eg[8], eu[8], et[8];
    u32x4 ov;
    SFOR(n, 0, 160)
      constexpr int a = n >> 5, ks = n & 31;
      if (n == 10) { WAITV(7); RAWBAR(); }
      if (n == 26) { WAITV(8); BAR(); }
      if (n == 56) { WAITV(4); RAWBAR(); }
      if (n == 100) WAITV(3);
      if (n == 121) { WAITV(4); RAWBAR(); signal_early(); }
      if constexpr (n < 20 && n % 2 == 0) YPIECE(6 + n / 2);
      if (n == 36 || n == 68 || n == 100) RAWBAR();
      if constexpr (n == 60) {
        const int rr = pm3 * 224 + w * 28 + (l < 28 ? l : 27);
        const int r2 = rr + (rr < 256 ? 768 : 896) + r5 * 128;
        GTOUCH((u32)(r2 >= 1792 ? r2 - 1792 : r2) * 1024, sd, (u32)(pn3 * 128), 0, lb + SCR);
      }
      if constexpr (n >= 36 && n < 52 && n % 4 == 0) WDPIECE((n - 36) / 8, (n - 36) / 8, (n - 36) / 4 & 1);
      if constexpr (n >= 68 && n < 84 && n % 4 == 0) WDPIECE(2 + (n - 68) / 8, 2 + (n - 68) / 8, (n - 68) / 4 & 1);
      if constexpr (n == 84 || n == 88) WDPIECE(8, 8, (n - 84) / 4);
      if constexpr (n >= 100 && n < 116 && n % 4 == 0) WDPIECE(4 + (n - 100) / 8, 4 + (n - 100) / 8, (n - 100) / 4 & 1);
      if constexpr (n >= 100 && n < 116) DFRAG1((n - 100) >> 2, (n - 100) & 3);
      if constexpr (n >= 121 && n < 129) WDPIECE(10 + (n - 121) / 2, (n - 121) / 2, n & 1);
      if constexpr (n == 12) {
        const LAS f32x2* sp = (const LAS f32x2*)((LAS char*)smem + 131072 + (t >> 7) * 2048 + (t & 3) * 512 + (t >> 2 & 31) * 8);
        const f32x2 p0 = sp[0], p1 = sp[32];
        float mu = p0[0] + p1[0];
        mu += dpp_xor1(mu); mu += dpp_xor2(mu);
        mu *= 0.125f;
        float q = p0[1] + p1[1] + 64.f * ((p0[0] - mu) * (p0[0] - mu) + (p1[0] - mu) * (p1[0] - mu));
        q += dpp_xor1(q); q += dpp_xor2(q);
        const float rs = __builtin_amdgcn_rsqf(q * (1.f / 512.f) + 1e-5f);
        ((LAS f32x2*)((LAS char*)smem + RST4))[t >> 2] = (f32x2){rs, -mu * rs};
      }
      if (ks == 28 && a < 4) stp = ((const LAS f32x2*)((LAS char*)smem + RST4))[a * 32 + (l & 31)];
      if (ks == 0 && a > 0) { accp = acc; acc = zero; }
      if (n == 128) STAMP(20);
      if constexpr (n + 4 < 128) yf[n + 4] = YFRAG(n + 4);
      if constexpr (n < 128) acc = MFMA32(wf[ks], yf[n], acc);
      if constexpr (a > 0 && ks >= 2 && ks < 26) {
        constexpr int i = (ks - 2) / 3, ph = (ks - 2) % 3;
        if (ph == 0) {
          eg[i] = __builtin_fmaf(accp[i], stp[0], __builtin_fmaf(uu[i], stp[1], vv[i]));
          eu[i] = __builtin_fmaf(accp[8 + i], stp[0], __builtin_fmaf(uu[8 + i], stp[1], vv[8 + i]));
          et[i] = -1.4426950408889634f * eg[i];
          if (a < 4) { PIN(eg[i]); PIN(eu[i]); PIN(et[i]); }
        } else if (ph == 1) {
          et[i] = 1.f + __builtin_amdgcn_exp2f(et[i]);
          eg[i] *= eu[i];
          if (a < 4) { PIN(eg[i]); PIN(et[i]); }
        } else {
          eg[i] *= __builtin_amdgcn_rcpf(et[i]);
          if (a < 4) PIN(eg[i]);
          if constexpr (i & 1) ov[i >> 1] = pack2(eg[i - 1], eg[i]);
        }
      }
      if constexpr (a > 0 && ks == 26) GST_SC1("x4", ov, lo16, sws, asb, WS_ACT + (a - 1) * 112 * 1024);
      if (n < 128) SB();
    SEND
    STAMP(21);
  } else {
    WDISSUE(0, 0); WDISSUE(1, 1); WDISSUE(2, 2); WDISSUE(3, 3); WDISSUE(8, 8); WDISSUE(4, 4); WDISSUE(5, 5);
    WAITV(0);
    RAWBAR();
    SFOR(n, 0, 16) DFRAG1(n >> 2, n & 3); SEND
    BAR();
    WDISSUE(10, 0); WDISSUE(11, 1); WDISSUE(12, 2); WDISSUE(13, 3);
  }

  {
    u32 wra[3][2];
#pragma unroll
    for (int i = 0; i < 3; ++i) { wra[i][0] = wr5 + i * 65536; wra[i][1] = wra[i][0] ^ 64u; PIN(wra[i][0]); PIN(wra[i][1]); }
#define DFRAG(c) _Pragma("unroll") for (int i_ = 0; i_ < 4; ++i_) {                                              \
      const u32 o_ = wra[((c) % 10) >> 2][i_ & 1] + (((c) % 10) & 3) * 16384 + (i_ >> 1) * 2048; dq[c][i_] = lds_tr2(smem, o_, o_ + 1024); }
    f32x4 gb[2];
#pragma unroll
    for (int e = 0; e < 2; ++e) gb[e] = *(const LAS f32x4*)((LAS char*)smem + MT + 512 + (l & 7) * 32 + e * 16);
    const int pan5 = xcd * 2 + (pm3 >> 1);
    wait_cnt(ws, 48 + pan5, 14, w);
    WAITV(0);
    BAR();
    if (loc < 28) signal_cnt(ws, 32 + pan4);
    STAMP(22);
#define WD2(c, slot) do { WDISSUE(c, slot); WDPIECE(c, slot, -8); WDPIECE(c, slot, -7); } while (0)
#define BAR_IF(lo) asm volatile("s_cmp_lt_u32 %0, 4\n\ts_cbranch_scc" lo " 1f\n\ts_barrier\n1:" :: "s"(w) : "memory", "scc")
    if (w >= 4) {
      WD2(6, 6); WD2(7, 7); WD2(9, 9);
      WAITV(0);
      RAWBAR();
      wait_cnt(ws, 32 + pan5, 14, 0);
    }
    STAMP(23);
    const u32 asb = (u32)(rte * 112 + kq * 2) * 1024;
    u32x4 af[14][2];
#define AISSUE(c) do { GLD128_SC1(af[c][0], lo16, sws, asb + CH5(c) * 8192, WS_ACT); GLD128_SC1(af[c][1], lo16, sws, asb + CH5(c) * 8192, WS_ACT + 1024); } while (0)
    SFOR(c, 0, AWIN) AISSUE(c); SEND
    f32x16 acc[2] = {zero, zero};
    u32x4 x1p;
    SFOR(c, 0, 14)
      constexpr int young = c + AWIN <= 14 ? 2 * (AWIN - 1) : (c < 13 ? 2 * (13 - c) + 1 : 0);
      asm volatile("s_waitcnt vmcnt(%0)" :: "i"(young) : "memory");
      if (c == 0) STAMP(24);
      if (c == 5) STAMP(37);
      if (c == 10) STAMP(38);
      if (c == 2) BAR_IF("0");
      if (c == 10) BAR_IF("1");
      if constexpr (c + AWIN < 14) AISSUE(c + AWIN);
      if (c + AWIN == 14) GLD128_SC1(x1p, (u32)t * 16, sws, (u32)bid * 8192, WS_X1);
      if constexpr (c >= 3 && c < 13) { DFRAG(c + 1); }
      PIN(af[c][0]); PIN(af[c][1]);
      SB();
#pragma unroll
      for (int i = 0; i < 4; ++i) acc[i & 1] = MFMA32(dq[c][i], __builtin_bit_cast(bf16x8, af[c][i >> 1]), acc[i & 1]);
      SB();
    SEND
    STAMP(25);
    BAR_IF("0");
    {
      LAS char* eb = (LAS char*)smem + 65536 + kq * 17408 + ((w >> 2) * 32 + (l & 31)) * 272 + hb * 16;
#pragma unroll
      for (int tn = 0; tn < 2; ++tn)
#pragma unroll
        for (int e = 0; e < 4; ++e)
          *(LAS f32x4*)(eb + tn * 128 + e * 32) = (f32x4){acc[tn][4 * e], acc[tn][4 * e + 1], acc[tn][4 * e + 2], acc[tn][4 * e + 3]};
    }
    STAMP(40);
    BAR();
    STAMP(29);
    PIN(x1p);
    u32x4 ov;
#pragma unroll
    for (int e = 0; e < 2; ++e) {
      const LAS char* eb = (const LAS char*)smem + 65536 + (t >> 3) * 272 + (l & 7) * 32 + e * 16;
      const f32x4 s = (*(const LAS f32x4*)eb + *(const LAS f32x4*)(eb + 17408)) + (*(const LAS f32x4*)(eb + 2 * 17408) + *(const LAS f32x4*)(eb + 3 * 17408));
      ov[2 * e] = pack2(bf_lo(x1p[2 * e]) + gb[e][0] * s[0], bf_hi(x1p[2 * e]) + gb[e][1] * s[1]);
      ov[2 * e + 1] = pack2(bf_lo(x1p[2 * e + 1]) + gb[e][2] * s[2], bf_hi(x1p[2 * e + 1]) + gb[e][3] * s[3]);
    }
    *(u32x4*)((char*)out + (size_t)(rte * 32 + (t >> 3 & 31)) * 1024 + pn3 * 128 + (l & 7) * 16) = ov;
  }
  ST_OUT(2, ws);
  asm volatile(".Lcode_end:");
}

extern "C" void kernel_launch(void* const* d_in, const int* in_sizes, int n_in, void* d_out, int out_size, void* d_ws, size_t ws_size,
                              hipStream_t stream) {
  adaln_block<<<256, 512, 0, stream>>>((const u16*)d_in[0], (const u16*)d_in[1], (const u16*)d_in[2], (const u16*)d_in[4], (const u16*)d_in[5],
                                       (const u16*)d_in[6], (const u16*)d_in[7], (const u16*)d_in[8], (char*)d_ws, (u16*)d_out);
}
```

```cpp
#include <hip/hip_runtime.h>
#include <hip/hip_bf16.h>

typedef unsigned short u16;
typedef unsigned int u32;
typedef u32 u32x2 __attribute__((ext_vector_type(2)));
typedef u32 u32x4 __attribute__((ext_vector_type(4)));
typedef float f32x2 __attribute__((ext_vector_type(2)));
typedef float f32x4 __attribute__((ext_vector_type(4)));
typedef float f32x8 __attribute__((ext_vector_type(8)));
typedef float f32x16 __attribute__((ext_vector_type(16)));
typedef __bf16 bf16x2 __attribute__((ext_vector_type(2)));
typedef __bf16 bf16x8 __attribute__((ext_vector_type(8)));
typedef __bf16 bf16x16 __attribute__((ext_vector_type(16)));
typedef short s16x4 __attribute__((ext_vector_type(4)));
typedef short s16x8 __attribute__((ext_vector_type(8)));
#define PRIO_MODE 1
#define GATHER_AT 5
#define AORD 0
#define E1 9
#define AWIN 8
#define OUT_WT 0
#define CODE_PF 8
#define LAS __attribute__((address_space(3)))
#define DI __device__ __forceinline__

constexpr u32 WS_GRAN = 0;
constexpr u32 WS_EPOCH = 0x18000;
constexpr u32 WS_CNT  = 0x20000;
constexpr u32 WS_QF   = 0x100000;
constexpr u32 WS_KV   = 0x300000;
constexpr u32 WS_OF   = 0x800000;
constexpr u32 WS_X1   = 0xA00000;
constexpr u32 WS_Y    = 0xC00000;
constexpr u32 WS_ST2  = 0xE00000;
constexpr u32 WS_ACT  = 0xF00000;
constexpr u32 WS_BYTES = 0x1600000;
constexpr int KV_TILE = 2112, KV_HEAD = 64 * KV_TILE;

template <int I> struct IC { static constexpr int value = I; };
template <int B, int E, class F> DI void static_for(F&& f) { if constexpr (B < E) { f(IC<B>{}); static_for<B + 1, E>(f); } }
#define SFOR(n, B, E) static_for<B, E>([&](auto ic_) __attribute__((always_inline)) { constexpr int n = decltype(ic_)::value;
#define SEND });

DI float bf_lo(u32 u) { return __uint_as_float(u << 16); }
DI float bf_hi(u32 u) { return __uint_as_float(u & 0xffff0000u); }
DI u32 pack2(float a, float b) { bf16x2 r = {(__bf16)a, (__bf16)b}; return __builtin_bit_cast(u32, r); }
DI float bfround(float a) { return (float)(__bf16)a; }
DI float ln_rstd(float var) { return __builtin_amdgcn_rcpf(bfround(__builtin_amdgcn_sqrtf(bfround(bfround(var) + 1e-5f)))); }
DI float xor32_sum(float v) {
  const auto r = __builtin_amdgcn_permlane32_swap(__float_as_uint(v), __float_as_uint(v), false, false);
  return __uint_as_float(r[0]) + __uint_as_float(r[1]);
}
DI float dpp_xor1(float v) { return __int_as_float(__builtin_amdgcn_mov_dpp(__float_as_int(v), 0xB1, 0xF, 0xF, true)); }
DI float dpp_xor2(float v) { return __int_as_float(__builtin_amdgcn_mov_dpp(__float_as_int(v), 0x4E, 0xF, 0xF, true)); }
DI float shfl_idx(float v, int srcLane) { return __int_as_float(__builtin_amdgcn_ds_bpermute(srcLane << 2, __float_as_int(v))); }

DI u32x4 make_srd(const void* p, u32 bytes) {
  const size_t a = (size_t)p;
  u32x4 r = {(u32)a, (u32)(a >> 32) & 0xffffu, bytes, 0x20000u};
  return r;
}
#define GLDS(voff, srd, sb, GO, lb, LO)                                                                                   \
  do { u32 tmp_;                                                                                                          \
    asm volatile("s_add_u32 m0, %4, %6\n\ts_add_u32 %0, %3, %5\n\tbuffer_load_dwordx4 %1, %2, %0 offen lds"            \
                 : "=&s"(tmp_) : "v"(voff), "s"(srd), "s"(sb), "s"(lb), "i"(GO), "i"(LO) : "memory", "scc"); } while (0)
#define GLD(W, dst, voff, srd, sb, GO)                                                                                    \
  do { u32 tmp_;                                                                                                          \
    asm volatile("s_add_u32 %1, %4, %5\n\tbuffer_load_dword" W " %0, %2, %3, %1 offen"                                   \
                 : "=v"(dst), "=&s"(tmp_) : "v"(voff), "s"(srd), "s"(sb), "i"(GO) : "memory", "scc"); } while (0)
#define GLD128(dst, voff, srd, sb, GO) GLD("x4", dst, voff, srd, sb, GO)
#define GLD64(dst, voff, srd, sb, GO) GLD("x2", dst, voff, srd, sb, GO)
#define GLD32(dst, voff, srd, sb, GO) GLD("", dst, voff, srd, sb, GO)
#define WAITV(N) asm volatile("s_waitcnt vmcnt(" #N ")" ::: "memory")
#define PIN(x) asm volatile("" : "+v"(x))
#define BAR() do { asm volatile("s_waitcnt lgkmcnt(0)" ::: "memory"); __builtin_amdgcn_s_barrier(); asm volatile("" ::: "memory"); } while (0)

#define ST_DECL
#define STAMP(i)
#define ST_OUT(kid, wsp)

DI bf16x8 lds_frag(const char* smem, u32 off) { return *(const LAS bf16x8*)((const LAS char*)smem + off); }
DI bf16x8 lds_tr2(const char* smem, u32 off0, u32 off1) {
  s16x4 a = __builtin_amdgcn_ds_read_tr16_b64_v4i16((LAS s16x4*)((LAS char*)smem + off0));
  s16x4 b = __builtin_amdgcn_ds_read_tr16_b64_v4i16((LAS s16x4*)((LAS char*)smem + off1));
  s16x8 r = __builtin_shufflevector(a, b, 0, 1, 2, 3, 4, 5, 6, 7);
  return __builtin_bit_cast(bf16x8, r);
}
DI u32 lds_base(const char* smem) { return (u32)(size_t)(const LAS char*)smem; }
#define SB() __builtin_amdgcn_sched_barrier(0)
#define RAWBAR() do { asm volatile("" ::: "memory"); __builtin_amdgcn_s_barrier(); asm volatile("" ::: "memory"); } while (0)
#define MFMA32(a, b, c) __builtin_amdgcn_mfma_f32_32x32x16_bf16((a), (b), (c), 0, 0, 0)
#define MFMA16(a, b, c) __builtin_amdgcn_mfma_f32_16x16x32_bf16((a), (b), (c), 0, 0, 0)

DI u32x4 modulate8(const f32x8 n, const float* c1, const float* sh) {
  u32x4 o;
#pragma unroll
  for (int e = 0; e < 4; ++e) o[e] = pack2(__builtin_fmaf(n[2 * e], c1[2 * e], sh[2 * e]), __builtin_fmaf(n[2 * e + 1], c1[2 * e + 1], sh[2 * e + 1]));
  return o;
}

#define GLDS_SC1(voff, srd, sb, GO, lb, LO)                                                                               \
  do { u32 tmp_;                                                                                                          \
    asm volatile("s_add_u32 m0, %4, %6\n\ts_add_u32 %0, %3, %5\n\tbuffer_load_dwordx4 %1, %2, %0 offen sc1 lds"         \
                 : "=&s"(tmp_) : "v"(voff), "s"(srd), "s"(sb), "s"(lb), "i"(GO), "i"(LO) : "memory", "scc"); } while (0)
#define GLD128_SC1(dst, voff, srd, sb, GO)                                                                                \
  do { u32 tmp_;                                                                                                          \
    asm volatile("s_add_u32 %1, %4, %5\n\tbuffer_load_dwordx4 %0, %2, %3, %1 offen sc1"                                 \
                 : "=v"(dst), "=&s"(tmp_) : "v"(voff), "s"(srd), "s"(sb), "i"(GO) : "memory", "scc"); } while (0)
#define GST_SC1(W, val, voff, srd, sb, GO)                                                                                \
  do { u32 tmp_;                                                                                                          \
    asm volatile("s_add_u32 %0, %4, %5\n\tbuffer_store_dword" W " %1, %2, %3, %0 offen sc1\n\ts_nop 1"                  \
                 : "=&s"(tmp_) : "v"(val), "v"(voff), "s"(srd), "s"(sb), "i"(GO) : "memory", "scc"); } while (0)
#define GTOUCH(voff, srd, sb, GO, lb)                                                                                     \
  do { u32 tmp_;                                                                                                          \
    asm volatile("s_mov_b32 m0, %4\n\ts_add_u32 %0, %3, %5\n\tbuffer_load_dword %1, %2, %0 offen lds"                   \
                 : "=&s"(tmp_) : "v"(voff), "s"(srd), "s"(sb), "s"(lb), "i"(GO) : "memory", "scc"); } while (0)
#define GLDS64(vaddr, lb, LO)                                                                                             \
    asm volatile("s_add_u32 m0, %1, %2\n\ts_nop 0\n\tglobal_load_lds_dwordx4 %0, off" :: "v"(vaddr), "s"(lb), "i"(LO) : "memory", "scc")

DI void wave_sum8(const float* v, float* z) {
  float u[4];
#pragma unroll
  for (int i = 0; i < 4; ++i) {
    const auto a = __builtin_amdgcn_permlane32_swap(__float_as_uint(v[i]), __float_as_uint(v[i + 4]), false, false);
    u[i] = __uint_as_float(a[0]) + __uint_as_float(a[1]);
  }
#pragma unroll
  for (int i = 0; i < 2; ++i) {
    const auto a = __builtin_amdgcn_permlane16_swap(__float_as_uint(u[i]), __float_as_uint(u[i + 2]), false, false);
    float s = __uint_as_float(a[0]) + __uint_as_float(a[1]);
    s += dpp_xor1(s); s += dpp_xor2(s);
    s += __int_as_float(__builtin_amdgcn_mov_dpp(__float_as_int(s), 0x141, 0xF, 0xF, true));
    s += __int_as_float(__builtin_amdgcn_mov_dpp(__float_as_int(s), 0x140, 0xF, 0xF, true));
    z[i] = s;
  }
}
#define ROW_OF(z, r) __int_as_float(__builtin_amdgcn_readlane(__float_as_int(z[(r) & 1]), ((r) >> 1) * 16))
DI float col8_sum(float v) {
  v += __int_as_float(__builtin_amdgcn_mov_dpp(__float_as_int(v), 0x128, 0xF, 0xF, true));
  const auto a = __builtin_amdgcn_permlane16_swap(__float_as_uint(v), __float_as_uint(v), false, false);
  return xor32_sum(__uint_as_float(a[0]) + __uint_as_float(a[1]));
}

DI void signal_cnt(char* ws, int idx) {
  if (threadIdx.x == 0) __hip_atomic_fetch_add((u32*)(ws + WS_CNT) + idx * 64, 1u, __ATOMIC_RELAXED, __HIP_MEMORY_SCOPE_AGENT);
}
DI void wait_cnt(char* ws, int idx, u32 want, int w) {
  if (w == 0) {
    u32* c = (u32*)(ws + WS_CNT) + idx * 64;
#pragma unroll 1
    for (u32 spins = 0; spins < (1u << 17); ++spins) {
      if ((u32)__builtin_amdgcn_readfirstlane(__hip_atomic_load(c, __ATOMIC_RELAXED, __HIP_MEMORY_SCOPE_AGENT)) >= want) break;
      __builtin_amdgcn_s_sleep(1);
    }
    asm volatile("" ::: "memory");
  }
}
DI float gsum(const u32x4 a) { return __uint_as_float(a[0]) + __uint_as_float(a[2]); }
DI bool gok(const u32x4 a, u32 tag) { return a[1] == tag && a[3] == tag; }
#define SMF(v, p, o) __builtin_amdgcn_smfmac_f32_32x32x32_bf16((v), __builtin_bit_cast(bf16x16, (p)), (o), 0xDDDD, 0, 0)
#define LO8(s) __builtin_shufflevector(s, s, 0, 1, 2, 3, 4, 5, 6, 7)
#define HI8(s) __builtin_shufflevector(s, s, 8, 9, 10, 11, 12, 13, 14, 15)

extern "C" __global__ __launch_bounds__(512) void adaln_block(const u16* __restrict__ x, const u16* __restrict__ wqkv, const u16* __restrict__ wo,
                                                              const u16* __restrict__ wg, const u16* __restrict__ wu, const u16* __restrict__ wd,
                                                              const u16* __restrict__ c, const u16* __restrict__ wmod, char* __restrict__ ws,
                                                              u16* __restrict__ out) {
  __shared__ __attribute__((aligned(16))) char smem[163840];
  ST_DECL STAMP(0);
  const int bid = blockIdx.x, xcd = bid & 7, loc = bid >> 3;
  const int t = threadIdx.x, l = t & 63;
  const int w = __builtin_amdgcn_readfirstlane(t >> 6);
  const u32 lb = lds_base(smem);
  {
    unsigned long long pc;
    u32 len;
    asm volatile("s_getpc_b64 %0\n.Lcode_mark:\n\ts_mov_b32 %1, .Lcode_end-.Lcode_mark" : "=s"(pc), "=s"(len));
    const u32x4 scode = make_srd((const void*)pc, loc < CODE_PF ? len : 0u);
    GTOUCH((u32)(loc * 512 + t) * 128 / CODE_PF, scode, 0u, 0, lb + 57344);
    SB();
  }
  const u32x4 sws = make_srd(ws, WS_BYTES), sx = make_srd(x, 2048 * 1024);
  const u32 lo16 = (u32)l * 16, zs = 0;
  const int hb = l >> 5;
  const int pm3 = loc >> 3, pn3 = loc & 7;
  constexpr u32 TAB = 159744, MT = TAB + 2048, SCR = TAB;
  u32 tag;
  asm volatile("" :: "s"(x), "s"(wqkv), "s"(wo), "s"(wg), "s"(wu), "s"(wd), "s"(c), "s"(wmod), "s"(ws), "s"(out));

  {
    constexpr u32 WSLOT = 24576, RED = 49152, MTAB = 53248;
    const int pm = (xcd >> 1) * 8 + (loc >> 2), cg = (xcd & 1) * 4 + (loc & 3);
    const u32x4 sw = make_srd(wqkv, 512 * 3072), sm = make_srd(wmod, bid < 96 ? 512 * 6144 : 0), sc = make_srd(c, bid < 96 ? 1024 : 0);
    const int mcb = bid >> 1, mkq = bid & 1;
    u32 ev;
    u32x2 cv;
    u32x4 mv[4], xa[8];
    if (bid == 0 && w == 7) GST_SC1("", zs, (u32)l * 256, sws, zs, WS_CNT);
    asm volatile("buffer_load_dword %0, %1, %2, %3 offen sc1" : "=v"(ev) : "v"(zs), "s"(sws), "s"(WS_EPOCH) : "memory");
#pragma unroll
    for (int i = 0; i < 4; ++i) GLD128(mv[i], (u32)((t >> 3) * 4 * 6144 + (t & 7) * 16), sm, (u32)(mkq * 256 * 6144 + mcb * 128), i * 6144);
    GLD64(cv, (u32)((t >> 3) * 8), sc, (u32)(mkq * 512), 0);
    RAWBAR();
    const int rx = loc & 3, rw = loc >> 2;
    const u32 xsb = (u32)(pm * 64 + w * 2) * 1024;
#define XROW(i) GLD128(xa[i], lo16, sx, xsb + (u32)(((i) >> 1) ^ rx) * 16384, ((i) & 1) * 1024)
    const u32 wvo = (u32)((l >> 3) * 3072 + ((((l >> 2) ^ (l >> 4)) & 1) * 32 + (l & 3) * 8) * 2);
    const u32 wsb = (u32)(w * 8 * 3072 + cg * 128), wlb = lb + w * 1024;
#define KA_SLOT(kc) (((kc) >> 1 == 1 ? 0 : ((kc) >> 1 == 2 ? 65536 + 2 * WSLOT : 65536)) + ((kc) & 1) * WSLOT)
#define KA_PIECE(n) GLDS(wvo, sw, wsb + (u32)(((n) / 3 + rw) & 7) * (64 * 3072), ((n) % 3) * 1024, wlb, KA_SLOT((n) / 3) + ((n) % 3) * 8192)
    STAMP(26);
    XROW(0); XROW(1); XROW(2);
    STAMP(27);
    WAITV(3);
    STAMP(28);
    PIN(ev); PIN(cv);
#pragma unroll
    for (int i = 0; i < 4; ++i) PIN(mv[i]);
    tag = (u32)__builtin_amdgcn_readfirstlane(ev) + 1u;
    XROW(3); XROW(4); XROW(5);
    if (bid < 96) {
      float acc[8];
#pragma unroll
      for (int e = 0; e < 8; ++e) acc[e] = 0.f;
#pragma unroll
      for (int i = 0; i < 4; ++i) {
        const float cc = (i & 1) ? bf_hi(cv[i >> 1]) : bf_lo(cv[i >> 1]);
        const float sv = bfround(cc * __builtin_amdgcn_rcpf(1.f + __builtin_amdgcn_exp2f(-1.4426950408889634f * cc)));
#pragma unroll
        for (int e = 0; e < 4; ++e) { acc[2 * e] += sv * bf_lo(mv[i][e]); acc[2 * e + 1] += sv * bf_hi(mv[i][e]); }
      }
      float u[4], z[2];
#pragma unroll
      for (int i = 0; i < 4; ++i) {
        const auto a = __builtin_amdgcn_permlane32_swap(__float_as_uint(acc[i]), __float_as_uint(acc[i + 4]), false, false);
        u[i] = __uint_as_float(a[0]) + __uint_as_float(a[1]);
      }
#pragma unroll
      for (int i = 0; i < 2; ++i) {
        const auto a = __builtin_amdgcn_permlane16_swap(__float_as_uint(u[i]), __float_as_uint(u[i + 2]), false, false);
        z[i] = __uint_as_float(a[0]) + __uint_as_float(a[1]);
        z[i] += __int_as_float(__builtin_amdgcn_mov_dpp(__float_as_int(z[i]), 0x128, 0xF, 0xF, true));
      }
      *(LAS f32x2*)((LAS char*)smem + RED + w * 256 + (l & 7) * 32 + (l >> 4) * 8) = (f32x2){z[0], z[1]};
      BAR();
      if (w == 7) {
        const LAS float* rp = (const LAS float*)((LAS char*)smem + RED) + l;
        const float v = ((rp[0] + rp[64]) + (rp[128] + rp[192])) + ((rp[256] + rp[320]) + (rp[384] + rp[448]));
        const u32x2 gr = {__float_as_uint(v), tag};
        GST_SC1("x2", gr, lo16, sws, (u32)(mcb * 1024 + mkq * 8), WS_GRAN);
      }
    }
    STAMP(1);
    XROW(6); XROW(7);
    SFOR(n, 0, 6) KA_PIECE(n); SEND
    u32x4 gq[2];
#define GATHER(tail)                                                                                                            \
    asm volatile("buffer_load_dwordx4 %0, %2, %3, %4 offen sc1\n\tbuffer_load_dwordx4 %1, %2, %3, %5 offen sc1" tail            \
                 : "=&v"(gq[0]), "=&v"(gq[1]) : "v"((u32)t * 16), "s"(sws), "s"(WS_GRAN), "s"(WS_GRAN + 8192) : "memory")
    const int kind = w < 4 ? (w >> 1) : 2, hp = w < 4 ? (w & 1) : (w >> 1 & 1);
    u32 brd0 = (u32)(kind * 8192 + (hb * 8 + (l >> 2 & 3)) * 128 + ((hp ^ (l >> 3)) & 1) * 64 + (l >> 4 & 1) * 32 + (l & 3) * 8);
    u32 brd1 = brd0 + 65536, brd2 = brd0 + 65536 + 2 * WSLOT;
    PIN(brd1); PIN(brd2);
    bf16x8 wq[32];
#define KA_FRAG(kc) _Pragma("unroll") for (int c_ = 0; c_ < 4; ++c_) {                                                    \
      const u32 o_ = ((kc) >> 1 == 1 ? brd0 : ((kc) >> 1 == 2 ? brd2 : brd1)) + ((kc) & 1) * WSLOT + c_ * 2048; wq[(kc) * 4 + c_] = lds_tr2(smem, o_, o_ + 512); }
    if (w < 4) { SFOR(n, 6, 18) KA_PIECE(n); SEND WAITV(12); RAWBAR(); KA_FRAG(0); KA_FRAG(1); BAR(); } else WAITV(6);
    STAMP(30);
    float rs[8], mr[8];
    {
      float v[8], z[2];
      SFOR(i, 0, 8)
        PIN(xa[i]);
        v[i] = ((bf_lo(xa[i][0]) + bf_hi(xa[i][0])) + (bf_lo(xa[i][1]) + bf_hi(xa[i][1]))) + ((bf_lo(xa[i][2]) + bf_hi(xa[i][2])) + (bf_lo(xa[i][3]) + bf_hi(xa[i][3])));
      SEND
      STAMP(31);
      wave_sum8(v, z);
      z[0] *= (1.f / 512.f); z[1] *= (1.f / 512.f);
#define VAR_ROWS(B, E)                                                                                                   \
      SFOR(i, B, E)                                                                                                      \
        const float mu = ROW_OF(z, i);                                                                                   \
        float sq = 0.f;                                                                                                  \
        _Pragma("unroll") for (int e = 0; e < 4; ++e) {                                                                  \
          const float a = bf_lo(xa[i][e]) - mu, b = bf_hi(xa[i][e]) - mu; sq = __builtin_fmaf(a, a, sq); sq = __builtin_fmaf(b, b, sq); } \
        v[i] = sq;                                                                                                       \
      SEND
      VAR_ROWS(0, 8)
      float q[2];
      wave_sum8(v, q);
#pragma unroll
      for (int j = 0; j < 2; ++j) { q[j] = ln_rstd(q[j] * (1.f / 512.f)); z[j] = -bfround(z[j]) * q[j]; }
      SFOR(i, 0, 8) rs[i] = ROW_OF(q, i); mr[i] = ROW_OF(z, i); SEND
    }
    f32x8 nv[8];
#pragma unroll
    for (int i = 0; i < 8; ++i) {
#pragma unroll
      for (int e = 0; e < 4; ++e) { nv[i][2 * e] = __builtin_fmaf(bf_lo(xa[i][e]), rs[i], mr[i]); nv[i][2 * e + 1] = __builtin_fmaf(bf_hi(xa[i][e]), rs[i], mr[i]); }
      PIN(nv[i]);
    }
    if (w >= 4) { WAITV(0); RAWBAR(); KA_FRAG(0); KA_FRAG(1); BAR(); SFOR(n, 6, 18) KA_PIECE(n); SEND }
    GATHER("");
#define KB_PIECE(n, d) GLDS(wvo, sw, wsb + (u32)(((n) / 3 + rw) & 7) * (64 * 3072), ((n) % 3) * 1024 - (d) * 32 * 3072, wlb, KA_SLOT((n) / 3) + ((n) % 3) * 8192 - (d) * 4096)
    if (w >= 4) { SFOR(n, 18, 24) KB_PIECE(n, 0); KB_PIECE(n, 1); SEND WAITV(20); } else WAITV(8);
    STAMP(2);
    RAWBAR();
    KA_FRAG(2); KA_FRAG(3);
    if (w >= 4) WAITV(12); else WAITV(0);
    PIN(gq[0]); PIN(gq[1]);
    STAMP(3);
#pragma unroll 1
    for (u32 spins = 0; spins < (1u << 17); ++spins) {
      if (__all(gok(gq[0], tag) && gok(gq[1], tag))) break;
      __builtin_amdgcn_s_sleep(2);
      GATHER("\n\ts_waitcnt vmcnt(0)");
    }
    ((LAS float*)((LAS char*)smem + MTAB))[t] = bfround(1.f + bfround(gsum(gq[1])));
    ((LAS float*)((LAS char*)smem + MTAB))[512 + t] = bfround(gsum(gq[0]));
    BAR();
    STAMP(33);
    float c1[8], sh[8];
    {
      const LAS f32x4* tc = (const LAS f32x4*)((LAS char*)smem + MTAB + l * 32);
      const f32x4 a0 = tc[0], a1 = tc[1], b0 = tc[128], b1 = tc[129];
#pragma unroll
      for (int e = 0; e < 4; ++e) { c1[e] = a0[e]; c1[4 + e] = a1[e]; sh[e] = b0[e]; sh[4 + e] = b1[e]; }
    }
    u32 awr[4];
#pragma unroll
    for (int i = 0; i < 4; ++i) awr[i] = (u32)((((l >> 1) - 4 * rw) & 31) * 1024 + (l & 1) * 512 + (((i >> 1) ^ rx) & 1) * 256 + (((w * 2 + (i & 1)) ^ (l & 7)) << 4));
#define KA_PUT(i) *(LAS u32x4*)((LAS char*)smem + awr[(i) & 3] + ((i) >> 2) * 32768) = hv[i]
    u32x4 hv[8];
#pragma unroll
    for (int i = 0; i < 4; ++i) { hv[i] = modulate8(nv[i], c1, sh); KA_PUT(i); }
    const int ta = w < 4 ? 0 : (w & 1), tx = rx >> 1;
    u32 ard[4];
#pragma unroll
    for (int i = 0; i < 4; ++i) ard[i] = (u32)(ta * 32768 + i * 1024 + hb * 512 + (((l & 31) ^ (i * 2 + hb)) << 4));
#define HFRAG(n) lds_frag(smem, ard[(n) & 3] + ((n) >> 2) * 4096)
    const int head = cg * 4 + hp * 2;
    const u32 kvb = WS_KV + (u32)(head * KV_HEAD + pm * 2 * KV_TILE);
    f32x16 z16;
#pragma unroll
    for (int i = 0; i < 16; ++i) z16[i] = 0.f;
    BAR();
    STAMP(4);
    if (w < 4) {
      const float qs = kind == 0 ? 0.25f * 1.4426950408889634f : 1.f;
      const u32 so = kind == 0 ? WS_QF + (u32)(head * 64 + pm * 2) * 1024 + lo16 : kvb + (u32)((l & 51) | (l & 4) << 1 | (l & 8) >> 1) * 16;
      const u32 sh_ = kind == 0 ? 65536u : (u32)KV_HEAD, st_ = kind == 0 ? 1024u : (u32)KV_TILE;
      bf16x8 hf[64];
      f32x16 acc = z16, accp = z16;
      u32x4 ov[2];
#pragma unroll
      for (int i = 0; i < 4; ++i) hf[i] = HFRAG(i);
      SB();
      SFOR(n, 0, 76)
        if constexpr (n < 4) { hv[4 + n] = modulate8(nv[4 + n], c1, sh); KA_PUT(4 + n); }
        if constexpr (n == 5) { KA_FRAG(4); }
        if constexpr (n == 9) RAWBAR();
        if constexpr (n == 11) { KA_FRAG(5); }
        if constexpr (n == 15) { KA_FRAG(6); }
        if constexpr (n == 19) { KA_FRAG(7); }
        if constexpr (n == 32 || n == 64) { accp = acc; acc = z16; }
        if constexpr (n + 4 < 64) hf[n + 4] = HFRAG(n + 4);
        if constexpr (n < 64) acc = MFMA32(wq[n & 31], hf[n], acc);
        if constexpr (n >= 32 && (n & 31) >= 2 && (n & 31) < 10) {
          constexpr int e = (n & 31) - 2;
          ov[e >> 2][e & 3] = pack2(accp[2 * e] * qs, accp[2 * e + 1] * qs);
        }
        if constexpr (n == 42 || n == 74) GST_SC1("x4", ov[0], so, sws, st_ * ((n / 32 - 1) ^ tx), 0);
        if constexpr (n == 43 || n == 75) GST_SC1("x4", ov[1], so, sws, st_ * ((n / 32 - 1) ^ tx) + sh_, 0);
        if (n < 64) SB();
      SEND
    } else {
      bf16x8 hf[32];
      f32x16 acc = z16;
#pragma unroll
      for (int i = 4; i < 8; ++i) { hv[i] = modulate8(nv[i], c1, sh); KA_PUT(i); }
      WAITV(0);
      BAR();
#pragma unroll
      for (int i = 0; i < 4; ++i) hf[i] = HFRAG(i);
      SB();
      SFOR(n, 0, 32)
        if constexpr (n >= 2 && n < 18 && (n & 3) == 2) { KA_FRAG(4 + n / 4); }
        if constexpr (n + 4 < 32) hf[n + 4] = HFRAG(n + 4);
        acc = MFMA32(hf[n], wq[n], acc);
        SB();
      SEND
      const u32 kvt = kvb + (u32)((ta ^ tx) * KV_TILE);
      const u32 vo = kvt + (u32)((l >> 4 & 1) * KV_HEAD + 1024 + hb * 272 + (l & 15) * 16);
#pragma unroll
      for (int e = 0; e < 2; ++e) {
        const u32x4 ov = {pack2(acc[8 * e], acc[8 * e + 1]), pack2(acc[8 * e + 2], acc[8 * e + 3]), pack2(acc[8 * e + 4], acc[8 * e + 5]), pack2(acc[8 * e + 6], acc[8 * e + 7])};
        GST_SC1("x4", ov, vo, sws, zs, e * 544);
      }
      const u32x4 one = {0x3f803f80u, 0x3f803f80u, 0x3f803f80u, 0x3f803f80u};
      GST_SC1("x4", one, l < 8 ? kvt + (u32)((l >> 2) * KV_HEAD + 1024 + (l & 3) * 272 + 256) : 0x40000000u, sws, zs, 0);
    }
    STAMP(5);
    u32x4 tq[3];
    const u32 msb = WS_GRAN + (u32)(((w == 0 ? 1024 : (w == 1 ? 2048 : 2560)) + pn3 * 64) * 16);
#define TGATHER(tail)                                                                                                           \
    asm volatile("buffer_load_dwordx4 %0, %3, %5, %6 offen sc1\n\tbuffer_load_dwordx4 %1, %3, %5, %7 offen sc1\n\t"             \
                 "buffer_load_dwordx4 %2, %4, %5, %8 offen sc1" tail                                                            \
                 : "=&v"(tq[0]), "=&v"(tq[1]), "=&v"(tq[2])                                                                     \
                 : "v"((u32)t * 16), "v"(lo16), "s"(sws), "s"(WS_GRAN + 1536 * 16), "s"(WS_GRAN + 2048 * 16), "s"(msb) : "memory")
    TGATHER("");
    WAITV(3);
    STAMP(6);
    BAR();
    signal_cnt(ws, cg);
    WAITV(0);
#pragma unroll
    for (int i = 0; i < 3; ++i) PIN(tq[i]);
#pragma unroll 1
    for (u32 spins = 0; spins < (1u << 17); ++spins) {
      if (__all(gok(tq[0], tag) && gok(tq[1], tag) && gok(tq[2], tag))) break;
      __builtin_amdgcn_s_sleep(2);
      TGATHER("\n\ts_waitcnt vmcnt(0)");
    }
    *(LAS u16*)((LAS char*)smem + TAB + t * 2) = (u16)(__float_as_uint(bfround(1.f + bfround(gsum(tq[1])))) >> 16);
    *(LAS u16*)((LAS char*)smem + TAB + 1024 + t * 2) = (u16)(__float_as_uint(bfround(gsum(tq[0]))) >> 16);
    const float mv_ = bfround(gsum(tq[2]));
    ((LAS float*)((LAS char*)smem + MT))[t] = (w == 1) ? bfround(1.f + mv_) : mv_;
  }

  const int lq = loc >= 28 ? 27 : loc;
  const int pan4 = (xcd >> 1) * 4 + lq / 7, pn4 = (xcd & 1) * 7 + lq % 7;
  constexpr u32 STG = KV_HEAD;
  f32x16 zero;
#pragma unroll
  for (int i = 0; i < 16; ++i) zero[i] = 0.f;
  bf16x8 wf[32];

  {
    const char* wsrc = (const char*)((l & 2) ? wu : wg) + (size_t)(l >> 2) * 3584 + (pn4 * 128 + w * 16 + (l & 1) * 8) * 2;
    const u32 stg_lb = lb + w * 3072;
    u32 stg_rd0 = (u32)(w * 3072 + (hb * 8 + (l >> 2 & 3)) * 64 + (l >> 4 & 1) * 32 + (l & 3) * 8), stg_rd1 = stg_rd0 + STG;
    PIN(stg_rd1);
#define WREAD(p)                                                                                        \
    do {                                                                                                \
      if constexpr ((p) >= 5) {                                                                         \
        if ((p) <= 32) WAITV(4); else if ((p) == 33) WAITV(3); else if ((p) == 34) WAITV(2); else if ((p) == 35) WAITV(1); else WAITV(0); \
        const u32 o_ = ((((p) - 5) % 6) < 3 ? stg_rd1 : stg_rd0) + (((p) - 5) % 3) * 1024;              \
        wf[(p) - 5] = lds_tr2(smem, o_, o_ + 256);                                                      \
      }                                                                                                 \
    } while (0)
#define WLOAD(p) do { if ((p) < 32) { GLDS64(wsrc, stg_lb, (((p) % 6) < 3 ? STG : 0u) + ((p) % 3) * 1024u); wsrc += 16 * 3584; } } while (0)
    const int head = xcd * 4 + (loc >> 3), qt = (loc & 7) * 8 + w;
    const u32 ksb = (u32)(head * KV_HEAD + w * 1024), wlb = lb + w * 1024;
    const int r = l & 31;
    u32 krd = lo16;
    u32 vrd = (u32)(1024 + hb * 272 + (r < 16 ? r * 16 : (r == 16 ? 256 : 0x40000)));
    STAMP(7);
    wait_cnt(ws, xcd, 32, w);
    BAR();
    STAMP(8);
#define KVR(r) GLDS_SC1(lo16, sws, ksb, WS_KV + (r) * 8192, wlb, (r) * 8192)
    u32x4 qraw;
    GLD128_SC1(qraw, lo16, sws, (u32)(head * 64 + qt) * 1024, WS_QF);
    KVR(0); KVR(1);
    if (w < 4) KVR(16);
    KVR(2); KVR(3);
    WAITV(2);
    PIN(qraw);
    RAWBAR();
    STAMP(9);
    const bf16x8 qf = __builtin_bit_cast(bf16x8, qraw);
    float m;
    bf16x8 kf = lds_frag(smem, krd);
    {
      f32x16 s0 = MFMA32(kf, qf, zero);
      m = s0[0];
#pragma unroll
      for (int i = 1; i < 16; ++i) m = fmaxf(m, s0[i]);
      const auto a = __builtin_amdgcn_permlane32_swap(__float_as_uint(m), __float_as_uint(m), false, false);
      m = fmaxf(__uint_as_float(a[0]), __uint_as_float(a[1]));
    }
    f32x16 negm;
#pragma unroll
    for (int i = 0; i < 16; ++i) negm[i] = -m;
    f32x16 o = zero;
    f32x16 sc = MFMA32(kf, qf, negm);
    kf = lds_frag(smem, krd + KV_TILE);
    bf16x8 v0 = lds_frag(smem, vrd), v1;
    SFOR(j, 0, 64)
      if constexpr ((j & 1) == 0) __builtin_amdgcn_s_setprio(3 - (j >> 1 & 3));
      if (j == 5) { WAITV(10); RAWBAR(); }
      if (j == 13) { WAITV(0); RAWBAR(); }
      if (j == 14) STAMP(10);
      constexpr bool wst = j >= 14 && j < 63 && (j - 14) % 4 != 3;
      constexpr int wp = (j - 14) - (j - 14) / 4;
      if (j == 28 || j == 56) { krd += 28 * KV_TILE; vrd += 28 * KV_TILE; PIN(krd); PIN(vrd); }
      const u32 jo = (u32)(j % 28) * KV_TILE;
#define EXPS(a, b) _Pragma("unroll") for (int i = (a); i < (b); ++i) sc[i] = __builtin_amdgcn_exp2f(sc[i])
      f32x16 sn;
      v0 = lds_frag(smem, vrd + jo); v1 = lds_frag(smem, vrd + jo + 544);
      if (j < 63) sn = MFMA32(kf, qf, negm);
      if (j < 62) kf = lds_frag(smem, krd + jo + 2 * KV_TILE);
      SB();
      EXPS(0, E1);
      SB();
      o = SMF(v0, LO8(sc), o);
      if constexpr (wst) { WREAD(wp); WLOAD(wp); }
      if constexpr (j < 6) { KVR(4 + 2 * j); KVR(5 + 2 * j); }
      SB();
      EXPS(E1, 16);
      o = SMF(v1, HI8(sc), o);
      SB();
      if (j < 63) sc = sn;
    SEND
    float lsum = __uint_as_float(__builtin_amdgcn_permlane32_swap(__float_as_uint(o[8]), __float_as_uint(o[8]), false, false)[0]);
    bool ok = lsum < 1e18f;
#pragma unroll
    for (int i = 0; i < 8; ++i) ok = ok && (fabsf(o[i]) < 3e38f);
    if (!__all(ok)) {
      asm volatile("" ::: "memory");
      const __amdgpu_buffer_rsrc_t rk = __builtin_amdgcn_make_buffer_rsrc(ws + WS_KV + (size_t)head * KV_HEAD, 0, KV_HEAD, 0x20000);
      const int vg = 1024 + hb * 272 + (r == 16 ? 256 : (r & 15) * 16);
#define KVLD(off, j) __builtin_bit_cast(bf16x8, __builtin_amdgcn_raw_buffer_load_b128(rk, (off), (j) * KV_TILE, 16))
#pragma unroll 1
      for (int j = 0; j < 64; ++j) {
        const f32x16 sv = MFMA32(KVLD(lo16, j), qf, zero);
#pragma unroll
        for (int i = 0; i < 16; ++i) m = fmaxf(m, sv[i]);
      }
      m = fmaxf(m, __shfl_xor(m, 32));
#pragma unroll
      for (int i = 0; i < 16; ++i) negm[i] = -m;
      o = zero;
#pragma unroll 1
      for (int j = 0; j < 64; ++j) {
        f32x16 sv = MFMA32(KVLD(lo16, j), qf, negm);
        const bf16x8 v0 = KVLD(vg, j), v1 = KVLD(vg + 544, j);
#pragma unroll
        for (int i = 0; i < 16; ++i) sv[i] = __builtin_amdgcn_exp2f(sv[i]);
        o = SMF(v0, LO8(sv), o);
        o = SMF(v1, HI8(sv), o);
      }
      lsum = shfl_idx(o[8], r);
    }
    const float inv = 1.f / lsum;
    u32x4 ov;
#pragma unroll
    for (int e = 0; e < 4; ++e) ov[e] = pack2(o[2 * e] * inv, o[2 * e + 1] * inv);
    GST_SC1("x4", ov, lo16, sws, (u32)(qt * 32 + head) * 1024, WS_OF);
    STAMP(11);
    WAITV(0);
    BAR();
    signal_cnt(ws, 8 + (loc & 7));
    STAMP(12);
  }

  const int tm = w & 1;
  const u32 wvo = (u32)((l >> 3) * 1024 + ((((l >> 2) ^ (l >> 4)) & 1) * 32 + (l & 3) * 8) * 2);
  const u32 wrd0 = (u32)((hb * 4 + (l >> 2 & 3)) * 128 + (l >> 3 & 1) * 64 + (l >> 4 & 1) * 32 + (l & 3) * 8);
  const int rte = xcd * 8 + (pm3 >> 1) * 4 + (pm3 & 1) + (w >> 2) * 2;
  const int erow = w * 8 + (l & 7), ecol = l >> 3;
  f32x16 uv = zero;
  const u32 trd = TAB + (u32)((l >> 4 & 1) * 1024 + hb * 16);
#define UV_PART(k0)                                                                                          \
  do { bf16x8 tf_[16];                                                                                       \
    _Pragma("unroll") for (int i_ = 0; i_ < 4; ++i_) tf_[i_] = lds_frag(smem, trd + ((k0) + i_) * 32);       \
    _Pragma("unroll") for (int i_ = 0; i_ < 16; ++i_) {                                                      \
      if (i_ + 4 < 16) tf_[i_ + 4] = lds_frag(smem, trd + ((k0) + i_ + 4) * 32);                             \
      uv = MFMA32(wf[(k0) + i_], tf_[i_], uv); SB(); }                                                       \
    PIN(uv); } while (0)

  {
    const u32x4 so = make_srd(wo, 512 * 1024);
    const int ro = pn3 & 3;
    SFOR(j, 0, 8) GLDS(wvo, so, (u32)(w * 8192 + pn3 * 128 + ((j + 2 * ro) & 7) * 65536), 0, lb + w * 1024, j * 8192); SEND
    u32x4 xr;
    GLD128(xr, (u32)((erow & 31) * 1024 + ecol * 16), sx, (u32)(rte * 32768 + pn3 * 128), 0);
    UV_PART(0);
    WAITV(0);
    PIN(xr);
    STAMP(13);
    wait_cnt(ws, 8 + xcd, 32, w);
    RAWBAR();
    STAMP(14);
    if (bid == 0 && t == 0) __hip_atomic_store((u32*)(ws + WS_EPOCH), tag, __ATOMIC_RELAXED, __HIP_MEMORY_SCOPE_AGENT);
    const u32 olo = (u32)((w >> 2) * 32768 + (w & 3) * 2048);
    const u32 osb = (u32)(rte * 32768 + (w & 3) * 2048), olb = lb + olo;
    SFOR(j, 0, 8) GLDS_SC1(lo16, sws, osb + (u32)(((j >> 1) + ro) & 3) * 8192, WS_OF + (j & 1) * 1024, olb, 65536 + (j >> 1) * 8192 + (j & 1) * 1024); SEND
    const int tn = w >> 1 & 1, kh = w >> 2;
    u32 ord = 65536 + (u32)(tm * 32768 + kh * 4096) + lo16;
    PIN(ord);
    const u32 wrd = (wrd0 ^ (u32)(tn * 64)) + kh * 8192;
    f32x16 acc = zero;
    bf16x8 wq[16];
#define WOFRAG(n) wq[n] = lds_tr2(smem, wrd + ((n) >> 2) * 16384 + ((n) & 3) * 2048, wrd + ((n) >> 2) * 16384 + ((n) & 3) * 2048 + 1024)
#pragma unroll
    for (int n = 0; n < 8; ++n) WOFRAG(n);
    SFOR(s, 0, 4)
      if (s == 0) WAITV(6); else if (s == 1) WAITV(4); else if (s == 2) WAITV(2); else WAITV(0);
      RAWBAR();
      bf16x8 oq[4];
#pragma unroll
      for (int j = 0; j < 4; ++j) oq[j] = lds_frag(smem, ord + s * 8192 + j * 1024);
      if constexpr (s < 2) {
#pragma unroll
        for (int j = 0; j < 4; ++j) WOFRAG(8 + s * 4 + j);
      }
      SB();
#pragma unroll
      for (int j = 0; j < 4; ++j) acc = MFMA32(wq[s * 4 + j], oq[j], acc);
      SB();
    SEND
    STAMP(15);
    {
      LAS char* eb = (LAS char*)smem + kh * 17408 + (tm * 32 + (l & 31)) * 272 + (tn * 32 + hb * 4) * 4;
#pragma unroll
      for (int e = 0; e < 4; ++e) *(LAS f32x4*)(eb + e * 32) = (f32x4){acc[4 * e], acc[4 * e + 1], acc[4 * e + 2], acc[4 * e + 3]};
    }
    f32x4 ga[2], cb[2];
#pragma unroll
    for (int e = 0; e < 2; ++e) { ga[e] = *(const LAS f32x4*)((LAS char*)smem + MT + ecol * 32 + e * 16); cb[e] = *(const LAS f32x4*)((LAS char*)smem + MT + 256 + ecol * 32 + e * 16); }
    BAR();
    float xv[8];
    u32x4 x1p, yp;
#pragma unroll
    for (int e = 0; e < 2; ++e) {
      const LAS char* eb = (const LAS char*)smem + erow * 272 + ecol * 32 + e * 16;
      const f32x4 a = *(const LAS f32x4*)eb + *(const LAS f32x4*)(eb + 17408);
#pragma unroll
      for (int h = 0; h < 2; ++h) {
        const u32 pk = pack2(bf_lo(xr[2 * e + h]) + ga[e][2 * h] * a[2 * h], bf_hi(xr[2 * e + h]) + ga[e][2 * h + 1] * a[2 * h + 1]);
        x1p[2 * e + h] = pk;
        xv[4 * e + 2 * h] = bf_lo(pk); xv[4 * e + 2 * h + 1] = bf_hi(pk);
        yp[2 * e + h] = pack2(bf_lo(pk) * cb[e][2 * h], bf_hi(pk) * cb[e][2 * h + 1]);
      }
    }
    GST_SC1("x4", yp, (u32)((ecol >> 1) * 1024 + ((ecol & 1) * 32 + (erow & 31)) * 16), sws, (u32)(rte * 32 + pn3 * 4) * 1024, WS_Y);
    float sum = ((xv[0] + xv[1]) + (xv[2] + xv[3])) + ((xv[4] + xv[5]) + (xv[6] + xv[7]));
    const float mean = col8_sum(sum) * (1.f / 64.f);
    float sq = 0.f;
#pragma unroll
    for (int i = 0; i < 8; ++i) { const float d = xv[i] - mean; sq = __builtin_fmaf(d, d, sq); }
    const f32x2 o2 = {mean, col8_sum(sq)};
    GST_SC1("x2", o2, l < 8 ? (u32)((erow & 31) * 8) : 0x40000000u, sws, (u32)(rte * 2048 + pn3 * 256), WS_ST2);
    STAMP(16);
    WAITV(0);
    RAWBAR();
    signal_cnt(ws, 16 + xcd * 2 + (pm3 >> 1));
    STAMP(17);
    GST_SC1("x4", x1p, (u32)(erow * 8 + ecol) * 16, sws, (u32)bid * 8192, WS_X1);
  }

  const u32x4 sd = make_srd(wd, 1792 * 1024);
  const u32 dsb = (u32)(w * 16384 + pn3 * 128), dlb = lb + w * 2048;
  const int r5 = (pn3 * 7) >> 2;
#define CH5(c) (u32)((c) + r5 >= 14 ? (c) + r5 - 14 : (c) + r5)
#define WDPIECE(c, slot, h) GLDS(wvo, sd, dsb + CH5(c) * 131072, (h) * 8192, dlb, (slot) * 16384 + (h) * 1024)
#define WDISSUE(c, slot) do { WDPIECE(c, slot, 0); WDPIECE(c, slot, 1); } while (0)
  const int kq = w & 3;
  const u32 wr5 = wrd0 + kq * 4096;
  bf16x8 dq[14][4];
#define DFRAG1(c, i) dq[c][i] = lds_tr2(smem, (wr5 ^ (((i) & 1) * 64u)) + (c) * 16384 + ((i) >> 1) * 2048, (wr5 ^ (((i) & 1) * 64u)) + (c) * 16384 + ((i) >> 1) * 2048 + 1024)
  if (loc < 28) {
    constexpr u32 RST4 = 157696;
    UV_PART(16);
    float uu[16], vv[16];
#pragma unroll
    for (int i = 0; i < 16; ++i) {
      const auto r = __builtin_amdgcn_permlane16_swap(__float_as_uint(uv[i]), __float_as_uint(uv[i]), false, false);
      uu[i] = __uint_as_float(r[0]); vv[i] = __uint_as_float(r[1]);
      PIN(uu[i]); PIN(vv[i]);
    }
    wait_cnt(ws, 16 + pan4, 16, w);
    RAWBAR();
    STAMP(18);
    const int rt4 = pan4 * 4;
    const u32 ysb = (u32)(rt4 * 32768 + w * 1024), ylb = lb + w * 1024;
#define YPIECE(i) GLDS_SC1(lo16, sws, ysb, WS_Y + (i) * 8192, ylb, (i) * 8192)
    YPIECE(0); YPIECE(1); YPIECE(2); YPIECE(3);
    GLDS_SC1(lo16, sws, (u32)(rt4 * 2048 + w * 1024), WS_ST2, ylb, 131072);
    YPIECE(4); YPIECE(5);
    u32 yrd0 = lo16, yrd1 = lo16 + 65536;
    PIN(yrd1);
    const u32 asb = (u32)(rt4 * 112 + pn4 * 8 + w) * 1024;
#define YFRAG(n) lds_frag(smem, ((n) < 64 ? yrd0 : yrd1) + ((n) & 63) * 1024)
    WAITV(5);
    RAWBAR();
    STAMP(19);
    bf16x8 yf[128];
#pragma unroll
    for (int n = 0; n < 4; ++n) yf[n] = YFRAG(n);
    SB();
#define signal_early()                                                                                               \
    do { unsigned long long ex_;                                                                                     \
      asm volatile("s_cmp_eq_u32 %5, 0\n\ts_cbranch_scc0 1f\n\ts_mov_b64 %0, exec\n\ts_mov_b64 exec, 1\n\t"          \
                   "buffer_atomic_add %1, %2, %3, %4 offen\n\ts_mov_b64 exec, %0\n1:"                                \
                   : "=&s"(ex_) : "v"(1u), "v"(0u), "s"(sws), "s"(WS_CNT + (u32)(48 + pan4) * 256), "s"(w) : "memory", "scc"); } while (0)
    f32x16 acc = zero, accp = zero;
    f32x2 stp = {0.f, 0.f};
    float eg[8], eu[8], et[8];
    u32x4 ov;
    SFOR(n, 0, 160)
      constexpr int a = n >> 5, ks = n & 31;
      if (n == 10) { WAITV(7); RAWBAR(); }
      if (n == 26) { WAITV(8); BAR(); }
      if (n == 56) { WAITV(4); RAWBAR(); }
      if (n == 100) WAITV(3);
      if (n == 121) { WAITV(4); RAWBAR(); signal_early(); }
      if constexpr (n < 20 && n % 2 == 0) YPIECE(6 + n / 2);
      if (n == 36 || n == 68 || n == 100) RAWBAR();
      if constexpr (n == 60) {
        const int rr = pm3 * 224 + w * 28 + (l < 28 ? l : 27);
        const int r2 = rr + (rr < 256 ? 768 : 896) + r5 * 128;
        GTOUCH((u32)(r2 >= 1792 ? r2 - 1792 : r2) * 1024, sd, (u32)(pn3 * 128), 0, lb + SCR);
      }
      if constexpr (n >= 36 && n < 52 && n % 4 == 0) WDPIECE((n - 36) / 8, (n - 36) / 8, (n - 36) / 4 & 1);
      if constexpr (n >= 68 && n < 84 && n % 4 == 0) WDPIECE(2 + (n - 68) / 8, 2 + (n - 68) / 8, (n - 68) / 4 & 1);
      if constexpr (n == 84 || n == 88) WDPIECE(8, 8, (n - 84) / 4);
      if constexpr (n >= 100 && n < 116 && n % 4 == 0) WDPIECE(4 + (n - 100) / 8, 4 + (n - 100) / 8, (n - 100) / 4 & 1);
      if constexpr (n >= 100 && n < 116) DFRAG1((n - 100) >> 2, (n - 100) & 3);
      if constexpr (n >= 121 && n < 129) WDPIECE(10 + (n - 121) / 2, (n - 121) / 2, n & 1);
      if constexpr (n == 12) {
        const LAS f32x2* sp = (const LAS f32x2*)((LAS char*)smem + 131072 + (t >> 7) * 2048 + (t & 3) * 512 + (t >> 2 & 31) * 8);
        const f32x2 p0 = sp[0], p1 = sp[32];
        float mu = p0[0] + p1[0];
        mu += dpp_xor1(mu); mu += dpp_xor2(mu);
        mu *= 0.125f;
        float q = p0[1] + p1[1] + 64.f * ((p0[0] - mu) * (p0[0] - mu) + (p1[0] - mu) * (p1[0] - mu));
        q += dpp_xor1(q); q += dpp_xor2(q);
        const float rs = __builtin_amdgcn_rsqf(q * (1.f / 512.f) + 1e-5f);
        ((LAS f32x2*)((LAS char*)smem + RST4))[t >> 2] = (f32x2){rs, -mu * rs};
      }
      if (ks == 28 && a < 4) stp = ((const LAS f32x2*)((LAS char*)smem + RST4))[a * 32 + (l & 31)];
      if (ks == 0 && a > 0) { accp = acc; acc = zero; }
      if (n == 128) STAMP(20);
      if constexpr (n + 4 < 128) yf[n + 4] = YFRAG(n + 4);
      if constexpr (n < 128) acc = MFMA32(wf[ks], yf[n], acc);
      if constexpr (a > 0 && ks >= 2 && ks < 26) {
        constexpr int i = (ks - 2) / 3, ph = (ks - 2) % 3;
        if (ph == 0) {
          eg[i] = __builtin_fmaf(accp[i], stp[0], __builtin_fmaf(uu[i], stp[1], vv[i]));
          eu[i] = __builtin_fmaf(accp[8 + i], stp[0], __builtin_fmaf(uu[8 + i], stp[1], vv[8 + i]));
          et[i] = -1.4426950408889634f * eg[i];
          if (a < 4) { PIN(eg[i]); PIN(eu[i]); PIN(et[i]); }
        } else if (ph == 1) {
          et[i] = 1.f + __builtin_amdgcn_exp2f(et[i]);
          eg[i] *= eu[i];
          if (a < 4) { PIN(eg[i]); PIN(et[i]); }
        } else {
          eg[i] *= __builtin_amdgcn_rcpf(et[i]);
          if (a < 4) PIN(eg[i]);
          if constexpr (i & 1) ov[i >> 1] = pack2(eg[i - 1], eg[i]);
        }
      }
      if constexpr (a > 0 && ks == 26) GST_SC1("x4", ov, lo16, sws, asb, WS_ACT + (a - 1) * 112 * 1024);
      if (n < 128) SB();
    SEND
    STAMP(21);
  } else {
    WDISSUE(0, 0); WDISSUE(1, 1); WDISSUE(2, 2); WDISSUE(3, 3); WDISSUE(8, 8); WDISSUE(4, 4); WDISSUE(5, 5);
    WAITV(0);
    RAWBAR();
    SFOR(n, 0, 16) DFRAG1(n >> 2, n & 3); SEND
    BAR();
    WDISSUE(10, 0); WDISSUE(11, 1); WDISSUE(12, 2); WDISSUE(13, 3);
  }

  {
    u32 wra[3][2];
#pragma unroll
    for (int i = 0; i < 3; ++i) { wra[i][0] = wr5 + i * 65536; wra[i][1] = wra[i][0] ^ 64u; PIN(wra[i][0]); PIN(wra[i][1]); }
#define DFRAG(c) _Pragma("unroll") for (int i_ = 0; i_ < 4; ++i_) {                                              \
      const u32 o_ = wra[((c) % 10) >> 2][i_ & 1] + (((c) % 10) & 3) * 16384 + (i_ >> 1) * 2048; dq[c][i_] = lds_tr2(smem, o_, o_ + 1024); }
    f32x4 gb[2];
#pragma unroll
    for (int e = 0; e < 2; ++e) gb[e] = *(const LAS f32x4*)((LAS char*)smem + MT + 512 + (l & 7) * 32 + e * 16);
    const int pan5 = xcd * 2 + (pm3 >> 1);
    wait_cnt(ws, 48 + pan5, 14, w);
    WAITV(0);
    BAR();
    if (loc < 28) signal_cnt(ws, 32 + pan4);
    STAMP(22);
#define WD2(c, slot) do { WDISSUE(c, slot); WDPIECE(c, slot, -8); WDPIECE(c, slot, -7); } while (0)
#define BAR_IF(lo) asm volatile("s_cmp_lt_u32 %0, 4\n\ts_cbranch_scc" lo " 1f\n\ts_barrier\n1:" :: "s"(w) : "memory", "scc")
    if (w >= 4) {
      WD2(6, 6); WD2(7, 7); WD2(9, 9);
      WAITV(0);
      RAWBAR();
      wait_cnt(ws, 32 + pan5, 14, 0);
    }
    STAMP(23);
    const u32 asb = (u32)(rte * 112 + kq * 2) * 1024;
    u32x4 af[14][2];
#define AISSUE(c) do { GLD128_SC1(af[c][0], lo16, sws, asb + CH5(c) * 8192, WS_ACT); GLD128_SC1(af[c][1], lo16, sws, asb + CH5(c) * 8192, WS_ACT + 1024); } while (0)
    SFOR(c, 0, AWIN) AISSUE(c); SEND
    f32x16 acc[2] = {zero, zero};
    u32x4 x1p;
    SFOR(c, 0, 14)
      constexpr int young = c + AWIN <= 14 ? 2 * (AWIN - 1) : (c < 13 ? 2 * (13 - c) + 1 : 0);
      asm volatile("s_waitcnt vmcnt(%0)" :: "i"(young) : "memory");
      if (c == 0) STAMP(24);
      if (c == 5) STAMP(37);
      if (c == 10) STAMP(38);
      if (c == 2) BAR_IF("0");
      if (c == 10) BAR_IF("1");
      if constexpr (c + AWIN < 14) AISSUE(c + AWIN);
      if (c + AWIN == 14) GLD128_SC1(x1p, (u32)t * 16, sws, (u32)bid * 8192, WS_X1);
      if constexpr (c >= 3 && c < 13) { DFRAG(c + 1); }
      PIN(af[c][0]); PIN(af[c][1]);
      SB();
#pragma unroll
      for (int i = 0; i < 4; ++i) acc[i & 1] = MFMA32(dq[c][i], __builtin_bit_cast(bf16x8, af[c][i >> 1]), acc[i & 1]);
      SB();
    SEND
    STAMP(25);
    BAR_IF("0");
    {
      LAS char* eb = (LAS char*)smem + 65536 + kq * 17408 + ((w >> 2) * 32 + (l & 31)) * 272 + hb * 16;
#pragma unroll
      for (int tn = 0; tn < 2; ++tn)
#pragma unroll
        for (int e = 0; e < 4; ++e)
          *(LAS f32x4*)(eb + tn * 128 + e * 32) = (f32x4){acc[tn][4 * e], acc[tn][4 * e + 1], acc[tn][4 * e + 2], acc[tn][4 * e + 3]};
    }
    STAMP(40);
    BAR();
    STAMP(29);
    PIN(x1p);
    u32x4 ov;
#pragma unroll
    for (int e = 0; e < 2; ++e) {
      const LAS char* eb = (const LAS char*)smem + 65536 + (t >> 3) * 272 + (l & 7) * 32 + e * 16;
      const f32x4 s = (*(const LAS f32x4*)eb + *(const LAS f32x4*)(eb + 17408)) + (*(const LAS f32x4*)(eb + 2 * 17408) + *(const LAS f32x4*)(eb + 3 * 17408));
      ov[2 * e] = pack2(bf_lo(x1p[2 * e]) + gb[e][0] * s[0], bf_hi(x1p[2 * e]) + gb[e][1] * s[1]);
      ov[2 * e + 1] = pack2(bf_lo(x1p[2 * e + 1]) + gb[e][2] * s[2], bf_hi(x1p[2 * e + 1]) + gb[e][3] * s[3]);
    }
    *(u32x4*)((char*)out + (size_t)(rte * 32 + (t >> 3 & 31)) * 1024 + pn3 * 128 + (l & 7) * 16) = ov;
  }
  ST_OUT(2, ws);
  asm volatile(".Lcode_end:");
}

extern "C" void kernel_launch(void* const* d_in, const int* in_sizes, int n_in, void* d_out, int out_size, void* d_ws, size_t ws_size,
                              hipStream_t stream) {
  adaln_block<<<256, 512, 0, stream>>>((const u16*)d_in[0], (const u16*)d_in[1], (const u16*)d_in[2], (const u16*)d_in[4], (const u16*)d_in[5],
                                       (const u16*)d_in[6], (const u16*)d_in[7], (const u16*)d_in[8], (char*)d_ws, (u16*)d_out);
}
```

```cpp
#include <hip/hip_runtime.h>
#include <hip/hip_bf16.h>

typedef unsigned short u16;
typedef unsigned int u32;
typedef u32 u32x2 __attribute__((ext_vector_type(2)));
typedef u32 u32x4 __attribute__((ext_vector_type(4)));
typedef float f32x2 __attribute__((ext_vector_type(2)));
typedef float f32x4 __attribute__((ext_vector_type(4)));
typedef float f32x8 __attribute__((ext_vector_type(8)));
typedef float f32x16 __attribute__((ext_vector_type(16)));
typedef __bf16 bf16x2 __attribute__((ext_vector_type(2)));
typedef __bf16 bf16x8 __attribute__((ext_vector_type(8)));
typedef __bf16 bf16x16 __attribute__((ext_vector_type(16)));
typedef short s16x4 __attribute__((ext_vector_type(4)));
typedef short s16x8 __attribute__((ext_vector_type(8)));
#define PRIO_MODE 1
#define GATHER_AT 5
#define AORD 0
#define E1 9
#define AWIN 8
#define OUT_WT 0
#define CODE_PF 8
#define L2_WARM 0
#define LAS __attribute__((address_space(3)))
#define DI __device__ __forceinline__

constexpr u32 WS_GRAN = 0;
constexpr u32 WS_EPOCH = 0x18000;
constexpr u32 WS_CNT  = 0x20000;
constexpr u32 WS_QF   = 0x100000;
constexpr u32 WS_KV   = 0x300000;
constexpr u32 WS_OF   = 0x800000;
constexpr u32 WS_X1   = 0xA00000;
constexpr u32 WS_Y    = 0xC00000;
constexpr u32 WS_ST2  = 0xE00000;
constexpr u32 WS_ACT  = 0xF00000;
constexpr u32 WS_BYTES = 0x1600000;
constexpr int KV_TILE = 2112, KV_HEAD = 64 * KV_TILE;

template <int I> struct IC { static constexpr int value = I; };
template <int B, int E, class F> DI void static_for(F&& f) { if constexpr (B < E) { f(IC<B>{}); static_for<B + 1, E>(f); } }
#define SFOR(n, B, E) static_for<B, E>([&](auto ic_) __attribute__((always_inline)) { constexpr int n = decltype(ic_)::value;
#define SEND });

DI float bf_lo(u32 u) { return __uint_as_float(u << 16); }
DI float bf_hi(u32 u) { return __uint_as_float(u & 0xffff0000u); }
DI u32 pack2(float a, float b) { bf16x2 r = {(__bf16)a, (__bf16)b}; return __builtin_bit_cast(u32, r); }
DI float bfround(float a) { return (float)(__bf16)a; }
DI float ln_rstd(float var) { return __builtin_amdgcn_rcpf(bfround(__builtin_amdgcn_sqrtf(bfround(bfround(var) + 1e-5f)))); }
DI float xor32_sum(float v) {
  const auto r = __builtin_amdgcn_permlane32_swap(__float_as_uint(v), __float_as_uint(v), false, false);
  return __uint_as_float(r[0]) + __uint_as_float(r[1]);
}
DI float dpp_xor1(float v) { return __int_as_float(__builtin_amdgcn_mov_dpp(__float_as_int(v), 0xB1, 0xF, 0xF, true)); }
DI float dpp_xor2(float v) { return __int_as_float(__builtin_amdgcn_mov_dpp(__float_as_int(v), 0x4E, 0xF, 0xF, true)); }
DI float shfl_idx(float v, int srcLane) { return __int_as_float(__builtin_amdgcn_ds_bpermute(srcLane << 2, __float_as_int(v))); }

DI u32x4 make_srd(const void* p, u32 bytes) {
  const size_t a = (size_t)p;
  u32x4 r = {(u32)a, (u32)(a >> 32) & 0xffffu, bytes, 0x20000u};
  return r;
}
#define GLDS(voff, srd, sb, GO, lb, LO)                                                                                   \
  do { u32 tmp_;                                                                                                          \
    asm volatile("s_add_u32 m0, %4, %6\n\ts_add_u32 %0, %3, %5\n\tbuffer_load_dwordx4 %1, %2, %0 offen lds"            \
                 : "=&s"(tmp_) : "v"(voff), "s"(srd), "s"(sb), "s"(lb), "i"(GO), "i"(LO) : "memory", "scc"); } while (0)
#define GLD(W, dst, voff, srd, sb, GO)                                                                                    \
  do { u32 tmp_;                                                                                                          \
    asm volatile("s_add_u32 %1, %4, %5\n\tbuffer_load_dword" W " %0, %2, %3, %1 offen"                                   \
                 : "=v"(dst), "=&s"(tmp_) : "v"(voff), "s"(srd), "s"(sb), "i"(GO) : "memory", "scc"); } while (0)
#define GLD128(dst, voff, srd, sb, GO) GLD("x4", dst, voff, srd, sb, GO)
#define GLD64(dst, voff, srd, sb, GO) GLD("x2", dst, voff, srd, sb, GO)
#define GLD32(dst, voff, srd, sb, GO) GLD("", dst, voff, srd, sb, GO)
#define WAITV(N) asm volatile("s_waitcnt vmcnt(" #N ")" ::: "memory")
#define PIN(x) asm volatile("" : "+v"(x))
#define BAR() do { asm volatile("s_waitcnt lgkmcnt(0)" ::: "memory"); __builtin_amdgcn_s_barrier(); asm volatile("" ::: "memory"); } while (0)

#define ST_DECL
#define STAMP(i)
#define ST_OUT(kid, wsp)

DI bf16x8 lds_frag(const char* smem, u32 off) { return *(const LAS bf16x8*)((const LAS char*)smem + off); }
DI bf16x8 lds_tr2(const char* smem, u32 off0, u32 off1) {
  s16x4 a = __builtin_amdgcn_ds_read_tr16_b64_v4i16((LAS s16x4*)((LAS char*)smem + off0));
  s16x4 b = __builtin_amdgcn_ds_read_tr16_b64_v4i16((LAS s16x4*)((LAS char*)smem + off1));
  s16x8 r = __builtin_shufflevector(a, b, 0, 1, 2, 3, 4, 5, 6, 7);
  return __builtin_bit_cast(bf16x8, r);
}
DI u32 lds_base(const char* smem) { return (u32)(size_t)(const LAS char*)smem; }
#define SB() __builtin_amdgcn_sched_barrier(0)
#define RAWBAR() do { asm volatile("" ::: "memory"); __builtin_amdgcn_s_barrier(); asm volatile("" ::: "memory"); } while (0)
#define MFMA32(a, b, c) __builtin_amdgcn_mfma_f32_32x32x16_bf16((a), (b), (c), 0, 0, 0)
#define MFMA16(a, b, c) __builtin_amdgcn_mfma_f32_16x16x32_bf16((a), (b), (c), 0, 0, 0)

DI u32x4 modulate8(const f32x8 n, const float* c1, const float* sh) {
  u32x4 o;
#pragma unroll
  for (int e = 0; e < 4; ++e) o[e] = pack2(__builtin_fmaf(n[2 * e], c1[2 * e], sh[2 * e]), __builtin_fmaf(n[2 * e + 1], c1[2 * e + 1], sh[2 * e + 1]));
  return o;
}

#define GLDS_SC1(voff, srd, sb, GO, lb, LO)                                                                               \
  do { u32 tmp_;                                                                                                          \
    asm volatile("s_add_u32 m0, %4, %6\n\ts_add_u32 %0, %3, %5\n\tbuffer_load_dwordx4 %1, %2, %0 offen sc1 lds"         \
                 : "=&s"(tmp_) : "v"(voff), "s"(srd), "s"(sb), "s"(lb), "i"(GO), "i"(LO) : "memory", "scc"); } while (0)
#define GLD128_SC1(dst, voff, srd, sb, GO)                                                                                \
  do { u32 tmp_;                                                                                                          \
    asm volatile("s_add_u32 %1, %4, %5\n\tbuffer_load_dwordx4 %0, %2, %3, %1 offen sc1"                                 \
                 : "=v"(dst), "=&s"(tmp_) : "v"(voff), "s"(srd), "s"(sb), "i"(GO) : "memory", "scc"); } while (0)
#define GST_SC1(W, val, voff, srd, sb, GO)                                                                                \
  do { u32 tmp_;                                                                                                          \
    asm volatile("s_add_u32 %0, %4, %5\n\tbuffer_store_dword" W " %1, %2, %3, %0 offen sc1\n\ts_nop 1"                  \
                 : "=&s"(tmp_) : "v"(val), "v"(voff), "s"(srd), "s"(sb), "i"(GO) : "memory", "scc"); } while (0)
#define GTOUCH(voff, srd, sb, GO, lb)                                                                                     \
  do { u32 tmp_;                                                                                                          \
    asm volatile("s_mov_b32 m0, %4\n\ts_add_u32 %0, %3, %5\n\tbuffer_load_dword %1, %2, %0 offen lds"                   \
                 : "=&s"(tmp_) : "v"(voff), "s"(srd), "s"(sb), "s"(lb), "i"(GO) : "memory", "scc"); } while (0)
#define GLDS64(vaddr, lb, LO)                                                                                             \
    asm volatile("s_add_u32 m0, %1, %2\n\ts_nop 0\n\tglobal_load_lds_dwordx4 %0, off" :: "v"(vaddr), "s"(lb), "i"(LO) : "memory", "scc")

DI void wave_sum8(const float* v, float* z) {
  float u[4];
#pragma unroll
  for (int i = 0; i < 4; ++i) {
    const auto a = __builtin_amdgcn_permlane32_swap(__float_as_uint(v[i]), __float_as_uint(v[i + 4]), false, false);
    u[i] = __uint_as_float(a[0]) + __uint_as_float(a[1]);
  }
#pragma unroll
  for (int i = 0; i < 2; ++i) {
    const auto a = __builtin_amdgcn_permlane16_swap(__float_as_uint(u[i]), __float_as_uint(u[i + 2]), false, false);
    float s = __uint_as_float(a[0]) + __uint_as_float(a[1]);
    s += dpp_xor1(s); s += dpp_xor2(s);
    s += __int_as_float(__builtin_amdgcn_mov_dpp(__float_as_int(s), 0x141, 0xF, 0xF, true));
    s += __int_as_float(__builtin_amdgcn_mov_dpp(__float_as_int(s), 0x140, 0xF, 0xF, true));
    z[i] = s;
  }
}
#define ROW_OF(z, r) __int_as_float(__builtin_amdgcn_readlane(__float_as_int(z[(r) & 1]), ((r) >> 1) * 16))
DI float col8_sum(float v) {
  v += __int_as_float(__builtin_amdgcn_mov_dpp(__float_as_int(v), 0x128, 0xF, 0xF, true));
  const auto a = __builtin_amdgcn_permlane16_swap(__float_as_uint(v), __float_as_uint(v), false, false);
  return xor32_sum(__uint_as_float(a[0]) + __uint_as_float(a[1]));
}

DI void signal_cnt(char* ws, int idx) {
  if (threadIdx.x == 0) __hip_atomic_fetch_add((u32*)(ws + WS_CNT) + idx * 64, 1u, __ATOMIC_RELAXED, __HIP_MEMORY_SCOPE_AGENT);
}
DI void wait_cnt(char* ws, int idx, u32 want, int w) {
  if (w == 0) {
    u32* c = (u32*)(ws + WS_CNT) + idx * 64;
#pragma unroll 1
    for (u32 spins = 0; spins < (1u << 17); ++spins) {
      if ((u32)__builtin_amdgcn_readfirstlane(__hip_atomic_load(c, __ATOMIC_RELAXED, __HIP_MEMORY_SCOPE_AGENT)) >= want) break;
      __builtin_amdgcn_s_sleep(1);
    }
    asm volatile("" ::: "memory");
  }
}
constexpr int TOUCH0 = 2, NTOUCH = L2_WARM ? 9 : 0;
constexpr int wyoung(int p) {
  int n = (p <= 32 ? p : 32) - (p - 4);
  for (int m = (p - 5) / 3; m < p / 3; ++m) n += (m >= TOUCH0 && m < TOUCH0 + NTOUCH) ? 1 : 0;
  return n;
}
DI float gsum(const u32x4 a) { return __uint_as_float(a[0]) + __uint_as_float(a[2]); }
DI bool gok(const u32x4 a, u32 tag) { return a[1] == tag && a[3] == tag; }
#define SMF(v, p, o) __builtin_amdgcn_smfmac_f32_32x32x32_bf16((v), __builtin_bit_cast(bf16x16, (p)), (o), 0xDDDD, 0, 0)
#define LO8(s) __builtin_shufflevector(s, s, 0, 1, 2, 3, 4, 5, 6, 7)
#define HI8(s) __builtin_shufflevector(s, s, 8, 9, 10, 11, 12, 13, 14, 15)

extern "C" __global__ __launch_bounds__(512) void adaln_block(const u16* __restrict__ x, const u16* __restrict__ wqkv, const u16* __restrict__ wo,
                                                              const u16* __restrict__ wg, const u16* __restrict__ wu, const u16* __restrict__ wd,
                                                              const u16* __restrict__ c, const u16* __restrict__ wmod, char* __restrict__ ws,
                                                              u16* __restrict__ out) {
  __shared__ __attribute__((aligned(16))) char smem[163840];
  ST_DECL STAMP(0);
  const int bid = blockIdx.x, xcd = bid & 7, loc = bid >> 3;
  const int t = threadIdx.x, l = t & 63;
  const int w = __builtin_amdgcn_readfirstlane(t >> 6);
  const u32 lb = lds_base(smem);
  {
    unsigned long long pc;
    u32 len;
    asm volatile("s_getpc_b64 %0\n.Lcode_mark:\n\ts_mov_b32 %1, .Lcode_end-.Lcode_mark" : "=s"(pc), "=s"(len));
    const u32x4 scode = make_srd((const void*)pc, loc < CODE_PF ? len : 0u);
    GTOUCH((u32)(loc * 512 + t) * 128 / CODE_PF, scode, 0u, 0, lb + 57344);
    SB();
  }
  const u32x4 sws = make_srd(ws, WS_BYTES), sx = make_srd(x, 2048 * 1024);
  const u32 lo16 = (u32)l * 16, zs = 0;
  const int hb = l >> 5;
  const int pm3 = loc >> 3, pn3 = loc & 7;
  constexpr u32 TAB = 159744, MT = TAB + 2048, SCR = TAB;
  u32 tag;
  asm volatile("" :: "s"(x), "s"(wqkv), "s"(wo), "s"(wg), "s"(wu), "s"(wd), "s"(c), "s"(wmod), "s"(ws), "s"(out));

  {
    constexpr u32 WSLOT = 24576, RED = 49152, MTAB = 53248;
    const int pm = (xcd >> 1) * 8 + (loc >> 2), cg = (xcd & 1) * 4 + (loc & 3);
    const u32x4 sw = make_srd(wqkv, 512 * 3072), sm = make_srd(wmod, bid < 96 ? 512 * 6144 : 0), sc = make_srd(c, bid < 96 ? 1024 : 0);
    const int mcb = bid >> 1, mkq = bid & 1;
    u32 ev;
    u32x2 cv;
    u32x4 mv[4], xa[8];
    if (bid == 0 && w == 7) GST_SC1("", zs, (u32)l * 256, sws, zs, WS_CNT);
    asm volatile("buffer_load_dword %0, %1, %2, %3 offen sc1" : "=v"(ev) : "v"(zs), "s"(sws), "s"(WS_EPOCH) : "memory");
#pragma unroll
    for (int i = 0; i < 4; ++i) GLD128(mv[i], (u32)((t >> 3) * 4 * 6144 + (t & 7) * 16), sm, (u32)(mkq * 256 * 6144 + mcb * 128), i * 6144);
    GLD64(cv, (u32)((t >> 3) * 8), sc, (u32)(mkq * 512), 0);
    RAWBAR();
    const int rx = loc & 3, rw = loc >> 2;
    const u32 xsb = (u32)(pm * 64 + w * 2) * 1024;
#define XROW(i) GLD128(xa[i], lo16, sx, xsb + (u32)(((i) >> 1) ^ rx) * 16384, ((i) & 1) * 1024)
    const u32 wvo = (u32)((l >> 3) * 3072 + ((((l >> 2) ^ (l >> 4)) & 1) * 32 + (l & 3) * 8) * 2);
    const u32 wsb = (u32)(w * 8 * 3072 + cg * 128), wlb = lb + w * 1024;
#define KA_SLOT(kc) (((kc) >> 1 == 1 ? 0 : ((kc) >> 1 == 2 ? 65536 + 2 * WSLOT : 65536)) + ((kc) & 1) * WSLOT)
#define KA_PIECE(n) GLDS(wvo, sw, wsb + (u32)(((n) / 3 + rw) & 7) * (64 * 3072), ((n) % 3) * 1024, wlb, KA_SLOT((n) / 3) + ((n) % 3) * 8192)
    STAMP(26);
    XROW(0); XROW(1); XROW(2);
    STAMP(27);
    WAITV(3);
    STAMP(28);
    PIN(ev); PIN(cv);
#pragma unroll
    for (int i = 0; i < 4; ++i) PIN(mv[i]);
    tag = (u32)__builtin_amdgcn_readfirstlane(ev) + 1u;
    XROW(3); XROW(4); XROW(5);
    if (bid < 96) {
      float acc[8];
#pragma unroll
      for (int e = 0; e < 8; ++e) acc[e] = 0.f;
#pragma unroll
      for (int i = 0; i < 4; ++i) {
        const float cc = (i & 1) ? bf_hi(cv[i >> 1]) : bf_lo(cv[i >> 1]);
        const float sv = bfround(cc * __builtin_amdgcn_rcpf(1.f + __builtin_amdgcn_exp2f(-1.4426950408889634f * cc)));
#pragma unroll
        for (int e = 0; e < 4; ++e) { acc[2 * e] += sv * bf_lo(mv[i][e]); acc[2 * e + 1] += sv * bf_hi(mv[i][e]); }
      }
      float u[4], z[2];
#pragma unroll
      for (int i = 0; i < 4; ++i) {
        const auto a = __builtin_amdgcn_permlane32_swap(__float_as_uint(acc[i]), __float_as_uint(acc[i + 4]), false, false);
        u[i] = __uint_as_float(a[0]) + __uint_as_float(a[1]);
      }
#pragma unroll
      for (int i = 0; i < 2; ++i) {
        const auto a = __builtin_amdgcn_permlane16_swap(__float_as_uint(u[i]), __float_as_uint(u[i + 2]), false, false);
        z[i] = __uint_as_float(a[0]) + __uint_as_float(a[1]);
        z[i] += __int_as_float(__builtin_amdgcn_mov_dpp(__float_as_int(z[i]), 0x128, 0xF, 0xF, true));
      }
      *(LAS f32x2*)((LAS char*)smem + RED + w * 256 + (l & 7) * 32 + (l >> 4) * 8) = (f32x2){z[0], z[1]};
      BAR();
      if (w == 7) {
        const LAS float* rp = (const LAS float*)((LAS char*)smem + RED) + l;
        const float v = ((rp[0] + rp[64]) + (rp[128] + rp[192])) + ((rp[256] + rp[320]) + (rp[384] + rp[448]));
        const u32x2 gr = {__float_as_uint(v), tag};
        GST_SC1("x2", gr, lo16, sws, (u32)(mcb * 1024 + mkq * 8), WS_GRAN);
      }
    }
    STAMP(1);
    XROW(6); XROW(7);
    SFOR(n, 0, 6) KA_PIECE(n); SEND
    u32x4 gq[2];
#define GATHER(tail)                                                                                                            \
    asm volatile("buffer_load_dwordx4 %0, %2, %3, %4 offen sc1\n\tbuffer_load_dwordx4 %1, %2, %3, %5 offen sc1" tail            \
                 : "=&v"(gq[0]), "=&v"(gq[1]) : "v"((u32)t * 16), "s"(sws), "s"(WS_GRAN), "s"(WS_GRAN + 8192) : "memory")
    const int kind = w < 4 ? (w >> 1) : 2, hp = w < 4 ? (w & 1) : (w >> 1 & 1);
    u32 brd0 = (u32)(kind * 8192 + (hb * 8 + (l >> 2 & 3)) * 128 + ((hp ^ (l >> 3)) & 1) * 64 + (l >> 4 & 1) * 32 + (l & 3) * 8);
    u32 brd1 = brd0 + 65536, brd2 = brd0 + 65536 + 2 * WSLOT;
    PIN(brd1); PIN(brd2);
    bf16x8 wq[32];
#define KA_FRAG(kc) _Pragma("unroll") for (int c_ = 0; c_ < 4; ++c_) {                                                    \
      const u32 o_ = ((kc) >> 1 == 1 ? brd0 : ((kc) >> 1 == 2 ? brd2 : brd1)) + ((kc) & 1) * WSLOT + c_ * 2048; wq[(kc) * 4 + c_] = lds_tr2(smem, o_, o_ + 512); }
    if (w < 4) { SFOR(n, 6, 18) KA_PIECE(n); SEND WAITV(12); RAWBAR(); KA_FRAG(0); KA_FRAG(1); BAR(); } else WAITV(6);
    STAMP(30);
    float rs[8], mr[8];
    {
      float v[8], z[2];
      SFOR(i, 0, 8)
        PIN(xa[i]);
        v[i] = ((bf_lo(xa[i][0]) + bf_hi(xa[i][0])) + (bf_lo(xa[i][1]) + bf_hi(xa[i][1]))) + ((bf_lo(xa[i][2]) + bf_hi(xa[i][2])) + (bf_lo(xa[i][3]) + bf_hi(xa[i][3])));
      SEND
      STAMP(31);
      wave_sum8(v, z);
      z[0] *= (1.f / 512.f); z[1] *= (1.f / 512.f);
#define VAR_ROWS(B, E)                                                                                                   \
      SFOR(i, B, E)                                                                                                      \
        const float mu = ROW_OF(z, i);                                                                                   \
        float sq = 0.f;                                                                                                  \
        _Pragma("unroll") for (int e = 0; e < 4; ++e) {                                                                  \
          const float a = bf_lo(xa[i][e]) - mu, b = bf_hi(xa[i][e]) - mu; sq = __builtin_fmaf(a, a, sq); sq = __builtin_fmaf(b, b, sq); } \
        v[i] = sq;                                                                                                       \
      SEND
      VAR_ROWS(0, 8)
      float q[2];
      wave_sum8(v, q);
#pragma unroll
      for (int j = 0; j < 2; ++j) { q[j] = ln_rstd(q[j] * (1.f / 512.f)); z[j] = -bfround(z[j]) * q[j]; }
      SFOR(i, 0, 8) rs[i] = ROW_OF(q, i); mr[i] = ROW_OF(z, i); SEND
    }
    f32x8 nv[8];
#pragma unroll
    for (int i = 0; i < 8; ++i) {
#pragma unroll
      for (int e = 0; e < 4; ++e) { nv[i][2 * e] = __builtin_fmaf(bf_lo(xa[i][e]), rs[i], mr[i]); nv[i][2 * e + 1] = __builtin_fmaf(bf_hi(xa[i][e]), rs[i], mr[i]); }
      PIN(nv[i]);
    }
    if (w >= 4) { WAITV(0); RAWBAR(); KA_FRAG(0); KA_FRAG(1); BAR(); SFOR(n, 6, 18) KA_PIECE(n); SEND }
    GATHER("");
#define KB_PIECE(n, d) GLDS(wvo, sw, wsb + (u32)(((n) / 3 + rw) & 7) * (64 * 3072), ((n) % 3) * 1024 - (d) * 32 * 3072, wlb, KA_SLOT((n) / 3) + ((n) % 3) * 8192 - (d) * 4096)
    if (w >= 4) { SFOR(n, 18, 24) KB_PIECE(n, 0); KB_PIECE(n, 1); SEND WAITV(20); } else WAITV(8);
    STAMP(2);
    RAWBAR();
    KA_FRAG(2); KA_FRAG(3);
    if (w >= 4) WAITV(12); else WAITV(0);
    PIN(gq[0]); PIN(gq[1]);
    STAMP(3);
#pragma unroll 1
    for (u32 spins = 0; spins < (1u << 17); ++spins) {
      if (__all(gok(gq[0], tag) && gok(gq[1], tag))) break;
      __builtin_amdgcn_s_sleep(2);
      GATHER("\n\ts_waitcnt vmcnt(0)");
    }
    ((LAS float*)((LAS char*)smem + MTAB))[t] = bfround(1.f + bfround(gsum(gq[1])));
    ((LAS float*)((LAS char*)smem + MTAB))[512 + t] = bfround(gsum(gq[0]));
    BAR();
    STAMP(33);
    float c1[8], sh[8];
    {
      const LAS f32x4* tc = (const LAS f32x4*)((LAS char*)smem + MTAB + l * 32);
      const f32x4 a0 = tc[0], a1 = tc[1], b0 = tc[128], b1 = tc[129];
#pragma unroll
      for (int e = 0; e < 4; ++e) { c1[e] = a0[e]; c1[4 + e] = a1[e]; sh[e] = b0[e]; sh[4 + e] = b1[e]; }
    }
    u32 awr[4];
#pragma unroll
    for (int i = 0; i < 4; ++i) awr[i] = (u32)((((l >> 1) - 4 * rw) & 31) * 1024 + (l & 1) * 512 + (((i >> 1) ^ rx) & 1) * 256 + (((w * 2 + (i & 1)) ^ (l & 7)) << 4));
#define KA_PUT(i) *(LAS u32x4*)((LAS char*)smem + awr[(i) & 3] + ((i) >> 2) * 32768) = hv[i]
    u32x4 hv[8];
#pragma unroll
    for (int i = 0; i < 4; ++i) { hv[i] = modulate8(nv[i], c1, sh); KA_PUT(i); }
    const int ta = w < 4 ? 0 : (w & 1), tx = rx >> 1;
    u32 ard[4];
#pragma unroll
    for (int i = 0; i < 4; ++i) ard[i] = (u32)(ta * 32768 + i * 1024 + hb * 512 + (((l & 31) ^ (i * 2 + hb)) << 4));
#define HFRAG(n) lds_frag(smem, ard[(n) & 3] + ((n) >> 2) * 4096)
    const int head = cg * 4 + hp * 2;
    const u32 kvb = WS_KV + (u32)(head * KV_HEAD + pm * 2 * KV_TILE);
    f32x16 z16;
#pragma unroll
    for (int i = 0; i < 16; ++i) z16[i] = 0.f;
    BAR();
    STAMP(4);
    if (w < 4) {
      const float qs = kind == 0 ? 0.25f * 1.4426950408889634f : 1.f;
      const u32 so = kind == 0 ? WS_QF + (u32)(head * 64 + pm * 2) * 1024 + lo16 : kvb + (u32)((l & 51) | (l & 4) << 1 | (l & 8) >> 1) * 16;
      const u32 sh_ = kind == 0 ? 65536u : (u32)KV_HEAD, st_ = kind == 0 ? 1024u : (u32)KV_TILE;
      bf16x8 hf[64];
      f32x16 acc = z16, accp = z16;
      u32x4 ov[2];
#pragma unroll
      for (int i = 0; i < 4; ++i) hf[i] = HFRAG(i);
      SB();
      SFOR(n, 0, 76)
        if constexpr (n < 4) { hv[4 + n] = modulate8(nv[4 + n], c1, sh); KA_PUT(4 + n); }
        if constexpr (n == 5) { KA_FRAG(4); }
        if constexpr (n == 9) RAWBAR();
        if constexpr (n == 11) { KA_FRAG(5); }
        if constexpr (n == 15) { KA_FRAG(6); }
        if constexpr (n == 19) { KA_FRAG(7); }
        if constexpr (n == 32 || n == 64) { accp = acc; acc = z16; }
        if constexpr (n + 4 < 64) hf[n + 4] = HFRAG(n + 4);
        if constexpr (n < 64) acc = MFMA32(wq[n & 31], hf[n], acc);
        if constexpr (n >= 32 && (n & 31) >= 2 && (n & 31) < 10) {
          constexpr int e = (n & 31) - 2;
          ov[e >> 2][e & 3] = pack2(accp[2 * e] * qs, accp[2 * e + 1] * qs);
        }
        if constexpr (n == 42 || n == 74) GST_SC1("x4", ov[0], so, sws, st_ * ((n / 32 - 1) ^ tx), 0);
        if constexpr (n == 43 || n == 75) GST_SC1("x4", ov[1], so, sws, st_ * ((n / 32 - 1) ^ tx) + sh_, 0);
        if (n < 64) SB();
      SEND
    } else {
      bf16x8 hf[32];
      f32x16 acc = z16;
#pragma unroll
      for (int i = 4; i < 8; ++i) { hv[i] = modulate8(nv[i], c1, sh); KA_PUT(i); }
      WAITV(0);
      BAR();
#pragma unroll
      for (int i = 0; i < 4; ++i) hf[i] = HFRAG(i);
      SB();
      SFOR(n, 0, 32)
        if constexpr (n >= 2 && n < 18 && (n & 3) == 2) { KA_FRAG(4 + n / 4); }
        if constexpr (n + 4 < 32) hf[n + 4] = HFRAG(n + 4);
        acc = MFMA32(hf[n], wq[n], acc);
        SB();
      SEND
      const u32 kvt = kvb + (u32)((ta ^ tx) * KV_TILE);
      const u32 vo = kvt + (u32)((l >> 4 & 1) * KV_HEAD + 1024 + hb * 272 + (l & 15) * 16);
#pragma unroll
      for (int e = 0; e < 2; ++e) {
        const u32x4 ov = {pack2(acc[8 * e], acc[8 * e + 1]), pack2(acc[8 * e + 2], acc[8 * e + 3]), pack2(acc[8 * e + 4], acc[8 * e + 5]), pack2(acc[8 * e + 6], acc[8 * e + 7])};
        GST_SC1("x4", ov, vo, sws, zs, e * 544);
      }
      const u32x4 one = {0x3f803f80u, 0x3f803f80u, 0x3f803f80u, 0x3f803f80u};
      GST_SC1("x4", one, l < 8 ? kvt + (u32)((l >> 2) * KV_HEAD + 1024 + (l & 3) * 272 + 256) : 0x40000000u, sws, zs, 0);
    }
    STAMP(5);
    u32x4 tq[3];
    const u32 msb = WS_GRAN + (u32)(((w == 0 ? 1024 : (w == 1 ? 2048 : 2560)) + pn3 * 64) * 16);
#define TGATHER(tail)                                                                                                           \
    asm volatile("buffer_load_dwordx4 %0, %3, %5, %6 offen sc1\n\tbuffer_load_dwordx4 %1, %3, %5, %7 offen sc1\n\t"             \
                 "buffer_load_dwordx4 %2, %4, %5, %8 offen sc1" tail                                                            \
                 : "=&v"(tq[0]), "=&v"(tq[1]), "=&v"(tq[2])                                                                     \
                 : "v"((u32)t * 16), "v"(lo16), "s"(sws), "s"(WS_GRAN + 1536 * 16), "s"(WS_GRAN + 2048 * 16), "s"(msb) : "memory")
    TGATHER("");
    WAITV(3);
    STAMP(6);
    BAR();
    signal_cnt(ws, cg);
    WAITV(0);
#pragma unroll
    for (int i = 0; i < 3; ++i) PIN(tq[i]);
#pragma unroll 1
    for (u32 spins = 0; spins < (1u << 17); ++spins) {
      if (__all(gok(tq[0], tag) && gok(tq[1], tag) && gok(tq[2], tag))) break;
      __builtin_amdgcn_s_sleep(2);
      TGATHER("\n\ts_waitcnt vmcnt(0)");
    }
    *(LAS u16*)((LAS char*)smem + TAB + t * 2) = (u16)(__float_as_uint(bfround(1.f + bfround(gsum(tq[1])))) >> 16);
    *(LAS u16*)((LAS char*)smem + TAB + 1024 + t * 2) = (u16)(__float_as_uint(bfround(gsum(tq[0]))) >> 16);
    const float mv_ = bfround(gsum(tq[2]));
    ((LAS float*)((LAS char*)smem + MT))[t] = (w == 1) ? bfround(1.f + mv_) : mv_;
  }

  const int lq = loc >= 28 ? 27 : loc;
  const int pan4 = (xcd >> 1) * 4 + lq / 7, pn4 = (xcd & 1) * 7 + lq % 7;
  constexpr u32 STG = KV_HEAD;
  f32x16 zero;
#pragma unroll
  for (int i = 0; i < 16; ++i) zero[i] = 0.f;
  bf16x8 wf[32];
  const u32x4 so = make_srd(wo, 512 * 1024), sd = make_srd(wd, 1792 * 1024);

  {
    const char* wsrc = (const char*)((l & 2) ? wu : wg) + (size_t)(l >> 2) * 3584 + (pn4 * 128 + w * 16 + (l & 1) * 8) * 2;
    const u32 stg_lb = lb + w * 3072;
    u32 stg_rd0 = (u32)(w * 3072 + (hb * 8 + (l >> 2 & 3)) * 64 + (l >> 4 & 1) * 32 + (l & 3) * 8), stg_rd1 = stg_rd0 + STG;
    PIN(stg_rd1);
#define WREAD(p)                                                                                        \
    do {                                                                                                \
      if constexpr ((p) >= 5) {                                                                         \
        asm volatile("s_waitcnt vmcnt(%0)" :: "i"(wyoung(p)) : "memory");                               \
        const u32 o_ = ((((p) - 5) % 6) < 3 ? stg_rd1 : stg_rd0) + (((p) - 5) % 3) * 1024;              \
        wf[(p) - 5] = lds_tr2(smem, o_, o_ + 256);                                                      \
      }                                                                                                 \
    } while (0)
#define WLOAD(p) do { if ((p) < 32) { GLDS64(wsrc, stg_lb, (((p) % 6) < 3 ? STG : 0u) + ((p) % 3) * 1024u); wsrc += 16 * 3584; } } while (0)
    const int head = xcd * 4 + (loc >> 3), qt = (loc & 7) * 8 + w;
    const u32 ksb = (u32)(head * KV_HEAD + w * 1024), wlb = lb + w * 1024;
    const int r = l & 31;
    u32 krd = lo16;
    u32 vrd = (u32)(1024 + hb * 272 + (r < 16 ? r * 16 : (r == 16 ? 256 : 0x40000)));
    STAMP(7);
    wait_cnt(ws, xcd, 32, w);
    BAR();
    STAMP(8);
#define KVR(r) GLDS_SC1(lo16, sws, ksb, WS_KV + (r) * 8192, wlb, (r) * 8192)
    u32x4 qraw;
    GLD128_SC1(qraw, lo16, sws, (u32)(head * 64 + qt) * 1024, WS_QF);
    KVR(0); KVR(1);
    if (w < 4) KVR(16);
    KVR(2); KVR(3);
    WAITV(2);
    PIN(qraw);
    RAWBAR();
    STAMP(9);
    const bf16x8 qf = __builtin_bit_cast(bf16x8, qraw);
    float m;
    bf16x8 kf = lds_frag(smem, krd);
    {
      f32x16 s0 = MFMA32(kf, qf, zero);
      m = s0[0];
#pragma unroll
      for (int i = 1; i < 16; ++i) m = fmaxf(m, s0[i]);
      const auto a = __builtin_amdgcn_permlane32_swap(__float_as_uint(m), __float_as_uint(m), false, false);
      m = fmaxf(__uint_as_float(a[0]), __uint_as_float(a[1]));
    }
    f32x16 negm;
#pragma unroll
    for (int i = 0; i < 16; ++i) negm[i] = -m;
    f32x16 o = zero;
    f32x16 sc = MFMA32(kf, qf, negm);
    kf = lds_frag(smem, krd + KV_TILE);
    bf16x8 v0 = lds_frag(smem, vrd), v1;
    const u32 tvo = (u32)((l & 7) * 1024 + pn3 * 128), tsb = (u32)(pm3 * 8 + w) * 8192;
    SFOR(j, 0, 64)
      if constexpr ((j & 1) == 0) __builtin_amdgcn_s_setprio(3 - (j >> 1 & 3));
      if (j == 5) { WAITV(10); RAWBAR(); }
      if (j == 13) { WAITV(0); RAWBAR(); }
      if (j == 14) STAMP(10);
      constexpr bool wst = j >= 14 && j < 63 && (j - 14) % 4 != 3;
      constexpr int wp = (j - 14) - (j - 14) / 4;
      if (j == 28 || j == 56) { krd += 28 * KV_TILE; vrd += 28 * KV_TILE; PIN(krd); PIN(vrd); }
      const u32 jo = (u32)(j % 28) * KV_TILE;
#define EXPS(a, b) _Pragma("unroll") for (int i = (a); i < (b); ++i) sc[i] = __builtin_amdgcn_exp2f(sc[i])
      f32x16 sn;
      v0 = lds_frag(smem, vrd + jo); v1 = lds_frag(smem, vrd + jo + 544);
      if (j < 63) sn = MFMA32(kf, qf, negm);
      if (j < 62) kf = lds_frag(smem, krd + jo + 2 * KV_TILE);
      SB();
      EXPS(0, E1);
      SB();
      o = SMF(v0, LO8(sc), o);
      if constexpr (wst) { WREAD(wp); WLOAD(wp); }
      if constexpr (j >= 14 && (j - 14) % 4 == 3) {
        constexpr int m = (j - 14) / 4 - TOUCH0;
        if (m >= 0 && m < 7 && NTOUCH) GTOUCH(tvo, sd, tsb * 7, (m < 0 ? 0 : m) * 8192, lb + 25600);
        if (m >= 7 && m < NTOUCH) GTOUCH(tvo, so, tsb * 2, (m < 7 ? 0 : m - 7) * 8192, lb + 25600);
      }
      if constexpr (j < 6) { KVR(4 + 2 * j); KVR(5 + 2 * j); }
      SB();
      EXPS(E1, 16);
      o = SMF(v1, HI8(sc), o);
      SB();
      if (j < 63) sc = sn;
    SEND
    float lsum = __uint_as_float(__builtin_amdgcn_permlane32_swap(__float_as_uint(o[8]), __float_as_uint(o[8]), false, false)[0]);
    bool ok = lsum < 1e18f;
#pragma unroll
    for (int i = 0; i < 8; ++i) ok = ok && (fabsf(o[i]) < 3e38f);
    if (!__all(ok)) {
      asm volatile("" ::: "memory");
      const __amdgpu_buffer_rsrc_t rk = __builtin_amdgcn_make_buffer_rsrc(ws + WS_KV + (size_t)head * KV_HEAD, 0, KV_HEAD, 0x20000);
      const int vg = 1024 + hb * 272 + (r == 16 ? 256 : (r & 15) * 16);
#define KVLD(off, j) __builtin_bit_cast(bf16x8, __builtin_amdgcn_raw_buffer_load_b128(rk, (off), (j) * KV_TILE, 16))
#pragma unroll 1
      for (int j = 0; j < 64; ++j) {
        const f32x16 sv = MFMA32(KVLD(lo16, j), qf, zero);
#pragma unroll
        for (int i = 0; i < 16; ++i) m = fmaxf(m, sv[i]);
      }
      m = fmaxf(m, __shfl_xor(m, 32));
#pragma unroll
      for (int i = 0; i < 16; ++i) negm[i] = -m;
      o = zero;
#pragma unroll 1
      for (int j = 0; j < 64; ++j) {
        f32x16 sv = MFMA32(KVLD(lo16, j), qf, negm);
        const bf16x8 v0 = KVLD(vg, j), v1 = KVLD(vg + 544, j);
#pragma unroll
        for (int i = 0; i < 16; ++i) sv[i] = __builtin_amdgcn_exp2f(sv[i]);
        o = SMF(v0, LO8(sv), o);
        o = SMF(v1, HI8(sv), o);
      }
      lsum = shfl_idx(o[8], r);
    }
    const float inv = 1.f / lsum;
    u32x4 ov;
#pragma unroll
    for (int e = 0; e < 4; ++e) ov[e] = pack2(o[2 * e] * inv, o[2 * e + 1] * inv);
    GST_SC1("x4", ov, lo16, sws, (u32)(qt * 32 + head) * 1024, WS_OF);
    STAMP(11);
    WAITV(0);
    BAR();
    signal_cnt(ws, 8 + (loc & 7));
    STAMP(12);
  }

  const int tm = w & 1;
  const u32 wvo = (u32)((l >> 3) * 1024 + ((((l >> 2) ^ (l >> 4)) & 1) * 32 + (l & 3) * 8) * 2);
  const u32 wrd0 = (u32)((hb * 4 + (l >> 2 & 3)) * 128 + (l >> 3 & 1) * 64 + (l >> 4 & 1) * 32 + (l & 3) * 8);
  const int rte = xcd * 8 + (pm3 >> 1) * 4 + (pm3 & 1) + (w >> 2) * 2;
  const int erow = w * 8 + (l & 7), ecol = l >> 3;
  f32x16 uv = zero;
  const u32 trd = TAB + (u32)((l >> 4 & 1) * 1024 + hb * 16);
#define UV_PART(k0)                                                                                          \
  do { bf16x8 tf_[16];                                                                                       \
    _Pragma("unroll") for (int i_ = 0; i_ < 4; ++i_) tf_[i_] = lds_frag(smem, trd + ((k0) + i_) * 32);       \
    _Pragma("unroll") for (int i_ = 0; i_ < 16; ++i_) {                                                      \
      if (i_ + 4 < 16) tf_[i_ + 4] = lds_frag(smem, trd + ((k0) + i_ + 4) * 32);                             \
      uv = MFMA32(wf[(k0) + i_], tf_[i_], uv); SB(); }                                                       \
    PIN(uv); } while (0)

  {
    const int ro = pn3 & 3;
    SFOR(j, 0, 8) GLDS(wvo, so, (u32)(w * 8192 + pn3 * 128 + ((j + 2 * ro) & 7) * 65536), 0, lb + w * 1024, j * 8192); SEND
    u32x4 xr;
    GLD128(xr, (u32)((erow & 31) * 1024 + ecol * 16), sx, (u32)(rte * 32768 + pn3 * 128), 0);
    UV_PART(0);
    WAITV(0);
    PIN(xr);
    STAMP(13);
    wait_cnt(ws, 8 + xcd, 32, w);
    RAWBAR();
    STAMP(14);
    if (bid == 0 && t == 0) __hip_atomic_store((u32*)(ws + WS_EPOCH), tag, __ATOMIC_RELAXED, __HIP_MEMORY_SCOPE_AGENT);
    const u32 olo = (u32)((w >> 2) * 32768 + (w & 3) * 2048);
    const u32 osb = (u32)(rte * 32768 + (w & 3) * 2048), olb = lb + olo;
    SFOR(j, 0, 8) GLDS_SC1(lo16, sws, osb + (u32)(((j >> 1) + ro) & 3) * 8192, WS_OF + (j & 1) * 1024, olb, 65536 + (j >> 1) * 8192 + (j & 1) * 1024); SEND
    const int tn = w >> 1 & 1, kh = w >> 2;
    u32 ord = 65536 + (u32)(tm * 32768 + kh * 4096) + lo16;
    PIN(ord);
    const u32 wrd = (wrd0 ^ (u32)(tn * 64)) + kh * 8192;
    f32x16 acc = zero;
    bf16x8 wq[16];
#define WOFRAG(n) wq[n] = lds_tr2(smem, wrd + ((n) >> 2) * 16384 + ((n) & 3) * 2048, wrd + ((n) >> 2) * 16384 + ((n) & 3) * 2048 + 1024)
#pragma unroll
    for (int n = 0; n < 8; ++n) WOFRAG(n);
    SFOR(s, 0, 4)
      if (s == 0) WAITV(6); else if (s == 1) WAITV(4); else if (s == 2) WAITV(2); else WAITV(0);
      RAWBAR();
      bf16x8 oq[4];
#pragma unroll
      for (int j = 0; j < 4; ++j) oq[j] = lds_frag(smem, ord + s * 8192 + j * 1024);
      if constexpr (s < 2) {
#pragma unroll
        for (int j = 0; j < 4; ++j) WOFRAG(8 + s * 4 + j);
      }
      SB();
#pragma unroll
      for (int j = 0; j < 4; ++j) acc = MFMA32(wq[s * 4 + j], oq[j], acc);
      SB();
    SEND
    STAMP(15);
    {
      LAS char* eb = (LAS char*)smem + kh * 17408 + (tm * 32 + (l & 31)) * 272 + (tn * 32 + hb * 4) * 4;
#pragma unroll
      for (int e = 0; e < 4; ++e) *(LAS f32x4*)(eb + e * 32) = (f32x4){acc[4 * e], acc[4 * e + 1], acc[4 * e + 2], acc[4 * e + 3]};
    }
    f32x4 ga[2], cb[2];
#pragma unroll
    for (int e = 0; e < 2; ++e) { ga[e] = *(const LAS f32x4*)((LAS char*)smem + MT + ecol * 32 + e * 16); cb[e] = *(const LAS f32x4*)((LAS char*)smem + MT + 256 + ecol * 32 + e * 16); }
    BAR();
    float xv[8];
    u32x4 x1p, yp;
#pragma unroll
    for (int e = 0; e < 2; ++e) {
      const LAS char* eb = (const LAS char*)smem + erow * 272 + ecol * 32 + e * 16;
      const f32x4 a = *(const LAS f32x4*)eb + *(const LAS f32x4*)(eb + 17408);
#pragma unroll
      for (int h = 0; h < 2; ++h) {
        const u32 pk = pack2(bf_lo(xr[2 * e + h]) + ga[e][2 * h] * a[2 * h], bf_hi(xr[2 * e + h]) + ga[e][2 * h + 1] * a[2 * h + 1]);
        x1p[2 * e + h] = pk;
        xv[4 * e + 2 * h] = bf_lo(pk); xv[4 * e + 2 * h + 1] = bf_hi(pk);
        yp[2 * e + h] = pack2(bf_lo(pk) * cb[e][2 * h], bf_hi(pk) * cb[e][2 * h + 1]);
      }
    }
    GST_SC1("x4", yp, (u32)((ecol >> 1) * 1024 + ((ecol & 1) * 32 + (erow & 31)) * 16), sws, (u32)(rte * 32 + pn3 * 4) * 1024, WS_Y);
    float sum = ((xv[0] + xv[1]) + (xv[2] + xv[3])) + ((xv[4] + xv[5]) + (xv[6] + xv[7]));
    const float mean = col8_sum(sum) * (1.f / 64.f);
    float sq = 0.f;
#pragma unroll
    for (int i = 0; i < 8; ++i) { const float d = xv[i] - mean; sq = __builtin_fmaf(d, d, sq); }
    const f32x2 o2 = {mean, col8_sum(sq)};
    GST_SC1("x2", o2, l < 8 ? (u32)((erow & 31) * 8) : 0x40000000u, sws, (u32)(rte * 2048 + pn3 * 256), WS_ST2);
    STAMP(16);
    WAITV(0);
    RAWBAR();
    signal_cnt(ws, 16 + xcd * 2 + (pm3 >> 1));
    STAMP(17);
    GST_SC1("x4", x1p, (u32)(erow * 8 + ecol) * 16, sws, (u32)bid * 8192, WS_X1);
  }

  const u32 dsb = (u32)(w * 16384 + pn3 * 128), dlb = lb + w * 2048;
  const int r5 = (pn3 * 7) >> 2;
#define CH5(c) (u32)((c) + r5 >= 14 ? (c) + r5 - 14 : (c) + r5)
#define WDPIECE(c, slot, h) GLDS(wvo, sd, dsb + CH5(c) * 131072, (h) * 8192, dlb, (slot) * 16384 + (h) * 1024)
#define WDISSUE(c, slot) do { WDPIECE(c, slot, 0); WDPIECE(c, slot, 1); } while (0)
  const int kq = w & 3;
  const u32 wr5 = wrd0 + kq * 4096;
  bf16x8 dq[14][4];
#define DFRAG1(c, i) dq[c][i] = lds_tr2(smem, (wr5 ^ (((i) & 1) * 64u)) + (c) * 16384 + ((i) >> 1) * 2048, (wr5 ^ (((i) & 1) * 64u)) + (c) * 16384 + ((i) >> 1) * 2048 + 1024)
  if (loc < 28) {
    constexpr u32 RST4 = 157696;
    UV_PART(16);
    float uu[16], vv[16];
#pragma unroll
    for (int i = 0; i < 16; ++i) {
      const auto r = __builtin_amdgcn_permlane16_swap(__float_as_uint(uv[i]), __float_as_uint(uv[i]), false, false);
      uu[i] = __uint_as_float(r[0]); vv[i] = __uint_as_float(r[1]);
      PIN(uu[i]); PIN(vv[i]);
    }
    wait_cnt(ws, 16 + pan4, 16, w);
    RAWBAR();
    STAMP(18);
    const int rt4 = pan4 * 4;
    const u32 ysb = (u32)(rt4 * 32768 + w * 1024), ylb = lb + w * 1024;
#define YPIECE(i) GLDS_SC1(lo16, sws, ysb, WS_Y + (i) * 8192, ylb, (i) * 8192)
    YPIECE(0); YPIECE(1); YPIECE(2); YPIECE(3);
    GLDS_SC1(lo16, sws, (u32)(rt4 * 2048 + w * 1024), WS_ST2, ylb, 131072);
    YPIECE(4); YPIECE(5);
    u32 yrd0 = lo16, yrd1 = lo16 + 65536;
    PIN(yrd1);
    const u32 asb = (u32)(rt4 * 112 + pn4 * 8 + w) * 1024;
#define YFRAG(n) lds_frag(smem, ((n) < 64 ? yrd0 : yrd1) + ((n) & 63) * 1024)
    WAITV(5);
    RAWBAR();
    STAMP(19);
    bf16x8 yf[128];
#pragma unroll
    for (int n = 0; n < 4; ++n) yf[n] = YFRAG(n);
    SB();
#define signal_early()                                                                                               \
    do { unsigned long long ex_;                                                                                     \
      asm volatile("s_cmp_eq_u32 %5, 0\n\ts_cbranch_scc0 1f\n\ts_mov_b64 %0, exec\n\ts_mov_b64 exec, 1\n\t"          \
                   "buffer_atomic_add %1, %2, %3, %4 offen\n\ts_mov_b64 exec, %0\n1:"                                \
                   : "=&s"(ex_) : "v"(1u), "v"(0u), "s"(sws), "s"(WS_CNT + (u32)(48 + pan4) * 256), "s"(w) : "memory", "scc"); } while (0)
    f32x16 acc = zero, accp = zero;
    f32x2 stp = {0.f, 0.f};
    float eg[8], eu[8], et[8];
    u32x4 ov;
    SFOR(n, 0, 160)
      constexpr int a = n >> 5, ks = n & 31;
      if (n == 10) { WAITV(7); RAWBAR(); }
      if (n == 26) { WAITV(8); BAR(); }
      if (n == 56) { WAITV(4); RAWBAR(); }
      if (n == 100) WAITV(3);
      if (n == 121) { WAITV(4); RAWBAR(); signal_early(); }
      if constexpr (n < 20 && n % 2 == 0) YPIECE(6 + n / 2);
      if (n == 36 || n == 68 || n == 100) RAWBAR();
      if constexpr (n == 60 && !L2_WARM) {
        const int rr = pm3 * 224 + w * 28 + (l < 28 ? l : 27);
        const int r2 = rr + (rr < 256 ? 768 : 896) + r5 * 128;
        GTOUCH((u32)(r2 >= 1792 ? r2 - 1792 : r2) * 1024, sd, (u32)(pn3 * 128), 0, lb + SCR);
      }
      if constexpr (n >= 36 && n < 52 && n % 4 == 0) WDPIECE((n - 36) / 8, (n - 36) / 8, (n - 36) / 4 & 1);
      if constexpr (n >= 68 && n < 84 && n % 4 == 0) WDPIECE(2 + (n - 68) / 8, 2 + (n - 68) / 8, (n - 68) / 4 & 1);
      if constexpr (n == 84 || n == 88) WDPIECE(8, 8, (n - 84) / 4);
      if constexpr (n >= 100 && n < 116 && n % 4 == 0) WDPIECE(4 + (n - 100) / 8, 4 + (n - 100) / 8, (n - 100) / 4 & 1);
      if constexpr (n >= 100 && n < 116) DFRAG1((n - 100) >> 2, (n - 100) & 3);
      if constexpr (n >= 121 && n < 129) WDPIECE(10 + (n - 121) / 2, (n - 121) / 2, n & 1);
      if constexpr (n == 12) {
        const LAS f32x2* sp = (const LAS f32x2*)((LAS char*)smem + 131072 + (t >> 7) * 2048 + (t & 3) * 512 + (t >> 2 & 31) * 8);
        const f32x2 p0 = sp[0], p1 = sp[32];
        float mu = p0[0] + p1[0];
        mu += dpp_xor1(mu); mu += dpp_xor2(mu);
        mu *= 0.125f;
        float q = p0[1] + p1[1] + 64.f * ((p0[0] - mu) * (p0[0] - mu) + (p1[0] - mu) * (p1[0] - mu));
        q += dpp_xor1(q); q += dpp_xor2(q);
        const float rs = __builtin_amdgcn_rsqf(q * (1.f / 512.f) + 1e-5f);
        ((LAS f32x2*)((LAS char*)smem + RST4))[t >> 2] = (f32x2){rs, -mu * rs};
      }
      if (ks == 28 && a < 4) stp = ((const LAS f32x2*)((LAS char*)smem + RST4))[a * 32 + (l & 31)];
      if (ks == 0 && a > 0) { accp = acc; acc = zero; }
      if (n == 128) STAMP(20);
      if constexpr (n + 4 < 128) yf[n + 4] = YFRAG(n + 4);
      if constexpr (n < 128) acc = MFMA32(wf[ks], yf[n], acc);
      if constexpr (a > 0 && ks >= 2 && ks < 26) {
        constexpr int i = (ks - 2) / 3, ph = (ks - 2) % 3;
        if (ph == 0) {
          eg[i] = __builtin_fmaf(accp[i], stp[0], __builtin_fmaf(uu[i], stp[1], vv[i]));
          eu[i] = __builtin_fmaf(accp[8 + i], stp[0], __builtin_fmaf(uu[8 + i], stp[1], vv[8 + i]));
          et[i] = -1.4426950408889634f * eg[i];
          if (a < 4) { PIN(eg[i]); PIN(eu[i]); PIN(et[i]); }
        } else if (ph == 1) {
          et[i] = 1.f + __builtin_amdgcn_exp2f(et[i]);
          eg[i] *= eu[i];
          if (a < 4) { PIN(eg[i]); PIN(et[i]); }
        } else {
          eg[i] *= __builtin_amdgcn_rcpf(et[i]);
          if (a < 4) PIN(eg[i]);
          if constexpr (i & 1) ov[i >> 1] = pack2(eg[i - 1], eg[i]);
        }
      }
      if constexpr (a > 0 && ks == 26) GST_SC1("x4", ov, lo16, sws, asb, WS_ACT + (a - 1) * 112 * 1024);
      if (n < 128) SB();
    SEND
    STAMP(21);
  } else {
    WDISSUE(0, 0); WDISSUE(1, 1); WDISSUE(2, 2); WDISSUE(3, 3); WDISSUE(8, 8); WDISSUE(4, 4); WDISSUE(5, 5);
    WAITV(0);
    RAWBAR();
    SFOR(n, 0, 16) DFRAG1(n >> 2, n & 3); SEND
    BAR();
    WDISSUE(10, 0); WDISSUE(11, 1); WDISSUE(12, 2); WDISSUE(13, 3);
  }

  {
    u32 wra[3][2];
#pragma unroll
    for (int i = 0; i < 3; ++i) { wra[i][0] = wr5 + i * 65536; wra[i][1] = wra[i][0] ^ 64u; PIN(wra[i][0]); PIN(wra[i][1]); }
#define DFRAG(c) _Pragma("unroll") for (int i_ = 0; i_ < 4; ++i_) {                                              \
      const u32 o_ = wra[((c) % 10) >> 2][i_ & 1] + (((c) % 10) & 3) * 16384 + (i_ >> 1) * 2048; dq[c][i_] = lds_tr2(smem, o_, o_ + 1024); }
    f32x4 gb[2];
#pragma unroll
    for (int e = 0; e < 2; ++e) gb[e] = *(const LAS f32x4*)((LAS char*)smem + MT + 512 + (l & 7) * 32 + e * 16);
    const int pan5 = xcd * 2 + (pm3 >> 1);
    wait_cnt(ws, 48 + pan5, 14, w);
    WAITV(0);
    BAR();
    if (loc < 28) signal_cnt(ws, 32 + pan4);
    STAMP(22);
#define WD2(c, slot) do { WDISSUE(c, slot); WDPIECE(c, slot, -8); WDPIECE(c, slot, -7); } while (0)
#define BAR_IF(lo) asm volatile("s_cmp_lt_u32 %0, 4\n\ts_cbranch_scc" lo " 1f\n\ts_barrier\n1:" :: "s"(w) : "memory", "scc")
    if (w >= 4) {
      WD2(6, 6); WD2(7, 7); WD2(9, 9);
      WAITV(0);
      RAWBAR();
      wait_cnt(ws, 32 + pan5, 14, 0);
    }
    STAMP(23);
    const u32 asb = (u32)(rte * 112 + kq * 2) * 1024;
    u32x4 af[14][2];
#define AISSUE(c) do { GLD128_SC1(af[c][0], lo16, sws, asb + CH5(c) * 8192, WS_ACT); GLD128_SC1(af[c][1], lo16, sws, asb + CH5(c) * 8192, WS_ACT + 1024); } while (0)
    SFOR(c, 0, AWIN) AISSUE(c); SEND
    f32x16 acc[2] = {zero, zero};
    u32x4 x1p;
    SFOR(c, 0, 14)
      constexpr int young = c + AWIN <= 14 ? 2 * (AWIN - 1) : (c < 13 ? 2 * (13 - c) + 1 : 0);
      asm volatile("s_waitcnt vmcnt(%0)" :: "i"(young) : "memory");
      if (c == 0) STAMP(24);
      if (c == 5) STAMP(37);
      if (c == 10) STAMP(38);
      if (c == 2) BAR_IF("0");
      if (c == 10) BAR_IF("1");
      if constexpr (c + AWIN < 14) AISSUE(c + AWIN);
      if (c + AWIN == 14) GLD128_SC1(x1p, (u32)t * 16, sws, (u32)bid * 8192, WS_X1);
      if constexpr (c >= 3 && c < 13) { DFRAG(c + 1); }
      PIN(af[c][0]); PIN(af[c][1]);
      SB();
#pragma unroll
      for (int i = 0; i < 4; ++i) acc[i & 1] = MFMA32(dq[c][i], __builtin_bit_cast(bf16x8, af[c][i >> 1]), acc[i & 1]);
      SB();
    SEND
    STAMP(25);
    BAR_IF("0");
    {
      LAS char* eb = (LAS char*)smem + 65536 + kq * 17408 + ((w >> 2) * 32 + (l & 31)) * 272 + hb * 16;
#pragma unroll
      for (int tn = 0; tn < 2; ++tn)
#pragma unroll
        for (int e = 0; e < 4; ++e)
          *(LAS f32x4*)(eb + tn * 128 + e * 32) = (f32x4){acc[tn][4 * e], acc[tn][4 * e + 1], acc[tn][4 * e + 2], acc[tn][4 * e + 3]};
    }
    STAMP(40);
    BAR();
    STAMP(29);
    PIN(x1p);
    u32x4 ov;
#pragma unroll
    for (int e = 0; e < 2; ++e) {
      const LAS char* eb = (const LAS char*)smem + 65536 + (t >> 3) * 272 + (l & 7) * 32 + e * 16;
      const f32x4 s = (*(const LAS f32x4*)eb + *(const LAS f32x4*)(eb + 17408)) + (*(const LAS f32x4*)(eb + 2 * 17408) + *(const LAS f32x4*)(eb + 3 * 17408));
      ov[2 * e] = pack2(bf_lo(x1p[2 * e]) + gb[e][0] * s[0], bf_hi(x1p[2 * e]) + gb[e][1] * s[1]);
      ov[2 * e + 1] = pack2(bf_lo(x1p[2 * e + 1]) + gb[e][2] * s[2], bf_hi(x1p[2 * e + 1]) + gb[e][3] * s[3]);
    }
    *(u32x4*)((char*)out + (size_t)(rte * 32 + (t >> 3 & 31)) * 1024 + pn3 * 128 + (l & 7) * 16) = ov;
  }
  ST_OUT(2, ws);
  asm volatile(".Lcode_end:");
}

extern "C" void kernel_launch(void* const* d_in, const int* in_sizes, int n_in, void* d_out, int out_size, void* d_ws, size_t ws_size,
                              hipStream_t stream) {
  adaln_block<<<256, 512, 0, stream>>>((const u16*)d_in[0], (const u16*)d_in[1], (const u16*)d_in[2], (const u16*)d_in[4], (const u16*)d_in[5],
                                       (const u16*)d_in[6], (const u16*)d_in[7], (const u16*)d_in[8], (char*)d_ws, (u16*)d_out);
}
```

```cpp
#include <hip/hip_runtime.h>
#include <hip/hip_bf16.h>

typedef unsigned short u16;
typedef unsigned int u32;
typedef u32 u32x2 __attribute__((ext_vector_type(2)));
typedef u32 u32x4 __attribute__((ext_vector_type(4)));
typedef float f32x2 __attribute__((ext_vector_type(2)));
typedef float f32x4 __attribute__((ext_vector_type(4)));
typedef float f32x8 __attribute__((ext_vector_type(8)));
typedef float f32x16 __attribute__((ext_vector_type(16)));
typedef __bf16 bf16x2 __attribute__((ext_vector_type(2)));
typedef __bf16 bf16x8 __attribute__((ext_vector_type(8)));
typedef __bf16 bf16x16 __attribute__((ext_vector_type(16)));
typedef short s16x4 __attribute__((ext_vector_type(4)));
typedef short s16x8 __attribute__((ext_vector_type(8)));
#define PRIO_MODE 1
#define GATHER_AT 5
#define AORD 3
#define E1 10
#define AWIN 8
#define OUT_WT 0
#define CODE_PF 8
#define L2_WARM 1
#define UV_EARLY 0
#define B_PRIO 0
#define POLL_SLEEP 4
#define NREP 1
#define P4_FIRST 1
#define WD_LATE 2
#define E0 5
#define LAS __attribute__((address_space(3)))
#define DI __device__ __forceinline__

constexpr u32 WS_GRAN = 0;
constexpr u32 WS_EPOCH = 0x18000;
constexpr u32 WS_CNT  = 0x20000;
constexpr u32 WS_QF   = 0x100000;
constexpr u32 WS_KV   = 0x300000;
constexpr u32 WS_OF   = 0x800000;
constexpr u32 WS_X1   = 0xA00000;
constexpr u32 WS_Y    = 0xC00000;
constexpr u32 WS_ST2  = 0xE00000;
constexpr u32 WS_ACT  = 0xF00000;
constexpr u32 WS_BYTES = 0x1600000;
constexpr int KV_TILE = 2112, KV_HEAD = 64 * KV_TILE;

template <int I> struct IC { static constexpr int value = I; };
template <int B, int E, class F> DI void static_for(F&& f) { if constexpr (B < E) { f(IC<B>{}); static_for<B + 1, E>(f); } }
#define SFOR(n, B, E) static_for<B, E>([&](auto ic_) __attribute__((always_inline)) { constexpr int n = decltype(ic_)::value;
#define SEND });

DI float bf_lo(u32 u) { return __uint_as_float(u << 16); }
DI float bf_hi(u32 u) { return __uint_as_float(u & 0xffff0000u); }
DI u32 pack2(float a, float b) { bf16x2 r = {(__bf16)a, (__bf16)b}; return __builtin_bit_cast(u32, r); }
DI float bfround(float a) { return (float)(__bf16)a; }
DI float ln_rstd(float var) { return __builtin_amdgcn_rcpf(bfround(__builtin_amdgcn_sqrtf(bfround(bfround(var) + 1e-5f)))); }
DI float xor32_sum(float v) {
  const auto r = __builtin_amdgcn_permlane32_swap(__float_as_uint(v), __float_as_uint(v), false, false);
  return __uint_as_float(r[0]) + __uint_as_float(r[1]);
}
DI float dpp_xor1(float v) { return __int_as_float(__builtin_amdgcn_mov_dpp(__float_as_int(v), 0xB1, 0xF, 0xF, true)); }
DI float dpp_xor2(float v) { return __int_as_float(__builtin_amdgcn_mov_dpp(__float_as_int(v), 0x4E, 0xF, 0xF, true)); }
DI float shfl_idx(float v, int srcLane) { return __int_as_float(__builtin_amdgcn_ds_bpermute(srcLane << 2, __float_as_int(v))); }

DI u32x4 make_srd(const void* p, u32 bytes) {
  const size_t a = (size_t)p;
  u32x4 r = {(u32)a, (u32)(a >> 32) & 0xffffu, bytes, 0x20000u};
  return r;
}
#define GLDS(voff, srd, sb, GO, lb, LO)                                                                                   \
  do { u32 tmp_;                                                                                                          \
    asm volatile("s_add_u32 m0, %4, %6\n\ts_add_u32 %0, %3, %5\n\tbuffer_load_dwordx4 %1, %2, %0 offen lds"            \
                 : "=&s"(tmp_) : "v"(voff), "s"(srd), "s"(sb), "s"(lb), "i"(GO), "i"(LO) : "memory", "scc"); } while (0)
#define GLD(W, dst, voff, srd, sb, GO)                                                                                    \
  do { u32 tmp_;                                                                                                          \
    asm volatile("s_add_u32 %1, %4, %5\n\tbuffer_load_dword" W " %0, %2, %3, %1 offen"                                   \
                 : "=v"(dst), "=&s"(tmp_) : "v"(voff), "s"(srd), "s"(sb), "i"(GO) : "memory", "scc"); } while (0)
#define GLD128(dst, voff, srd, sb, GO) GLD("x4", dst, voff, srd, sb, GO)
#define GLD64(dst, voff, srd, sb, GO) GLD("x2", dst, voff, srd, sb, GO)
#define GLD32(dst, voff, srd, sb, GO) GLD("", dst, voff, srd, sb, GO)
#define WAITV(N) asm volatile("s_waitcnt vmcnt(" #N ")" ::: "memory")
#define PIN(x) asm volatile("" : "+v"(x))
#define BAR() do { asm volatile("s_waitcnt lgkmcnt(0)" ::: "memory"); __builtin_amdgcn_s_barrier(); asm volatile("" ::: "memory"); } while (0)

#define ST_DECL
#define STAMP(i)
#define ST_OUT(kid, wsp)

DI bf16x8 lds_frag(const char* smem, u32 off) { return *(const LAS bf16x8*)((const LAS char*)smem + off); }
DI bf16x8 lds_tr2(const char* smem, u32 off0, u32 off1) {
  s16x4 a = __builtin_amdgcn_ds_read_tr16_b64_v4i16((LAS s16x4*)((LAS char*)smem + off0));
  s16x4 b = __builtin_amdgcn_ds_read_tr16_b64_v4i16((LAS s16x4*)((LAS char*)smem + off1));
  s16x8 r = __builtin_shufflevector(a, b, 0, 1, 2, 3, 4, 5, 6, 7);
  return __builtin_bit_cast(bf16x8, r);
}
DI u32 lds_base(const char* smem) { return (u32)(size_t)(const LAS char*)smem; }
#define SB() __builtin_amdgcn_sched_barrier(0)
#define RAWBAR() do { asm volatile("" ::: "memory"); __builtin_amdgcn_s_barrier(); asm volatile("" ::: "memory"); } while (0)
#define MFMA32(a, b, c) __builtin_amdgcn_mfma_f32_32x32x16_bf16((a), (b), (c), 0, 0, 0)
#define MFMA16(a, b, c) __builtin_amdgcn_mfma_f32_16x16x32_bf16((a), (b), (c), 0, 0, 0)

DI u32x4 modulate8(const f32x2* n, const f32x2* c1, const f32x2* sh) {
  u32x4 o;
#pragma unroll
  for (int e = 0; e < 4; ++e) { const f32x2 r = __builtin_elementwise_fma(n[e], c1[e], sh[e]); o[e] = pack2(r[0], r[1]); }
  return o;
}

#define GLDS_SC1(voff, srd, sb, GO, lb, LO)                                                                               \
  do { u32 tmp_;                                                                                                          \
    asm volatile("s_add_u32 m0, %4, %6\n\ts_add_u32 %0, %3, %5\n\tbuffer_load_dwordx4 %1, %2, %0 offen sc1 lds"         \
                 : "=&s"(tmp_) : "v"(voff), "s"(srd), "s"(sb), "s"(lb), "i"(GO), "i"(LO) : "memory", "scc"); } while (0)
#define GLD128_SC1(dst, voff, srd, sb, GO)                                                                                \
  do { u32 tmp_;                                                                                                          \
    asm volatile("s_add_u32 %1, %4, %5\n\tbuffer_load_dwordx4 %0, %2, %3, %1 offen sc1"                                 \
                 : "=v"(dst), "=&s"(tmp_) : "v"(voff), "s"(srd), "s"(sb), "i"(GO) : "memory", "scc"); } while (0)
#define GST_SC1(W, val, voff, srd, sb, GO)                                                                                \
  do { u32 tmp_;                                                                                                          \
    asm volatile("s_add_u32 %0, %4, %5\n\tbuffer_store_dword" W " %1, %2, %3, %0 offen sc1\n\ts_nop 1"                  \
                 : "=&s"(tmp_) : "v"(val), "v"(voff), "s"(srd), "s"(sb), "i"(GO) : "memory", "scc"); } while (0)
#define GTOUCH(voff, srd, sb, GO, lb)                                                                                     \
  do { u32 tmp_;                                                                                                          \
    asm volatile("s_mov_b32 m0, %4\n\ts_add_u32 %0, %3, %5\n\tbuffer_load_dword %1, %2, %0 offen lds"                   \
                 : "=&s"(tmp_) : "v"(voff), "s"(srd), "s"(sb), "s"(lb), "i"(GO) : "memory", "scc"); } while (0)
#define GLDS64(vaddr, lb, LO)                                                                                             \
    asm volatile("s_add_u32 m0, %1, %2\n\ts_nop 0\n\tglobal_load_lds_dwordx4 %0, off" :: "v"(vaddr), "s"(lb), "i"(LO) : "memory", "scc")

DI void wave_sum8(const float* v, float* z) {
  float u[4];
#pragma unroll
  for (int i = 0; i < 4; ++i) {
    const auto a = __builtin_amdgcn_permlane32_swap(__float_as_uint(v[i]), __float_as_uint(v[i + 4]), false, false);
    u[i] = __uint_as_float(a[0]) + __uint_as_float(a[1]);
  }
#pragma unroll
  for (int i = 0; i < 2; ++i) {
    const auto a = __builtin_amdgcn_permlane16_swap(__float_as_uint(u[i]), __float_as_uint(u[i + 2]), false, false);
    float s = __uint_as_float(a[0]) + __uint_as_float(a[1]);
    s += dpp_xor1(s); s += dpp_xor2(s);
    s += __int_as_float(__builtin_amdgcn_mov_dpp(__float_as_int(s), 0x141, 0xF, 0xF, true));
    s += __int_as_float(__builtin_amdgcn_mov_dpp(__float_as_int(s), 0x140, 0xF, 0xF, true));
    z[i] = s;
  }
}
#define ROW_OF(z, r) __int_as_float(__builtin_amdgcn_readlane(__float_as_int(z[(r) & 1]), ((r) >> 1) * 16))
DI float col8_sum(float v) {
  v += __int_as_float(__builtin_amdgcn_mov_dpp(__float_as_int(v), 0x128, 0xF, 0xF, true));
  const auto a = __builtin_amdgcn_permlane16_swap(__float_as_uint(v), __float_as_uint(v), false, false);
  return xor32_sum(__uint_as_float(a[0]) + __uint_as_float(a[1]));
}

DI void signal_cnt(char* ws, int idx) {
  if (threadIdx.x < NREP) __hip_atomic_fetch_add((u32*)(ws + WS_CNT) + (threadIdx.x * 64 + idx) * 64, 1u, __ATOMIC_RELAXED, __HIP_MEMORY_SCOPE_AGENT);
}
DI void wait_cnt(char* ws, int idx, u32 want, int w) {
  if (w == 0) {
    u32* c = (u32*)(ws + WS_CNT) + (((blockIdx.x >> 3) % NREP) * 64 + idx) * 64;
#pragma unroll 1
    for (u32 spins = 0; spins < (1u << 17); ++spins) {
      if ((u32)__builtin_amdgcn_readfirstlane(__hip_atomic_load(c, __ATOMIC_RELAXED, __HIP_MEMORY_SCOPE_AGENT)) >= want) break;
      __builtin_amdgcn_s_sleep(POLL_SLEEP);
    }
    asm volatile("" ::: "memory");
  }
}
constexpr int TOUCH0 = 2, NTOUCH = L2_WARM ? 9 : 0;
constexpr int wyoung(int p) {
  int n = (p <= 32 ? p : 32) - (p - 4);
  for (int m = (p - 5) / 3; m < p / 3; ++m) n += (m >= TOUCH0 && m < TOUCH0 + NTOUCH) ? 1 : 0;
  return n;
}
DI float gsum(const u32x4 a) { return __uint_as_float(a[0]) + __uint_as_float(a[2]); }
DI bool gok(const u32x4 a, u32 tag) { return a[1] == tag && a[3] == tag; }
#define SMF(v, p, o) __builtin_amdgcn_smfmac_f32_32x32x32_bf16((v), __builtin_bit_cast(bf16x16, (p)), (o), 0xDDDD, 0, 0)
#define LO8(s) __builtin_shufflevector(s, s, 0, 1, 2, 3, 4, 5, 6, 7)
#define HI8(s) __builtin_shufflevector(s, s, 8, 9, 10, 11, 12, 13, 14, 15)

extern "C" __global__ __launch_bounds__(512) void adaln_block(const u16* __restrict__ x, const u16* __restrict__ wqkv, const u16* __restrict__ wo,
                                                              const u16* __restrict__ wg, const u16* __restrict__ wu, const u16* __restrict__ wd,
                                                              const u16* __restrict__ c, const u16* __restrict__ wmod, char* __restrict__ ws,
                                                              u16* __restrict__ out) {
  __shared__ __attribute__((aligned(16))) char smem[163840];
  ST_DECL STAMP(0);
  const int bid = blockIdx.x, xcd = bid & 7, loc = bid >> 3;
  const int t = threadIdx.x, l = t & 63;
  const int w = __builtin_amdgcn_readfirstlane(t >> 6);
  const u32 lb = lds_base(smem);
  {
    unsigned long long pc;
    u32 len;
    asm volatile("s_getpc_b64 %0\n.Lcode_mark:\n\ts_mov_b32 %1, .Lcode_end-.Lcode_mark" : "=s"(pc), "=s"(len));
    const u32x4 scode = make_srd((const void*)pc, loc < CODE_PF ? len : 0u);
    GTOUCH((u32)(loc * 512 + t) * 128 / CODE_PF, scode, 0u, 0, lb + 57344);
    SB();
  }
  const u32x4 sws = make_srd(ws, WS_BYTES), sx = make_srd(x, 2048 * 1024);
  const u32 lo16 = (u32)l * 16, zs = 0;
  const int hb = l >> 5;
  const int pm3 = loc >> 3, pn3 = loc & 7;
  constexpr u32 TAB = 159744, MT = TAB + 2048, SCR = TAB;
  u32 tag;
  asm volatile("" :: "s"(x), "s"(wqkv), "s"(wo), "s"(wg), "s"(wu), "s"(wd), "s"(c), "s"(wmod), "s"(ws), "s"(out));

  {
    constexpr u32 WSLOT = 24576, RED = 49152, MTAB = 53248;
    const int pm = (xcd >> 1) * 8 + (loc >> 2), cg = (xcd & 1) * 4 + (loc & 3);
    const u32x4 sw = make_srd(wqkv, 512 * 3072), sm = make_srd(wmod, bid < 96 ? 512 * 6144 : 0), sc = make_srd(c, bid < 96 ? 1024 : 0);
    const int mcb = bid >> 1, mkq = bid & 1;
    u32 ev;
    u32x2 cv;
    u32x4 mv[4], xa[8];
    if (bid == 0 && w < NREP) GST_SC1("", zs, (u32)t * 256, sws, zs, WS_CNT);
    asm volatile("buffer_load_dword %0, %1, %2, %3 offen sc1" : "=v"(ev) : "v"(zs), "s"(sws), "s"(WS_EPOCH) : "memory");
#pragma unroll
    for (int i = 0; i < 4; ++i) GLD128(mv[i], (u32)((t >> 3) * 4 * 6144 + (t & 7) * 16), sm, (u32)(mkq * 256 * 6144 + mcb * 128), i * 6144);
    GLD64(cv, (u32)((t >> 3) * 8), sc, (u32)(mkq * 512), 0);
    RAWBAR();
    const int rx = loc & 3, rw = loc >> 2;
    const u32 xsb = (u32)(pm * 64 + w * 2) * 1024;
#define XROW(i) GLD128(xa[i], lo16, sx, xsb + (u32)(((i) >> 1) ^ rx) * 16384, ((i) & 1) * 1024)
    const u32 wvo = (u32)((l >> 3) * 3072 + ((((l >> 2) ^ (l >> 4)) & 1) * 32 + (l & 3) * 8) * 2);
    const u32 wsb = (u32)((w & 3) * 8 * 3072 + cg * 128), wlb = lb + (w & 3) * 1024;
#define KA_SLOT(kc) (((kc) >> 1 == 1 ? 0 : ((kc) >> 1 == 2 ? 65536 + 2 * WSLOT : 65536)) + ((kc) & 1) * WSLOT)
#define KA_CHUNK(kc) SFOR(n_, 0, 6) GLDS(wvo, sw, wsb + (u32)(((kc) + rw) & 7) * (64 * 3072), (n_ >> 1) * 1024 + (n_ & 1) * 32 * 3072, wlb, KA_SLOT(kc) + (n_ >> 1) * 8192 + (n_ & 1) * 4096); SEND
    STAMP(26);
    XROW(0); XROW(1); XROW(2);
    STAMP(27);
    WAITV(3);
    STAMP(28);
    PIN(ev); PIN(cv);
#pragma unroll
    for (int i = 0; i < 4; ++i) PIN(mv[i]);
    tag = (u32)__builtin_amdgcn_readfirstlane(ev) + 1u;
    XROW(3); XROW(4); XROW(5);
    if (bid < 96) {
      float acc[8];
#pragma unroll
      for (int e = 0; e < 8; ++e) acc[e] = 0.f;
#pragma unroll
      for (int i = 0; i < 4; ++i) {
        const float cc = (i & 1) ? bf_hi(cv[i >> 1]) : bf_lo(cv[i >> 1]);
        const float sv = bfround(cc * __builtin_amdgcn_rcpf(1.f + __builtin_amdgcn_exp2f(-1.4426950408889634f * cc)));
#pragma unroll
        for (int e = 0; e < 4; ++e) { acc[2 * e] += sv * bf_lo(mv[i][e]); acc[2 * e + 1] += sv * bf_hi(mv[i][e]); }
      }
      float u[4], z[2];
#pragma unroll
      for (int i = 0; i < 4; ++i) {
        const auto a = __builtin_amdgcn_permlane32_swap(__float_as_uint(acc[i]), __float_as_uint(acc[i + 4]), false, false);
        u[i] = __uint_as_float(a[0]) + __uint_as_float(a[1]);
      }
#pragma unroll
      for (int i = 0; i < 2; ++i) {
        const auto a = __builtin_amdgcn_permlane16_swap(__float_as_uint(u[i]), __float_as_uint(u[i + 2]), false, false);
        z[i] = __uint_as_float(a[0]) + __uint_as_float(a[1]);
        z[i] += __int_as_float(__builtin_amdgcn_mov_dpp(__float_as_int(z[i]), 0x128, 0xF, 0xF, true));
      }
      *(LAS f32x2*)((LAS char*)smem + RED + w * 256 + (l & 7) * 32 + (l >> 4) * 8) = (f32x2){z[0], z[1]};
      BAR();
      if (w == 7) {
        const LAS float* rp = (const LAS float*)((LAS char*)smem + RED) + l;
        const float v = ((rp[0] + rp[64]) + (rp[128] + rp[192])) + ((rp[256] + rp[320]) + (rp[384] + rp[448]));
        const u32x2 gr = {__float_as_uint(v), tag};
        GST_SC1("x2", gr, lo16, sws, (u32)(mcb * 1024 + mkq * 8), WS_GRAN);
      }
    }
    STAMP(1);
    XROW(6); XROW(7);
    RAWBAR();
    u32x4 gq[2];
#define GATHER(tail)                                                                                                            \
    asm volatile("buffer_load_dwordx4 %0, %2, %3, %4 offen sc1\n\tbuffer_load_dwordx4 %1, %2, %3, %5 offen sc1" tail            \
                 : "=&v"(gq[0]), "=&v"(gq[1]) : "v"((u32)t * 16), "s"(sws), "s"(WS_GRAN), "s"(WS_GRAN + 8192) : "memory")
    const int kind = w < 4 ? (w >> 1) : 2, hp = w < 4 ? (w & 1) : (w >> 1 & 1);
    u32 brd0 = (u32)(kind * 8192 + (hb * 8 + (l >> 2 & 3)) * 128 + ((hp ^ (l >> 3)) & 1) * 64 + (l >> 4 & 1) * 32 + (l & 3) * 8);
    u32 brd1 = brd0 + 65536, brd2 = brd0 + 65536 + 2 * WSLOT;
    PIN(brd1); PIN(brd2);
    bf16x8 wq[32];
#define KA_FRAG(kc) _Pragma("unroll") for (int c_ = 0; c_ < 4; ++c_) {                                                    \
      const u32 o_ = ((kc) >> 1 == 1 ? brd0 : ((kc) >> 1 == 2 ? brd2 : brd1)) + ((kc) & 1) * WSLOT + c_ * 2048; wq[(kc) * 4 + c_] = lds_tr2(smem, o_, o_ + 512); }
    if (w < 4) { KA_CHUNK(0) KA_CHUNK(1) KA_CHUNK(2) KA_CHUNK(3) WAITV(12); RAWBAR(); KA_FRAG(0); KA_FRAG(1); BAR(); } else WAITV(0);
    STAMP(30);
    f32x2 nv[8][4];
    {
      float v[8], z[2], q[2];
#pragma unroll
      for (int i = 0; i < 8; ++i) {
        PIN(xa[i]);
#pragma unroll
        for (int e = 0; e < 4; ++e) nv[i][e] = (f32x2){bf_lo(xa[i][e]), bf_hi(xa[i][e])};
        const f32x2 s2 = (nv[i][0] + nv[i][1]) + (nv[i][2] + nv[i][3]);
        v[i] = s2[0] + s2[1];
      }
      STAMP(31);
      wave_sum8(v, z);
      z[0] *= (1.f / 512.f); z[1] *= (1.f / 512.f);
      SFOR(i, 0, 8)
        const float mu = ROW_OF(z, i);
        f32x2 s2 = {0.f, 0.f};
        _Pragma("unroll") for (int e = 0; e < 4; ++e) { nv[i][e] -= (f32x2){mu, mu}; s2 = __builtin_elementwise_fma(nv[i][e], nv[i][e], s2); }
        v[i] = s2[0] + s2[1];
      SEND
      wave_sum8(v, q);
#pragma unroll
      for (int j = 0; j < 2; ++j) { q[j] = ln_rstd(q[j] * (1.f / 512.f)); z[j] = (z[j] - bfround(z[j])) * q[j]; }
      SFOR(i, 0, 8)
        const float rs = ROW_OF(q, i), mr = ROW_OF(z, i);
        _Pragma("unroll") for (int e = 0; e < 4; ++e) { nv[i][e] = __builtin_elementwise_fma(nv[i][e], (f32x2){rs, rs}, (f32x2){mr, mr}); }
      SEND
#pragma unroll
      for (int i = 0; i < 8; ++i)
#pragma unroll
        for (int e = 0; e < 4; ++e) PIN(nv[i][e]);
    }
    if (w >= 4) { RAWBAR(); KA_FRAG(0); KA_FRAG(1); BAR(); KA_CHUNK(4) KA_CHUNK(5) }
    GATHER("");
    if (w >= 4) { KA_CHUNK(6) KA_CHUNK(7) } else WAITV(2);
    STAMP(2);
    RAWBAR();
    KA_FRAG(2); KA_FRAG(3);
    if (w >= 4) WAITV(12); else WAITV(0);
    PIN(gq[0]); PIN(gq[1]);
    STAMP(3);
#pragma unroll 1
    for (u32 spins = 0; spins < (1u << 17); ++spins) {
      if (__all(gok(gq[0], tag) && gok(gq[1], tag))) break;
      __builtin_amdgcn_s_sleep(2);
      GATHER("\n\ts_waitcnt vmcnt(0)");
    }
    ((LAS float*)((LAS char*)smem + MTAB))[t] = bfround(1.f + bfround(gsum(gq[1])));
    ((LAS float*)((LAS char*)smem + MTAB))[512 + t] = bfround(gsum(gq[0]));
    BAR();
    STAMP(33);
    f32x2 c1[4], sh[4];
    {
      const LAS f32x4* tc = (const LAS f32x4*)((LAS char*)smem + MTAB + l * 32);
      const f32x4 a0 = tc[0], a1 = tc[1], b0 = tc[128], b1 = tc[129];
#pragma unroll
      for (int e = 0; e < 2; ++e) {
        c1[e] = (f32x2){a0[2 * e], a0[2 * e + 1]}; c1[2 + e] = (f32x2){a1[2 * e], a1[2 * e + 1]};
        sh[e] = (f32x2){b0[2 * e], b0[2 * e + 1]}; sh[2 + e] = (f32x2){b1[2 * e], b1[2 * e + 1]};
      }
    }
    u32 awr[4];
#pragma unroll
    for (int i = 0; i < 4; ++i) awr[i] = (u32)((((l >> 1) - 4 * rw) & 31) * 1024 + (l & 1) * 512 + (((i >> 1) ^ rx) & 1) * 256 + (((w * 2 + (i & 1)) ^ (l & 7)) << 4));
#define KA_PUT(i) *(LAS u32x4*)((LAS char*)smem + awr[(i) & 3] + ((i) >> 2) * 32768) = hv[i]
    u32x4 hv[8];
#pragma unroll
    for (int i = 0; i < 4; ++i) { hv[i] = modulate8(nv[i], c1, sh); KA_PUT(i); }
    const int ta = w < 4 ? 0 : (w & 1), tx = rx >> 1;
    u32 ard[4];
#pragma unroll
    for (int i = 0; i < 4; ++i) ard[i] = (u32)(ta * 32768 + i * 1024 + hb * 512 + (((l & 31) ^ (i * 2 + hb)) << 4));
#define HFRAG(n) lds_frag(smem, ard[(n) & 3] + ((n) >> 2) * 4096)
    const int head = cg * 4 + hp * 2;
    const u32 kvb = WS_KV + (u32)(head * KV_HEAD + pm * 2 * KV_TILE);
    f32x16 z16;
#pragma unroll
    for (int i = 0; i < 16; ++i) z16[i] = 0.f;
    BAR();
    STAMP(4);
    if (w < 4) {
      const float qs = kind == 0 ? 0.25f * 1.4426950408889634f : 1.f;
      const u32 so = kind == 0 ? WS_QF + (u32)(head * 64 + pm * 2) * 1024 + lo16 : kvb + (u32)((l & 51) | (l & 4) << 1 | (l & 8) >> 1) * 16;
      const u32 sh_ = kind == 0 ? 65536u : (u32)KV_HEAD, st_ = kind == 0 ? 1024u : (u32)KV_TILE;
      bf16x8 hf[64];
      f32x16 acc = z16, accp = z16;
      u32x4 ov[2];
#pragma unroll
      for (int i = 0; i < 4; ++i) hf[i] = HFRAG(i);
      SB();
      SFOR(n, 0, 76)
        if constexpr (n < 4) { hv[4 + n] = modulate8(nv[4 + n], c1, sh); KA_PUT(4 + n); }
        if constexpr (n == 5) { KA_FRAG(4); }
        if constexpr (n == 9) RAWBAR();
        if constexpr (n == 11) { KA_FRAG(5); }
        if constexpr (n == 15) { KA_FRAG(6); }
        if constexpr (n == 19) { KA_FRAG(7); }
        if constexpr (n == 32 || n == 64) { accp = acc; acc = z16; }
        if constexpr (n + 4 < 64) hf[n + 4] = HFRAG(n + 4);
        if constexpr (n < 64) acc = MFMA32(wq[n & 31], hf[n], acc);
        if constexpr (n >= 32 && (n & 31) >= 2 && (n & 31) < 10) {
          constexpr int e = (n & 31) - 2;
          ov[e >> 2][e & 3] = pack2(accp[2 * e] * qs, accp[2 * e + 1] * qs);
        }
        if constexpr (n == 42 || n == 74) GST_SC1("x4", ov[0], so, sws, st_ * ((n / 32 - 1) ^ tx), 0);
        if constexpr (n == 43 || n == 75) GST_SC1("x4", ov[1], so, sws, st_ * ((n / 32 - 1) ^ tx) + sh_, 0);
        if (n < 64) SB();
      SEND
    } else {
      bf16x8 hf[32];
      f32x16 acc = z16;
#pragma unroll
      for (int i = 4; i < 8; ++i) { hv[i] = modulate8(nv[i], c1, sh); KA_PUT(i); }
      WAITV(0);
      BAR();
      __builtin_amdgcn_s_setprio(B_PRIO);
#pragma unroll
      for (int i = 0; i < 4; ++i) hf[i] = HFRAG(i);
      SB();
      SFOR(n, 0, 32)
        if constexpr (n >= 2 && n < 18 && (n & 3) == 2) { KA_FRAG(4 + n / 4); }
        if constexpr (n + 4 < 32) hf[n + 4] = HFRAG(n + 4);
        acc = MFMA32(hf[n], wq[n], acc);
        SB();
      SEND
      const u32 kvt = kvb + (u32)((ta ^ tx) * KV_TILE);
      const u32 vo = kvt + (u32)((l >> 4 & 1) * KV_HEAD + 1024 + hb * 272 + (l & 15) * 16);
#pragma unroll
      for (int e = 0; e < 2; ++e) {
        const u32x4 ov = {pack2(acc[8 * e], acc[8 * e + 1]), pack2(acc[8 * e + 2], acc[8 * e + 3]), pack2(acc[8 * e + 4], acc[8 * e + 5]), pack2(acc[8 * e + 6], acc[8 * e + 7])};
        GST_SC1("x4", ov, vo, sws, zs, e * 544);
      }
      const u32x4 one = {0x3f803f80u, 0x3f803f80u, 0x3f803f80u, 0x3f803f80u};
      GST_SC1("x4", one, l < 8 ? kvt + (u32)((l >> 2) * KV_HEAD + 1024 + (l & 3) * 272 + 256) : 0x40000000u, sws, zs, 0);
    }
    __builtin_amdgcn_s_setprio(0);
    STAMP(5);
    u32x4 tq[3];
    const u32 msb = WS_GRAN + (u32)(((w == 0 ? 1024 : (w == 1 ? 2048 : 2560)) + pn3 * 64) * 16);
#define TGATHER(tail)                                                                                                           \
    asm volatile("buffer_load_dwordx4 %0, %3, %5, %6 offen sc1\n\tbuffer_load_dwordx4 %1, %3, %5, %7 offen sc1\n\t"             \
                 "buffer_load_dwordx4 %2, %4, %5, %8 offen sc1" tail                                                            \
                 : "=&v"(tq[0]), "=&v"(tq[1]), "=&v"(tq[2])                                                                     \
                 : "v"((u32)t * 16), "v"(lo16), "s"(sws), "s"(WS_GRAN + 1536 * 16), "s"(WS_GRAN + 2048 * 16), "s"(msb) : "memory")
    TGATHER("");
    WAITV(3);
    STAMP(6);
    BAR();
    signal_cnt(ws, cg);
    WAITV(0);
#pragma unroll
    for (int i = 0; i < 3; ++i) PIN(tq[i]);
#pragma unroll 1
    for (u32 spins = 0; spins < (1u << 17); ++spins) {
      if (__all(gok(tq[0], tag) && gok(tq[1], tag) && gok(tq[2], tag))) break;
      __builtin_amdgcn_s_sleep(2);
      TGATHER("\n\ts_waitcnt vmcnt(0)");
    }
    *(LAS u16*)((LAS char*)smem + TAB + t * 2) = (u16)(__float_as_uint(bfround(1.f + bfround(gsum(tq[1])))) >> 16);
    *(LAS u16*)((LAS char*)smem + TAB + 1024 + t * 2) = (u16)(__float_as_uint(bfround(gsum(tq[0]))) >> 16);
    const float mv_ = bfround(gsum(tq[2]));
    ((LAS float*)((LAS char*)smem + MT))[t] = (w == 1) ? bfround(1.f + mv_) : mv_;
  }

  const int lq = loc >= 28 ? 27 : loc;
  const int pan4 = (xcd >> 1) * 4 + lq / 7, pn4 = (xcd & 1) * 7 + lq % 7;
  constexpr u32 STG = KV_HEAD;
  f32x16 zero;
#pragma unroll
  for (int i = 0; i < 16; ++i) zero[i] = 0.f;
  bf16x8 wf[32];
  const u32x4 so = make_srd(wo, 512 * 1024), sd = make_srd(wd, 1792 * 1024);

  {
    const char* wsrc = (const char*)((l & 2) ? wu : wg) + (size_t)(l >> 2) * 3584 + (pn4 * 128 + w * 16 + (l & 1) * 8) * 2;
    const u32 stg_lb = lb + w * 3072;
    u32 stg_rd0 = (u32)(w * 3072 + (hb * 8 + (l >> 2 & 3)) * 64 + (l >> 4 & 1) * 32 + (l & 3) * 8), stg_rd1 = stg_rd0 + STG;
    PIN(stg_rd1);
#define WREAD(p)                                                                                        \
    do {                                                                                                \
      if constexpr ((p) >= 5) {                                                                         \
        asm volatile("s_waitcnt vmcnt(%0)" :: "i"(wyoung(p)) : "memory");                               \
        const u32 o_ = ((((p) - 5) % 6) < 3 ? stg_rd1 : stg_rd0) + (((p) - 5) % 3) * 1024;              \
        wf[(p) - 5] = lds_tr2(smem, o_, o_ + 256);                                                      \
      }                                                                                                 \
    } while (0)
#define WLOAD(p) do { if ((p) < 32) { GLDS64(wsrc, stg_lb, (((p) % 6) < 3 ? STG : 0u) + ((p) % 3) * 1024u); wsrc += 16 * 3584; } } while (0)
    const int head = xcd * 4 + (loc >> 3), qt = (loc & 7) * 8 + w;
    const u32 ksb = (u32)(head * KV_HEAD + w * 1024), wlb = lb + w * 1024;
    const int r = l & 31;
    u32 krd = lo16;
    u32 vrd = (u32)(1024 + hb * 272 + (r < 16 ? r * 16 : (r == 16 ? 256 : 0x40000)));
    STAMP(7);
    wait_cnt(ws, xcd, 32, w);
    BAR();
    STAMP(8);
#define KVR(r) GLDS_SC1(lo16, sws, ksb, WS_KV + (r) * 8192, wlb, (r) * 8192)
    u32x4 qraw;
    GLD128_SC1(qraw, lo16, sws, (u32)(head * 64 + qt) * 1024, WS_QF);
    KVR(0); KVR(1);
    if (w < 4) KVR(16);
    KVR(2); KVR(3);
    WAITV(2);
    PIN(qraw);
    RAWBAR();
    STAMP(9);
    const bf16x8 qf = __builtin_bit_cast(bf16x8, qraw);
    float m;
    bf16x8 kf = lds_frag(smem, krd);
    {
      f32x16 s0 = MFMA32(kf, qf, zero);
      m = s0[0];
#pragma unroll
      for (int i = 1; i < 16; ++i) m = fmaxf(m, s0[i]);
      const auto a = __builtin_amdgcn_permlane32_swap(__float_as_uint(m), __float_as_uint(m), false, false);
      m = fmaxf(__uint_as_float(a[0]), __uint_as_float(a[1]));
    }
    f32x16 negm;
#pragma unroll
    for (int i = 0; i < 16; ++i) negm[i] = -m;
    f32x16 o = zero;
    f32x16 sc = MFMA32(kf, qf, negm);
    kf = lds_frag(smem, krd + KV_TILE);
    bf16x8 v0 = lds_frag(smem, vrd), v1;
    const u32 tvo = (u32)((l & 7) * 1024 + pn3 * 128), tsb = (u32)(pm3 * 8 + w) * 8192;
    SFOR(j, 0, 64)
      if constexpr ((j & 1) == 0) __builtin_amdgcn_s_setprio(3 - (j >> 1 & 3));
      if (j == 5) { WAITV(10); RAWBAR(); }
      if (j == 13) { WAITV(0); RAWBAR(); }
      if (j == 14) STAMP(10);
      constexpr bool wst = j >= 14 && j < 63 && (j - 14) % 4 != 3;
      constexpr int wp = (j - 14) - (j - 14) / 4;
      if (j == 28 || j == 56) { krd += 28 * KV_TILE; vrd += 28 * KV_TILE; PIN(krd); PIN(vrd); }
      const u32 jo = (u32)(j % 28) * KV_TILE;
#define EXPS(a, b) _Pragma("unroll") for (int i = (a); i < (b); ++i) sc[i] = __builtin_amdgcn_exp2f(sc[i])
      f32x16 sn;
#define QKNEXT() do { if (j < 63) asm volatile("v_mfma_f32_32x32x16_bf16 %0, %1, %2, %3" : "=&v"(sn) : "v"(kf), "v"(qf), "v"(negm)); } while (0)
#define KREAD() do { if (j < 62) kf = lds_frag(smem, krd + jo + 2 * KV_TILE); } while (0)
#define SIDE()                                                                                                             \
      do {                                                                                                                 \
        if constexpr (wst) { WREAD(wp); WLOAD(wp); }                                                                       \
        if constexpr (j >= 14 && (j - 14) % 4 == 3) {     \
          constexpr int m = (j - 14) / 4 - TOUCH0;                                                                         \
          if (m >= 0 && m < 7 && NTOUCH) GTOUCH(tvo, sd, tsb * 7, (m < 0 ? 0 : m) * 8192, lb + 25600);                     \
          if (m >= 7 && m < NTOUCH) GTOUCH(tvo, so, tsb * 2, (m < 7 ? 0 : m - 7) * 8192, lb + 25600);                      \
        }                                                                                                                  \
        if constexpr (j < 6) { KVR(4 + 2 * j); KVR(5 + 2 * j); }                                                           \
      } while (0)
      v0 = lds_frag(smem, vrd + jo); v1 = lds_frag(smem, vrd + jo + 544);
      SB();
      EXPS(j == 0 ? 0 : 2, E0);
      PIN(sc);
      QKNEXT(); SB(); KREAD(); SB();
      EXPS(E0, E1);
      SB();
      o = SMF(v0, LO8(sc), o);
      SIDE();
      SB();
      EXPS(E1, 16);
      PIN(sc);
      if (j < 63) { sn[0] = __builtin_amdgcn_exp2f(sn[0]); sn[1] = __builtin_amdgcn_exp2f(sn[1]); PIN(sn); }
      SB();
      o = SMF(v1, HI8(sc), o);
      PIN(o);
      SB();
      if (j < 63) sc = sn;
    SEND
    float lsum = __uint_as_float(__builtin_amdgcn_permlane32_swap(__float_as_uint(o[8]), __float_as_uint(o[8]), false, false)[0]);
    bool ok = lsum < 1e18f;
#pragma unroll
    for (int i = 0; i < 8; ++i) ok = ok && (fabsf(o[i]) < 3e38f);
    if (!__all(ok)) {
      asm volatile("" ::: "memory");
      const __amdgpu_buffer_rsrc_t rk = __builtin_amdgcn_make_buffer_rsrc(ws + WS_KV + (size_t)head * KV_HEAD, 0, KV_HEAD, 0x20000);
      const int vg = 1024 + hb * 272 + (r == 16 ? 256 : (r & 15) * 16);
#define KVLD(off, j) __builtin_bit_cast(bf16x8, __builtin_amdgcn_raw_buffer_load_b128(rk, (off), (j) * KV_TILE, 16))
#pragma unroll 1
      for (int j = 0; j < 64; ++j) {
        const f32x16 sv = MFMA32(KVLD(lo16, j), qf, zero);
#pragma unroll
        for (int i = 0; i < 16; ++i) m = fmaxf(m, sv[i]);
      }
      m = fmaxf(m, __shfl_xor(m, 32));
#pragma unroll
      for (int i = 0; i < 16; ++i) negm[i] = -m;
      o = zero;
#pragma unroll 1
      for (int j = 0; j < 64; ++j) {
        f32x16 sv = MFMA32(KVLD(lo16, j), qf, negm);
        const bf16x8 v0 = KVLD(vg, j), v1 = KVLD(vg + 544, j);
#pragma unroll
        for (int i = 0; i < 16; ++i) sv[i] = __builtin_amdgcn_exp2f(sv[i]);
        o = SMF(v0, LO8(sv), o);
        o = SMF(v1, HI8(sv), o);
      }
      lsum = shfl_idx(o[8], r);
    }
    const float inv = 1.f / lsum;
    u32x4 ov;
#pragma unroll
    for (int e = 0; e < 4; ++e) ov[e] = pack2(o[2 * e] * inv, o[2 * e + 1] * inv);
    GST_SC1("x4", ov, lo16, sws, (u32)(qt * 32 + head) * 1024, WS_OF);
    STAMP(11);
    WAITV(0);
    BAR();
    signal_cnt(ws, 8 + (loc & 7));
    STAMP(12);
  }

  const int tm = w & 1;
  const u32 wvo = (u32)((l >> 3) * 1024 + ((((l >> 2) ^ (l >> 4)) & 1) * 32 + (l & 3) * 8) * 2);
  const u32 wrd0 = (u32)((hb * 4 + (l >> 2 & 3)) * 128 + (l >> 3 & 1) * 64 + (l >> 4 & 1) * 32 + (l & 3) * 8);
  const int rte = xcd * 8 + (pm3 >> 1) * 4 + (pm3 & 1) + (w >> 2) * 2;
  const int erow = w * 8 + (l & 7), ecol = l >> 3;
  f32x16 uv = zero;
  const u32 trd = TAB + (u32)((l >> 4 & 1) * 1024 + hb * 16);
#define UV_PART(k0)                                                                                          \
  do { bf16x8 tf_[16];                                                                                       \
    _Pragma("unroll") for (int i_ = 0; i_ < 4; ++i_) tf_[i_] = lds_frag(smem, trd + ((k0) + i_) * 32);       \
    _Pragma("unroll") for (int i_ = 0; i_ < 16; ++i_) {                                                      \
      if (i_ + 4 < 16) tf_[i_ + 4] = lds_frag(smem, trd + ((k0) + i_ + 4) * 32);                             \
      uv = MFMA32(wf[(k0) + i_], tf_[i_], uv); SB(); }                                                       \
    PIN(uv); } while (0)

  {
    const int ro = pn3 & 3;
    SFOR(j, 0, 8) GLDS(wvo, so, (u32)(w * 8192 + pn3 * 128 + ((j + 2 * ro) & 7) * 65536), 0, lb + w * 1024, j * 8192); SEND
    u32x4 xr;
    GLD128(xr, (u32)((erow & 31) * 1024 + ecol * 16), sx, (u32)(rte * 32768 + pn3 * 128), 0);
    UV_PART(0);
    WAITV(0);
    PIN(xr);
    STAMP(13);
    wait_cnt(ws, 8 + xcd, 32, w);
    RAWBAR();
    STAMP(14);
    if (bid == 0 && t == 0) __hip_atomic_store((u32*)(ws + WS_EPOCH), tag, __ATOMIC_RELAXED, __HIP_MEMORY_SCOPE_AGENT);
    const u32 olo = (u32)((w >> 2) * 32768 + (w & 3) * 2048);
    const u32 osb = (u32)(rte * 32768 + (w & 3) * 2048), olb = lb + olo;
    SFOR(j, 0, 8) GLDS_SC1(lo16, sws, osb + (u32)(((j >> 1) + ro) & 3) * 8192, WS_OF + (j & 1) * 1024, olb, 65536 + (j >> 1) * 8192 + (j & 1) * 1024); SEND
    const int tn = w >> 1 & 1, kh = w >> 2;
    u32 ord = 65536 + (u32)(tm * 32768 + kh * 4096) + lo16;
    PIN(ord);
    const u32 wrd = (wrd0 ^ (u32)(tn * 64)) + kh * 8192;
    f32x16 acc = zero;
    bf16x8 wq[16];
#define WOFRAG(n) wq[n] = lds_tr2(smem, wrd + ((n) >> 2) * 16384 + ((n) & 3) * 2048, wrd + ((n) >> 2) * 16384 + ((n) & 3) * 2048 + 1024)
#pragma unroll
    for (int n = 0; n < 8; ++n) WOFRAG(n);
    SFOR(s, 0, 4)
      if (s == 0) WAITV(6); else if (s == 1) WAITV(4); else if (s == 2) WAITV(2); else WAITV(0);
      RAWBAR();
      bf16x8 oq[4];
#pragma unroll
      for (int j = 0; j < 4; ++j) oq[j] = lds_frag(smem, ord + s * 8192 + j * 1024);
      if constexpr (s < 2) {
#pragma unroll
        for (int j = 0; j < 4; ++j) WOFRAG(8 + s * 4 + j);
      }
      SB();
#pragma unroll
      for (int j = 0; j < 4; ++j) acc = MFMA32(wq[s * 4 + j], oq[j], acc);
      SB();
    SEND
    STAMP(15);
    {
      LAS char* eb = (LAS char*)smem + kh * 17408 + (tm * 32 + (l & 31)) * 272 + (tn * 32 + hb * 4) * 4;
#pragma unroll
      for (int e = 0; e < 4; ++e) *(LAS f32x4*)(eb + e * 32) = (f32x4){acc[4 * e], acc[4 * e + 1], acc[4 * e + 2], acc[4 * e + 3]};
    }
    f32x4 ga[2], cb[2];
#pragma unroll
    for (int e = 0; e < 2; ++e) { ga[e] = *(const LAS f32x4*)((LAS char*)smem + MT + ecol * 32 + e * 16); cb[e] = *(const LAS f32x4*)((LAS char*)smem + MT + 256 + ecol * 32 + e * 16); }
    BAR();
    float xv[8];
    u32x4 x1p, yp;
#pragma unroll
    for (int e = 0; e < 2; ++e) {
      const LAS char* eb = (const LAS char*)smem + erow * 272 + ecol * 32 + e * 16;
      const f32x4 a = *(const LAS f32x4*)eb + *(const LAS f32x4*)(eb + 17408);
#pragma unroll
      for (int h = 0; h < 2; ++h) {
        const u32 pk = pack2(bf_lo(xr[2 * e + h]) + ga[e][2 * h] * a[2 * h], bf_hi(xr[2 * e + h]) + ga[e][2 * h + 1] * a[2 * h + 1]);
        x1p[2 * e + h] = pk;
        xv[4 * e + 2 * h] = bf_lo(pk); xv[4 * e + 2 * h + 1] = bf_hi(pk);
        yp[2 * e + h] = pack2(bf_lo(pk) * cb[e][2 * h], bf_hi(pk) * cb[e][2 * h + 1]);
      }
    }
    GST_SC1("x4", yp, (u32)((ecol >> 1) * 1024 + ((ecol & 1) * 32 + (erow & 31)) * 16), sws, (u32)(rte * 32 + pn3 * 4) * 1024, WS_Y);
    float sum = ((xv[0] + xv[1]) + (xv[2] + xv[3])) + ((xv[4] + xv[5]) + (xv[6] + xv[7]));
    const float mean = col8_sum(sum) * (1.f / 64.f);
    float sq = 0.f;
#pragma unroll
    for (int i = 0; i < 8; ++i) { const float d = xv[i] - mean; sq = __builtin_fmaf(d, d, sq); }
    const f32x2 o2 = {mean, col8_sum(sq)};
    GST_SC1("x2", o2, l < 8 ? (u32)((erow & 31) * 8) : 0x40000000u, sws, (u32)(rte * 2048 + pn3 * 256), WS_ST2);
    STAMP(16);
    WAITV(0);
    RAWBAR();
    signal_cnt(ws, 16 + xcd * 2 + (pm3 >> 1));
    STAMP(17);
    GST_SC1("x4", x1p, (u32)(erow * 8 + ecol) * 16, sws, (u32)bid * 8192, WS_X1);
  }

  const u32 dsb = (u32)(w * 16384 + pn3 * 128), dlb = lb + w * 2048;
  const int r5 = (pn3 * 7) >> 2;
#define CH5(c) (u32)((c) + r5 >= 14 ? (c) + r5 - 14 : (c) + r5)
#define WDPIECE(c, slot, h) GLDS(wvo, sd, dsb + CH5(c) * 131072, (h) * 8192, dlb, (slot) * 16384 + (h) * 1024)
#define WDISSUE(c, slot) do { WDPIECE(c, slot, 0); WDPIECE(c, slot, 1); } while (0)
  const int kq = w & 3;
  const u32 wr5 = wrd0 + kq * 4096;
  bf16x8 dq[14][4];
#define DFRAG1(c, i) dq[c][i] = lds_tr2(smem, (wr5 ^ (((i) & 1) * 64u)) + (c) * 16384 + ((i) >> 1) * 2048, (wr5 ^ (((i) & 1) * 64u)) + (c) * 16384 + ((i) >> 1) * 2048 + 1024)
  if (loc < 28) {
    constexpr u32 RST4 = 157696;
    UV_PART(16);
    wait_cnt(ws, 16 + pan4, 16, w);
    RAWBAR();
    STAMP(18);
    const int rt4 = pan4 * 4;
    const u32 ysb = (u32)(rt4 * 32768 + w * 1024), ylb = lb + w * 1024;
#define YPIECE(i) GLDS_SC1(lo16, sws, ysb, WS_Y + (i) * 8192, ylb, (i) * 8192)
    YPIECE(0); YPIECE(1); YPIECE(2); YPIECE(3);
    GLDS_SC1(lo16, sws, (u32)(rt4 * 2048 + w * 1024), WS_ST2, ylb, 131072);
    YPIECE(4); YPIECE(5);
    float uu[16], vv[16];
#pragma unroll
    for (int i = 0; i < 16; ++i) {
      const auto r = __builtin_amdgcn_permlane16_swap(__float_as_uint(uv[i]), __float_as_uint(uv[i]), false, false);
      uu[i] = __uint_as_float(r[0]); vv[i] = __uint_as_float(r[1]);
      PIN(uu[i]); PIN(vv[i]);
    }
    u32 yrd0 = lo16, yrd1 = lo16 + 65536;
    PIN(yrd1);
    const u32 asb = (u32)(rt4 * 112 + pn4 * 8 + w) * 1024;
#define YFRAG(n) lds_frag(smem, ((n) < 64 ? yrd0 : yrd1) + ((n) & 63) * 1024)
    WAITV(5);
    RAWBAR();
    STAMP(19);
    bf16x8 yf[128];
#pragma unroll
    for (int n = 0; n < 4; ++n) yf[n] = YFRAG(n);
    SB();
#define signal_early()                                                                                               \
    do { unsigned long long ex_;                                                                                     \
      asm volatile("s_cmp_eq_u32 %5, 0\n\ts_cbranch_scc0 1f\n\ts_mov_b64 %0, exec\n\ts_mov_b64 exec, %6\n\t"          \
                   "buffer_atomic_add %1, %2, %3, %4 offen\n\ts_mov_b64 exec, %0\n1:"                                \
                   : "=&s"(ex_) : "v"(1u), "v"(lo16 << 10), "s"(sws), "s"(WS_CNT + (u32)(48 + pan4) * 256), "s"(w), "i"((1 << NREP) - 1) : "memory", "scc"); } while (0)
    f32x16 acc = zero, accp = zero;
    f32x2 stp = {0.f, 0.f};
    float eg[8], eu[8], et[8];
    u32x4 ov;
    SFOR(n, 0, 160)
      constexpr int a = n >> 5, ks = n & 31;
      if (ks == 0 && a > 0) { accp = acc; acc = zero; }
      if constexpr (n < 128) { acc = MFMA32(wf[ks], yf[n], acc); SB(); }
      if (n == 10) { WAITV(7); RAWBAR(); }
      if (n == 26) { WAITV(8); BAR(); }
      if (n == 56) { WAITV(4); RAWBAR(); }
      if (n == 100) WAITV(3);
      if (n == 121) { WAITV(4); RAWBAR(); signal_early(); }
      if constexpr (n < 20 && n % 2 == 0) YPIECE(6 + n / 2);
      if (n == 36 || n == 68 || n == 100) RAWBAR();
      if constexpr (n == 60 && !L2_WARM) {
        const int rr = pm3 * 224 + w * 28 + (l < 28 ? l : 27);
        const int r2 = rr + (rr < 256 ? 768 : 896) + r5 * 128;
        GTOUCH((u32)(r2 >= 1792 ? r2 - 1792 : r2) * 1024, sd, (u32)(pn3 * 128), 0, lb + SCR);
      }
      if constexpr (n >= 36 && n < 52 && n % 4 == 0) WDPIECE((n - 36) / 8, (n - 36) / 8, (n - 36) / 4 & 1);
      if constexpr (n >= 68 && n < 84 && n % 4 == 0) WDPIECE(2 + (n - 68) / 8, 2 + (n - 68) / 8, (n - 68) / 4 & 1);
      if constexpr (n == 84 || n == 88) WDPIECE(8, 8, (n - 84) / 4);
      if constexpr (n >= 100 && n < 116 && n % 4 == 0) WDPIECE(4 + (n - 100) / 8, 4 + (n - 100) / 8, (n - 100) / 4 & 1);
      if constexpr (n >= 100 && n < 116) DFRAG1((n - 100) >> 2, (n - 100) & 3);
      if constexpr (n == 122 || n == 125) WDPIECE(10, 0, n & 1);
      if constexpr (n == 12) {
        const LAS f32x2* sp = (const LAS f32x2*)((LAS char*)smem + 131072 + (t >> 7) * 2048 + (t & 3) * 512 + (t >> 2 & 31) * 8);
        const f32x2 p0 = sp[0], p1 = sp[32];
        float mu = p0[0] + p1[0];
        mu += dpp_xor1(mu); mu += dpp_xor2(mu);
        mu *= 0.125f;
        float q = p0[1] + p1[1] + 64.f * ((p0[0] - mu) * (p0[0] - mu) + (p1[0] - mu) * (p1[0] - mu));
        q += dpp_xor1(q); q += dpp_xor2(q);
        const float rs = __builtin_amdgcn_rsqf(q * (1.f / 512.f) + 1e-5f);
        ((LAS f32x2*)((LAS char*)smem + RST4))[t >> 2] = (f32x2){rs, -mu * rs};
      }
      if (ks == 28 && a < 4) stp = ((const LAS f32x2*)((LAS char*)smem + RST4))[a * 32 + (l & 31)];
      if (n == 128) STAMP(20);
      if (n == 32) STAMP(34);
      if (n == 64) STAMP(35);
      if (n == 96) STAMP(36);
      if (n == 120) STAMP(39);
      if constexpr (n + 4 < 128) yf[n + 4] = YFRAG(n + 4);
      if constexpr (a > 0 && ks >= 2 && ks < 26) {
        constexpr int i = (ks - 2) / 3, ph = (ks - 2) % 3;
        if (ph == 0) {
          eg[i] = __builtin_fmaf(accp[i], stp[0], __builtin_fmaf(uu[i], stp[1], vv[i]));
          eu[i] = __builtin_fmaf(accp[8 + i], stp[0], __builtin_fmaf(uu[8 + i], stp[1], vv[8 + i]));
          et[i] = -1.4426950408889634f * eg[i];
          if (a < 4) { PIN(eg[i]); PIN(eu[i]); PIN(et[i]); }
        } else if (ph == 1) {
          et[i] = 1.f + __builtin_amdgcn_exp2f(et[i]);
          eg[i] *= eu[i];
          if (a < 4) { PIN(eg[i]); PIN(et[i]); }
        } else {
          eg[i] *= __builtin_amdgcn_rcpf(et[i]);
          if (a < 4) PIN(eg[i]);
          if constexpr (i & 1) ov[i >> 1] = pack2(eg[i - 1], eg[i]);
        }
      }
      if constexpr (a > 0 && ks == 26) GST_SC1("x4", ov, lo16, sws, asb, WS_ACT + (a - 1) * 112 * 1024);
      if (n < 128) SB();
    SEND
    STAMP(21);
    WDISSUE(11, 1); WDISSUE(12, 2); WDISSUE(13, 3);
  } else {
    WDISSUE(0, 0); WDISSUE(1, 1); WDISSUE(2, 2); WDISSUE(3, 3); WDISSUE(8, 8); WDISSUE(4, 4); WDISSUE(5, 5);
    WAITV(0);
    RAWBAR();
    SFOR(n, 0, 16) DFRAG1(n >> 2, n & 3); SEND
    BAR();
    WDISSUE(10, 0); WDISSUE(11, 1); WDISSUE(12, 2); WDISSUE(13, 3);
  }

  {
    u32 wra[3][2];
#pragma unroll
    for (int i = 0; i < 3; ++i) { wra[i][0] = wr5 + i * 65536; wra[i][1] = wra[i][0] ^ 64u; PIN(wra[i][0]); PIN(wra[i][1]); }
#define DFRAG(c) _Pragma("unroll") for (int i_ = 0; i_ < 4; ++i_) {                                              \
      const u32 o_ = wra[((c) % 10) >> 2][i_ & 1] + (((c) % 10) & 3) * 16384 + (i_ >> 1) * 2048; dq[c][i_] = lds_tr2(smem, o_, o_ + 1024); }
    f32x4 gb[2];
#pragma unroll
    for (int e = 0; e < 2; ++e) gb[e] = *(const LAS f32x4*)((LAS char*)smem + MT + 512 + (l & 7) * 32 + e * 16);
    const int pan5 = xcd * 2 + (pm3 >> 1);
    wait_cnt(ws, 48 + pan5, 14, w);
    WAITV(0);
    BAR();
    if (loc < 28) signal_cnt(ws, 32 + pan4);
    STAMP(22);
#define WD2(c, slot) do { WDISSUE(c, slot); WDPIECE(c, slot, -8); WDPIECE(c, slot, -7); } while (0)
#define BAR_IF(lo) asm volatile("s_cmp_lt_u32 %0, 4\n\ts_cbranch_scc" lo " 1f\n\ts_barrier\n1:" :: "s"(w) : "memory", "scc")
    if (w >= 4) {
      WD2(6, 6); WD2(7, 7); WD2(9, 9);
      WAITV(0);
      RAWBAR();
      wait_cnt(ws, 32 + pan5, 14, 0);
    }
    STAMP(23);
    const u32 asb = (u32)(rte * 112 + kq * 2) * 1024;
    u32x4 af[14][2];
#define AISSUE(c) do { GLD128_SC1(af[c][0], lo16, sws, asb + CH5(c) * 8192, WS_ACT); GLD128_SC1(af[c][1], lo16, sws, asb + CH5(c) * 8192, WS_ACT + 1024); } while (0)
    SFOR(c, 0, AWIN) AISSUE(c); SEND
    f32x16 acc[2] = {zero, zero};
    u32x4 x1p;
    SFOR(c, 0, 14)
      constexpr int young = c + AWIN <= 14 ? 2 * (AWIN - 1) : (c < 13 ? 2 * (13 - c) + 1 : 0);
      asm volatile("s_waitcnt vmcnt(%0)" :: "i"(young) : "memory");
      if (c == 0) STAMP(24);
      if (c == 5) STAMP(37);
      if (c == 10) STAMP(38);
      if (c == 2) BAR_IF("0");
      if (c == 10) BAR_IF("1");
      if constexpr (c + AWIN < 14) AISSUE(c + AWIN);
      if (c + AWIN == 14) GLD128_SC1(x1p, (u32)t * 16, sws, (u32)bid * 8192, WS_X1);
      if constexpr (c >= 3 && c < 13) { DFRAG(c + 1); }
      PIN(af[c][0]); PIN(af[c][1]);
      SB();
#pragma unroll
      for (int i = 0; i < 4; ++i) acc[i & 1] = MFMA32(dq[c][i], __builtin_bit_cast(bf16x8, af[c][i >> 1]), acc[i & 1]);
      SB();
    SEND
    STAMP(25);
    BAR_IF("0");
    {
      LAS char* eb = (LAS char*)smem + 65536 + kq * 17408 + ((w >> 2) * 32 + (l & 31)) * 272 + hb * 16;
#pragma unroll
      for (int tn = 0; tn < 2; ++tn)
#pragma unroll
        for (int e = 0; e < 4; ++e)
          *(LAS f32x4*)(eb + tn * 128 + e * 32) = (f32x4){acc[tn][4 * e], acc[tn][4 * e + 1], acc[tn][4 * e + 2], acc[tn][4 * e + 3]};
    }
    STAMP(40);
    BAR();
    STAMP(29);
    PIN(x1p);
    u32x4 ov;
#pragma unroll
    for (int e = 0; e < 2; ++e) {
      const LAS char* eb = (const LAS char*)smem + 65536 + (t >> 3) * 272 + (l & 7) * 32 + e * 16;
      const f32x4 s = (*(const LAS f32x4*)eb + *(const LAS f32x4*)(eb + 17408)) + (*(const LAS f32x4*)(eb + 2 * 17408) + *(const LAS f32x4*)(eb + 3 * 17408));
      ov[2 * e] = pack2(bf_lo(x1p[2 * e]) + gb[e][0] * s[0], bf_hi(x1p[2 * e]) + gb[e][1] * s[1]);
      ov[2 * e + 1] = pack2(bf_lo(x1p[2 * e + 1]) + gb[e][2] * s[2], bf_hi(x1p[2 * e + 1]) + gb[e][3] * s[3]);
    }
    *(u32x4*)((char*)out + (size_t)(rte * 32 + (t >> 3 & 31)) * 1024 + pn3 * 128 + (l & 7) * 16) = ov;
  }
  ST_OUT(2, ws);
  asm volatile(".Lcode_end:");
}

extern "C" void kernel_launch(void* const* d_in, const int* in_sizes, int n_in, void* d_out, int out_size, void* d_ws, size_t ws_size,
                              hipStream_t stream) {
  adaln_block<<<256, 512, 0, stream>>>((const u16*)d_in[0], (const u16*)d_in[1], (const u16*)d_in[2], (const u16*)d_in[4], (const u16*)d_in[5],
                                       (const u16*)d_in[6], (const u16*)d_in[7], (const u16*)d_in[8], (char*)d_ws, (u16*)d_out);
}
```

```cpp
#define DG_A 12
#define DG_B 19
#define DG_INS "s_memrealtime"
#define DG_MUL 4
#define DG_DIV 1
#define DG_BLK 128
#include <hip/hip_runtime.h>
#include <hip/hip_bf16.h>

typedef unsigned short u16;
typedef unsigned int u32;
typedef u32 u32x2 __attribute__((ext_vector_type(2)));
typedef u32 u32x4 __attribute__((ext_vector_type(4)));
typedef float f32x2 __attribute__((ext_vector_type(2)));
typedef float f32x4 __attribute__((ext_vector_type(4)));
typedef float f32x8 __attribute__((ext_vector_type(8)));
typedef float f32x16 __attribute__((ext_vector_type(16)));
typedef __bf16 bf16x2 __attribute__((ext_vector_type(2)));
typedef __bf16 bf16x8 __attribute__((ext_vector_type(8)));
typedef __bf16 bf16x16 __attribute__((ext_vector_type(16)));
typedef short s16x4 __attribute__((ext_vector_type(4)));
typedef short s16x8 __attribute__((ext_vector_type(8)));
#define PRIO_MODE 1
#define GATHER_AT 5
#define AORD 3
#define E1 10
#define AWIN 8
#define OUT_WT 0
#define CODE_PF 8
#define L2_WARM 1
#define UV_EARLY 0
#define B_PRIO 0
#define POLL_SLEEP 4
#define NREP 1
#define P4_FIRST 1
#define WD_LATE 2
#define E0 5
#define LAS __attribute__((address_space(3)))
#define DI __device__ __forceinline__

constexpr u32 WS_GRAN = 0;
constexpr u32 WS_EPOCH = 0x18000;
constexpr u32 WS_CNT  = 0x20000;
constexpr u32 WS_QF   = 0x100000;
constexpr u32 WS_KV   = 0x300000;
constexpr u32 WS_OF   = 0x800000;
constexpr u32 WS_X1   = 0xA00000;
constexpr u32 WS_Y    = 0xC00000;
constexpr u32 WS_ST2  = 0xE00000;
constexpr u32 WS_ACT  = 0xF00000;
constexpr u32 WS_BYTES = 0x1600000;
constexpr int KV_TILE = 2112, KV_HEAD = 64 * KV_TILE;

template <int I> struct IC { static constexpr int value = I; };
template <int B, int E, class F> DI void static_for(F&& f) { if constexpr (B < E) { f(IC<B>{}); static_for<B + 1, E>(f); } }
#define SFOR(n, B, E) static_for<B, E>([&](auto ic_) __attribute__((always_inline)) { constexpr int n = decltype(ic_)::value;
#define SEND });

DI float bf_lo(u32 u) { return __uint_as_float(u << 16); }
DI float bf_hi(u32 u) { return __uint_as_float(u & 0xffff0000u); }
DI u32 pack2(float a, float b) { bf16x2 r = {(__bf16)a, (__bf16)b}; return __builtin_bit_cast(u32, r); }
DI float bfround(float a) { return (float)(__bf16)a; }
DI float ln_rstd(float var) { return __builtin_amdgcn_rcpf(bfround(__builtin_amdgcn_sqrtf(bfround(bfround(var) + 1e-5f)))); }
DI float xor32_sum(float v) {
  const auto r = __builtin_amdgcn_permlane32_swap(__float_as_uint(v), __float_as_uint(v), false, false);
  return __uint_as_float(r[0]) + __uint_as_float(r[1]);
}
DI float dpp_xor1(float v) { return __int_as_float(__builtin_amdgcn_mov_dpp(__float_as_int(v), 0xB1, 0xF, 0xF, true)); }
DI float dpp_xor2(float v) { return __int_as_float(__builtin_amdgcn_mov_dpp(__float_as_int(v), 0x4E, 0xF, 0xF, true)); }
DI float shfl_idx(float v, int srcLane) { return __int_as_float(__builtin_amdgcn_ds_bpermute(srcLane << 2, __float_as_int(v))); }

DI u32x4 make_srd(const void* p, u32 bytes) {
  const size_t a = (size_t)p;
  u32x4 r = {(u32)a, (u32)(a >> 32) & 0xffffu, bytes, 0x20000u};
  return r;
}
#define GLDS(voff, srd, sb, GO, lb, LO)                                                                                   \
  do { u32 tmp_;                                                                                                          \
    asm volatile("s_add_u32 m0, %4, %6\n\ts_add_u32 %0, %3, %5\n\tbuffer_load_dwordx4 %1, %2, %0 offen lds"            \
                 : "=&s"(tmp_) : "v"(voff), "s"(srd), "s"(sb), "s"(lb), "i"(GO), "i"(LO) : "memory", "scc"); } while (0)
#define GLD(W, dst, voff, srd, sb, GO)                                                                                    \
  do { u32 tmp_;                                                                                                          \
    asm volatile("s_add_u32 %1, %4, %5\n\tbuffer_load_dword" W " %0, %2, %3, %1 offen"                                   \
                 : "=v"(dst), "=&s"(tmp_) : "v"(voff), "s"(srd), "s"(sb), "i"(GO) : "memory", "scc"); } while (0)
#define GLD128(dst, voff, srd, sb, GO) GLD("x4", dst, voff, srd, sb, GO)
#define GLD64(dst, voff, srd, sb, GO) GLD("x2", dst, voff, srd, sb, GO)
#define GLD32(dst, voff, srd, sb, GO) GLD("", dst, voff, srd, sb, GO)
#define WAITV(N) asm volatile("s_waitcnt vmcnt(" #N ")" ::: "memory")
#define PIN(x) asm volatile("" : "+v"(x))
#define BAR() do { asm volatile("s_waitcnt lgkmcnt(0)" ::: "memory"); __builtin_amdgcn_s_barrier(); asm volatile("" ::: "memory"); } while (0)

#define RTS(d) asm volatile(DG_INS " %0\n\ts_waitcnt lgkmcnt(0)" : "=s"(d) :: "memory")
#define ST_DECL unsigned long long dga_ = 0, dgb_ = 0;
#define STAMP(i) do { if ((i) == DG_A) RTS(dga_); if ((i) == DG_B) RTS(dgb_); } while (0)
#define ST_OUT(kid, wsp) do { STAMP(99); if (blockIdx.x == DG_BLK && threadIdx.x < 64) { const unsigned long long e_ = __builtin_amdgcn_s_memrealtime() + (dgb_ - dga_) * DG_MUL / DG_DIV; while (__builtin_amdgcn_s_memrealtime() < e_) __builtin_amdgcn_s_sleep(2); } } while (0)

DI bf16x8 lds_frag(const char* smem, u32 off) { return *(const LAS bf16x8*)((const LAS char*)smem + off); }
DI bf16x8 lds_tr2(const char* smem, u32 off0, u32 off1) {
  s16x4 a = __builtin_amdgcn_ds_read_tr16_b64_v4i16((LAS s16x4*)((LAS char*)smem + off0));
  s16x4 b = __builtin_amdgcn_ds_read_tr16_b64_v4i16((LAS s16x4*)((LAS char*)smem + off1));
  s16x8 r = __builtin_shufflevector(a, b, 0, 1, 2, 3, 4, 5, 6, 7);
  return __builtin_bit_cast(bf16x8, r);
}
DI u32 lds_base(const char* smem) { return (u32)(size_t)(const LAS char*)smem; }
#define SB() __builtin_amdgcn_sched_barrier(0)
#define RAWBAR() do { asm volatile("" ::: "memory"); __builtin_amdgcn_s_barrier(); asm volatile("" ::: "memory"); } while (0)
#define MFMA32(a, b, c) __builtin_amdgcn_mfma_f32_32x32x16_bf16((a), (b), (c), 0, 0, 0)
#define MFMA16(a, b, c) __builtin_amdgcn_mfma_f32_16x16x32_bf16((a), (b), (c), 0, 0, 0)

DI u32x4 modulate8(const f32x2* n, const f32x2* c1, const f32x2* sh) {
  u32x4 o;
#pragma unroll
  for (int e = 0; e < 4; ++e) { const f32x2 r = __builtin_elementwise_fma(n[e], c1[e], sh[e]); o[e] = pack2(r[0], r[1]); }
  return o;
}

#define GLDS_SC1(voff, srd, sb, GO, lb, LO)                                                                               \
  do { u32 tmp_;                                                                                                          \
    asm volatile("s_add_u32 m0, %4, %6\n\ts_add_u32 %0, %3, %5\n\tbuffer_load_dwordx4 %1, %2, %0 offen sc1 lds"         \
                 : "=&s"(tmp_) : "v"(voff), "s"(srd), "s"(sb), "s"(lb), "i"(GO), "i"(LO) : "memory", "scc"); } while (0)
#define GLD128_SC1(dst, voff, srd, sb, GO)                                                                                \
  do { u32 tmp_;                                                                                                          \
    asm volatile("s_add_u32 %1, %4, %5\n\tbuffer_load_dwordx4 %0, %2, %3, %1 offen sc1"                                 \
                 : "=v"(dst), "=&s"(tmp_) : "v"(voff), "s"(srd), "s"(sb), "i"(GO) : "memory", "scc"); } while (0)
#define GST_SC1(W, val, voff, srd, sb, GO)                                                                                \
  do { u32 tmp_;                                                                                                          \
    asm volatile("s_add_u32 %0, %4, %5\n\tbuffer_store_dword" W " %1, %2, %3, %0 offen sc1\n\ts_nop 1"                  \
                 : "=&s"(tmp_) : "v"(val), "v"(voff), "s"(srd), "s"(sb), "i"(GO) : "memory", "scc"); } while (0)
#define GTOUCH(voff, srd, sb, GO, lb)                                                                                     \
  do { u32 tmp_;                                                                                                          \
    asm volatile("s_mov_b32 m0, %4\n\ts_add_u32 %0, %3, %5\n\tbuffer_load_dword %1, %2, %0 offen lds"                   \
                 : "=&s"(tmp_) : "v"(voff), "s"(srd), "s"(sb), "s"(lb), "i"(GO) : "memory", "scc"); } while (0)
#define GLDS64(vaddr, lb, LO)                                                                                             \
    asm volatile("s_add_u32 m0, %1, %2\n\ts_nop 0\n\tglobal_load_lds_dwordx4 %0, off" :: "v"(vaddr), "s"(lb), "i"(LO) : "memory", "scc")

DI void wave_sum8(const float* v, float* z) {
  float u[4];
#pragma unroll
  for (int i = 0; i < 4; ++i) {
    const auto a = __builtin_amdgcn_permlane32_swap(__float_as_uint(v[i]), __float_as_uint(v[i + 4]), false, false);
    u[i] = __uint_as_float(a[0]) + __uint_as_float(a[1]);
  }
#pragma unroll
  for (int i = 0; i < 2; ++i) {
    const auto a = __builtin_amdgcn_permlane16_swap(__float_as_uint(u[i]), __float_as_uint(u[i + 2]), false, false);
    float s = __uint_as_float(a[0]) + __uint_as_float(a[1]);
    s += dpp_xor1(s); s += dpp_xor2(s);
    s += __int_as_float(__builtin_amdgcn_mov_dpp(__float_as_int(s), 0x141, 0xF, 0xF, true));
    s += __int_as_float(__builtin_amdgcn_mov_dpp(__float_as_int(s), 0x140, 0xF, 0xF, true));
    z[i] = s;
  }
}
#define ROW_OF(z, r) __int_as_float(__builtin_amdgcn_readlane(__float_as_int(z[(r) & 1]), ((r) >> 1) * 16))
DI float col8_sum(float v) {
  v += __int_as_float(__builtin_amdgcn_mov_dpp(__float_as_int(v), 0x128, 0xF, 0xF, true));
  const auto a = __builtin_amdgcn_permlane16_swap(__float_as_uint(v), __float_as_uint(v), false, false);
  return xor32_sum(__uint_as_float(a[0]) + __uint_as_float(a[1]));
}

DI void signal_cnt(char* ws, int idx) {
  if (threadIdx.x < NREP) __hip_atomic_fetch_add((u32*)(ws + WS_CNT) + (threadIdx.x * 64 + idx) * 64, 1u, __ATOMIC_RELAXED, __HIP_MEMORY_SCOPE_AGENT);
}
DI void wait_cnt(char* ws, int idx, u32 want, int w) {
  if (w == 0) {
    u32* c = (u32*)(ws + WS_CNT) + (((blockIdx.x >> 3) % NREP) * 64 + idx) * 64;
#pragma unroll 1
    for (u32 spins = 0; spins < (1u << 17); ++spins) {
      if ((u32)__builtin_amdgcn_readfirstlane(__hip_atomic_load(c, __ATOMIC_RELAXED, __HIP_MEMORY_SCOPE_AGENT)) >= want) break;
      __builtin_amdgcn_s_sleep(POLL_SLEEP);
    }
    asm volatile("" ::: "memory");
  }
}
constexpr int TOUCH0 = 2, NTOUCH = L2_WARM ? 9 : 0;
constexpr int wyoung(int p) {
  int n = (p <= 32 ? p : 32) - (p - 4);
  for (int m = (p - 5) / 3; m < p / 3; ++m) n += (m >= TOUCH0 && m < TOUCH0 + NTOUCH) ? 1 : 0;
  return n;
}
DI float gsum(const u32x4 a) { return __uint_as_float(a[0]) + __uint_as_float(a[2]); }
DI bool gok(const u32x4 a, u32 tag) { return a[1] == tag && a[3] == tag; }
#define SMF(v, p, o) __builtin_amdgcn_smfmac_f32_32x32x32_bf16((v), __builtin_bit_cast(bf16x16, (p)), (o), 0xDDDD, 0, 0)
#define LO8(s) __builtin_shufflevector(s, s, 0, 1, 2, 3, 4, 5, 6, 7)
#define HI8(s) __builtin_shufflevector(s, s, 8, 9, 10, 11, 12, 13, 14, 15)

extern "C" __global__ __launch_bounds__(512) void adaln_block(const u16* __restrict__ x, const u16* __restrict__ wqkv, const u16* __restrict__ wo,
                                                              const u16* __restrict__ wg, const u16* __restrict__ wu, const u16* __restrict__ wd,
                                                              const u16* __restrict__ c, const u16* __restrict__ wmod, char* __restrict__ ws,
                                                              u16* __restrict__ out) {
  __shared__ __attribute__((aligned(16))) char smem[163840];
  ST_DECL STAMP(0);
  const int bid = blockIdx.x, xcd = bid & 7, loc = bid >> 3;
  const int t = threadIdx.x, l = t & 63;
  const int w = __builtin_amdgcn_readfirstlane(t >> 6);
  const u32 lb = lds_base(smem);
  {
    unsigned long long pc;
    u32 len;
    asm volatile("s_getpc_b64 %0\n.Lcode_mark:\n\ts_mov_b32 %1, .Lcode_end-.Lcode_mark" : "=s"(pc), "=s"(len));
    const u32x4 scode = make_srd((const void*)pc, loc < CODE_PF ? len : 0u);
    GTOUCH((u32)(loc * 512 + t) * 128 / CODE_PF, scode, 0u, 0, lb + 57344);
    SB();
  }
  const u32x4 sws = make_srd(ws, WS_BYTES), sx = make_srd(x, 2048 * 1024);
  const u32 lo16 = (u32)l * 16, zs = 0;
  const int hb = l >> 5;
  const int pm3 = loc >> 3, pn3 = loc & 7;
  constexpr u32 TAB = 159744, MT = TAB + 2048, SCR = TAB;
  u32 tag;
  asm volatile("" :: "s"(x), "s"(wqkv), "s"(wo), "s"(wg), "s"(wu), "s"(wd), "s"(c), "s"(wmod), "s"(ws), "s"(out));

  {
    constexpr u32 WSLOT = 24576, RED = 49152, MTAB = 53248;
    const int pm = (xcd >> 1) * 8 + (loc >> 2), cg = (xcd & 1) * 4 + (loc & 3);
    const u32x4 sw = make_srd(wqkv, 512 * 3072), sm = make_srd(wmod, bid < 96 ? 512 * 6144 : 0), sc = make_srd(c, bid < 96 ? 1024 : 0);
    const int mcb = bid >> 1, mkq = bid & 1;
    u32 ev;
    u32x2 cv;
    u32x4 mv[4], xa[8];
    if (bid == 0 && w < NREP) GST_SC1("", zs, (u32)t * 256, sws, zs, WS_CNT);
    asm volatile("buffer_load_dword %0, %1, %2, %3 offen sc1" : "=v"(ev) : "v"(zs), "s"(sws), "s"(WS_EPOCH) : "memory");
#pragma unroll
    for (int i = 0; i < 4; ++i) GLD128(mv[i], (u32)((t >> 3) * 4 * 6144 + (t & 7) * 16), sm, (u32)(mkq * 256 * 6144 + mcb * 128), i * 6144);
    GLD64(cv, (u32)((t >> 3) * 8), sc, (u32)(mkq * 512), 0);
    RAWBAR();
    const int rx = loc & 3, rw = loc >> 2;
    const u32 xsb = (u32)(pm * 64 + w * 2) * 1024;
#define XROW(i) GLD128(xa[i], lo16, sx, xsb + (u32)(((i) >> 1) ^ rx) * 16384, ((i) & 1) * 1024)
    const u32 wvo = (u32)((l >> 3) * 3072 + ((((l >> 2) ^ (l >> 4)) & 1) * 32 + (l & 3) * 8) * 2);
    const u32 wsb = (u32)((w & 3) * 8 * 3072 + cg * 128), wlb = lb + (w & 3) * 1024;
#define KA_SLOT(kc) (((kc) >> 1 == 1 ? 0 : ((kc) >> 1 == 2 ? 65536 + 2 * WSLOT : 65536)) + ((kc) & 1) * WSLOT)
#define KA_CHUNK(kc) SFOR(n_, 0, 6) GLDS(wvo, sw, wsb + (u32)(((kc) + rw) & 7) * (64 * 3072), (n_ >> 1) * 1024 + (n_ & 1) * 32 * 3072, wlb, KA_SLOT(kc) + (n_ >> 1) * 8192 + (n_ & 1) * 4096); SEND
    STAMP(26);
    XROW(0); XROW(1); XROW(2);
    STAMP(27);
    WAITV(3);
    STAMP(28);
    PIN(ev); PIN(cv);
#pragma unroll
    for (int i = 0; i < 4; ++i) PIN(mv[i]);
    tag = (u32)__builtin_amdgcn_readfirstlane(ev) + 1u;
    XROW(3); XROW(4); XROW(5);
    if (bid < 96) {
      float acc[8];
#pragma unroll
      for (int e = 0; e < 8; ++e) acc[e] = 0.f;
#pragma unroll
      for (int i = 0; i < 4; ++i) {
        const float cc = (i & 1) ? bf_hi(cv[i >> 1]) : bf_lo(cv[i >> 1]);
        const float sv = bfround(cc * __builtin_amdgcn_rcpf(1.f + __builtin_amdgcn_exp2f(-1.4426950408889634f * cc)));
#pragma unroll
        for (int e = 0; e < 4; ++e) { acc[2 * e] += sv * bf_lo(mv[i][e]); acc[2 * e + 1] += sv * bf_hi(mv[i][e]); }
      }
      float u[4], z[2];
#pragma unroll
      for (int i = 0; i < 4; ++i) {
        const auto a = __builtin_amdgcn_permlane32_swap(__float_as_uint(acc[i]), __float_as_uint(acc[i + 4]), false, false);
        u[i] = __uint_as_float(a[0]) + __uint_as_float(a[1]);
      }
#pragma unroll
      for (int i = 0; i < 2; ++i) {
        const auto a = __builtin_amdgcn_permlane16_swap(__float_as_uint(u[i]), __float_as_uint(u[i + 2]), false, false);
        z[i] = __uint_as_float(a[0]) + __uint_as_float(a[1]);
        z[i] += __int_as_float(__builtin_amdgcn_mov_dpp(__float_as_int(z[i]), 0x128, 0xF, 0xF, true));
      }
      *(LAS f32x2*)((LAS char*)smem + RED + w * 256 + (l & 7) * 32 + (l >> 4) * 8) = (f32x2){z[0], z[1]};
      BAR();
      if (w == 7) {
        const LAS float* rp = (const LAS float*)((LAS char*)smem + RED) + l;
        const float v = ((rp[0] + rp[64]) + (rp[128] + rp[192])) + ((rp[256] + rp[320]) + (rp[384] + rp[448]));
        const u32x2 gr = {__float_as_uint(v), tag};
        GST_SC1("x2", gr, lo16, sws, (u32)(mcb * 1024 + mkq * 8), WS_GRAN);
      }
    }
    STAMP(1);
    XROW(6); XROW(7);
    RAWBAR();
    u32x4 gq[2];
#define GATHER(tail)                                                                                                            \
    asm volatile("buffer_load_dwordx4 %0, %2, %3, %4 offen sc1\n\tbuffer_load_dwordx4 %1, %2, %3, %5 offen sc1" tail            \
                 : "=&v"(gq[0]), "=&v"(gq[1]) : "v"((u32)t * 16), "s"(sws), "s"(WS_GRAN), "s"(WS_GRAN + 8192) : "memory")
    const int kind = w < 4 ? (w >> 1) : 2, hp = w < 4 ? (w & 1) : (w >> 1 & 1);
    u32 brd0 = (u32)(kind * 8192 + (hb * 8 + (l >> 2 & 3)) * 128 + ((hp ^ (l >> 3)) & 1) * 64 + (l >> 4 & 1) * 32 + (l & 3) * 8);
    u32 brd1 = brd0 + 65536, brd2 = brd0 + 65536 + 2 * WSLOT;
    PIN(brd1); PIN(brd2);
    bf16x8 wq[32];
#define KA_FRAG(kc) _Pragma("unroll") for (int c_ = 0; c_ < 4; ++c_) {                                                    \
      const u32 o_ = ((kc) >> 1 == 1 ? brd0 : ((kc) >> 1 == 2 ? brd2 : brd1)) + ((kc) & 1) * WSLOT + c_ * 2048; wq[(kc) * 4 + c_] = lds_tr2(smem, o_, o_ + 512); }
    if (w < 4) { KA_CHUNK(0) KA_CHUNK(1) KA_CHUNK(2) KA_CHUNK(3) WAITV(12); RAWBAR(); KA_FRAG(0); KA_FRAG(1); BAR(); } else WAITV(0);
    STAMP(30);
    f32x2 nv[8][4];
    {
      float v[8], z[2], q[2];
#pragma unroll
      for (int i = 0; i < 8; ++i) {
        PIN(xa[i]);
#pragma unroll
        for (int e = 0; e < 4; ++e) nv[i][e] = (f32x2){bf_lo(xa[i][e]), bf_hi(xa[i][e])};
        const f32x2 s2 = (nv[i][0] + nv[i][1]) + (nv[i][2] + nv[i][3]);
        v[i] = s2[0] + s2[1];
      }
      STAMP(31);
      wave_sum8(v, z);
      z[0] *= (1.f / 512.f); z[1] *= (1.f / 512.f);
      SFOR(i, 0, 8)
        const float mu = ROW_OF(z, i);
        f32x2 s2 = {0.f, 0.f};
        _Pragma("unroll") for (int e = 0; e < 4; ++e) { nv[i][e] -= (f32x2){mu, mu}; s2 = __builtin_elementwise_fma(nv[i][e], nv[i][e], s2); }
        v[i] = s2[0] + s2[1];
      SEND
      wave_sum8(v, q);
#pragma unroll
      for (int j = 0; j < 2; ++j) { q[j] = ln_rstd(q[j] * (1.f / 512.f)); z[j] = (z[j] - bfround(z[j])) * q[j]; }
      SFOR(i, 0, 8)
        const float rs = ROW_OF(q, i), mr = ROW_OF(z, i);
        _Pragma("unroll") for (int e = 0; e < 4; ++e) { nv[i][e] = __builtin_elementwise_fma(nv[i][e], (f32x2){rs, rs}, (f32x2){mr, mr}); }
      SEND
#pragma unroll
      for (int i = 0; i < 8; ++i)
#pragma unroll
        for (int e = 0; e < 4; ++e) PIN(nv[i][e]);
    }
    if (w >= 4) { RAWBAR(); KA_FRAG(0); KA_FRAG(1); BAR(); KA_CHUNK(4) KA_CHUNK(5) }
    GATHER("");
    if (w >= 4) { KA_CHUNK(6) KA_CHUNK(7) } else WAITV(2);
    STAMP(2);
    RAWBAR();
    KA_FRAG(2); KA_FRAG(3);
    if (w >= 4) WAITV(12); else WAITV(0);
    PIN(gq[0]); PIN(gq[1]);
    STAMP(3);
#pragma unroll 1
    for (u32 spins = 0; spins < (1u << 17); ++spins) {
      if (__all(gok(gq[0], tag) && gok(gq[1], tag))) break;
      __builtin_amdgcn_s_sleep(2);
      GATHER("\n\ts_waitcnt vmcnt(0)");
    }
    ((LAS float*)((LAS char*)smem + MTAB))[t] = bfround(1.f + bfround(gsum(gq[1])));
    ((LAS float*)((LAS char*)smem + MTAB))[512 + t] = bfround(gsum(gq[0]));
    BAR();
    STAMP(33);
    f32x2 c1[4], sh[4];
    {
      const LAS f32x4* tc = (const LAS f32x4*)((LAS char*)smem + MTAB + l * 32);
      const f32x4 a0 = tc[0], a1 = tc[1], b0 = tc[128], b1 = tc[129];
#pragma unroll
      for (int e = 0; e < 2; ++e) {
        c1[e] = (f32x2){a0[2 * e], a0[2 * e + 1]}; c1[2 + e] = (f32x2){a1[2 * e], a1[2 * e + 1]};
        sh[e] = (f32x2){b0[2 * e], b0[2 * e + 1]}; sh[2 + e] = (f32x2){b1[2 * e], b1[2 * e + 1]};
      }
    }
    u32 awr[4];
#pragma unroll
    for (int i = 0; i < 4; ++i) awr[i] = (u32)((((l >> 1) - 4 * rw) & 31) * 1024 + (l & 1) * 512 + (((i >> 1) ^ rx) & 1) * 256 + (((w * 2 + (i & 1)) ^ (l & 7)) << 4));
#define KA_PUT(i) *(LAS u32x4*)((LAS char*)smem + awr[(i) & 3] + ((i) >> 2) * 32768) = hv[i]
    u32x4 hv[8];
#pragma unroll
    for (int i = 0; i < 4; ++i) { hv[i] = modulate8(nv[i], c1, sh); KA_PUT(i); }
    const int ta = w < 4 ? 0 : (w & 1), tx = rx >> 1;
    u32 ard[4];
#pragma unroll
    for (int i = 0; i < 4; ++i) ard[i] = (u32)(ta * 32768 + i * 1024 + hb * 512 + (((l & 31) ^ (i * 2 + hb)) << 4));
#define HFRAG(n) lds_frag(smem, ard[(n) & 3] + ((n) >> 2) * 4096)
    const int head = cg * 4 + hp * 2;
    const u32 kvb = WS_KV + (u32)(head * KV_HEAD + pm * 2 * KV_TILE);
    f32x16 z16;
#pragma unroll
    for (int i = 0; i < 16; ++i) z16[i] = 0.f;
    BAR();
    STAMP(4);
    if (w < 4) {
      const float qs = kind == 0 ? 0.25f * 1.4426950408889634f : 1.f;
      const u32 so = kind == 0 ? WS_QF + (u32)(head * 64 + pm * 2) * 1024 + lo16 : kvb + (u32)((l & 51) | (l & 4) << 1 | (l & 8) >> 1) * 16;
      const u32 sh_ = kind == 0 ? 65536u : (u32)KV_HEAD, st_ = kind == 0 ? 1024u : (u32)KV_TILE;
      bf16x8 hf[64];
      f32x16 acc = z16, accp = z16;
      u32x4 ov[2];
#pragma unroll
      for (int i = 0; i < 4; ++i) hf[i] = HFRAG(i);
      SB();
      SFOR(n, 0, 76)
        if constexpr (n < 4) { hv[4 + n] = modulate8(nv[4 + n], c1, sh); KA_PUT(4 + n); }
        if constexpr (n == 5) { KA_FRAG(4); }
        if constexpr (n == 9) RAWBAR();
        if constexpr (n == 11) { KA_FRAG(5); }
        if constexpr (n == 15) { KA_FRAG(6); }
        if constexpr (n == 19) { KA_FRAG(7); }
        if constexpr (n == 32 || n == 64) { accp = acc; acc = z16; }
        if constexpr (n + 4 < 64) hf[n + 4] = HFRAG(n + 4);
        if constexpr (n < 64) acc = MFMA32(wq[n & 31], hf[n], acc);
        if constexpr (n >= 32 && (n & 31) >= 2 && (n & 31) < 10) {
          constexpr int e = (n & 31) - 2;
          ov[e >> 2][e & 3] = pack2(accp[2 * e] * qs, accp[2 * e + 1] * qs);
        }
        if constexpr (n == 42 || n == 74) GST_SC1("x4", ov[0], so, sws, st_ * ((n / 32 - 1) ^ tx), 0);
        if constexpr (n == 43 || n == 75) GST_SC1("x4", ov[1], so, sws, st_ * ((n / 32 - 1) ^ tx) + sh_, 0);
        if (n < 64) SB();
      SEND
    } else {
      bf16x8 hf[32];
      f32x16 acc = z16;
#pragma unroll
      for (int i = 4; i < 8; ++i) { hv[i] = modulate8(nv[i], c1, sh); KA_PUT(i); }
      WAITV(0);
      BAR();
      __builtin_amdgcn_s_setprio(B_PRIO);
#pragma unroll
      for (int i = 0; i < 4; ++i) hf[i] = HFRAG(i);
      SB();
      SFOR(n, 0, 32)
        if constexpr (n >= 2 && n < 18 && (n & 3) == 2) { KA_FRAG(4 + n / 4); }
        if constexpr (n + 4 < 32) hf[n + 4] = HFRAG(n + 4);
        acc = MFMA32(hf[n], wq[n], acc);
        SB();
      SEND
      const u32 kvt = kvb + (u32)((ta ^ tx) * KV_TILE);
      const u32 vo = kvt + (u32)((l >> 4 & 1) * KV_HEAD + 1024 + hb * 272 + (l & 15) * 16);
#pragma unroll
      for (int e = 0; e < 2; ++e) {
        const u32x4 ov = {pack2(acc[8 * e], acc[8 * e + 1]), pack2(acc[8 * e + 2], acc[8 * e + 3]), pack2(acc[8 * e + 4], acc[8 * e + 5]), pack2(acc[8 * e + 6], acc[8 * e + 7])};
        GST_SC1("x4", ov, vo, sws, zs, e * 544);
      }
      const u32x4 one = {0x3f803f80u, 0x3f803f80u, 0x3f803f80u, 0x3f803f80u};
      GST_SC1("x4", one, l < 8 ? kvt + (u32)((l >> 2) * KV_HEAD + 1024 + (l & 3) * 272 + 256) : 0x40000000u, sws, zs, 0);
    }
    __builtin_amdgcn_s_setprio(0);
    STAMP(5);
    u32x4 tq[3];
    const u32 msb = WS_GRAN + (u32)(((w == 0 ? 1024 : (w == 1 ? 2048 : 2560)) + pn3 * 64) * 16);
#define TGATHER(tail)                                                                                                           \
    asm volatile("buffer_load_dwordx4 %0, %3, %5, %6 offen sc1\n\tbuffer_load_dwordx4 %1, %3, %5, %7 offen sc1\n\t"             \
                 "buffer_load_dwordx4 %2, %4, %5, %8 offen sc1" tail                                                            \
                 : "=&v"(tq[0]), "=&v"(tq[1]), "=&v"(tq[2])                                                                     \
                 : "v"((u32)t * 16), "v"(lo16), "s"(sws), "s"(WS_GRAN + 1536 * 16), "s"(WS_GRAN + 2048 * 16), "s"(msb) : "memory")
    TGATHER("");
    WAITV(3);
    STAMP(6);
    BAR();
    signal_cnt(ws, cg);
    WAITV(0);
#pragma unroll
    for (int i = 0; i < 3; ++i) PIN(tq[i]);
#pragma unroll 1
    for (u32 spins = 0; spins < (1u << 17); ++spins) {
      if (__all(gok(tq[0], tag) && gok(tq[1], tag) && gok(tq[2], tag))) break;
      __builtin_amdgcn_s_sleep(2);
      TGATHER("\n\ts_waitcnt vmcnt(0)");
    }
    *(LAS u16*)((LAS char*)smem + TAB + t * 2) = (u16)(__float_as_uint(bfround(1.f + bfround(gsum(tq[1])))) >> 16);
    *(LAS u16*)((LAS char*)smem + TAB + 1024 + t * 2) = (u16)(__float_as_uint(bfround(gsum(tq[0]))) >> 16);
    const float mv_ = bfround(gsum(tq[2]));
    ((LAS float*)((LAS char*)smem + MT))[t] = (w == 1) ? bfround(1.f + mv_) : mv_;
  }

  const int lq = loc >= 28 ? 27 : loc;
  const int pan4 = (xcd >> 1) * 4 + lq / 7, pn4 = (xcd & 1) * 7 + lq % 7;
  constexpr u32 STG = KV_HEAD;
  f32x16 zero;
#pragma unroll
  for (int i = 0; i < 16; ++i) zero[i] = 0.f;
  bf16x8 wf[32];
  const u32x4 so = make_srd(wo, 512 * 1024), sd = make_srd(wd, 1792 * 1024);

  {
    const char* wsrc = (const char*)((l & 2) ? wu : wg) + (size_t)(l >> 2) * 3584 + (pn4 * 128 + w * 16 + (l & 1) * 8) * 2;
    const u32 stg_lb = lb + w * 3072;
    u32 stg_rd0 = (u32)(w * 3072 + (hb * 8 + (l >> 2 & 3)) * 64 + (l >> 4 & 1) * 32 + (l & 3) * 8), stg_rd1 = stg_rd0 + STG;
    PIN(stg_rd1);
#define WREAD(p)                                                                                        \
    do {                                                                                                \
      if constexpr ((p) >= 5) {                                                                         \
        asm volatile("s_waitcnt vmcnt(%0)" :: "i"(wyoung(p)) : "memory");                               \
        const u32 o_ = ((((p) - 5) % 6) < 3 ? stg_rd1 : stg_rd0) + (((p) - 5) % 3) * 1024;              \
        wf[(p) - 5] = lds_tr2(smem, o_, o_ + 256);                                                      \
      }                                                                                                 \
    } while (0)
#define WLOAD(p) do { if ((p) < 32) { GLDS64(wsrc, stg_lb, (((p) % 6) < 3 ? STG : 0u) + ((p) % 3) * 1024u); wsrc += 16 * 3584; } } while (0)
    const int head = xcd * 4 + (loc >> 3), qt = (loc & 7) * 8 + w;
    const u32 ksb = (u32)(head * KV_HEAD + w * 1024), wlb = lb + w * 1024;
    const int r = l & 31;
    u32 krd = lo16;
    u32 vrd = (u32)(1024 + hb * 272 + (r < 16 ? r * 16 : (r == 16 ? 256 : 0x40000)));
    STAMP(7);
    wait_cnt(ws, xcd, 32, w);
    BAR();
    STAMP(8);
#define KVR(r) GLDS_SC1(lo16, sws, ksb, WS_KV + (r) * 8192, wlb, (r) * 8192)
    u32x4 qraw;
    GLD128_SC1(qraw, lo16, sws, (u32)(head * 64 + qt) * 1024, WS_QF);
    KVR(0); KVR(1);
    if (w < 4) KVR(16);
    KVR(2); KVR(3);
    WAITV(2);
    PIN(qraw);
    RAWBAR();
    STAMP(9);
    const bf16x8 qf = __builtin_bit_cast(bf16x8, qraw);
    float m;
    bf16x8 kf = lds_frag(smem, krd);
    {
      f32x16 s0 = MFMA32(kf, qf, zero);
      m = s0[0];
#pragma unroll
      for (int i = 1; i < 16; ++i) m = fmaxf(m, s0[i]);
      const auto a = __builtin_amdgcn_permlane32_swap(__float_as_uint(m), __float_as_uint(m), false, false);
      m = fmaxf(__uint_as_float(a[0]), __uint_as_float(a[1]));
    }
    f32x16 negm;
#pragma unroll
    for (int i = 0; i < 16; ++i) negm[i] = -m;
    f32x16 o = zero;
    f32x16 sc = MFMA32(kf, qf, negm);
    kf = lds_frag(smem, krd + KV_TILE);
    bf16x8 v0 = lds_frag(smem, vrd), v1;
    const u32 tvo = (u32)((l & 7) * 1024 + pn3 * 128), tsb = (u32)(pm3 * 8 + w) * 8192;
    SFOR(j, 0, 64)
      if constexpr ((j & 1) == 0) __builtin_amdgcn_s_setprio(3 - (j >> 1 & 3));
      if (j == 5) { WAITV(10); RAWBAR(); }
      if (j == 13) { WAITV(0); RAWBAR(); }
      if (j == 14) STAMP(10);
      constexpr bool wst = j >= 14 && j < 63 && (j - 14) % 4 != 3;
      constexpr int wp = (j - 14) - (j - 14) / 4;
      if (j == 28 || j == 56) { krd += 28 * KV_TILE; vrd += 28 * KV_TILE; PIN(krd); PIN(vrd); }
      const u32 jo = (u32)(j % 28) * KV_TILE;
#define EXPS(a, b) _Pragma("unroll") for (int i = (a); i < (b); ++i) sc[i] = __builtin_amdgcn_exp2f(sc[i])
      f32x16 sn;
#define QKNEXT() do { if (j < 63) asm volatile("v_mfma_f32_32x32x16_bf16 %0, %1, %2, %3" : "=&v"(sn) : "v"(kf), "v"(qf), "v"(negm)); } while (0)
#define KREAD() do { if (j < 62) kf = lds_frag(smem, krd + jo + 2 * KV_TILE); } while (0)
#define SIDE()                                                                                                             \
      do {                                                                                                                 \
        if constexpr (wst) { WREAD(wp); WLOAD(wp); }                                                                       \
        if constexpr (j >= 14 && (j - 14) % 4 == 3) {     \
          constexpr int m = (j - 14) / 4 - TOUCH0;                                                                         \
          if (m >= 0 && m < 7 && NTOUCH) GTOUCH(tvo, sd, tsb * 7, (m < 0 ? 0 : m) * 8192, lb + 25600);                     \
          if (m >= 7 && m < NTOUCH) GTOUCH(tvo, so, tsb * 2, (m < 7 ? 0 : m - 7) * 8192, lb + 25600);                      \
        }                                                                                                                  \
        if constexpr (j < 6) { KVR(4 + 2 * j); KVR(5 + 2 * j); }                                                           \
      } while (0)
      v0 = lds_frag(smem, vrd + jo); v1 = lds_frag(smem, vrd + jo + 544);
      SB();
      EXPS(j == 0 ? 0 : 2, E0);
      PIN(sc);
      QKNEXT(); SB(); KREAD(); SB();
      EXPS(E0, E1);
      SB();
      o = SMF(v0, LO8(sc), o);
      SIDE();
      SB();
      EXPS(E1, 16);
      PIN(sc);
      if (j < 63) { sn[0] = __builtin_amdgcn_exp2f(sn[0]); sn[1] = __builtin_amdgcn_exp2f(sn[1]); PIN(sn); }
      SB();
      o = SMF(v1, HI8(sc), o);
      PIN(o);
      SB();
      if (j < 63) sc = sn;
    SEND
    float lsum = __uint_as_float(__builtin_amdgcn_permlane32_swap(__float_as_uint(o[8]), __float_as_uint(o[8]), false, false)[0]);
    bool ok = lsum < 1e18f;
#pragma unroll
    for (int i = 0; i < 8; ++i) ok = ok && (fabsf(o[i]) < 3e38f);
    if (!__all(ok)) {
      asm volatile("" ::: "memory");
      const __amdgpu_buffer_rsrc_t rk = __builtin_amdgcn_make_buffer_rsrc(ws + WS_KV + (size_t)head * KV_HEAD, 0, KV_HEAD, 0x20000);
      const int vg = 1024 + hb * 272 + (r == 16 ? 256 : (r & 15) * 16);
#define KVLD(off, j) __builtin_bit_cast(bf16x8, __builtin_amdgcn_raw_buffer_load_b128(rk, (off), (j) * KV_TILE, 16))
#pragma unroll 1
      for (int j = 0; j < 64; ++j) {
        const f32x16 sv = MFMA32(KVLD(lo16, j), qf, zero);
#pragma unroll
        for (int i = 0; i < 16; ++i) m = fmaxf(m, sv[i]);
      }
      m = fmaxf(m, __shfl_xor(m, 32));
#pragma unroll
      for (int i = 0; i < 16; ++i) negm[i] = -m;
      o = zero;
#pragma unroll 1
      for (int j = 0; j < 64; ++j) {
        f32x16 sv = MFMA32(KVLD(lo16, j), qf, negm);
        const bf16x8 v0 = KVLD(vg, j), v1 = KVLD(vg + 544, j);
#pragma unroll
        for (int i = 0; i < 16; ++i) sv[i] = __builtin_amdgcn_exp2f(sv[i]);
        o = SMF(v0, LO8(sv), o);
        o = SMF(v1, HI8(sv), o);
      }
      lsum = shfl_idx(o[8], r);
    }
    const float inv = 1.f / lsum;
    u32x4 ov;
#pragma unroll
    for (int e = 0; e < 4; ++e) ov[e] = pack2(o[2 * e] * inv, o[2 * e + 1] * inv);
    GST_SC1("x4", ov, lo16, sws, (u32)(qt * 32 + head) * 1024, WS_OF);
    STAMP(11);
    WAITV(0);
    BAR();
    signal_cnt(ws, 8 + (loc & 7));
    STAMP(12);
  }

  const int tm = w & 1;
  const u32 wvo = (u32)((l >> 3) * 1024 + ((((l >> 2) ^ (l >> 4)) & 1) * 32 + (l & 3) * 8) * 2);
  const u32 wrd0 = (u32)((hb * 4 + (l >> 2 & 3)) * 128 + (l >> 3 & 1) * 64 + (l >> 4 & 1) * 32 + (l & 3) * 8);
  const int rte = xcd * 8 + (pm3 >> 1) * 4 + (pm3 & 1) + (w >> 2) * 2;
  const int erow = w * 8 + (l & 7), ecol = l >> 3;
  f32x16 uv = zero;
  const u32 trd = TAB + (u32)((l >> 4 & 1) * 1024 + hb * 16);
#define UV_PART(k0)                                                                                          \
  do { bf16x8 tf_[16];                                                                                       \
    _Pragma("unroll") for (int i_ = 0; i_ < 4; ++i_) tf_[i_] = lds_frag(smem, trd + ((k0) + i_) * 32);       \
    _Pragma("unroll") for (int i_ = 0; i_ < 16; ++i_) {                                                      \
      if (i_ + 4 < 16) tf_[i_ + 4] = lds_frag(smem, trd + ((k0) + i_ + 4) * 32);                             \
      uv = MFMA32(wf[(k0) + i_], tf_[i_], uv); SB(); }                                                       \
    PIN(uv); } while (0)

  {
    const int ro = pn3 & 3;
    SFOR(j, 0, 8) GLDS(wvo, so, (u32)(w * 8192 + pn3 * 128 + ((j + 2 * ro) & 7) * 65536), 0, lb + w * 1024, j * 8192); SEND
    u32x4 xr;
    GLD128(xr, (u32)((erow & 31) * 1024 + ecol * 16), sx, (u32)(rte * 32768 + pn3 * 128), 0);
    UV_PART(0);
    WAITV(0);
    PIN(xr);
    STAMP(13);
    wait_cnt(ws, 8 + xcd, 32, w);
    RAWBAR();
    STAMP(14);
    if (bid == 0 && t == 0) __hip_atomic_store((u32*)(ws + WS_EPOCH), tag, __ATOMIC_RELAXED, __HIP_MEMORY_SCOPE_AGENT);
    const u32 olo = (u32)((w >> 2) * 32768 + (w & 3) * 2048);
    const u32 osb = (u32)(rte * 32768 + (w & 3) * 2048), olb = lb + olo;
    SFOR(j, 0, 8) GLDS_SC1(lo16, sws, osb + (u32)(((j >> 1) + ro) & 3) * 8192, WS_OF + (j & 1) * 1024, olb, 65536 + (j >> 1) * 8192 + (j & 1) * 1024); SEND
    const int tn = w >> 1 & 1, kh = w >> 2;
    u32 ord = 65536 + (u32)(tm * 32768 + kh * 4096) + lo16;
    PIN(ord);
    const u32 wrd = (wrd0 ^ (u32)(tn * 64)) + kh * 8192;
    f32x16 acc = zero;
    bf16x8 wq[16];
#define WOFRAG(n) wq[n] = lds_tr2(smem, wrd + ((n) >> 2) * 16384 + ((n) & 3) * 2048, wrd + ((n) >> 2) * 16384 + ((n) & 3) * 2048 + 1024)
#pragma unroll
    for (int n = 0; n < 8; ++n) WOFRAG(n);
    SFOR(s, 0, 4)
      if (s == 0) WAITV(6); else if (s == 1) WAITV(4); else if (s == 2) WAITV(2); else WAITV(0);
      RAWBAR();
      bf16x8 oq[4];
#pragma unroll
      for (int j = 0; j < 4; ++j) oq[j] = lds_frag(smem, ord + s * 8192 + j * 1024);
      if constexpr (s < 2) {
#pragma unroll
        for (int j = 0; j < 4; ++j) WOFRAG(8 + s * 4 + j);
      }
      SB();
#pragma unroll
      for (int j = 0; j < 4; ++j) acc = MFMA32(wq[s * 4 + j], oq[j], acc);
      SB();
    SEND
    STAMP(15);
    {
      LAS char* eb = (LAS char*)smem + kh * 17408 + (tm * 32 + (l & 31)) * 272 + (tn * 32 + hb * 4) * 4;
#pragma unroll
      for (int e = 0; e < 4; ++e) *(LAS f32x4*)(eb + e * 32) = (f32x4){acc[4 * e], acc[4 * e + 1], acc[4 * e + 2], acc[4 * e + 3]};
    }
    f32x4 ga[2], cb[2];
#pragma unroll
    for (int e = 0; e < 2; ++e) { ga[e] = *(const LAS f32x4*)((LAS char*)smem + MT + ecol * 32 + e * 16); cb[e] = *(const LAS f32x4*)((LAS char*)smem + MT + 256 + ecol * 32 + e * 16); }
    BAR();
    float xv[8];
    u32x4 x1p, yp;
#pragma unroll
    for (int e = 0; e < 2; ++e) {
      const LAS char* eb = (const LAS char*)smem + erow * 272 + ecol * 32 + e * 16;
      const f32x4 a = *(const LAS f32x4*)eb + *(const LAS f32x4*)(eb + 17408);
#pragma unroll
      for (int h = 0; h < 2; ++h) {
        const u32 pk = pack2(bf_lo(xr[2 * e + h]) + ga[e][2 * h] * a[2 * h], bf_hi(xr[2 * e + h]) + ga[e][2 * h + 1] * a[2 * h + 1]);
        x1p[2 * e + h] = pk;
        xv[4 * e + 2 * h] = bf_lo(pk); xv[4 * e + 2 * h + 1] = bf_hi(pk);
        yp[2 * e + h] = pack2(bf_lo(pk) * cb[e][2 * h], bf_hi(pk) * cb[e][2 * h + 1]);
      }
    }
    GST_SC1("x4", yp, (u32)((ecol >> 1) * 1024 + ((ecol & 1) * 32 + (erow & 31)) * 16), sws, (u32)(rte * 32 + pn3 * 4) * 1024, WS_Y);
    float sum = ((xv[0] + xv[1]) + (xv[2] + xv[3])) + ((xv[4] + xv[5]) + (xv[6] + xv[7]));
    const float mean = col8_sum(sum) * (1.f / 64.f);
    float sq = 0.f;
#pragma unroll
    for (int i = 0; i < 8; ++i) { const float d = xv[i] - mean; sq = __builtin_fmaf(d, d, sq); }
    const f32x2 o2 = {mean, col8_sum(sq)};
    GST_SC1("x2", o2, l < 8 ? (u32)((erow & 31) * 8) : 0x40000000u, sws, (u32)(rte * 2048 + pn3 * 256), WS_ST2);
    STAMP(16);
    WAITV(0);
    RAWBAR();
    signal_cnt(ws, 16 + xcd * 2 + (pm3 >> 1));
    STAMP(17);
    GST_SC1("x4", x1p, (u32)(erow * 8 + ecol) * 16, sws, (u32)bid * 8192, WS_X1);
  }

  const u32 dsb = (u32)(w * 16384 + pn3 * 128), dlb = lb + w * 2048;
  const int r5 = (pn3 * 7) >> 2;
#define CH5(c) (u32)((c) + r5 >= 14 ? (c) + r5 - 14 : (c) + r5)
#define WDPIECE(c, slot, h) GLDS(wvo, sd, dsb + CH5(c) * 131072, (h) * 8192, dlb, (slot) * 16384 + (h) * 1024)
#define WDISSUE(c, slot) do { WDPIECE(c, slot, 0); WDPIECE(c, slot, 1); } while (0)
  const int kq = w & 3;
  const u32 wr5 = wrd0 + kq * 4096;
  bf16x8 dq[14][4];
#define DFRAG1(c, i) dq[c][i] = lds_tr2(smem, (wr5 ^ (((i) & 1) * 64u)) + (c) * 16384 + ((i) >> 1) * 2048, (wr5 ^ (((i) & 1) * 64u)) + (c) * 16384 + ((i) >> 1) * 2048 + 1024)
  if (loc < 28) {
    constexpr u32 RST4 = 157696;
    UV_PART(16);
    wait_cnt(ws, 16 + pan4, 16, w);
    RAWBAR();
    STAMP(18);
    const int rt4 = pan4 * 4;
    const u32 ysb = (u32)(rt4 * 32768 + w * 1024), ylb = lb + w * 1024;
#define YPIECE(i) GLDS_SC1(lo16, sws, ysb, WS_Y + (i) * 8192, ylb, (i) * 8192)
    YPIECE(0); YPIECE(1); YPIECE(2); YPIECE(3);
    GLDS_SC1(lo16, sws, (u32)(rt4 * 2048 + w * 1024), WS_ST2, ylb, 131072);
    YPIECE(4); YPIECE(5);
    float uu[16], vv[16];
#pragma unroll
    for (int i = 0; i < 16; ++i) {
      const auto r = __builtin_amdgcn_permlane16_swap(__float_as_uint(uv[i]), __float_as_uint(uv[i]), false, false);
      uu[i] = __uint_as_float(r[0]); vv[i] = __uint_as_float(r[1]);
      PIN(uu[i]); PIN(vv[i]);
    }
    u32 yrd0 = lo16, yrd1 = lo16 + 65536;
    PIN(yrd1);
    const u32 asb = (u32)(rt4 * 112 + pn4 * 8 + w) * 1024;
#define YFRAG(n) lds_frag(smem, ((n) < 64 ? yrd0 : yrd1) + ((n) & 63) * 1024)
    WAITV(5);
    RAWBAR();
    STAMP(19);
    bf16x8 yf[128];
#pragma unroll
    for (int n = 0; n < 4; ++n) yf[n] = YFRAG(n);
    SB();
#define signal_early()                                                                                               \
    do { unsigned long long ex_;                                                                                     \
      asm volatile("s_cmp_eq_u32 %5, 0\n\ts_cbranch_scc0 1f\n\ts_mov_b64 %0, exec\n\ts_mov_b64 exec, %6\n\t"          \
                   "buffer_atomic_add %1, %2, %3, %4 offen\n\ts_mov_b64 exec, %0\n1:"                                \
                   : "=&s"(ex_) : "v"(1u), "v"(lo16 << 10), "s"(sws), "s"(WS_CNT + (u32)(48 + pan4) * 256), "s"(w), "i"((1 << NREP) - 1) : "memory", "scc"); } while (0)
    f32x16 acc = zero, accp = zero;
    f32x2 stp = {0.f, 0.f};
    float eg[8], eu[8], et[8];
    u32x4 ov;
    SFOR(n, 0, 160)
      constexpr int a = n >> 5, ks = n & 31;
      if (ks == 0 && a > 0) { accp = acc; acc = zero; }
      if constexpr (n < 128) { acc = MFMA32(wf[ks], yf[n], acc); SB(); }
      if (n == 10) { WAITV(7); RAWBAR(); }
      if (n == 26) { WAITV(8); BAR(); }
      if (n == 56) { WAITV(4); RAWBAR(); }
      if (n == 100) WAITV(3);
      if (n == 121) { WAITV(4); RAWBAR(); signal_early(); }
      if constexpr (n < 20 && n % 2 == 0) YPIECE(6 + n / 2);
      if (n == 36 || n == 68 || n == 100) RAWBAR();
      if constexpr (n == 60 && !L2_WARM) {
        const int rr = pm3 * 224 + w * 28 + (l < 28 ? l : 27);
        const int r2 = rr + (rr < 256 ? 768 : 896) + r5 * 128;
        GTOUCH((u32)(r2 >= 1792 ? r2 - 1792 : r2) * 1024, sd, (u32)(pn3 * 128), 0, lb + SCR);
      }
      if constexpr (n >= 36 && n < 52 && n % 4 == 0) WDPIECE((n - 36) / 8, (n - 36) / 8, (n - 36) / 4 & 1);
      if constexpr (n >= 68 && n < 84 && n % 4 == 0) WDPIECE(2 + (n - 68) / 8, 2 + (n - 68) / 8, (n - 68) / 4 & 1);
      if constexpr (n == 84 || n == 88) WDPIECE(8, 8, (n - 84) / 4);
      if constexpr (n >= 100 && n < 116 && n % 4 == 0) WDPIECE(4 + (n - 100) / 8, 4 + (n - 100) / 8, (n - 100) / 4 & 1);
      if constexpr (n >= 100 && n < 116) DFRAG1((n - 100) >> 2, (n - 100) & 3);
      if constexpr (n == 122 || n == 125) WDPIECE(10, 0, n & 1);
      if constexpr (n == 12) {
        const LAS f32x2* sp = (const LAS f32x2*)((LAS char*)smem + 131072 + (t >> 7) * 2048 + (t & 3) * 512 + (t >> 2 & 31) * 8);
        const f32x2 p0 = sp[0], p1 = sp[32];
        float mu = p0[0] + p1[0];
        mu += dpp_xor1(mu); mu += dpp_xor2(mu);
        mu *= 0.125f;
        float q = p0[1] + p1[1] + 64.f * ((p0[0] - mu) * (p0[0] - mu) + (p1[0] - mu) * (p1[0] - mu));
        q += dpp_xor1(q); q += dpp_xor2(q);
        const float rs = __builtin_amdgcn_rsqf(q * (1.f / 512.f) + 1e-5f);
        ((LAS f32x2*)((LAS char*)smem + RST4))[t >> 2] = (f32x2){rs, -mu * rs};
      }
      if (ks == 28 && a < 4) stp = ((const LAS f32x2*)((LAS char*)smem + RST4))[a * 32 + (l & 31)];
      if (n == 128) STAMP(20);
      if (n == 32) STAMP(34);
      if (n == 64) STAMP(35);
      if (n == 96) STAMP(36);
      if (n == 120) STAMP(39);
      if constexpr (n + 4 < 128) yf[n + 4] = YFRAG(n + 4);
      if constexpr (a > 0 && ks >= 2 && ks < 26) {
        constexpr int i = (ks - 2) / 3, ph = (ks - 2) % 3;
        if (ph == 0) {
          eg[i] = __builtin_fmaf(accp[i], stp[0], __builtin_fmaf(uu[i], stp[1], vv[i]));
          eu[i] = __builtin_fmaf(accp[8 + i], stp[0], __builtin_fmaf(uu[8 + i], stp[1], vv[8 + i]));
          et[i] = -1.4426950408889634f * eg[i];
          if (a < 4) { PIN(eg[i]); PIN(eu[i]); PIN(et[i]); }
        } else if (ph == 1) {
          et[i] = 1.f + __builtin_amdgcn_exp2f(et[i]);
          eg[i] *= eu[i];
          if (a < 4) { PIN(eg[i]); PIN(et[i]); }
        } else {
          eg[i] *= __builtin_amdgcn_rcpf(et[i]);
          if (a < 4) PIN(eg[i]);
          if constexpr (i & 1) ov[i >> 1] = pack2(eg[i - 1], eg[i]);
        }
      }
      if constexpr (a > 0 && ks == 26) GST_SC1("x4", ov, lo16, sws, asb, WS_ACT + (a - 1) * 112 * 1024);
      if (n < 128) SB();
    SEND
    STAMP(21);
    WDISSUE(11, 1); WDISSUE(12, 2); WDISSUE(13, 3);
  } else {
    WDISSUE(0, 0); WDISSUE(1, 1); WDISSUE(2, 2); WDISSUE(3, 3); WDISSUE(8, 8); WDISSUE(4, 4); WDISSUE(5, 5);
    WAITV(0);
    RAWBAR();
    SFOR(n, 0, 16) DFRAG1(n >> 2, n & 3); SEND
    BAR();
    WDISSUE(10, 0); WDISSUE(11, 1); WDISSUE(12, 2); WDISSUE(13, 3);
  }

  {
    u32 wra[3][2];
#pragma unroll
    for (int i = 0; i < 3; ++i) { wra[i][0] = wr5 + i * 65536; wra[i][1] = wra[i][0] ^ 64u; PIN(wra[i][0]); PIN(wra[i][1]); }
#define DFRAG(c) _Pragma("unroll") for (int i_ = 0; i_ < 4; ++i_) {                                              \
      const u32 o_ = wra[((c) % 10) >> 2][i_ & 1] + (((c) % 10) & 3) * 16384 + (i_ >> 1) * 2048; dq[c][i_] = lds_tr2(smem, o_, o_ + 1024); }
    f32x4 gb[2];
#pragma unroll
    for (int e = 0; e < 2; ++e) gb[e] = *(const LAS f32x4*)((LAS char*)smem + MT + 512 + (l & 7) * 32 + e * 16);
    const int pan5 = xcd * 2 + (pm3 >> 1);
    wait_cnt(ws, 48 + pan5, 14, w);
    WAITV(0);
    BAR();
    if (loc < 28) signal_cnt(ws, 32 + pan4);
    STAMP(22);
#define WD2(c, slot) do { WDISSUE(c, slot); WDPIECE(c, slot, -8); WDPIECE(c, slot, -7); } while (0)
#define BAR_IF(lo) asm volatile("s_cmp_lt_u32 %0, 4\n\ts_cbranch_scc" lo " 1f\n\ts_barrier\n1:" :: "s"(w) : "memory", "scc")
    if (w >= 4) {
      WD2(6, 6); WD2(7, 7); WD2(9, 9);
      WAITV(0);
      RAWBAR();
      wait_cnt(ws, 32 + pan5, 14, 0);
    }
    STAMP(23);
    const u32 asb = (u32)(rte * 112 + kq * 2) * 1024;
    u32x4 af[14][2];
#define AISSUE(c) do { GLD128_SC1(af[c][0], lo16, sws, asb + CH5(c) * 8192, WS_ACT); GLD128_SC1(af[c][1], lo16, sws, asb + CH5(c) * 8192, WS_ACT + 1024); } while (0)
    SFOR(c, 0, AWIN) AISSUE(c); SEND
    f32x16 acc[2] = {zero, zero};
    u32x4 x1p;
    SFOR(c, 0, 14)
      constexpr int young = c + AWIN <= 14 ? 2 * (AWIN - 1) : (c < 13 ? 2 * (13 - c) + 1 : 0);
      asm volatile("s_waitcnt vmcnt(%0)" :: "i"(young) : "memory");
      if (c == 0) STAMP(24);
      if (c == 5) STAMP(37);
      if (c == 10) STAMP(38);
      if (c == 2) BAR_IF("0");
      if (c == 10) BAR_IF("1");
      if constexpr (c + AWIN < 14) AISSUE(c + AWIN);
      if (c + AWIN == 14) GLD128_SC1(x1p, (u32)t * 16, sws, (u32)bid * 8192, WS_X1);
      if constexpr (c >= 3 && c < 13) { DFRAG(c + 1); }
      PIN(af[c][0]); PIN(af[c][1]);
      SB();
#pragma unroll
      for (int i = 0; i < 4; ++i) acc[i & 1] = MFMA32(dq[c][i], __builtin_bit_cast(bf16x8, af[c][i >> 1]), acc[i & 1]);
      SB();
    SEND
    STAMP(25);
    BAR_IF("0");
    {
      LAS char* eb = (LAS char*)smem + 65536 + kq * 17408 + ((w >> 2) * 32 + (l & 31)) * 272 + hb * 16;
#pragma unroll
      for (int tn = 0; tn < 2; ++tn)
#pragma unroll
        for (int e = 0; e < 4; ++e)
          *(LAS f32x4*)(eb + tn * 128 + e * 32) = (f32x4){acc[tn][4 * e], acc[tn][4 * e + 1], acc[tn][4 * e + 2], acc[tn][4 * e + 3]};
    }
    STAMP(40);
    BAR();
    STAMP(29);
    PIN(x1p);
    u32x4 ov;
#pragma unroll
    for (int e = 0; e < 2; ++e) {
      const LAS char* eb = (const LAS char*)smem + 65536 + (t >> 3) * 272 + (l & 7) * 32 + e * 16;
      const f32x4 s = (*(const LAS f32x4*)eb + *(const LAS f32x4*)(eb + 17408)) + (*(const LAS f32x4*)(eb + 2 * 17408) + *(const LAS f32x4*)(eb + 3 * 17408));
      ov[2 * e] = pack2(bf_lo(x1p[2 * e]) + gb[e][0] * s[0], bf_hi(x1p[2 * e]) + gb[e][1] * s[1]);
      ov[2 * e + 1] = pack2(bf_lo(x1p[2 * e + 1]) + gb[e][2] * s[2], bf_hi(x1p[2 * e + 1]) + gb[e][3] * s[3]);
    }
    *(u32x4*)((char*)out + (size_t)(rte * 32 + (t >> 3 & 31)) * 1024 + pn3 * 128 + (l & 7) * 16) = ov;
  }
  ST_OUT(2, ws);
  asm volatile(".Lcode_end:");
}

extern "C" void kernel_launch(void* const* d_in, const int* in_sizes, int n_in, void* d_out, int out_size, void* d_ws, size_t ws_size,
                              hipStream_t stream) {
  adaln_block<<<256, 512, 0, stream>>>((const u16*)d_in[0], (const u16*)d_in[1], (const u16*)d_in[2], (const u16*)d_in[4], (const u16*)d_in[5],
                                       (const u16*)d_in[6], (const u16*)d_in[7], (const u16*)d_in[8], (char*)d_ws, (u16*)d_out);
}
```

```cpp
#define DG_A 777
#define DG_B 98
#define DG_INS "s_memrealtime"
#define DG_MUL 0
#define DG_DIV 1
#define DG_BLK 128
#include <hip/hip_runtime.h>
#include <hip/hip_bf16.h>

typedef unsigned short u16;
typedef unsigned int u32;
typedef u32 u32x2 __attribute__((ext_vector_type(2)));
typedef u32 u32x4 __attribute__((ext_vector_type(4)));
typedef float f32x2 __attribute__((ext_vector_type(2)));
typedef float f32x4 __attribute__((ext_vector_type(4)));
typedef float f32x8 __attribute__((ext_vector_type(8)));
typedef float f32x16 __attribute__((ext_vector_type(16)));
typedef __bf16 bf16x2 __attribute__((ext_vector_type(2)));
typedef __bf16 bf16x8 __attribute__((ext_vector_type(8)));
typedef __bf16 bf16x16 __attribute__((ext_vector_type(16)));
typedef short s16x4 __attribute__((ext_vector_type(4)));
typedef short s16x8 __attribute__((ext_vector_type(8)));
#define PRIO_MODE 1
#define GATHER_AT 5
#define AORD 3
#define E1 10
#define AWIN 8
#define OUT_WT 0
#define CODE_PF 8
#define L2_WARM 1
#define UV_EARLY 0
#define B_PRIO 0
#define POLL_SLEEP 4
#define NREP 1
#define P4_FIRST 1
#define WD_LATE 2
#define E0 5
#define LAS __attribute__((address_space(3)))
#define DI __device__ __forceinline__

constexpr u32 WS_GRAN = 0;
constexpr u32 WS_EPOCH = 0x18000;
constexpr u32 WS_CNT  = 0x20000;
constexpr u32 WS_QF   = 0x100000;
constexpr u32 WS_KV   = 0x300000;
constexpr u32 WS_OF   = 0x800000;
constexpr u32 WS_X1   = 0xA00000;
constexpr u32 WS_Y    = 0xC00000;
constexpr u32 WS_ST2  = 0xE00000;
constexpr u32 WS_ACT  = 0xF00000;
constexpr u32 WS_BYTES = 0x1600000;
constexpr int KV_TILE = 2112, KV_HEAD = 64 * KV_TILE;

template <int I> struct IC { static constexpr int value = I; };
template <int B, int E, class F> DI void static_for(F&& f) { if constexpr (B < E) { f(IC<B>{}); static_for<B + 1, E>(f); } }
#define SFOR(n, B, E) static_for<B, E>([&](auto ic_) __attribute__((always_inline)) { constexpr int n = decltype(ic_)::value;
#define SEND });

DI float bf_lo(u32 u) { return __uint_as_float(u << 16); }
DI float bf_hi(u32 u) { return __uint_as_float(u & 0xffff0000u); }
DI u32 pack2(float a, float b) { bf16x2 r = {(__bf16)a, (__bf16)b}; return __builtin_bit_cast(u32, r); }
DI float bfround(float a) { return (float)(__bf16)a; }
DI float ln_rstd(float var) { return __builtin_amdgcn_rcpf(bfround(__builtin_amdgcn_sqrtf(bfround(bfround(var) + 1e-5f)))); }
DI float xor32_sum(float v) {
  const auto r = __builtin_amdgcn_permlane32_swap(__float_as_uint(v), __float_as_uint(v), false, false);
  return __uint_as_float(r[0]) + __uint_as_float(r[1]);
}
DI float dpp_xor1(float v) { return __int_as_float(__builtin_amdgcn_mov_dpp(__float_as_int(v), 0xB1, 0xF, 0xF, true)); }
DI float dpp_xor2(float v) { return __int_as_float(__builtin_amdgcn_mov_dpp(__float_as_int(v), 0x4E, 0xF, 0xF, true)); }
DI float shfl_idx(float v, int srcLane) { return __int_as_float(__builtin_amdgcn_ds_bpermute(srcLane << 2, __float_as_int(v))); }

DI u32x4 make_srd(const void* p, u32 bytes) {
  const size_t a = (size_t)p;
  u32x4 r = {(u32)a, (u32)(a >> 32) & 0xffffu, bytes, 0x20000u};
  return r;
}
#define GLDS(voff, srd, sb, GO, lb, LO)                                                                                   \
  do { u32 tmp_;                                                                                                          \
    asm volatile("s_add_u32 m0, %4, %6\n\ts_add_u32 %0, %3, %5\n\tbuffer_load_dwordx4 %1, %2, %0 offen lds"            \
                 : "=&s"(tmp_) : "v"(voff), "s"(srd), "s"(sb), "s"(lb), "i"(GO), "i"(LO) : "memory", "scc"); } while (0)
#define GLD(W, dst, voff, srd, sb, GO)                                                                                    \
  do { u32 tmp_;                                                                                                          \
    asm volatile("s_add_u32 %1, %4, %5\n\tbuffer_load_dword" W " %0, %2, %3, %1 offen"                                   \
                 : "=v"(dst), "=&s"(tmp_) : "v"(voff), "s"(srd), "s"(sb), "i"(GO) : "memory", "scc"); } while (0)
#define GLD128(dst, voff, srd, sb, GO) GLD("x4", dst, voff, srd, sb, GO)
#define GLD64(dst, voff, srd, sb, GO) GLD("x2", dst, voff, srd, sb, GO)
#define GLD32(dst, voff, srd, sb, GO) GLD("", dst, voff, srd, sb, GO)
#define WAITV(N) asm volatile("s_waitcnt vmcnt(" #N ")" ::: "memory")
#define PIN(x) asm volatile("" : "+v"(x))
#define BAR() do { asm volatile("s_waitcnt lgkmcnt(0)" ::: "memory"); __builtin_amdgcn_s_barrier(); asm volatile("" ::: "memory"); } while (0)

#define RTS(d) asm volatile(DG_INS " %0\n\ts_waitcnt lgkmcnt(0)" : "=s"(d) :: "memory")
#define ST_DECL unsigned long long dga_ = 0, dgb_ = 0;
#define STAMP(i) do { if ((i) == DG_A) RTS(dga_); if ((i) == DG_B) RTS(dgb_); } while (0)
#define ST_OUT(kid, wsp) do { STAMP(99); if (blockIdx.x == DG_BLK && threadIdx.x < 64) { const unsigned long long e_ = __builtin_amdgcn_s_memrealtime() + (dgb_ - dga_) * DG_MUL / DG_DIV; while (__builtin_amdgcn_s_memrealtime() < e_) __builtin_amdgcn_s_sleep(2); } } while (0)

DI bf16x8 lds_frag(const char* smem, u32 off) { return *(const LAS bf16x8*)((const LAS char*)smem + off); }
DI bf16x8 lds_tr2(const char* smem, u32 off0, u32 off1) {
  s16x4 a = __builtin_amdgcn_ds_read_tr16_b64_v4i16((LAS s16x4*)((LAS char*)smem + off0));
  s16x4 b = __builtin_amdgcn_ds_read_tr16_b64_v4i16((LAS s16x4*)((LAS char*)smem + off1));
  s16x8 r = __builtin_shufflevector(a, b, 0, 1, 2, 3, 4, 5, 6, 7);
  return __builtin_bit_cast(bf16x8, r);
}
DI u32 lds_base(const char* smem) { return (u32)(size_t)(const LAS char*)smem; }
#define SB() __builtin_amdgcn_sched_barrier(0)
#define RAWBAR() do { asm volatile("" ::: "memory"); __builtin_amdgcn_s_barrier(); asm volatile("" ::: "memory"); } while (0)
#define MFMA32(a, b, c) __builtin_amdgcn_mfma_f32_32x32x16_bf16((a), (b), (c), 0, 0, 0)
#define MFMA16(a, b, c) __builtin_amdgcn_mfma_f32_16x16x32_bf16((a), (b), (c), 0, 0, 0)

DI u32x4 modulate8(const f32x2* n, const f32x2* c1, const f32x2* sh) {
  u32x4 o;
#pragma unroll
  for (int e = 0; e < 4; ++e) { const f32x2 r = __builtin_elementwise_fma(n[e], c1[e], sh[e]); o[e] = pack2(r[0], r[1]); }
  return o;
}

#define GLDS_SC1(voff, srd, sb, GO, lb, LO)                                                                               \
  do { u32 tmp_;                                                                                                          \
    asm volatile("s_add_u32 m0, %4, %6\n\ts_add_u32 %0, %3, %5\n\tbuffer_load_dwordx4 %1, %2, %0 offen sc1 lds"         \
                 : "=&s"(tmp_) : "v"(voff), "s"(srd), "s"(sb), "s"(lb), "i"(GO), "i"(LO) : "memory", "scc"); } while (0)
#define GLD128_SC1(dst, voff, srd, sb, GO)                                                                                \
  do { u32 tmp_;                                                                                                          \
    asm volatile("s_add_u32 %1, %4, %5\n\tbuffer_load_dwordx4 %0, %2, %3, %1 offen sc1"                                 \
                 : "=v"(dst), "=&s"(tmp_) : "v"(voff), "s"(srd), "s"(sb), "i"(GO) : "memory", "scc"); } while (0)
#define GST_SC1(W, val, voff, srd, sb, GO)                                                                                \
  do { u32 tmp_;                                                                                                          \
    asm volatile("s_add_u32 %0, %4, %5\n\tbuffer_store_dword" W " %1, %2, %3, %0 offen sc1\n\ts_nop 1"                  \
                 : "=&s"(tmp_) : "v"(val), "v"(voff), "s"(srd), "s"(sb), "i"(GO) : "memory", "scc"); } while (0)
#define GTOUCH(voff, srd, sb, GO, lb)                                                                                     \
  do { u32 tmp_;                                                                                                          \
    asm volatile("s_mov_b32 m0, %4\n\ts_add_u32 %0, %3, %5\n\tbuffer_load_dword %1, %2, %0 offen lds"                   \
                 : "=&s"(tmp_) : "v"(voff), "s"(srd), "s"(sb), "s"(lb), "i"(GO) : "memory", "scc"); } while (0)
#define GLDS64(vaddr, lb, LO)                                                                                             \
    asm volatile("s_add_u32 m0, %1, %2\n\ts_nop 0\n\tglobal_load_lds_dwordx4 %0, off" :: "v"(vaddr), "s"(lb), "i"(LO) : "memory", "scc")

DI void wave_sum8(const float* v, float* z) {
  float u[4];
#pragma unroll
  for (int i = 0; i < 4; ++i) {
    const auto a = __builtin_amdgcn_permlane32_swap(__float_as_uint(v[i]), __float_as_uint(v[i + 4]), false, false);
    u[i] = __uint_as_float(a[0]) + __uint_as_float(a[1]);
  }
#pragma unroll
  for (int i = 0; i < 2; ++i) {
    const auto a = __builtin_amdgcn_permlane16_swap(__float_as_uint(u[i]), __float_as_uint(u[i + 2]), false, false);
    float s = __uint_as_float(a[0]) + __uint_as_float(a[1]);
    s += dpp_xor1(s); s += dpp_xor2(s);
    s += __int_as_float(__builtin_amdgcn_mov_dpp(__float_as_int(s), 0x141, 0xF, 0xF, true));
    s += __int_as_float(__builtin_amdgcn_mov_dpp(__float_as_int(s), 0x140, 0xF, 0xF, true));
    z[i] = s;
  }
}
#define ROW_OF(z, r) __int_as_float(__builtin_amdgcn_readlane(__float_as_int(z[(r) & 1]), ((r) >> 1) * 16))
DI float col8_sum(float v) {
  v += __int_as_float(__builtin_amdgcn_mov_dpp(__float_as_int(v), 0x128, 0xF, 0xF, true));
  const auto a = __builtin_amdgcn_permlane16_swap(__float_as_uint(v), __float_as_uint(v), false, false);
  return xor32_sum(__uint_as_float(a[0]) + __uint_as_float(a[1]));
}

DI void signal_cnt(char* ws, int idx) {
  if (threadIdx.x < NREP) __hip_atomic_fetch_add((u32*)(ws + WS_CNT) + (threadIdx.x * 64 + idx) * 64, 1u, __ATOMIC_RELAXED, __HIP_MEMORY_SCOPE_AGENT);
}
DI void wait_cnt(char* ws, int idx, u32 want, int w) {
  if (w == 0) {
    u32* c = (u32*)(ws + WS_CNT) + (((blockIdx.x >> 3) % NREP) * 64 + idx) * 64;
#pragma unroll 1
    for (u32 spins = 0; spins < (1u << 17); ++spins) {
      if ((u32)__builtin_amdgcn_readfirstlane(__hip_atomic_load(c, __ATOMIC_RELAXED, __HIP_MEMORY_SCOPE_AGENT)) >= want) break;
      __builtin_amdgcn_s_sleep(POLL_SLEEP);
    }
    asm volatile("" ::: "memory");
  }
}
constexpr int TOUCH0 = 2, NTOUCH = L2_WARM ? 9 : 0;
constexpr int wyoung(int p) {
  int n = (p <= 32 ? p : 32) - (p - 4);
  for (int m = (p - 5) / 3; m < p / 3; ++m) n += (m >= TOUCH0 && m < TOUCH0 + NTOUCH) ? 1 : 0;
  return n;
}
DI float gsum(const u32x4 a) { return __uint_as_float(a[0]) + __uint_as_float(a[2]); }
DI bool gok(const u32x4 a, u32 tag) { return a[1] == tag && a[3] == tag; }
#define SMF(v, p, o) __builtin_amdgcn_smfmac_f32_32x32x32_bf16((v), __builtin_bit_cast(bf16x16, (p)), (o), 0xDDDD, 0, 0)
#define LO8(s) __builtin_shufflevector(s, s, 0, 1, 2, 3, 4, 5, 6, 7)
#define HI8(s) __builtin_shufflevector(s, s, 8, 9, 10, 11, 12, 13, 14, 15)

extern "C" __global__ __launch_bounds__(512) void adaln_block(const u16* x_, const u16* a1, const u16* a2, const u16* a3, const u16* a4, const u16* a5, const u16* a6, const u16* a7, char* a8, u16* a9) {
  __shared__ __attribute__((aligned(16))) char smem[163840];
  ST_DECL
  typedef const __attribute__((address_space(4))) unsigned long long* kap_t;
  kap_t ka = (kap_t)__builtin_amdgcn_kernarg_segment_ptr();
  asm volatile("s_memrealtime %0\n\ts_waitcnt lgkmcnt(0)" : "=s"(dga_), "+s"(ka) :: "memory");
  const u16* x = (const u16*)ka[0]; const u16* wqkv = (const u16*)ka[1]; const u16* wo = (const u16*)ka[2]; const u16* wg = (const u16*)ka[3];
  const u16* wu = (const u16*)ka[4]; const u16* wd = (const u16*)ka[5]; const u16* c = (const u16*)ka[6]; const u16* wmod = (const u16*)ka[7];
  char* ws = (char*)ka[8]; u16* out = (u16*)ka[9];
  const int bid = blockIdx.x, xcd = bid & 7, loc = bid >> 3;
  const int t = threadIdx.x, l = t & 63;
  const int w = __builtin_amdgcn_readfirstlane(t >> 6);
  const u32 lb = lds_base(smem);
  {
    unsigned long long pc;
    u32 len;
    asm volatile("s_getpc_b64 %0\n.Lcode_mark:\n\ts_mov_b32 %1, .Lcode_end-.Lcode_mark" : "=s"(pc), "=s"(len));
    const u32x4 scode = make_srd((const void*)pc, loc < CODE_PF ? len : 0u);
    GTOUCH((u32)(loc * 512 + t) * 128 / CODE_PF, scode, 0u, 0, lb + 57344);
    SB();
  }
  const u32x4 sws = make_srd(ws, WS_BYTES), sx = make_srd(x, 2048 * 1024);
  const u32 lo16 = (u32)l * 16, zs = 0;
  const int hb = l >> 5;
  const int pm3 = loc >> 3, pn3 = loc & 7;
  constexpr u32 TAB = 159744, MT = TAB + 2048, SCR = TAB;
  u32 tag;
  asm volatile("" :: "s"(x), "s"(wqkv), "s"(wo), "s"(wg), "s"(wu), "s"(wd), "s"(c), "s"(wmod), "s"(ws), "s"(out));
  STAMP(98);

  {
    constexpr u32 WSLOT = 24576, RED = 49152, MTAB = 53248;
    const int pm = (xcd >> 1) * 8 + (loc >> 2), cg = (xcd & 1) * 4 + (loc & 3);
    const u32x4 sw = make_srd(wqkv, 512 * 3072), sm = make_srd(wmod, bid < 96 ? 512 * 6144 : 0), sc = make_srd(c, bid < 96 ? 1024 : 0);
    const int mcb = bid >> 1, mkq = bid & 1;
    u32 ev;
    u32x2 cv;
    u32x4 mv[4], xa[8];
    if (bid == 0 && w < NREP) GST_SC1("", zs, (u32)t * 256, sws, zs, WS_CNT);
    asm volatile("buffer_load_dword %0, %1, %2, %3 offen sc1" : "=v"(ev) : "v"(zs), "s"(sws), "s"(WS_EPOCH) : "memory");
#pragma unroll
    for (int i = 0; i < 4; ++i) GLD128(mv[i], (u32)((t >> 3) * 4 * 6144 + (t & 7) * 16), sm, (u32)(mkq * 256 * 6144 + mcb * 128), i * 6144);
    GLD64(cv, (u32)((t >> 3) * 8), sc, (u32)(mkq * 512), 0);
    RAWBAR();
    const int rx = loc & 3, rw = loc >> 2;
    const u32 xsb = (u32)(pm * 64 + w * 2) * 1024;
#define XROW(i) GLD128(xa[i], lo16, sx, xsb + (u32)(((i) >> 1) ^ rx) * 16384, ((i) & 1) * 1024)
    const u32 wvo = (u32)((l >> 3) * 3072 + ((((l >> 2) ^ (l >> 4)) & 1) * 32 + (l & 3) * 8) * 2);
    const u32 wsb = (u32)((w & 3) * 8 * 3072 + cg * 128), wlb = lb + (w & 3) * 1024;
#define KA_SLOT(kc) (((kc) >> 1 == 1 ? 0 : ((kc) >> 1 == 2 ? 65536 + 2 * WSLOT : 65536)) + ((kc) & 1) * WSLOT)
#define KA_CHUNK(kc) SFOR(n_, 0, 6) GLDS(wvo, sw, wsb + (u32)(((kc) + rw) & 7) * (64 * 3072), (n_ >> 1) * 1024 + (n_ & 1) * 32 * 3072, wlb, KA_SLOT(kc) + (n_ >> 1) * 8192 + (n_ & 1) * 4096); SEND
    STAMP(26);
    XROW(0); XROW(1); XROW(2);
    STAMP(27);
    WAITV(3);
    STAMP(28);
    PIN(ev); PIN(cv);
#pragma unroll
    for (int i = 0; i < 4; ++i) PIN(mv[i]);
    tag = (u32)__builtin_amdgcn_readfirstlane(ev) + 1u;
    XROW(3); XROW(4); XROW(5);
    if (bid < 96) {
      float acc[8];
#pragma unroll
      for (int e = 0; e < 8; ++e) acc[e] = 0.f;
#pragma unroll
      for (int i = 0; i < 4; ++i) {
        const float cc = (i & 1) ? bf_hi(cv[i >> 1]) : bf_lo(cv[i >> 1]);
        const float sv = bfround(cc * __builtin_amdgcn_rcpf(1.f + __builtin_amdgcn_exp2f(-1.4426950408889634f * cc)));
#pragma unroll
        for (int e = 0; e < 4; ++e) { acc[2 * e] += sv * bf_lo(mv[i][e]); acc[2 * e + 1] += sv * bf_hi(mv[i][e]); }
      }
      float u[4], z[2];
#pragma unroll
      for (int i = 0; i < 4; ++i) {
        const auto a = __builtin_amdgcn_permlane32_swap(__float_as_uint(acc[i]), __float_as_uint(acc[i + 4]), false, false);
        u[i] = __uint_as_float(a[0]) + __uint_as_float(a[1]);
      }
#pragma unroll
      for (int i = 0; i < 2; ++i) {
        const auto a = __builtin_amdgcn_permlane16_swap(__float_as_uint(u[i]), __float_as_uint(u[i + 2]), false, false);
        z[i] = __uint_as_float(a[0]) + __uint_as_float(a[1]);
        z[i] += __int_as_float(__builtin_amdgcn_mov_dpp(__float_as_int(z[i]), 0x128, 0xF, 0xF, true));
      }
      *(LAS f32x2*)((LAS char*)smem + RED + w * 256 + (l & 7) * 32 + (l >> 4) * 8) = (f32x2){z[0], z[1]};
      BAR();
      if (w == 7) {
        const LAS float* rp = (const LAS float*)((LAS char*)smem + RED) + l;
        const float v = ((rp[0] + rp[64]) + (rp[128] + rp[192])) + ((rp[256] + rp[320]) + (rp[384] + rp[448]));
        const u32x2 gr = {__float_as_uint(v), tag};
        GST_SC1("x2", gr, lo16, sws, (u32)(mcb * 1024 + mkq * 8), WS_GRAN);
      }
    }
    STAMP(1);
    XROW(6); XROW(7);
    RAWBAR();
    u32x4 gq[2];
#define GATHER(tail)                                                                                                            \
    asm volatile("buffer_load_dwordx4 %0, %2, %3, %4 offen sc1\n\tbuffer_load_dwordx4 %1, %2, %3, %5 offen sc1" tail            \
                 : "=&v"(gq[0]), "=&v"(gq[1]) : "v"((u32)t * 16), "s"(sws), "s"(WS_GRAN), "s"(WS_GRAN + 8192) : "memory")
    const int kind = w < 4 ? (w >> 1) : 2, hp = w < 4 ? (w & 1) : (w >> 1 & 1);
    u32 brd0 = (u32)(kind * 8192 + (hb * 8 + (l >> 2 & 3)) * 128 + ((hp ^ (l >> 3)) & 1) * 64 + (l >> 4 & 1) * 32 + (l & 3) * 8);
    u32 brd1 = brd0 + 65536, brd2 = brd0 + 65536 + 2 * WSLOT;
    PIN(brd1); PIN(brd2);
    bf16x8 wq[32];
#define KA_FRAG(kc) _Pragma("unroll") for (int c_ = 0; c_ < 4; ++c_) {                                                    \
      const u32 o_ = ((kc) >> 1 == 1 ? brd0 : ((kc) >> 1 == 2 ? brd2 : brd1)) + ((kc) & 1) * WSLOT + c_ * 2048; wq[(kc) * 4 + c_] = lds_tr2(smem, o_, o_ + 512); }
    if (w < 4) { KA_CHUNK(0) KA_CHUNK(1) KA_CHUNK(2) KA_CHUNK(3) WAITV(12); RAWBAR(); KA_FRAG(0); KA_FRAG(1); BAR(); } else WAITV(0);
    STAMP(30);
    f32x2 nv[8][4];
    {
      float v[8], z[2], q[2];
#pragma unroll
      for (int i = 0; i < 8; ++i) {
        PIN(xa[i]);
#pragma unroll
        for (int e = 0; e < 4; ++e) nv[i][e] = (f32x2){bf_lo(xa[i][e]), bf_hi(xa[i][e])};
        const f32x2 s2 = (nv[i][0] + nv[i][1]) + (nv[i][2] + nv[i][3]);
        v[i] = s2[0] + s2[1];
      }
      STAMP(31);
      wave_sum8(v, z);
      z[0] *= (1.f / 512.f); z[1] *= (1.f / 512.f);
      SFOR(i, 0, 8)
        const float mu = ROW_OF(z, i);
        f32x2 s2 = {0.f, 0.f};
        _Pragma("unroll") for (int e = 0; e < 4; ++e) { nv[i][e] -= (f32x2){mu, mu}; s2 = __builtin_elementwise_fma(nv[i][e], nv[i][e], s2); }
        v[i] = s2[0] + s2[1];
      SEND
      wave_sum8(v, q);
#pragma unroll
      for (int j = 0; j < 2; ++j) { q[j] = ln_rstd(q[j] * (1.f / 512.f)); z[j] = (z[j] - bfround(z[j])) * q[j]; }
      SFOR(i, 0, 8)
        const float rs = ROW_OF(q, i), mr = ROW_OF(z, i);
        _Pragma("unroll") for (int e = 0; e < 4; ++e) { nv[i][e] = __builtin_elementwise_fma(nv[i][e], (f32x2){rs, rs}, (f32x2){mr, mr}); }
      SEND
#pragma unroll
      for (int i = 0; i < 8; ++i)
#pragma unroll
        for (int e = 0; e < 4; ++e) PIN(nv[i][e]);
    }
    if (w >= 4) { RAWBAR(); KA_FRAG(0); KA_FRAG(1); BAR(); KA_CHUNK(4) KA_CHUNK(5) }
    GATHER("");
    if (w >= 4) { KA_CHUNK(6) KA_CHUNK(7) } else WAITV(2);
    STAMP(2);
    RAWBAR();
    KA_FRAG(2); KA_FRAG(3);
    if (w >= 4) WAITV(12); else WAITV(0);
    PIN(gq[0]); PIN(gq[1]);
    STAMP(3);
#pragma unroll 1
    for (u32 spins = 0; spins < (1u << 17); ++spins) {
      if (__all(gok(gq[0], tag) && gok(gq[1], tag))) break;
      __builtin_amdgcn_s_sleep(2);
      GATHER("\n\ts_waitcnt vmcnt(0)");
    }
    ((LAS float*)((LAS char*)smem + MTAB))[t] = bfround(1.f + bfround(gsum(gq[1])));
    ((LAS float*)((LAS char*)smem + MTAB))[512 + t] = bfround(gsum(gq[0]));
    BAR();
    STAMP(33);
    f32x2 c1[4], sh[4];
    {
      const LAS f32x4* tc = (const LAS f32x4*)((LAS char*)smem + MTAB + l * 32);
      const f32x4 a0 = tc[0], a1 = tc[1], b0 = tc[128], b1 = tc[129];
#pragma unroll
      for (int e = 0; e < 2; ++e) {
        c1[e] = (f32x2){a0[2 * e], a0[2 * e + 1]}; c1[2 + e] = (f32x2){a1[2 * e], a1[2 * e + 1]};
        sh[e] = (f32x2){b0[2 * e], b0[2 * e + 1]}; sh[2 + e] = (f32x2){b1[2 * e], b1[2 * e + 1]};
      }
    }
    u32 awr[4];
#pragma unroll
    for (int i = 0; i < 4; ++i) awr[i] = (u32)((((l >> 1) - 4 * rw) & 31) * 1024 + (l & 1) * 512 + (((i >> 1) ^ rx) & 1) * 256 + (((w * 2 + (i & 1)) ^ (l & 7)) << 4));
#define KA_PUT(i) *(LAS u32x4*)((LAS char*)smem + awr[(i) & 3] + ((i) >> 2) * 32768) = hv[i]
    u32x4 hv[8];
#pragma unroll
    for (int i = 0; i < 4; ++i) { hv[i] = modulate8(nv[i], c1, sh); KA_PUT(i); }
    const int ta = w < 4 ? 0 : (w & 1), tx = rx >> 1;
    u32 ard[4];
#pragma unroll
    for (int i = 0; i < 4; ++i) ard[i] = (u32)(ta * 32768 + i * 1024 + hb * 512 + (((l & 31) ^ (i * 2 + hb)) << 4));
#define HFRAG(n) lds_frag(smem, ard[(n) & 3] + ((n) >> 2) * 4096)
    const int head = cg * 4 + hp * 2;
    const u32 kvb = WS_KV + (u32)(head * KV_HEAD + pm * 2 * KV_TILE);
    f32x16 z16;
#pragma unroll
    for (int i = 0; i < 16; ++i) z16[i] = 0.f;
    BAR();
    STAMP(4);
    if (w < 4) {
      const float qs = kind == 0 ? 0.25f * 1.4426950408889634f : 1.f;
      const u32 so = kind == 0 ? WS_QF + (u32)(head * 64 + pm * 2) * 1024 + lo16 : kvb + (u32)((l & 51) | (l & 4) << 1 | (l & 8) >> 1) * 16;
      const u32 sh_ = kind == 0 ? 65536u : (u32)KV_HEAD, st_ = kind == 0 ? 1024u : (u32)KV_TILE;
      bf16x8 hf[64];
      f32x16 acc = z16, accp = z16;
      u32x4 ov[2];
#pragma unroll
      for (int i = 0; i < 4; ++i) hf[i] = HFRAG(i);
      SB();
      SFOR(n, 0, 76)
        if constexpr (n < 4) { hv[4 + n] = modulate8(nv[4 + n], c1, sh); KA_PUT(4 + n); }
        if constexpr (n == 5) { KA_FRAG(4); }
        if constexpr (n == 9) RAWBAR();
        if constexpr (n == 11) { KA_FRAG(5); }
        if constexpr (n == 15) { KA_FRAG(6); }
        if constexpr (n == 19) { KA_FRAG(7); }
        if constexpr (n == 32 || n == 64) { accp = acc; acc = z16; }
        if constexpr (n + 4 < 64) hf[n + 4] = HFRAG(n + 4);
        if constexpr (n < 64) acc = MFMA32(wq[n & 31], hf[n], acc);
        if constexpr (n >= 32 && (n & 31) >= 2 && (n & 31) < 10) {
          constexpr int e = (n & 31) - 2;
          ov[e >> 2][e & 3] = pack2(accp[2 * e] * qs, accp[2 * e + 1] * qs);
        }
        if constexpr (n == 42 || n == 74) GST_SC1("x4", ov[0], so, sws, st_ * ((n / 32 - 1) ^ tx), 0);
        if constexpr (n == 43 || n == 75) GST_SC1("x4", ov[1], so, sws, st_ * ((n / 32 - 1) ^ tx) + sh_, 0);
        if (n < 64) SB();
      SEND
    } else {
      bf16x8 hf[32];
      f32x16 acc = z16;
#pragma unroll
      for (int i = 4; i < 8; ++i) { hv[i] = modulate8(nv[i], c1, sh); KA_PUT(i); }
      WAITV(0);
      BAR();
      __builtin_amdgcn_s_setprio(B_PRIO);
#pragma unroll
      for (int i = 0; i < 4; ++i) hf[i] = HFRAG(i);
      SB();
      SFOR(n, 0, 32)
        if constexpr (n >= 2 && n < 18 && (n & 3) == 2) { KA_FRAG(4 + n / 4); }
        if constexpr (n + 4 < 32) hf[n + 4] = HFRAG(n + 4);
        acc = MFMA32(hf[n], wq[n], acc);
        SB();
      SEND
      const u32 kvt = kvb + (u32)((ta ^ tx) * KV_TILE);
      const u32 vo = kvt + (u32)((l >> 4 & 1) * KV_HEAD + 1024 + hb * 272 + (l & 15) * 16);
#pragma unroll
      for (int e = 0; e < 2; ++e) {
        const u32x4 ov = {pack2(acc[8 * e], acc[8 * e + 1]), pack2(acc[8 * e + 2], acc[8 * e + 3]), pack2(acc[8 * e + 4], acc[8 * e + 5]), pack2(acc[8 * e + 6], acc[8 * e + 7])};
        GST_SC1("x4", ov, vo, sws, zs, e * 544);
      }
      const u32x4 one = {0x3f803f80u, 0x3f803f80u, 0x3f803f80u, 0x3f803f80u};
      GST_SC1("x4", one, l < 8 ? kvt + (u32)((l >> 2) * KV_HEAD + 1024 + (l & 3) * 272 + 256) : 0x40000000u, sws, zs, 0);
    }
    __builtin_amdgcn_s_setprio(0);
    STAMP(5);
    u32x4 tq[3];
    const u32 msb = WS_GRAN + (u32)(((w == 0 ? 1024 : (w == 1 ? 2048 : 2560)) + pn3 * 64) * 16);
#define TGATHER(tail)                                                                                                           \
    asm volatile("buffer_load_dwordx4 %0, %3, %5, %6 offen sc1\n\tbuffer_load_dwordx4 %1, %3, %5, %7 offen sc1\n\t"             \
                 "buffer_load_dwordx4 %2, %4, %5, %8 offen sc1" tail                                                            \
                 : "=&v"(tq[0]), "=&v"(tq[1]), "=&v"(tq[2])                                                                     \
                 : "v"((u32)t * 16), "v"(lo16), "s"(sws), "s"(WS_GRAN + 1536 * 16), "s"(WS_GRAN + 2048 * 16), "s"(msb) : "memory")
    TGATHER("");
    WAITV(3);
    STAMP(6);
    BAR();
    signal_cnt(ws, cg);
    WAITV(0);
#pragma unroll
    for (int i = 0; i < 3; ++i) PIN(tq[i]);
#pragma unroll 1
    for (u32 spins = 0; spins < (1u << 17); ++spins) {
      if (__all(gok(tq[0], tag) && gok(tq[1], tag) && gok(tq[2], tag))) break;
      __builtin_amdgcn_s_sleep(2);
      TGATHER("\n\ts_waitcnt vmcnt(0)");
    }
    *(LAS u16*)((LAS char*)smem + TAB + t * 2) = (u16)(__float_as_uint(bfround(1.f + bfround(gsum(tq[1])))) >> 16);
    *(LAS u16*)((LAS char*)smem + TAB + 1024 + t * 2) = (u16)(__float_as_uint(bfround(gsum(tq[0]))) >> 16);
    const float mv_ = bfround(gsum(tq[2]));
    ((LAS float*)((LAS char*)smem + MT))[t] = (w == 1) ? bfround(1.f + mv_) : mv_;
  }

  const int lq = loc >= 28 ? 27 : loc;
  const int pan4 = (xcd >> 1) * 4 + lq / 7, pn4 = (xcd & 1) * 7 + lq % 7;
  constexpr u32 STG = KV_HEAD;
  f32x16 zero;
#pragma unroll
  for (int i = 0; i < 16; ++i) zero[i] = 0.f;
  bf16x8 wf[32];
  const u32x4 so = make_srd(wo, 512 * 1024), sd = make_srd(wd, 1792 * 1024);

  {
    const char* wsrc = (const char*)((l & 2) ? wu : wg) + (size_t)(l >> 2) * 3584 + (pn4 * 128 + w * 16 + (l & 1) * 8) * 2;
    const u32 stg_lb = lb + w * 3072;
    u32 stg_rd0 = (u32)(w * 3072 + (hb * 8 + (l >> 2 & 3)) * 64 + (l >> 4 & 1) * 32 + (l & 3) * 8), stg_rd1 = stg_rd0 + STG;
    PIN(stg_rd1);
#define WREAD(p)                                                                                        \
    do {                                                                                                \
      if constexpr ((p) >= 5) {                                                                         \
        asm volatile("s_waitcnt vmcnt(%0)" :: "i"(wyoung(p)) : "memory");                               \
        const u32 o_ = ((((p) - 5) % 6) < 3 ? stg_rd1 : stg_rd0) + (((p) - 5) % 3) * 1024;              \
        wf[(p) - 5] = lds_tr2(smem, o_, o_ + 256);                                                      \
      }                                                                                                 \
    } while (0)
#define WLOAD(p) do { if ((p) < 32) { GLDS64(wsrc, stg_lb, (((p) % 6) < 3 ? STG : 0u) + ((p) % 3) * 1024u); wsrc += 16 * 3584; } } while (0)
    const int head = xcd * 4 + (loc >> 3), qt = (loc & 7) * 8 + w;
    const u32 ksb = (u32)(head * KV_HEAD + w * 1024), wlb = lb + w * 1024;
    const int r = l & 31;
    u32 krd = lo16;
    u32 vrd = (u32)(1024 + hb * 272 + (r < 16 ? r * 16 : (r == 16 ? 256 : 0x40000)));
    STAMP(7);
    wait_cnt(ws, xcd, 32, w);
    BAR();
    STAMP(8);
#define KVR(r) GLDS_SC1(lo16, sws, ksb, WS_KV + (r) * 8192, wlb, (r) * 8192)
    u32x4 qraw;
    GLD128_SC1(qraw, lo16, sws, (u32)(head * 64 + qt) * 1024, WS_QF);
    KVR(0); KVR(1);
    if (w < 4) KVR(16);
    KVR(2); KVR(3);
    WAITV(2);
    PIN(qraw);
    RAWBAR();
    STAMP(9);
    const bf16x8 qf = __builtin_bit_cast(bf16x8, qraw);
    float m;
    bf16x8 kf = lds_frag(smem, krd);
    {
      f32x16 s0 = MFMA32(kf, qf, zero);
      m = s0[0];
#pragma unroll
      for (int i = 1; i < 16; ++i) m = fmaxf(m, s0[i]);
      const auto a = __builtin_amdgcn_permlane32_swap(__float_as_uint(m), __float_as_uint(m), false, false);
      m = fmaxf(__uint_as_float(a[0]), __uint_as_float(a[1]));
    }
    f32x16 negm;
#pragma unroll
    for (int i = 0; i < 16; ++i) negm[i] = -m;
    f32x16 o = zero;
    f32x16 sc = MFMA32(kf, qf, negm);
    kf = lds_frag(smem, krd + KV_TILE);
    bf16x8 v0 = lds_frag(smem, vrd), v1;
    const u32 tvo = (u32)((l & 7) * 1024 + pn3 * 128), tsb = (u32)(pm3 * 8 + w) * 8192;
    SFOR(j, 0, 64)
      if constexpr ((j & 1) == 0) __builtin_amdgcn_s_setprio(3 - (j >> 1 & 3));
      if (j == 5) { WAITV(10); RAWBAR(); }
      if (j == 13) { WAITV(0); RAWBAR(); }
      if (j == 14) STAMP(10);
      constexpr bool wst = j >= 14 && j < 63 && (j - 14) % 4 != 3;
      constexpr int wp = (j - 14) - (j - 14) / 4;
      if (j == 28 || j == 56) { krd += 28 * KV_TILE; vrd += 28 * KV_TILE; PIN(krd); PIN(vrd); }
      const u32 jo = (u32)(j % 28) * KV_TILE;
#define EXPS(a, b) _Pragma("unroll") for (int i = (a); i < (b); ++i) sc[i] = __builtin_amdgcn_exp2f(sc[i])
      f32x16 sn;
#define QKNEXT() do { if (j < 63) asm volatile("v_mfma_f32_32x32x16_bf16 %0, %1, %2, %3" : "=&v"(sn) : "v"(kf), "v"(qf), "v"(negm)); } while (0)
#define KREAD() do { if (j < 62) kf = lds_frag(smem, krd + jo + 2 * KV_TILE); } while (0)
#define SIDE()                                                                                                             \
      do {                                                                                                                 \
        if constexpr (wst) { WREAD(wp); WLOAD(wp); }                                                                       \
        if constexpr (j >= 14 && (j - 14) % 4 == 3) {     \
          constexpr int m = (j - 14) / 4 - TOUCH0;                                                                         \
          if (m >= 0 && m < 7 && NTOUCH) GTOUCH(tvo, sd, tsb * 7, (m < 0 ? 0 : m) * 8192, lb + 25600);                     \
          if (m >= 7 && m < NTOUCH) GTOUCH(tvo, so, tsb * 2, (m < 7 ? 0 : m - 7) * 8192, lb + 25600);                      \
        }                                                                                                                  \
        if constexpr (j < 6) { KVR(4 + 2 * j); KVR(5 + 2 * j); }                                                           \
      } while (0)
      v0 = lds_frag(smem, vrd + jo); v1 = lds_frag(smem, vrd + jo + 544);
      SB();
      EXPS(j == 0 ? 0 : 2, E0);
      PIN(sc);
      QKNEXT(); SB(); KREAD(); SB();
      EXPS(E0, E1);
      SB();
      o = SMF(v0, LO8(sc), o);
      SIDE();
      SB();
      EXPS(E1, 16);
      PIN(sc);
      if (j < 63) { sn[0] = __builtin_amdgcn_exp2f(sn[0]); sn[1] = __builtin_amdgcn_exp2f(sn[1]); PIN(sn); }
      SB();
      o = SMF(v1, HI8(sc), o);
      PIN(o);
      SB();
      if (j < 63) sc = sn;
    SEND
    float lsum = __uint_as_float(__builtin_amdgcn_permlane32_swap(__float_as_uint(o[8]), __float_as_uint(o[8]), false, false)[0]);
    bool ok = lsum < 1e18f;
#pragma unroll
    for (int i = 0; i < 8; ++i) ok = ok && (fabsf(o[i]) < 3e38f);
    if (!__all(ok)) {
      asm volatile("" ::: "memory");
      const __amdgpu_buffer_rsrc_t rk = __builtin_amdgcn_make_buffer_rsrc(ws + WS_KV + (size_t)head * KV_HEAD, 0, KV_HEAD, 0x20000);
      const int vg = 1024 + hb * 272 + (r == 16 ? 256 : (r & 15) * 16);
#define KVLD(off, j) __builtin_bit_cast(bf16x8, __builtin_amdgcn_raw_buffer_load_b128(rk, (off), (j) * KV_TILE, 16))
#pragma unroll 1
      for (int j = 0; j < 64; ++j) {
        const f32x16 sv = MFMA32(KVLD(lo16, j), qf, zero);
#pragma unroll
        for (int i = 0; i < 16; ++i) m = fmaxf(m, sv[i]);
      }
      m = fmaxf(m, __shfl_xor(m, 32));
#pragma unroll
      for (int i = 0; i < 16; ++i) negm[i] = -m;
      o = zero;
#pragma unroll 1
      for (int j = 0; j < 64; ++j) {
        f32x16 sv = MFMA32(KVLD(lo16, j), qf, negm);
        const bf16x8 v0 = KVLD(vg, j), v1 = KVLD(vg + 544, j);
#pragma unroll
        for (int i = 0; i < 16; ++i) sv[i] = __builtin_amdgcn_exp2f(sv[i]);
        o = SMF(v0, LO8(sv), o);
        o = SMF(v1, HI8(sv), o);
      }
      lsum = shfl_idx(o[8], r);
    }
    const float inv = 1.f / lsum;
    u32x4 ov;
#pragma unroll
    for (int e = 0; e < 4; ++e) ov[e] = pack2(o[2 * e] * inv, o[2 * e + 1] * inv);
    GST_SC1("x4", ov, lo16, sws, (u32)(qt * 32 + head) * 1024, WS_OF);
    STAMP(11);
    WAITV(0);
    BAR();
    signal_cnt(ws, 8 + (loc & 7));
    STAMP(12);
  }

  const int tm = w & 1;
  const u32 wvo = (u32)((l >> 3) * 1024 + ((((l >> 2) ^ (l >> 4)) & 1) * 32 + (l & 3) * 8) * 2);
  const u32 wrd0 = (u32)((hb * 4 + (l >> 2 & 3)) * 128 + (l >> 3 & 1) * 64 + (l >> 4 & 1) * 32 + (l & 3) * 8);
  const int rte = xcd * 8 + (pm3 >> 1) * 4 + (pm3 & 1) + (w >> 2) * 2;
  const int erow = w * 8 + (l & 7), ecol = l >> 3;
  f32x16 uv = zero;
  const u32 trd = TAB + (u32)((l >> 4 & 1) * 1024 + hb * 16);
#define UV_PART(k0)                                                                                          \
  do { bf16x8 tf_[16];                                                                                       \
    _Pragma("unroll") for (int i_ = 0; i_ < 4; ++i_) tf_[i_] = lds_frag(smem, trd + ((k0) + i_) * 32);       \
    _Pragma("unroll") for (int i_ = 0; i_ < 16; ++i_) {                                                      \
      if (i_ + 4 < 16) tf_[i_ + 4] = lds_frag(smem, trd + ((k0) + i_ + 4) * 32);                             \
      uv = MFMA32(wf[(k0) + i_], tf_[i_], uv); SB(); }                                                       \
    PIN(uv); } while (0)

  {
    const int ro = pn3 & 3;
    SFOR(j, 0, 8) GLDS(wvo, so, (u32)(w * 8192 + pn3 * 128 + ((j + 2 * ro) & 7) * 65536), 0, lb + w * 1024, j * 8192); SEND
    u32x4 xr;
    GLD128(xr, (u32)((erow & 31) * 1024 + ecol * 16), sx, (u32)(rte * 32768 + pn3 * 128), 0);
    UV_PART(0);
    WAITV(0);
    PIN(xr);
    STAMP(13);
    wait_cnt(ws, 8 + xcd, 32, w);
    RAWBAR();
    STAMP(14);
    if (bid == 0 && t == 0) __hip_atomic_store((u32*)(ws + WS_EPOCH), tag, __ATOMIC_RELAXED, __HIP_MEMORY_SCOPE_AGENT);
    const u32 olo = (u32)((w >> 2) * 32768 + (w & 3) * 2048);
    const u32 osb = (u32)(rte * 32768 + (w & 3) * 2048), olb = lb + olo;
    SFOR(j, 0, 8) GLDS_SC1(lo16, sws, osb + (u32)(((j >> 1) + ro) & 3) * 8192, WS_OF + (j & 1) * 1024, olb, 65536 + (j >> 1) * 8192 + (j & 1) * 1024); SEND
    const int tn = w >> 1 & 1, kh = w >> 2;
    u32 ord = 65536 + (u32)(tm * 32768 + kh * 4096) + lo16;
    PIN(ord);
    const u32 wrd = (wrd0 ^ (u32)(tn * 64)) + kh * 8192;
    f32x16 acc = zero;
    bf16x8 wq[16];
#define WOFRAG(n) wq[n] = lds_tr2(smem, wrd + ((n) >> 2) * 16384 + ((n) & 3) * 2048, wrd + ((n) >> 2) * 16384 + ((n) & 3) * 2048 + 1024)
#pragma unroll
    for (int n = 0; n < 8; ++n) WOFRAG(n);
    SFOR(s, 0, 4)
      if (s == 0) WAITV(6); else if (s == 1) WAITV(4); else if (s == 2) WAITV(2); else WAITV(0);
      RAWBAR();
      bf16x8 oq[4];
#pragma unroll
      for (int j = 0; j < 4; ++j) oq[j] = lds_frag(smem, ord + s * 8192 + j * 1024);
      if constexpr (s < 2) {
#pragma unroll
        for (int j = 0; j < 4; ++j) WOFRAG(8 + s * 4 + j);
      }
      SB();
#pragma unroll
      for (int j = 0; j < 4; ++j) acc = MFMA32(wq[s * 4 + j], oq[j], acc);
      SB();
    SEND
    STAMP(15);
    {
      LAS char* eb = (LAS char*)smem + kh * 17408 + (tm * 32 + (l & 31)) * 272 + (tn * 32 + hb * 4) * 4;
#pragma unroll
      for (int e = 0; e < 4; ++e) *(LAS f32x4*)(eb + e * 32) = (f32x4){acc[4 * e], acc[4 * e + 1], acc[4 * e + 2], acc[4 * e + 3]};
    }
    f32x4 ga[2], cb[2];
#pragma unroll
    for (int e = 0; e < 2; ++e) { ga[e] = *(const LAS f32x4*)((LAS char*)smem + MT + ecol * 32 + e * 16); cb[e] = *(const LAS f32x4*)((LAS char*)smem + MT + 256 + ecol * 32 + e * 16); }
    BAR();
    float xv[8];
    u32x4 x1p, yp;
#pragma unroll
    for (int e = 0; e < 2; ++e) {
      const LAS char* eb = (const LAS char*)smem + erow * 272 + ecol * 32 + e * 16;
      const f32x4 a = *(const LAS f32x4*)eb + *(const LAS f32x4*)(eb + 17408);
#pragma unroll
      for (int h = 0; h < 2; ++h) {
        const u32 pk = pack2(bf_lo(xr[2 * e + h]) + ga[e][2 * h] * a[2 * h], bf_hi(xr[2 * e + h]) + ga[e][2 * h + 1] * a[2 * h + 1]);
        x1p[2 * e + h] = pk;
        xv[4 * e + 2 * h] = bf_lo(pk); xv[4 * e + 2 * h + 1] = bf_hi(pk);
        yp[2 * e + h] = pack2(bf_lo(pk) * cb[e][2 * h], bf_hi(pk) * cb[e][2 * h + 1]);
      }
    }
    GST_SC1("x4", yp, (u32)((ecol >> 1) * 1024 + ((ecol & 1) * 32 + (erow & 31)) * 16), sws, (u32)(rte * 32 + pn3 * 4) * 1024, WS_Y);
    float sum = ((xv[0] + xv[1]) + (xv[2] + xv[3])) + ((xv[4] + xv[5]) + (xv[6] + xv[7]));
    const float mean = col8_sum(sum) * (1.f / 64.f);
    float sq = 0.f;
#pragma unroll
    for (int i = 0; i < 8; ++i) { const float d = xv[i] - mean; sq = __builtin_fmaf(d, d, sq); }
    const f32x2 o2 = {mean, col8_sum(sq)};
    GST_SC1("x2", o2, l < 8 ? (u32)((erow & 31) * 8) : 0x40000000u, sws, (u32)(rte * 2048 + pn3 * 256), WS_ST2);
    STAMP(16);
    WAITV(0);
    RAWBAR();
    signal_cnt(ws, 16 + xcd * 2 + (pm3 >> 1));
    STAMP(17);
    GST_SC1("x4", x1p, (u32)(erow * 8 + ecol) * 16, sws, (u32)bid * 8192, WS_X1);
  }

  const u32 dsb = (u32)(w * 16384 + pn3 * 128), dlb = lb + w * 2048;
  const int r5 = (pn3 * 7) >> 2;
#define CH5(c) (u32)((c) + r5 >= 14 ? (c) + r5 - 14 : (c) + r5)
#define WDPIECE(c, slot, h) GLDS(wvo, sd, dsb + CH5(c) * 131072, (h) * 8192, dlb, (slot) * 16384 + (h) * 1024)
#define WDISSUE(c, slot) do { WDPIECE(c, slot, 0); WDPIECE(c, slot, 1); } while (0)
  const int kq = w & 3;
  const u32 wr5 = wrd0 + kq * 4096;
  bf16x8 dq[14][4];
#define DFRAG1(c, i) dq[c][i] = lds_tr2(smem, (wr5 ^ (((i) & 1) * 64u)) + (c) * 16384 + ((i) >> 1) * 2048, (wr5 ^ (((i) & 1) * 64u)) + (c) * 16384 + ((i) >> 1) * 2048 + 1024)
  if (loc < 28) {
    constexpr u32 RST4 = 157696;
    UV_PART(16);
    wait_cnt(ws, 16 + pan4, 16, w);
    RAWBAR();
    STAMP(18);
    const int rt4 = pan4 * 4;
    const u32 ysb = (u32)(rt4 * 32768 + w * 1024), ylb = lb + w * 1024;
#define YPIECE(i) GLDS_SC1(lo16, sws, ysb, WS_Y + (i) * 8192, ylb, (i) * 8192)
    YPIECE(0); YPIECE(1); YPIECE(2); YPIECE(3);
    GLDS_SC1(lo16, sws, (u32)(rt4 * 2048 + w * 1024), WS_ST2, ylb, 131072);
    YPIECE(4); YPIECE(5);
    float uu[16], vv[16];
#pragma unroll
    for (int i = 0; i < 16; ++i) {
      const auto r = __builtin_amdgcn_permlane16_swap(__float_as_uint(uv[i]), __float_as_uint(uv[i]), false, false);
      uu[i] = __uint_as_float(r[0]); vv[i] = __uint_as_float(r[1]);
      PIN(uu[i]); PIN(vv[i]);
    }
    u32 yrd0 = lo16, yrd1 = lo16 + 65536;
    PIN(yrd1);
    const u32 asb = (u32)(rt4 * 112 + pn4 * 8 + w) * 1024;
#define YFRAG(n) lds_frag(smem, ((n) < 64 ? yrd0 : yrd1) + ((n) & 63) * 1024)
    WAITV(5);
    RAWBAR();
    STAMP(19);
    bf16x8 yf[128];
#pragma unroll
    for (int n = 0; n < 4; ++n) yf[n] = YFRAG(n);
    SB();
#define signal_early()                                                                                               \
    do { unsigned long long ex_;                                                                                     \
      asm volatile("s_cmp_eq_u32 %5, 0\n\ts_cbranch_scc0 1f\n\ts_mov_b64 %0, exec\n\ts_mov_b64 exec, %6\n\t"          \
                   "buffer_atomic_add %1, %2, %3, %4 offen\n\ts_mov_b64 exec, %0\n1:"                                \
                   : "=&s"(ex_) : "v"(1u), "v"(lo16 << 10), "s"(sws), "s"(WS_CNT + (u32)(48 + pan4) * 256), "s"(w), "i"((1 << NREP) - 1) : "memory", "scc"); } while (0)
    f32x16 acc = zero, accp = zero;
    f32x2 stp = {0.f, 0.f};
    float eg[8], eu[8], et[8];
    u32x4 ov;
    SFOR(n, 0, 160)
      constexpr int a = n >> 5, ks = n & 31;
      if (ks == 0 && a > 0) { accp = acc; acc = zero; }
      if constexpr (n < 128) { acc = MFMA32(wf[ks], yf[n], acc); SB(); }
      if (n == 10) { WAITV(7); RAWBAR(); }
      if (n == 26) { WAITV(8); BAR(); }
      if (n == 56) { WAITV(4); RAWBAR(); }
      if (n == 100) WAITV(3);
      if (n == 121) { WAITV(4); RAWBAR(); signal_early(); }
      if constexpr (n < 20 && n % 2 == 0) YPIECE(6 + n / 2);
      if (n == 36 || n == 68 || n == 100) RAWBAR();
      if constexpr (n == 60 && !L2_WARM) {
        const int rr = pm3 * 224 + w * 28 + (l < 28 ? l : 27);
        const int r2 = rr + (rr < 256 ? 768 : 896) + r5 * 128;
        GTOUCH((u32)(r2 >= 1792 ? r2 - 1792 : r2) * 1024, sd, (u32)(pn3 * 128), 0, lb + SCR);
      }
      if constexpr (n >= 36 && n < 52 && n % 4 == 0) WDPIECE((n - 36) / 8, (n - 36) / 8, (n - 36) / 4 & 1);
      if constexpr (n >= 68 && n < 84 && n % 4 == 0) WDPIECE(2 + (n - 68) / 8, 2 + (n - 68) / 8, (n - 68) / 4 & 1);
      if constexpr (n == 84 || n == 88) WDPIECE(8, 8, (n - 84) / 4);
      if constexpr (n >= 100 && n < 116 && n % 4 == 0) WDPIECE(4 + (n - 100) / 8, 4 + (n - 100) / 8, (n - 100) / 4 & 1);
      if constexpr (n >= 100 && n < 116) DFRAG1((n - 100) >> 2, (n - 100) & 3);
      if constexpr (n == 122 || n == 125) WDPIECE(10, 0, n & 1);
      if constexpr (n == 12) {
        const LAS f32x2* sp = (const LAS f32x2*)((LAS char*)smem + 131072 + (t >> 7) * 2048 + (t & 3) * 512 + (t >> 2 & 31) * 8);
        const f32x2 p0 = sp[0], p1 = sp[32];
        float mu = p0[0] + p1[0];
        mu += dpp_xor1(mu); mu += dpp_xor2(mu);
        mu *= 0.125f;
        float q = p0[1] + p1[1] + 64.f * ((p0[0] - mu) * (p0[0] - mu) + (p1[0] - mu) * (p1[0] - mu));
        q += dpp_xor1(q); q += dpp_xor2(q);
        const float rs = __builtin_amdgcn_rsqf(q * (1.f / 512.f) + 1e-5f);
        ((LAS f32x2*)((LAS char*)smem + RST4))[t >> 2] = (f32x2){rs, -mu * rs};
      }
      if (ks == 28 && a < 4) stp = ((const LAS f32x2*)((LAS char*)smem + RST4))[a * 32 + (l & 31)];
      if (n == 128) STAMP(20);
      if (n == 32) STAMP(34);
      if (n == 64) STAMP(35);
      if (n == 96) STAMP(36);
      if (n == 120) STAMP(39);
      if constexpr (n + 4 < 128) yf[n + 4] = YFRAG(n + 4);
      if constexpr (a > 0 && ks >= 2 && ks < 26) {
        constexpr int i = (ks - 2) / 3, ph = (ks - 2) % 3;
        if (ph == 0) {
          eg[i] = __builtin_fmaf(accp[i], stp[0], __builtin_fmaf(uu[i], stp[1], vv[i]));
          eu[i] = __builtin_fmaf(accp[8 + i], stp[0], __builtin_fmaf(uu[8 + i], stp[1], vv[8 + i]));
          et[i] = -1.4426950408889634f * eg[i];
          if (a < 4) { PIN(eg[i]); PIN(eu[i]); PIN(et[i]); }
        } else if (ph == 1) {
          et[i] = 1.f + __builtin_amdgcn_exp2f(et[i]);
          eg[i] *= eu[i];
          if (a < 4) { PIN(eg[i]); PIN(et[i]); }
        } else {
          eg[i] *= __builtin_amdgcn_rcpf(et[i]);
          if (a < 4) PIN(eg[i]);
          if constexpr (i & 1) ov[i >> 1] = pack2(eg[i - 1], eg[i]);
        }
      }
      if constexpr (a > 0 && ks == 26) GST_SC1("x4", ov, lo16, sws, asb, WS_ACT + (a - 1) * 112 * 1024);
      if (n < 128) SB();
    SEND
    STAMP(21);
    WDISSUE(11, 1); WDISSUE(12, 2); WDISSUE(13, 3);
  } else {
    WDISSUE(0, 0); WDISSUE(1, 1); WDISSUE(2, 2); WDISSUE(3, 3); WDISSUE(8, 8); WDISSUE(4, 4); WDISSUE(5, 5);
    WAITV(0);
    RAWBAR();
    SFOR(n, 0, 16) DFRAG1(n >> 2, n & 3); SEND
    BAR();
    WDISSUE(10, 0); WDISSUE(11, 1); WDISSUE(12, 2); WDISSUE(13, 3);
  }

  {
    u32 wra[3][2];
#pragma unroll
    for (int i = 0; i < 3; ++i) { wra[i][0] = wr5 + i * 65536; wra[i][1] = wra[i][0] ^ 64u; PIN(wra[i][0]); PIN(wra[i][1]); }
#define DFRAG(c) _Pragma("unroll") for (int i_ = 0; i_ < 4; ++i_) {                                              \
      const u32 o_ = wra[((c) % 10) >> 2][i_ & 1] + (((c) % 10) & 3) * 16384 + (i_ >> 1) * 2048; dq[c][i_] = lds_tr2(smem, o_, o_ + 1024); }
    f32x4 gb[2];
#pragma unroll
    for (int e = 0; e < 2; ++e) gb[e] = *(const LAS f32x4*)((LAS char*)smem + MT + 512 + (l & 7) * 32 + e * 16);
    const int pan5 = xcd * 2 + (pm3 >> 1);
    wait_cnt(ws, 48 + pan5, 14, w);
    WAITV(0);
    BAR();
    if (loc < 28) signal_cnt(ws, 32 + pan4);
    STAMP(22);
#define WD2(c, slot) do { WDISSUE(c, slot); WDPIECE(c, slot, -8); WDPIECE(c, slot, -7); } while (0)
#define BAR_IF(lo) asm volatile("s_cmp_lt_u32 %0, 4\n\ts_cbranch_scc" lo " 1f\n\ts_barrier\n1:" :: "s"(w) : "memory", "scc")
    if (w >= 4) {
      WD2(6, 6); WD2(7, 7); WD2(9, 9);
      WAITV(0);
      RAWBAR();
      wait_cnt(ws, 32 + pan5, 14, 0);
    }
    STAMP(23);
    const u32 asb = (u32)(rte * 112 + kq * 2) * 1024;
    u32x4 af[14][2];
#define AISSUE(c) do { GLD128_SC1(af[c][0], lo16, sws, asb + CH5(c) * 8192, WS_ACT); GLD128_SC1(af[c][1], lo16, sws, asb + CH5(c) * 8192, WS_ACT + 1024); } while (0)
    SFOR(c, 0, AWIN) AISSUE(c); SEND
    f32x16 acc[2] = {zero, zero};
    u32x4 x1p;
    SFOR(c, 0, 14)
      constexpr int young = c + AWIN <= 14 ? 2 * (AWIN - 1) : (c < 13 ? 2 * (13 - c) + 1 : 0);
      asm volatile("s_waitcnt vmcnt(%0)" :: "i"(young) : "memory");
      if (c == 0) STAMP(24);
      if (c == 5) STAMP(37);
      if (c == 10) STAMP(38);
      if (c == 2) BAR_IF("0");
      if (c == 10) BAR_IF("1");
      if constexpr (c + AWIN < 14) AISSUE(c + AWIN);
      if (c + AWIN == 14) GLD128_SC1(x1p, (u32)t * 16, sws, (u32)bid * 8192, WS_X1);
      if constexpr (c >= 3 && c < 13) { DFRAG(c + 1); }
      PIN(af[c][0]); PIN(af[c][1]);
      SB();
#pragma unroll
      for (int i = 0; i < 4; ++i) acc[i & 1] = MFMA32(dq[c][i], __builtin_bit_cast(bf16x8, af[c][i >> 1]), acc[i & 1]);
      SB();
    SEND
    STAMP(25);
    BAR_IF("0");
    {
      LAS char* eb = (LAS char*)smem + 65536 + kq * 17408 + ((w >> 2) * 32 + (l & 31)) * 272 + hb * 16;
#pragma unroll
      for (int tn = 0; tn < 2; ++tn)
#pragma unroll
        for (int e = 0; e < 4; ++e)
          *(LAS f32x4*)(eb + tn * 128 + e * 32) = (f32x4){acc[tn][4 * e], acc[tn][4 * e + 1], acc[tn][4 * e + 2], acc[tn][4 * e + 3]};
    }
    STAMP(40);
    BAR();
    STAMP(29);
    PIN(x1p);
    u32x4 ov;
#pragma unroll
    for (int e = 0; e < 2; ++e) {
      const LAS char* eb = (const LAS char*)smem + 65536 + (t >> 3) * 272 + (l & 7) * 32 + e * 16;
      const f32x4 s = (*(const LAS f32x4*)eb + *(const LAS f32x4*)(eb + 17408)) + (*(const LAS f32x4*)(eb + 2 * 17408) + *(const LAS f32x4*)(eb + 3 * 17408));
      ov[2 * e] = pack2(bf_lo(x1p[2 * e]) + gb[e][0] * s[0], bf_hi(x1p[2 * e]) + gb[e][1] * s[1]);
      ov[2 * e + 1] = pack2(bf_lo(x1p[2 * e + 1]) + gb[e][2] * s[2], bf_hi(x1p[2 * e + 1]) + gb[e][3] * s[3]);
    }
    *(u32x4*)((char*)out + (size_t)(rte * 32 + (t >> 3 & 31)) * 1024 + pn3 * 128 + (l & 7) * 16) = ov;
  }
  ST_OUT(2, ws);
  asm volatile(".Lcode_end:");
}

extern "C" void kernel_launch(void* const* d_in, const int* in_sizes, int n_in, void* d_out, int out_size, void* d_ws, size_t ws_size,
                              hipStream_t stream) {
  adaln_block<<<256, 512, 0, stream>>>((const u16*)d_in[0], (const u16*)d_in[1], (const u16*)d_in[2], (const u16*)d_in[4], (const u16*)d_in[5],
                                       (const u16*)d_in[6], (const u16*)d_in[7], (const u16*)d_in[8], (char*)d_ws, (u16*)d_out);
}
```

```cpp
#include <hip/hip_runtime.h>
#include <hip/hip_bf16.h>

typedef unsigned short u16;
typedef unsigned int u32;
typedef u32 u32x2 __attribute__((ext_vector_type(2)));
typedef u32 u32x4 __attribute__((ext_vector_type(4)));
typedef float f32x2 __attribute__((ext_vector_type(2)));
typedef float f32x4 __attribute__((ext_vector_type(4)));
typedef float f32x8 __attribute__((ext_vector_type(8)));
typedef float f32x16 __attribute__((ext_vector_type(16)));
typedef __bf16 bf16x2 __attribute__((ext_vector_type(2)));
typedef __bf16 bf16x8 __attribute__((ext_vector_type(8)));
typedef __bf16 bf16x16 __attribute__((ext_vector_type(16)));
typedef short s16x4 __attribute__((ext_vector_type(4)));
typedef short s16x8 __attribute__((ext_vector_type(8)));
#define PRIO_MODE 1
#define GATHER_AT 5
#define AORD 3
#define E1 10
#define AWIN 8
#define OUT_WT 0
#define CODE_PF 8
#define L2_WARM 1
#define UV_EARLY 0
#define B_PRIO 0
#define POLL_SLEEP 4
#define NREP 1
#define P4_FIRST 1
#define WD_LATE 2
#define E0 5
#define FRONT 0
#define SPLIT5 1
#define LAS __attribute__((address_space(3)))
#define DI __device__ __forceinline__

constexpr u32 WS_GRAN = 0;
constexpr u32 WS_EPOCH = 0x18000;
constexpr u32 WS_CNT  = 0x20000;
constexpr u32 WS_QF   = 0x100000;
constexpr u32 WS_KV   = 0x300000;
constexpr u32 WS_OF   = 0x800000;
constexpr u32 WS_X1   = 0xA00000;
constexpr u32 WS_Y    = 0xC00000;
constexpr u32 WS_ST2  = 0xE00000;
constexpr u32 WS_ACT  = 0xF00000;
constexpr u32 WS_WT   = 0x1600000;
constexpr u32 WS_BYTES = 0x1A00000;
constexpr int KV_TILE = 2112, KV_HEAD = 64 * KV_TILE;

template <int I> struct IC { static constexpr int value = I; };
template <int B, int E, class F> DI void static_for(F&& f) { if constexpr (B < E) { f(IC<B>{}); static_for<B + 1, E>(f); } }
#define SFOR(n, B, E) static_for<B, E>([&](auto ic_) __attribute__((always_inline)) { constexpr int n = decltype(ic_)::value;
#define SEND });

DI float bf_lo(u32 u) { return __uint_as_float(u << 16); }
DI float bf_hi(u32 u) { return __uint_as_float(u & 0xffff0000u); }
DI u32 pack2(float a, float b) { bf16x2 r = {(__bf16)a, (__bf16)b}; return __builtin_bit_cast(u32, r); }
DI float bfround(float a) { return (float)(__bf16)a; }
DI float ln_rstd(float var) { return __builtin_amdgcn_rcpf(bfround(__builtin_amdgcn_sqrtf(bfround(bfround(var) + 1e-5f)))); }
DI float xor32_sum(float v) {
  const auto r = __builtin_amdgcn_permlane32_swap(__float_as_uint(v), __float_as_uint(v), false, false);
  return __uint_as_float(r[0]) + __uint_as_float(r[1]);
}
DI float dpp_xor1(float v) { return __int_as_float(__builtin_amdgcn_mov_dpp(__float_as_int(v), 0xB1, 0xF, 0xF, true)); }
DI float dpp_xor2(float v) { return __int_as_float(__builtin_amdgcn_mov_dpp(__float_as_int(v), 0x4E, 0xF, 0xF, true)); }
DI float shfl_idx(float v, int srcLane) { return __int_as_float(__builtin_amdgcn_ds_bpermute(srcLane << 2, __float_as_int(v))); }

DI u32x4 make_srd(const void* p, u32 bytes) {
  const size_t a = (size_t)p;
  u32x4 r = {(u32)a, (u32)(a >> 32) & 0xffffu, bytes, 0x20000u};
  return r;
}
#define GLDS(voff, srd, sb, GO, lb, LO)                                                                                   \
  do { u32 tmp_;                                                                                                          \
    asm volatile("s_add_u32 m0, %4, %6\n\ts_add_u32 %0, %3, %5\n\tbuffer_load_dwordx4 %1, %2, %0 offen lds"            \
                 : "=&s"(tmp_) : "v"(voff), "s"(srd), "s"(sb), "s"(lb), "i"(GO), "i"(LO) : "memory", "scc"); } while (0)
#define GLD(W, dst, voff, srd, sb, GO)                                                                                    \
  do { u32 tmp_;                                                                                                          \
    asm volatile("s_add_u32 %1, %4, %5\n\tbuffer_load_dword" W " %0, %2, %3, %1 offen"                                   \
                 : "=v"(dst), "=&s"(tmp_) : "v"(voff), "s"(srd), "s"(sb), "i"(GO) : "memory", "scc"); } while (0)
#define GLD128(dst, voff, srd, sb, GO) GLD("x4", dst, voff, srd, sb, GO)
#define GLD64(dst, voff, srd, sb, GO) GLD("x2", dst, voff, srd, sb, GO)
#define GLD32(dst, voff, srd, sb, GO) GLD("", dst, voff, srd, sb, GO)
#define WAITV(N) asm volatile("s_waitcnt vmcnt(" #N ")" ::: "memory")
#define PIN(x) asm volatile("" : "+v"(x))
#define BAR() do { asm volatile("s_waitcnt lgkmcnt(0)" ::: "memory"); __builtin_amdgcn_s_barrier(); asm volatile("" ::: "memory"); } while (0)

#define ST_DECL
#define STAMP(i)
#define ST_OUT(kid, wsp)

DI bf16x8 lds_frag(const char* smem, u32 off) { return *(const LAS bf16x8*)((const LAS char*)smem + off); }
DI bf16x8 lds_tr2(const char* smem, u32 off0, u32 off1) {
  s16x4 a = __builtin_amdgcn_ds_read_tr16_b64_v4i16((LAS s16x4*)((LAS char*)smem + off0));
  s16x4 b = __builtin_amdgcn_ds_read_tr16_b64_v4i16((LAS s16x4*)((LAS char*)smem + off1));
  s16x8 r = __builtin_shufflevector(a, b, 0, 1, 2, 3, 4, 5, 6, 7);
  return __builtin_bit_cast(bf16x8, r);
}
DI u32 lds_base(const char* smem) { return (u32)(size_t)(const LAS char*)smem; }
#define SB() __builtin_amdgcn_sched_barrier(0)
#define RAWBAR() do { asm volatile("" ::: "memory"); __builtin_amdgcn_s_barrier(); asm volatile("" ::: "memory"); } while (0)
#define MFMA32(a, b, c) __builtin_amdgcn_mfma_f32_32x32x16_bf16((a), (b), (c), 0, 0, 0)
#define MFMA16(a, b, c) __builtin_amdgcn_mfma_f32_16x16x32_bf16((a), (b), (c), 0, 0, 0)

DI u32x4 modulate8(const f32x2* n, const f32x2* c1, const f32x2* sh) {
  u32x4 o;
#pragma unroll
  for (int e = 0; e < 4; ++e) { const f32x2 r = __builtin_elementwise_fma(n[e], c1[e], sh[e]); o[e] = pack2(r[0], r[1]); }
  return o;
}

#define GLDS_SC1(voff, srd, sb, GO, lb, LO)                                                                               \
  do { u32 tmp_;                                                                                                          \
    asm volatile("s_add_u32 m0, %4, %6\n\ts_add_u32 %0, %3, %5\n\tbuffer_load_dwordx4 %1, %2, %0 offen sc1 lds"         \
                 : "=&s"(tmp_) : "v"(voff), "s"(srd), "s"(sb), "s"(lb), "i"(GO), "i"(LO) : "memory", "scc"); } while (0)
#define GLD128_SC1(dst, voff, srd, sb, GO)                                                                                \
  do { u32 tmp_;                                                                                                          \
    asm volatile("s_add_u32 %1, %4, %5\n\tbuffer_load_dwordx4 %0, %2, %3, %1 offen sc1"                                 \
                 : "=v"(dst), "=&s"(tmp_) : "v"(voff), "s"(srd), "s"(sb), "i"(GO) : "memory", "scc"); } while (0)
#define GST_SC1(W, val, voff, srd, sb, GO)                                                                                \
  do { u32 tmp_;                                                                                                          \
    asm volatile("s_add_u32 %0, %4, %5\n\tbuffer_store_dword" W " %1, %2, %3, %0 offen sc1\n\ts_nop 1"                  \
                 : "=&s"(tmp_) : "v"(val), "v"(voff), "s"(srd), "s"(sb), "i"(GO) : "memory", "scc"); } while (0)
#define GTOUCH(voff, srd, sb, GO, lb)                                                                                     \
  do { u32 tmp_;                                                                                                          \
    asm volatile("s_mov_b32 m0, %4\n\ts_add_u32 %0, %3, %5\n\tbuffer_load_dword %1, %2, %0 offen lds"                   \
                 : "=&s"(tmp_) : "v"(voff), "s"(srd), "s"(sb), "s"(lb), "i"(GO) : "memory", "scc"); } while (0)
#define GLDS64(vaddr, lb, LO)                                                                                             \
    asm volatile("s_add_u32 m0, %1, %2\n\ts_nop 0\n\tglobal_load_lds_dwordx4 %0, off" :: "v"(vaddr), "s"(lb), "i"(LO) : "memory", "scc")

DI void wave_sum8(const float* v, float* z) {
  float u[4];
#pragma unroll
  for (int i = 0; i < 4; ++i) {
    const auto a = __builtin_amdgcn_permlane32_swap(__float_as_uint(v[i]), __float_as_uint(v[i + 4]), false, false);
    u[i] = __uint_as_float(a[0]) + __uint_as_float(a[1]);
  }
#pragma unroll
  for (int i = 0; i < 2; ++i) {
    const auto a = __builtin_amdgcn_permlane16_swap(__float_as_uint(u[i]), __float_as_uint(u[i + 2]), false, false);
    float s = __uint_as_float(a[0]) + __uint_as_float(a[1]);
    s += dpp_xor1(s); s += dpp_xor2(s);
    s += __int_as_float(__builtin_amdgcn_mov_dpp(__float_as_int(s), 0x141, 0xF, 0xF, true));
    s += __int_as_float(__builtin_amdgcn_mov_dpp(__float_as_int(s), 0x140, 0xF, 0xF, true));
    z[i] = s;
  }
}
#define ROW_OF(z, r) __int_as_float(__builtin_amdgcn_readlane(__float_as_int(z[(r) & 1]), ((r) >> 1) * 16))
DI float col8_sum(float v) {
  v += __int_as_float(__builtin_amdgcn_mov_dpp(__float_as_int(v), 0x128, 0xF, 0xF, true));
  const auto a = __builtin_amdgcn_permlane16_swap(__float_as_uint(v), __float_as_uint(v), false, false);
  return xor32_sum(__uint_as_float(a[0]) + __uint_as_float(a[1]));
}

DI void signal_cnt(char* ws, int idx) {
  if (threadIdx.x < NREP) __hip_atomic_fetch_add((u32*)(ws + WS_CNT) + (threadIdx.x * 64 + idx) * 64, 1u, __ATOMIC_RELAXED, __HIP_MEMORY_SCOPE_AGENT);
}
DI void wait_cnt(char* ws, int idx, u32 want, int w) {
  if (w == 0) {
    u32* c = (u32*)(ws + WS_CNT) + (((blockIdx.x >> 3) % NREP) * 64 + idx) * 64;
#pragma unroll 1
    for (u32 spins = 0; spins < (1u << 17); ++spins) {
      if ((u32)__builtin_amdgcn_readfirstlane(__hip_atomic_load(c, __ATOMIC_RELAXED, __HIP_MEMORY_SCOPE_AGENT)) >= want) break;
      __builtin_amdgcn_s_sleep(POLL_SLEEP);
    }
    asm volatile("" ::: "memory");
  }
}
constexpr int WT0 = 20;
#define SIGNAL_ASM(idx, sel)                                                                                         \
  do { unsigned long long ex_;                                                                                       \
    asm volatile("s_cmp_eq_u32 %5, 0\n\ts_cbranch_scc0 1f\n\ts_mov_b64 %0, exec\n\ts_mov_b64 exec, 1\n\t"            \
                 "buffer_atomic_add %1, %2, %3, %4 offen\n\ts_mov_b64 exec, %0\n1:"                                  \
                 : "=&s"(ex_) : "v"(1u), "v"(0u), "s"(sws), "s"(WS_CNT + (u32)(idx) * 256), "s"(sel) : "memory", "scc"); } while (0)
DI float gsum(const u32x4 a) { return __uint_as_float(a[0]) + __uint_as_float(a[2]); }
DI bool gok(const u32x4 a, u32 tag) { return a[1] == tag && a[3] == tag; }
#define SMF(v, p, o) __builtin_amdgcn_smfmac_f32_32x32x32_bf16((v), __builtin_bit_cast(bf16x16, (p)), (o), 0xDDDD, 0, 0)
#define LO8(s) __builtin_shufflevector(s, s, 0, 1, 2, 3, 4, 5, 6, 7)
#define HI8(s) __builtin_shufflevector(s, s, 8, 9, 10, 11, 12, 13, 14, 15)

extern "C" __global__ __launch_bounds__(512) void adaln_block(const u16* __restrict__ x, const u16* __restrict__ wqkv, const u16* __restrict__ wo,
                                                              const u16* __restrict__ wg, const u16* __restrict__ wu, const u16* __restrict__ wd,
                                                              const u16* __restrict__ c, const u16* __restrict__ wmod, char* __restrict__ ws,
                                                              u16* __restrict__ out) {
  __shared__ __attribute__((aligned(16))) char smem[163840];
  ST_DECL STAMP(0);
  const int bid = blockIdx.x, xcd = bid & 7, loc = bid >> 3;
  const int t = threadIdx.x, l = t & 63;
  const int w = __builtin_amdgcn_readfirstlane(t >> 6);
  const u32 lb = lds_base(smem);
  {
    unsigned long long pc;
    u32 len;
    asm volatile("s_getpc_b64 %0\n.Lcode_mark:\n\ts_mov_b32 %1, .Lcode_end-.Lcode_mark" : "=s"(pc), "=s"(len));
    const u32x4 scode = make_srd((const void*)pc, loc < CODE_PF ? len : 0u);
    GTOUCH((u32)(loc * 512 + t) * 128 / CODE_PF, scode, 0u, 0, lb + 57344);
    SB();
  }
  const u32x4 sws = make_srd(ws, WS_BYTES), sx = make_srd(x, 2048 * 1024);
  const u32 lo16 = (u32)l * 16, zs = 0;
  const int hb = l >> 5;
  const int pm3 = loc >> 3, pn3 = loc & 7;
  constexpr u32 TAB = 159744, MT = TAB + 2048, SCR = TAB;
  u32 tag;
  asm volatile("" :: "s"(x), "s"(wqkv), "s"(wo), "s"(wg), "s"(wu), "s"(wd), "s"(c), "s"(wmod), "s"(ws), "s"(out));

  {
    constexpr u32 WSLOT = 24576, RED = 49152, MTAB = 53248;
    const int pm = (xcd >> 1) * 8 + (loc >> 2), cg = (xcd & 1) * 4 + (loc & 3);
    const u32x4 sw = make_srd(wqkv, 512 * 3072), sm = make_srd(wmod, bid < 96 ? 512 * 6144 : 0), sc = make_srd(c, bid < 96 ? 1024 : 0);
    const int mcb = bid >> 1, mkq = bid & 1;
    u32 ev;
    u32x2 cv;
    u32x4 mv[4], xa[8];
    if (bid < 32 && w == 7) GST_SC1("", zs, l < 3 ? (u32)(bid * 3 + l) * 256 : 0x40000000u, sws, zs, WS_CNT);
    asm volatile("buffer_load_dword %0, %1, %2, %3 offen sc1" : "=v"(ev) : "v"(zs), "s"(sws), "s"(WS_EPOCH) : "memory");
#pragma unroll
    for (int i = 0; i < 4; ++i) GLD128(mv[i], (u32)((t >> 3) * 4 * 6144 + (t & 7) * 16), sm, (u32)(mkq * 256 * 6144 + mcb * 128), i * 6144);
    GLD64(cv, (u32)((t >> 3) * 8), sc, (u32)(mkq * 512), 0);
    RAWBAR();
    const int rx = loc & 3, rw = loc >> 2;
    const u32 xsb = (u32)(pm * 64 + w * 2) * 1024;
#define XROW(i) GLD128(xa[i], lo16, sx, xsb + (u32)(((i) >> 1) ^ rx) * 16384, ((i) & 1) * 1024)
    const u32 wvo = (u32)((l >> 3) * 3072 + ((((l >> 2) ^ (l >> 4)) & 1) * 32 + (l & 3) * 8) * 2);
    const u32 wsb = (u32)((w & 3) * 8 * 3072 + cg * 128), wlb = lb + (w & 3) * 1024;
#define KA_SLOT(kc) (((kc) >> 1 == 1 ? 0 : ((kc) >> 1 == 2 ? 65536 + 2 * WSLOT : 65536)) + ((kc) & 1) * WSLOT)
#define KA_CHUNK(kc) SFOR(n_, 0, 6) GLDS(wvo, sw, wsb + (u32)(((kc) + rw) & 7) * (64 * 3072), (n_ >> 1) * 1024 + (n_ & 1) * 32 * 3072, wlb, KA_SLOT(kc) + (n_ >> 1) * 8192 + (n_ & 1) * 4096); SEND
    STAMP(26);
    XROW(0); XROW(1); XROW(2);
    STAMP(27);
    WAITV(3);
    STAMP(28);
    PIN(ev); PIN(cv);
#pragma unroll
    for (int i = 0; i < 4; ++i) PIN(mv[i]);
    tag = (u32)__builtin_amdgcn_readfirstlane(ev) + 1u;
    XROW(3); XROW(4); XROW(5);
    if (bid < 96) {
      float acc[8];
#pragma unroll
      for (int e = 0; e < 8; ++e) acc[e] = 0.f;
#pragma unroll
      for (int i = 0; i < 4; ++i) {
        const float cc = (i & 1) ? bf_hi(cv[i >> 1]) : bf_lo(cv[i >> 1]);
        const float sv = bfround(cc * __builtin_amdgcn_rcpf(1.f + __builtin_amdgcn_exp2f(-1.4426950408889634f * cc)));
#pragma unroll
        for (int e = 0; e < 4; ++e) { acc[2 * e] += sv * bf_lo(mv[i][e]); acc[2 * e + 1] += sv * bf_hi(mv[i][e]); }
      }
      float u[4], z[2];
#pragma unroll
      for (int i = 0; i < 4; ++i) {
        const auto a = __builtin_amdgcn_permlane32_swap(__float_as_uint(acc[i]), __float_as_uint(acc[i + 4]), false, false);
        u[i] = __uint_as_float(a[0]) + __uint_as_float(a[1]);
      }
#pragma unroll
      for (int i = 0; i < 2; ++i) {
        const auto a = __builtin_amdgcn_permlane16_swap(__float_as_uint(u[i]), __float_as_uint(u[i + 2]), false, false);
        z[i] = __uint_as_float(a[0]) + __uint_as_float(a[1]);
        z[i] += __int_as_float(__builtin_amdgcn_mov_dpp(__float_as_int(z[i]), 0x128, 0xF, 0xF, true));
      }
      *(LAS f32x2*)((LAS char*)smem + RED + w * 256 + (l & 7) * 32 + (l >> 4) * 8) = (f32x2){z[0], z[1]};
      BAR();
      if (w == 7) {
        const LAS float* rp = (const LAS float*)((LAS char*)smem + RED) + l;
        const float v = ((rp[0] + rp[64]) + (rp[128] + rp[192])) + ((rp[256] + rp[320]) + (rp[384] + rp[448]));
        const u32x2 gr = {__float_as_uint(v), tag};
        GST_SC1("x2", gr, lo16, sws, (u32)(mcb * 1024 + mkq * 8), WS_GRAN);
      }
    }
    STAMP(1);
    XROW(6); XROW(7);
    if (FRONT == 0 || bid >= 96) RAWBAR();
    u32x4 gq[2];
#define GATHER(tail)                                                                                                            \
    asm volatile("buffer_load_dwordx4 %0, %2, %3, %4 offen sc1\n\tbuffer_load_dwordx4 %1, %2, %3, %5 offen sc1" tail            \
                 : "=&v"(gq[0]), "=&v"(gq[1]) : "v"((u32)t * 16), "s"(sws), "s"(WS_GRAN), "s"(WS_GRAN + 8192) : "memory")
    const int kind = w < 4 ? (w >> 1) : 2, hp = w < 4 ? (w & 1) : (w >> 1 & 1);
    u32 brd0 = (u32)(kind * 8192 + (hb * 8 + (l >> 2 & 3)) * 128 + ((hp ^ (l >> 3)) & 1) * 64 + (l >> 4 & 1) * 32 + (l & 3) * 8);
    u32 brd1 = brd0 + 65536, brd2 = brd0 + 65536 + 2 * WSLOT;
    PIN(brd1); PIN(brd2);
    bf16x8 wq[32];
#define KA_FRAG(kc) _Pragma("unroll") for (int c_ = 0; c_ < 4; ++c_) {                                                    \
      const u32 o_ = ((kc) >> 1 == 1 ? brd0 : ((kc) >> 1 == 2 ? brd2 : brd1)) + ((kc) & 1) * WSLOT + c_ * 2048; wq[(kc) * 4 + c_] = lds_tr2(smem, o_, o_ + 512); }
#define KA_FRAG1(f) do { const u32 o_ = ((f) >> 3 == 2 ? brd2 : brd1) + ((f) >> 2 & 1) * WSLOT + ((f) & 3) * 2048; wq[f] = lds_tr2(smem, o_, o_ + 512); } while (0)
    if (w < 4) { KA_CHUNK(0) KA_CHUNK(1) KA_CHUNK(2) KA_CHUNK(3)
      KA_CHUNK(4) WAITV(30);
    } else WAITV(0);
    STAMP(30);
    f32x2 nv[8][4];
    {
      float v[8], z[2], q[2];
#pragma unroll
      for (int i = 0; i < 8; ++i) {
        PIN(xa[i]);
#pragma unroll
        for (int e = 0; e < 4; ++e) nv[i][e] = (f32x2){bf_lo(xa[i][e]), bf_hi(xa[i][e])};
        const f32x2 s2 = (nv[i][0] + nv[i][1]) + (nv[i][2] + nv[i][3]);
        v[i] = s2[0] + s2[1];
      }
      STAMP(31);
      wave_sum8(v, z);
      z[0] *= (1.f / 512.f); z[1] *= (1.f / 512.f);
      PIN(z[0]); PIN(z[1]);
      if (w < 4) { if (SPLIT5) WAITV(18); else WAITV(12); RAWBAR(); KA_FRAG(0); KA_FRAG(1); BAR(); }
      SFOR(i, 0, 8)
        const float mu = ROW_OF(z, i);
        f32x2 s2 = {0.f, 0.f};
        _Pragma("unroll") for (int e = 0; e < 4; ++e) { nv[i][e] -= (f32x2){mu, mu}; s2 = __builtin_elementwise_fma(nv[i][e], nv[i][e], s2); }
        v[i] = s2[0] + s2[1];
      SEND
      wave_sum8(v, q);
#pragma unroll
      for (int j = 0; j < 2; ++j) { q[j] = ln_rstd(q[j] * (1.f / 512.f)); z[j] = (z[j] - bfround(z[j])) * q[j]; }
      SFOR(i, 0, 8)
        const float rs = ROW_OF(q, i), mr = ROW_OF(z, i);
        _Pragma("unroll") for (int e = 0; e < 4; ++e) { nv[i][e] = __builtin_elementwise_fma(nv[i][e], (f32x2){rs, rs}, (f32x2){mr, mr}); }
      SEND
#pragma unroll
      for (int i = 0; i < 8; ++i)
#pragma unroll
        for (int e = 0; e < 4; ++e) PIN(nv[i][e]);
    }
    if (w >= 4) { KA_CHUNK(5) RAWBAR(); KA_FRAG(0); KA_FRAG(1); BAR(); }
    GATHER("");
    if (w >= 4) { KA_CHUNK(6) KA_CHUNK(7) } else WAITV(2);
    STAMP(2);
    RAWBAR();
    KA_FRAG(2); KA_FRAG(3);
    if (w >= 4) WAITV(12); else WAITV(0);
    PIN(gq[0]); PIN(gq[1]);
    STAMP(3);
#pragma unroll 1
    for (u32 spins = 0; spins < (1u << 17); ++spins) {
      if (__all(gok(gq[0], tag) && gok(gq[1], tag))) break;
      __builtin_amdgcn_s_sleep(2);
      GATHER("\n\ts_waitcnt vmcnt(0)");
    }
    ((LAS float*)((LAS char*)smem + MTAB))[t] = bfround(1.f + bfround(gsum(gq[1])));
    ((LAS float*)((LAS char*)smem + MTAB))[512 + t] = bfround(gsum(gq[0]));
    BAR();
    STAMP(33);
    f32x2 c1[4], sh[4];
    {
      const LAS f32x4* tc = (const LAS f32x4*)((LAS char*)smem + MTAB + l * 32);
      const f32x4 a0 = tc[0], a1 = tc[1], b0 = tc[128], b1 = tc[129];
#pragma unroll
      for (int e = 0; e < 2; ++e) {
        c1[e] = (f32x2){a0[2 * e], a0[2 * e + 1]}; c1[2 + e] = (f32x2){a1[2 * e], a1[2 * e + 1]};
        sh[e] = (f32x2){b0[2 * e], b0[2 * e + 1]}; sh[2 + e] = (f32x2){b1[2 * e], b1[2 * e + 1]};
      }
    }
    u32 awr[4];
#pragma unroll
    for (int i = 0; i < 4; ++i) awr[i] = (u32)((((l >> 1) - 4 * rw) & 31) * 1024 + (l & 1) * 512 + (((i >> 1) ^ rx) & 1) * 256 + (((w * 2 + (i & 1)) ^ (l & 7)) << 4));
#define KA_PUT(i) *(LAS u32x4*)((LAS char*)smem + awr[(i) & 3] + ((i) >> 2) * 32768) = hv[i]
    u32x4 hv[8];
#pragma unroll
    for (int i = 0; i < 4; ++i) { hv[i] = modulate8(nv[i], c1, sh); KA_PUT(i); }
#pragma unroll
    for (int i = 4; i < 8; ++i) { hv[i] = modulate8(nv[i], c1, sh); PIN(hv[i]); }
    const int ta = w < 4 ? 0 : (w & 1), tx = rx >> 1;
    u32 ard[4];
#pragma unroll
    for (int i = 0; i < 4; ++i) ard[i] = (u32)(ta * 32768 + i * 1024 + hb * 512 + (((l & 31) ^ (i * 2 + hb)) << 4));
#define HFRAG(n) lds_frag(smem, ard[(n) & 3] + ((n) >> 2) * 4096)
    const int head = cg * 4 + hp * 2;
    const u32 kvb = WS_KV + (u32)(head * KV_HEAD + pm * 2 * KV_TILE);
    f32x16 z16;
#pragma unroll
    for (int i = 0; i < 16; ++i) z16[i] = 0.f;
    BAR();
    STAMP(4);
    if (w < 4) {
      const float qs = kind == 0 ? 0.25f * 1.4426950408889634f : 1.f;
      const u32 so = kind == 0 ? WS_QF + (u32)(head * 64 + pm * 2) * 1024 + lo16 : kvb + (u32)((l & 51) | (l & 4) << 1 | (l & 8) >> 1) * 16;
      const u32 sh_ = kind == 0 ? 65536u : (u32)KV_HEAD, st_ = kind == 0 ? 1024u : (u32)KV_TILE;
      bf16x8 hf[64];
      f32x16 acc = z16, accp = z16;
      u32x4 ov[2];
#pragma unroll
      for (int i = 0; i < 4; ++i) hf[i] = HFRAG(i);
      SB();
      SFOR(n, 0, 76)
        if constexpr (n == 32 || n == 64) { accp = acc; acc = z16; }
        if constexpr (n < 64) { acc = MFMA32(wq[n & 31], hf[n], acc); SB(); }
        if constexpr (n < 4) KA_PUT(4 + n);
        if constexpr (n == 9) RAWBAR();
        if constexpr (n >= 4 && n < 20) KA_FRAG1(12 + n);
        if constexpr (n + 4 < 64) hf[n + 4] = HFRAG(n + 4);
        if constexpr (n >= 32 && (n & 31) >= 2 && (n & 31) < 10) {
          constexpr int e = (n & 31) - 2;
          ov[e >> 2][e & 3] = pack2(accp[2 * e] * qs, accp[2 * e + 1] * qs);
        }
        if constexpr (n == 42 || n == 74) GST_SC1("x4", ov[0], so, sws, st_ * ((n / 32 - 1) ^ tx), 0);
        if constexpr (n == 43 || n == 75) GST_SC1("x4", ov[1], so, sws, st_ * ((n / 32 - 1) ^ tx) + sh_, 0);
        if (n < 64) SB();
      SEND
    } else {
      bf16x8 hf[32];
      f32x16 acc = z16;
#pragma unroll
      for (int i = 4; i < 8; ++i) KA_PUT(i);
      KA_FRAG(4); KA_FRAG(5);
      WAITV(0);
      BAR();
      __builtin_amdgcn_s_setprio(B_PRIO);
#pragma unroll
      for (int i = 0; i < 4; ++i) hf[i] = HFRAG(i);
      SB();
      SFOR(n, 0, 32)
        acc = MFMA32(hf[n], wq[n], acc);
        SB();
        if constexpr (n >= 2 && n < 18 && (n & 1) == 0) KA_FRAG1(23 + n / 2);
        if constexpr (n + 4 < 32) hf[n + 4] = HFRAG(n + 4);
        SB();
      SEND
      const u32 kvt = kvb + (u32)((ta ^ tx) * KV_TILE);
      const u32 vo = kvt + (u32)((l >> 4 & 1) * KV_HEAD + 1024 + hb * 272 + (l & 15) * 16);
#pragma unroll
      for (int e = 0; e < 2; ++e) {
        const u32x4 ov = {pack2(acc[8 * e], acc[8 * e + 1]), pack2(acc[8 * e + 2], acc[8 * e + 3]), pack2(acc[8 * e + 4], acc[8 * e + 5]), pack2(acc[8 * e + 6], acc[8 * e + 7])};
        GST_SC1("x4", ov, vo, sws, zs, e * 544);
      }
      const u32x4 one = {0x3f803f80u, 0x3f803f80u, 0x3f803f80u, 0x3f803f80u};
      GST_SC1("x4", one, l < 8 ? kvt + (u32)((l >> 2) * KV_HEAD + 1024 + (l & 3) * 272 + 256) : 0x40000000u, sws, zs, 0);
    }
    __builtin_amdgcn_s_setprio(0);
    STAMP(5);
    u32x4 tq[3];
    const u32 msb = WS_GRAN + (u32)(((w == 0 ? 1024 : (w == 1 ? 2048 : 2560)) + pn3 * 64) * 16);
#define TGATHER(tail)                                                                                                           \
    asm volatile("buffer_load_dwordx4 %0, %3, %5, %6 offen sc1\n\tbuffer_load_dwordx4 %1, %3, %5, %7 offen sc1\n\t"             \
                 "buffer_load_dwordx4 %2, %4, %5, %8 offen sc1" tail                                                            \
                 : "=&v"(tq[0]), "=&v"(tq[1]), "=&v"(tq[2])                                                                     \
                 : "v"((u32)t * 16), "v"(lo16), "s"(sws), "s"(WS_GRAN + 1536 * 16), "s"(WS_GRAN + 2048 * 16), "s"(msb) : "memory")
    TGATHER("");
    WAITV(3);
    STAMP(6);
    BAR();
    signal_cnt(ws, cg);
    WAITV(0);
#pragma unroll
    for (int i = 0; i < 3; ++i) PIN(tq[i]);
#pragma unroll 1
    for (u32 spins = 0; spins < (1u << 17); ++spins) {
      if (__all(gok(tq[0], tag) && gok(tq[1], tag) && gok(tq[2], tag))) break;
      __builtin_amdgcn_s_sleep(2);
      TGATHER("\n\ts_waitcnt vmcnt(0)");
    }
    *(LAS u16*)((LAS char*)smem + TAB + t * 2) = (u16)(__float_as_uint(bfround(1.f + bfround(gsum(tq[1])))) >> 16);
    *(LAS u16*)((LAS char*)smem + TAB + 1024 + t * 2) = (u16)(__float_as_uint(bfround(gsum(tq[0]))) >> 16);
    const float mv_ = bfround(gsum(tq[2]));
    ((LAS float*)((LAS char*)smem + MT))[t] = (w == 1) ? bfround(1.f + mv_) : mv_;
  }

  const int lq = loc >= 28 ? 27 : loc;
  const int pan4 = (xcd >> 1) * 4 + lq / 7, pn4 = (xcd & 1) * 7 + lq % 7;
  constexpr u32 STG = KV_HEAD;
  f32x16 zero;
#pragma unroll
  for (int i = 0; i < 16; ++i) zero[i] = 0.f;
  bf16x8 wf[32];
  const u32x4 so = make_srd(wo, 512 * 1024), sd = make_srd(wd, 1792 * 1024);

  {
    const u32x4 sg = make_srd(wg, 512 * 3584), su = make_srd(wu, 512 * 3584);
    const u32 rsb = (u32)((pan4 * 32 + w * 4) * 3584 + pn4 * 256);
    u32 rrd = STG + (u32)((l >> 4 & 1) * 8192 + (hb * 8 + (l >> 2 & 3)) * 256 + ((w ^ (2 * (l >> 2 & 3) + (l >> 4 & 1))) * 32) + (l & 3) * 8);
    PIN(rrd);
    const u32 wtb = (u32)(pn4 * 8 + w) * 32768;
    u32x4 wr[32];
    u32 pv;
    const int head = xcd * 4 + (loc >> 3), qt = (loc & 7) * 8 + w;
    const u32 ksb = (u32)(head * KV_HEAD + w * 1024), wlb = lb + w * 1024;
    const int r = l & 31;
    u32 krd = lo16;
    u32 vrd = (u32)(1024 + hb * 272 + (r < 16 ? r * 16 : (r == 16 ? 256 : 0x40000)));
    STAMP(7);
    wait_cnt(ws, xcd, 32, w);
    BAR();
    STAMP(8);
#define KVR(r) GLDS_SC1(lo16, sws, ksb, WS_KV + (r) * 8192, wlb, (r) * 8192)
    u32x4 qraw;
    GLD128_SC1(qraw, lo16, sws, (u32)(head * 64 + qt) * 1024, WS_QF);
    KVR(0); KVR(1);
    if (w < 4) KVR(16);
    KVR(2); KVR(3);
    GLDS((u32)((l >> 4) * 3584 + (((l >> 1 & 7) ^ (2 * (l >> 4))) * 16 + (l & 1) * 8) * 2), sg, rsb, 0, wlb, STG);
    GLDS((u32)((l >> 4) * 3584 + (((l >> 1 & 7) ^ (2 * (l >> 4) + 1)) * 16 + (l & 1) * 8) * 2), su, rsb, 0, wlb, STG + 8192);
    WAITV(4);
    PIN(qraw);
    RAWBAR();
    STAMP(9);
    const bf16x8 qf = __builtin_bit_cast(bf16x8, qraw);
    float m;
    bf16x8 kf = lds_frag(smem, krd);
    {
      f32x16 s0 = MFMA32(kf, qf, zero);
      m = s0[0];
#pragma unroll
      for (int i = 1; i < 16; ++i) m = fmaxf(m, s0[i]);
      const auto a = __builtin_amdgcn_permlane32_swap(__float_as_uint(m), __float_as_uint(m), false, false);
      m = fmaxf(__uint_as_float(a[0]), __uint_as_float(a[1]));
    }
    f32x16 negm;
#pragma unroll
    for (int i = 0; i < 16; ++i) negm[i] = -m;
    f32x16 o = zero;
    f32x16 sc = MFMA32(kf, qf, negm);
    kf = lds_frag(smem, krd + KV_TILE);
    bf16x8 v0 = lds_frag(smem, vrd), v1;
    const u32 tvo = (u32)((l & 7) * 1024 + pn3 * 128), tsb = (u32)(pm3 * 8 + w) * 8192;
    SFOR(j, 0, 64)
      if constexpr ((j & 1) == 0) __builtin_amdgcn_s_setprio(3 - (j >> 1 & 3));
      if (j == 5) { WAITV(10); RAWBAR(); }
      if (j == 13) { WAITV(0); RAWBAR(); SIGNAL_ASM(64 + pn4, w + (loc >= 28 ? 1 : 0)); }
      if (j == 14) STAMP(10);
      if (j == 28 || j == 56) { krd += 28 * KV_TILE; vrd += 28 * KV_TILE; PIN(krd); PIN(vrd); }
      const u32 jo = (u32)(j % 28) * KV_TILE;
#define EXPS(a, b) _Pragma("unroll") for (int i = (a); i < (b); ++i) sc[i] = __builtin_amdgcn_exp2f(sc[i])
      f32x16 sn;
#define QKNEXT() do { if (j < 63) asm volatile("v_mfma_f32_32x32x16_bf16 %0, %1, %2, %3" : "=&v"(sn) : "v"(kf), "v"(qf), "v"(negm)); } while (0)
#define KREAD() do { if (j < 62) kf = lds_frag(smem, krd + jo + 2 * KV_TILE); } while (0)
#define SIDE()                                                                                                             \
      do {                                                                                                                 \
        if (j == 6 || j == 7) {                                                                                            \
          const u32x4 f_ = __builtin_bit_cast(u32x4, lds_tr2(smem, rrd + (j & 1) * 4096, rrd + (j & 1) * 4096 + 1024));    \
          GST_SC1("x4", f_, loc < 28 ? lo16 : 0x40000000u, sws, wtb + (u32)pan4 * 2048, WS_WT + (j & 1) * 1024);           \
        }                                                                                                                  \
        if (j == WT0 - 2) asm volatile("buffer_load_dword %0, %1, %2, %3 offen sc1" : "=v"(pv) : "v"(zs), "s"(sws), "s"(WS_CNT + (u32)(64 + pn4) * 256) : "memory"); \
        if (j == WT0) {                                              \
          u32 c_, n_;                                                                                                      \
          asm volatile("s_mov_b32 %2, 0x20000\n1:\n\ts_waitcnt vmcnt(0)\n\tv_readfirstlane_b32 %1, %0\n\ts_nop 0\n\t"      \
                       "s_cmp_ge_u32 %1, 16\n\ts_cbranch_scc1 2f\n\ts_sleep 4\n\tbuffer_load_dword %0, %3, %4, %5 offen sc1\n\t" \
                       "s_sub_u32 %2, %2, 1\n\ts_cbranch_scc0 1b\n\ts_waitcnt vmcnt(0)\n2:"                               \
                       : "+v"(pv), "=&s"(c_), "=&s"(n_) : "v"(zs), "s"(sws), "s"(WS_CNT + (u32)(64 + pn4) * 256) : "memory", "scc"); \
        }                                                                                                                  \
        if constexpr (j >= WT0 && j < WT0 + 32) GLD128_SC1(wr[j - WT0], lo16, sws, wtb, WS_WT + (j - WT0) * 1024);         \
        if constexpr (j >= WT0 + 32 && j < WT0 + 41) {     \
          constexpr int m = j - WT0 - 32;                                                                                  \
          if (m >= 2) GTOUCH(tvo, sd, tsb * 7, (m < 2 ? 0 : m - 2) * 8192, lb + STG);                                      \
          else GTOUCH(tvo, so, tsb * 2, (m & 1) * 8192, lb + STG);                                                         \
        }                                                                                                                  \
        if constexpr (j < 6) { KVR(4 + 2 * j); KVR(5 + 2 * j); }                                                           \
      } while (0)
      v0 = lds_frag(smem, vrd + jo); v1 = lds_frag(smem, vrd + jo + 544);
      SB();
      EXPS(j == 0 ? 0 : 2, E0);
      PIN(sc);
      QKNEXT(); SB(); KREAD(); SB();
      EXPS(E0, E1);
      SB();
      o = SMF(v0, LO8(sc), o);
      SIDE();
      SB();
      EXPS(E1, 16);
      PIN(sc);
      if (j < 63) { sn[0] = __builtin_amdgcn_exp2f(sn[0]); sn[1] = __builtin_amdgcn_exp2f(sn[1]); PIN(sn); }
      SB();
      o = SMF(v1, HI8(sc), o);
      PIN(o);
      SB();
      if (j < 63) sc = sn;
    SEND
    WAITV(9);
#pragma unroll
    for (int i = 0; i < 32; ++i) { PIN(wr[i]); wf[i] = __builtin_bit_cast(bf16x8, wr[i]); }
    float lsum = __uint_as_float(__builtin_amdgcn_permlane32_swap(__float_as_uint(o[8]), __float_as_uint(o[8]), false, false)[0]);
    bool ok = lsum < 1e18f;
#pragma unroll
    for (int i = 0; i < 8; ++i) ok = ok && (fabsf(o[i]) < 3e38f);
    if (!__all(ok)) {
      asm volatile("" ::: "memory");
      const __amdgpu_buffer_rsrc_t rk = __builtin_amdgcn_make_buffer_rsrc(ws + WS_KV + (size_t)head * KV_HEAD, 0, KV_HEAD, 0x20000);
      const int vg = 1024 + hb * 272 + (r == 16 ? 256 : (r & 15) * 16);
#define KVLD(off, j) __builtin_bit_cast(bf16x8, __builtin_amdgcn_raw_buffer_load_b128(rk, (off), (j) * KV_TILE, 16))
#pragma unroll 1
      for (int j = 0; j < 64; ++j) {
        const f32x16 sv = MFMA32(KVLD(lo16, j), qf, zero);
#pragma unroll
        for (int i = 0; i < 16; ++i) m = fmaxf(m, sv[i]);
      }
      m = fmaxf(m, __shfl_xor(m, 32));
#pragma unroll
      for (int i = 0; i < 16; ++i) negm[i] = -m;
      o = zero;
#pragma unroll 1
      for (int j = 0; j < 64; ++j) {
        f32x16 sv = MFMA32(KVLD(lo16, j), qf, negm);
        const bf16x8 v0 = KVLD(vg, j), v1 = KVLD(vg + 544, j);
#pragma unroll
        for (int i = 0; i < 16; ++i) sv[i] = __builtin_amdgcn_exp2f(sv[i]);
        o = SMF(v0, LO8(sv), o);
        o = SMF(v1, HI8(sv), o);
      }
      lsum = shfl_idx(o[8], r);
    }
    const float inv = 1.f / lsum;
    u32x4 ov;
#pragma unroll
    for (int e = 0; e < 4; ++e) ov[e] = pack2(o[2 * e] * inv, o[2 * e + 1] * inv);
    GST_SC1("x4", ov, lo16, sws, (u32)(qt * 32 + head) * 1024, WS_OF);
    STAMP(11);
    WAITV(0);
    BAR();
    signal_cnt(ws, 8 + (loc & 7));
    STAMP(12);
  }

  const int tm = w & 1;
  const u32 wvo = (u32)((l >> 3) * 1024 + ((((l >> 2) ^ (l >> 4)) & 1) * 32 + (l & 3) * 8) * 2);
  const u32 wrd0 = (u32)((hb * 4 + (l >> 2 & 3)) * 128 + (l >> 3 & 1) * 64 + (l >> 4 & 1) * 32 + (l & 3) * 8);
  const int rte = xcd * 8 + (pm3 >> 1) * 4 + (pm3 & 1) + (w >> 2) * 2;
  const int erow = w * 8 + (l & 7), ecol = l >> 3;
  f32x16 uv = zero;
  const u32 trd = TAB + (u32)((l >> 4 & 1) * 1024 + hb * 16);
#define UV_PART(k0)                                                                                          \
  do { bf16x8 tf_[16];                                                                                       \
    _Pragma("unroll") for (int i_ = 0; i_ < 4; ++i_) tf_[i_] = lds_frag(smem, trd + ((k0) + i_) * 32);       \
    _Pragma("unroll") for (int i_ = 0; i_ < 16; ++i_) {                                                      \
      if (i_ + 4 < 16) tf_[i_ + 4] = lds_frag(smem, trd + ((k0) + i_ + 4) * 32);                             \
      uv = MFMA32(wf[(k0) + i_], tf_[i_], uv); SB(); }                                                       \
    PIN(uv); } while (0)

  {
    const int ro = pn3 & 3;
    SFOR(j, 0, 8) GLDS(wvo, so, (u32)(w * 8192 + pn3 * 128 + ((j + 2 * ro) & 7) * 65536), 0, lb + w * 1024, j * 8192); SEND
    u32x4 xr;
    GLD128(xr, (u32)((erow & 31) * 1024 + ecol * 16), sx, (u32)(rte * 32768 + pn3 * 128), 0);
    UV_PART(0);
    WAITV(0);
    PIN(xr);
    STAMP(13);
    wait_cnt(ws, 8 + xcd, 32, w);
    RAWBAR();
    STAMP(14);
    if (bid == 0 && t == 0) __hip_atomic_store((u32*)(ws + WS_EPOCH), tag, __ATOMIC_RELAXED, __HIP_MEMORY_SCOPE_AGENT);
    const u32 olo = (u32)((w >> 2) * 32768 + (w & 3) * 2048);
    const u32 osb = (u32)(rte * 32768 + (w & 3) * 2048), olb = lb + olo;
    SFOR(j, 0, 8) GLDS_SC1(lo16, sws, osb + (u32)(((j >> 1) + ro) & 3) * 8192, WS_OF + (j & 1) * 1024, olb, 65536 + (j >> 1) * 8192 + (j & 1) * 1024); SEND
    const int tn = w >> 1 & 1, kh = w >> 2;
    u32 ord = 65536 + (u32)(tm * 32768 + kh * 4096) + lo16;
    PIN(ord);
    const u32 wrd = (wrd0 ^ (u32)(tn * 64)) + kh * 8192;
    f32x16 acc = zero;
    bf16x8 wq[16];
#define WOFRAG(n) wq[n] = lds_tr2(smem, wrd + ((n) >> 2) * 16384 + ((n) & 3) * 2048, wrd + ((n) >> 2) * 16384 + ((n) & 3) * 2048 + 1024)
#pragma unroll
    for (int n = 0; n < 8; ++n) WOFRAG(n);
    SFOR(s, 0, 4)
      if (s == 0) WAITV(6); else if (s == 1) WAITV(4); else if (s == 2) WAITV(2); else WAITV(0);
      RAWBAR();
      bf16x8 oq[4];
#pragma unroll
      for (int j = 0; j < 4; ++j) oq[j] = lds_frag(smem, ord + s * 8192 + j * 1024);
      if constexpr (s < 2) {
#pragma unroll
        for (int j = 0; j < 4; ++j) WOFRAG(8 + s * 4 + j);
      }
      SB();
#pragma unroll
      for (int j = 0; j < 4; ++j) acc = MFMA32(wq[s * 4 + j], oq[j], acc);
      SB();
    SEND
    STAMP(15);
    {
      LAS char* eb = (LAS char*)smem + kh * 17408 + (tm * 32 + (l & 31)) * 272 + (tn * 32 + hb * 4) * 4;
#pragma unroll
      for (int e = 0; e < 4; ++e) *(LAS f32x4*)(eb + e * 32) = (f32x4){acc[4 * e], acc[4 * e + 1], acc[4 * e + 2], acc[4 * e + 3]};
    }
    f32x4 ga[2], cb[2];
#pragma unroll
    for (int e = 0; e < 2; ++e) { ga[e] = *(const LAS f32x4*)((LAS char*)smem + MT + ecol * 32 + e * 16); cb[e] = *(const LAS f32x4*)((LAS char*)smem + MT + 256 + ecol * 32 + e * 16); }
    BAR();
    float xv[8];
    u32x4 x1p, yp;
#pragma unroll
    for (int e = 0; e < 2; ++e) {
      const LAS char* eb = (const LAS char*)smem + erow * 272 + ecol * 32 + e * 16;
      const f32x4 a = *(const LAS f32x4*)eb + *(const LAS f32x4*)(eb + 17408);
#pragma unroll
      for (int h = 0; h < 2; ++h) {
        const u32 pk = pack2(bf_lo(xr[2 * e + h]) + ga[e][2 * h] * a[2 * h], bf_hi(xr[2 * e + h]) + ga[e][2 * h + 1] * a[2 * h + 1]);
        x1p[2 * e + h] = pk;
        xv[4 * e + 2 * h] = bf_lo(pk); xv[4 * e + 2 * h + 1] = bf_hi(pk);
        yp[2 * e + h] = pack2(bf_lo(pk) * cb[e][2 * h], bf_hi(pk) * cb[e][2 * h + 1]);
      }
    }
    GST_SC1("x4", yp, (u32)((ecol >> 1) * 1024 + ((ecol & 1) * 32 + (erow & 31)) * 16), sws, (u32)(rte * 32 + pn3 * 4) * 1024, WS_Y);
    float sum = ((xv[0] + xv[1]) + (xv[2] + xv[3])) + ((xv[4] + xv[5]) + (xv[6] + xv[7]));
    const float mean = col8_sum(sum) * (1.f / 64.f);
    float sq = 0.f;
#pragma unroll
    for (int i = 0; i < 8; ++i) { const float d = xv[i] - mean; sq = __builtin_fmaf(d, d, sq); }
    const f32x2 o2 = {mean, col8_sum(sq)};
    GST_SC1("x2", o2, l < 8 ? (u32)((erow & 31) * 8) : 0x40000000u, sws, (u32)(rte * 2048 + pn3 * 256), WS_ST2);
    STAMP(16);
    WAITV(0);
    RAWBAR();
    signal_cnt(ws, 16 + xcd * 2 + (pm3 >> 1));
    STAMP(17);
    GST_SC1("x4", x1p, (u32)(erow * 8 + ecol) * 16, sws, (u32)bid * 8192, WS_X1);
  }

  const u32 dsb = (u32)(w * 16384 + pn3 * 128), dlb = lb + w * 2048;
  const int r5 = (pn3 * 7) >> 2;
#define CH5(c) (u32)((c) + r5 >= 14 ? (c) + r5 - 14 : (c) + r5)
#define WDPIECE(c, slot, h) GLDS(wvo, sd, dsb + CH5(c) * 131072, (h) * 8192, dlb, (slot) * 16384 + (h) * 1024)
#define WDISSUE(c, slot) do { WDPIECE(c, slot, 0); WDPIECE(c, slot, 1); } while (0)
  const int kq = w & 3;
  const u32 wr5 = wrd0 + kq * 4096;
  bf16x8 dq[14][4];
#define DFRAG1(c, i) dq[c][i] = lds_tr2(smem, (wr5 ^ (((i) & 1) * 64u)) + (c) * 16384 + ((i) >> 1) * 2048, (wr5 ^ (((i) & 1) * 64u)) + (c) * 16384 + ((i) >> 1) * 2048 + 1024)
  if (loc < 28) {
    constexpr u32 RST4 = 157696;
    UV_PART(16);
    wait_cnt(ws, 16 + pan4, 16, w);
    RAWBAR();
    STAMP(18);
    const int rt4 = pan4 * 4;
    const u32 ysb = (u32)(rt4 * 32768 + w * 1024), ylb = lb + w * 1024;
#define YPIECE(i) GLDS_SC1(lo16, sws, ysb, WS_Y + (i) * 8192, ylb, (i) * 8192)
    YPIECE(0); YPIECE(1); YPIECE(2); YPIECE(3);
    GLDS_SC1(lo16, sws, (u32)(rt4 * 2048 + w * 1024), WS_ST2, ylb, 131072);
    YPIECE(4); YPIECE(5);
    float uu[16], vv[16];
#pragma unroll
    for (int i = 0; i < 16; ++i) {
      const auto r = __builtin_amdgcn_permlane16_swap(__float_as_uint(uv[i]), __float_as_uint(uv[i]), false, false);
      uu[i] = __uint_as_float(r[0]); vv[i] = __uint_as_float(r[1]);
      PIN(uu[i]); PIN(vv[i]);
    }
    u32 yrd0 = lo16, yrd1 = lo16 + 65536;
    PIN(yrd1);
    const u32 asb = (u32)(rt4 * 112 + pn4 * 8 + w) * 1024;
#define YFRAG(n) lds_frag(smem, ((n) < 64 ? yrd0 : yrd1) + ((n) & 63) * 1024)
    WAITV(5);
    RAWBAR();
    STAMP(19);
    bf16x8 yf[128];
#pragma unroll
    for (int n = 0; n < 4; ++n) yf[n] = YFRAG(n);
    SB();
    f32x16 acc = zero, accp = zero;
    f32x2 stp = {0.f, 0.f};
    float eg[8], eu[8], et[8];
    u32x4 ov;
    SFOR(n, 0, 160)
      constexpr int a = n >> 5, ks = n & 31;
      if (ks == 0 && a > 0) { accp = acc; acc = zero; }
      if constexpr (n < 128) { acc = MFMA32(wf[ks], yf[n], acc); SB(); }
      if (n == 10) { WAITV(7); RAWBAR(); }
      if (n == 26) { WAITV(8); BAR(); }
      if (n == 56) { WAITV(4); RAWBAR(); }
      if (n == 100) WAITV(3);
      if (n == 121) { WAITV(4); RAWBAR(); SIGNAL_ASM(48 + pan4, w); }
      if constexpr (n < 20 && n % 2 == 0) YPIECE(6 + n / 2);
      if (n == 36 || n == 68 || n == 100) RAWBAR();
      if constexpr (n == 60 && !L2_WARM) {
        const int rr = pm3 * 224 + w * 28 + (l < 28 ? l : 27);
        const int r2 = rr + (rr < 256 ? 768 : 896) + r5 * 128;
        GTOUCH((u32)(r2 >= 1792 ? r2 - 1792 : r2) * 1024, sd, (u32)(pn3 * 128), 0, lb + SCR);
      }
      if constexpr (n >= 36 && n < 52 && n % 4 == 0) WDPIECE((n - 36) / 8, (n - 36) / 8, (n - 36) / 4 & 1);
      if constexpr (n >= 68 && n < 84 && n % 4 == 0) WDPIECE(2 + (n - 68) / 8, 2 + (n - 68) / 8, (n - 68) / 4 & 1);
      if constexpr (n == 84 || n == 88) WDPIECE(8, 8, (n - 84) / 4);
      if constexpr (n >= 100 && n < 116 && n % 4 == 0) WDPIECE(4 + (n - 100) / 8, 4 + (n - 100) / 8, (n - 100) / 4 & 1);
      if constexpr (n >= 100 && n < 116) DFRAG1((n - 100) >> 2, (n - 100) & 3);
      if constexpr (n == 122 || n == 125) WDPIECE(10, 0, n & 1);
      if constexpr (n == 12) {
        const LAS f32x2* sp = (const LAS f32x2*)((LAS char*)smem + 131072 + (t >> 7) * 2048 + (t & 3) * 512 + (t >> 2 & 31) * 8);
        const f32x2 p0 = sp[0], p1 = sp[32];
        float mu = p0[0] + p1[0];
        mu += dpp_xor1(mu); mu += dpp_xor2(mu);
        mu *= 0.125f;
        float q = p0[1] + p1[1] + 64.f * ((p0[0] - mu) * (p0[0] - mu) + (p1[0] - mu) * (p1[0] - mu));
        q += dpp_xor1(q); q += dpp_xor2(q);
        const float rs = __builtin_amdgcn_rsqf(q * (1.f / 512.f) + 1e-5f);
        ((LAS f32x2*)((LAS char*)smem + RST4))[t >> 2] = (f32x2){rs, -mu * rs};
      }
      if (ks == 28 && a < 4) stp = ((const LAS f32x2*)((LAS char*)smem + RST4))[a * 32 + (l & 31)];
      if (n == 128) STAMP(20);
      if (n == 32) STAMP(34);
      if (n == 64) STAMP(35);
      if (n == 96) STAMP(36);
      if (n == 120) STAMP(39);
      if constexpr (n + 4 < 128) yf[n + 4] = YFRAG(n + 4);
      if constexpr (a > 0 && ks >= 2 && ks < 26) {
        constexpr int i = (ks - 2) / 3, ph = (ks - 2) % 3;
        if (ph == 0) {
          eg[i] = __builtin_fmaf(accp[i], stp[0], __builtin_fmaf(uu[i], stp[1], vv[i]));
          eu[i] = __builtin_fmaf(accp[8 + i], stp[0], __builtin_fmaf(uu[8 + i], stp[1], vv[8 + i]));
          et[i] = -1.4426950408889634f * eg[i];
          if (a < 4) { PIN(eg[i]); PIN(eu[i]); PIN(et[i]); }
        } else if (ph == 1) {
          et[i] = 1.f + __builtin_amdgcn_exp2f(et[i]);
          eg[i] *= eu[i];
          if (a < 4) { PIN(eg[i]); PIN(et[i]); }
        } else {
          eg[i] *= __builtin_amdgcn_rcpf(et[i]);
          if (a < 4) PIN(eg[i]);
          if constexpr (i & 1) ov[i >> 1] = pack2(eg[i - 1], eg[i]);
        }
      }
      if constexpr (a > 0 && ks == 26) GST_SC1("x4", ov, lo16, sws, asb, WS_ACT + (a - 1) * 112 * 1024);
      if (n < 128) SB();
    SEND
    STAMP(21);
    WDISSUE(11, 1); WDISSUE(12, 2); WDISSUE(13, 3);
  } else {
    WDISSUE(0, 0); WDISSUE(1, 1); WDISSUE(2, 2); WDISSUE(3, 3); WDISSUE(8, 8); WDISSUE(4, 4); WDISSUE(5, 5);
    WAITV(0);
    RAWBAR();
    SFOR(n, 0, 16) DFRAG1(n >> 2, n & 3); SEND
    BAR();
    WDISSUE(10, 0); WDISSUE(11, 1); WDISSUE(12, 2); WDISSUE(13, 3);
  }

  {
    u32 wra[3][2];
#pragma unroll
    for (int i = 0; i < 3; ++i) { wra[i][0] = wr5 + i * 65536; wra[i][1] = wra[i][0] ^ 64u; PIN(wra[i][0]); PIN(wra[i][1]); }
#define DFRAG(c) _Pragma("unroll") for (int i_ = 0; i_ < 4; ++i_) {                                              \
      const u32 o_ = wra[((c) % 10) >> 2][i_ & 1] + (((c) % 10) & 3) * 16384 + (i_ >> 1) * 2048; dq[c][i_] = lds_tr2(smem, o_, o_ + 1024); }
    f32x4 gb[2];
#pragma unroll
    for (int e = 0; e < 2; ++e) gb[e] = *(const LAS f32x4*)((LAS char*)smem + MT + 512 + (l & 7) * 32 + e * 16);
    const int pan5 = xcd * 2 + (pm3 >> 1);
    wait_cnt(ws, 48 + pan5, 14, w);
    WAITV(0);
    BAR();
    if (loc < 28) signal_cnt(ws, 32 + pan4);
    STAMP(22);
#define WD2(c, slot) do { WDISSUE(c, slot); WDPIECE(c, slot, -8); WDPIECE(c, slot, -7); } while (0)
#define BAR_IF(lo) asm volatile("s_cmp_lt_u32 %0, 4\n\ts_cbranch_scc" lo " 1f\n\ts_barrier\n1:" :: "s"(w) : "memory", "scc")
    if (w >= 4) {
      WD2(6, 6); WD2(7, 7); WD2(9, 9);
      WAITV(0);
      RAWBAR();
      wait_cnt(ws, 32 + pan5, 14, 0);
    }
    STAMP(23);
    const u32 asb = (u32)(rte * 112 + kq * 2) * 1024;
    u32x4 af[14][2];
#define AISSUE(c) do { GLD128_SC1(af[c][0], lo16, sws, asb + CH5(c) * 8192, WS_ACT); GLD128_SC1(af[c][1], lo16, sws, asb + CH5(c) * 8192, WS_ACT + 1024); } while (0)
    SFOR(c, 0, AWIN) AISSUE(c); SEND
    f32x16 acc[2] = {zero, zero};
    u32x4 x1p;
    SFOR(c, 0, 14)
      constexpr int young = c + AWIN <= 14 ? 2 * (AWIN - 1) : (c < 13 ? 2 * (13 - c) + 1 : 0);
      asm volatile("s_waitcnt vmcnt(%0)" :: "i"(young) : "memory");
      if (c == 0) STAMP(24);
      if (c == 5) STAMP(37);
      if (c == 10) STAMP(38);
      if (c == 2) BAR_IF("0");
      if (c == 10) BAR_IF("1");
      if constexpr (c + AWIN < 14) AISSUE(c + AWIN);
      if (c + AWIN == 14) GLD128_SC1(x1p, (u32)t * 16, sws, (u32)bid * 8192, WS_X1);
      if constexpr (c >= 3 && c < 13) { DFRAG(c + 1); }
      PIN(af[c][0]); PIN(af[c][1]);
      SB();
#pragma unroll
      for (int i = 0; i < 4; ++i) acc[i & 1] = MFMA32(dq[c][i], __builtin_bit_cast(bf16x8, af[c][i >> 1]), acc[i & 1]);
      SB();
    SEND
    STAMP(25);
    BAR_IF("0");
    {
      LAS char* eb = (LAS char*)smem + 65536 + kq * 17408 + ((w >> 2) * 32 + (l & 31)) * 272 + hb * 16;
#pragma unroll
      for (int tn = 0; tn < 2; ++tn)
#pragma unroll
        for (int e = 0; e < 4; ++e)
          *(LAS f32x4*)(eb + tn * 128 + e * 32) = (f32x4){acc[tn][4 * e], acc[tn][4 * e + 1], acc[tn][4 * e + 2], acc[tn][4 * e + 3]};
    }
    STAMP(40);
    BAR();
    STAMP(29);
    PIN(x1p);
    u32x4 ov;
#pragma unroll
    for (int e = 0; e < 2; ++e) {
      const LAS char* eb = (const LAS char*)smem + 65536 + (t >> 3) * 272 + (l & 7) * 32 + e * 16;
      const f32x4 s = (*(const LAS f32x4*)eb + *(const LAS f32x4*)(eb + 17408)) + (*(const LAS f32x4*)(eb + 2 * 17408) + *(const LAS f32x4*)(eb + 3 * 17408));
      ov[2 * e] = pack2(bf_lo(x1p[2 * e]) + gb[e][0] * s[0], bf_hi(x1p[2 * e]) + gb[e][1] * s[1]);
      ov[2 * e + 1] = pack2(bf_lo(x1p[2 * e + 1]) + gb[e][2] * s[2], bf_hi(x1p[2 * e + 1]) + gb[e][3] * s[3]);
    }
    *(u32x4*)((char*)out + (size_t)(rte * 32 + (t >> 3 & 31)) * 1024 + pn3 * 128 + (l & 7) * 16) = ov;
  }
  ST_OUT(2, ws);
  asm volatile(".Lcode_end:");
}

extern "C" void kernel_launch(void* const* d_in, const int* in_sizes, int n_in, void* d_out, int out_size, void* d_ws, size_t ws_size,
                              hipStream_t stream) {
  adaln_block<<<256, 512, 0, stream>>>((const u16*)d_in[0], (const u16*)d_in[1], (const u16*)d_in[2], (const u16*)d_in[4], (const u16*)d_in[5],
                                       (const u16*)d_in[6], (const u16*)d_in[7], (const u16*)d_in[8], (char*)d_ws, (u16*)d_out);
}
```

```cpp
#include <hip/hip_runtime.h>
#include <hip/hip_bf16.h>

typedef unsigned short u16;
typedef unsigned int u32;
typedef u32 u32x2 __attribute__((ext_vector_type(2)));
typedef u32 u32x4 __attribute__((ext_vector_type(4)));
typedef float f32x2 __attribute__((ext_vector_type(2)));
typedef float f32x4 __attribute__((ext_vector_type(4)));
typedef float f32x16 __attribute__((ext_vector_type(16)));
typedef __bf16 bf16x2 __attribute__((ext_vector_type(2)));
typedef __bf16 bf16x8 __attribute__((ext_vector_type(8)));
typedef __bf16 bf16x16 __attribute__((ext_vector_type(16)));
typedef short s16x4 __attribute__((ext_vector_type(4)));
typedef short s16x8 __attribute__((ext_vector_type(8)));
#define LAS __attribute__((address_space(3)))
#define DI __device__ __forceinline__

constexpr u32 WS_GRAN = 0;
constexpr u32 WS_EPOCH = 0x18000;
constexpr u32 WS_CNT  = 0x20000;
constexpr u32 WS_QF   = 0x100000;
constexpr u32 WS_KV   = 0x300000;
constexpr u32 WS_OF   = 0x800000;
constexpr u32 WS_X1   = 0xA00000;
constexpr u32 WS_Y    = 0xC00000;
constexpr u32 WS_ST2  = 0xE00000;
constexpr u32 WS_ACT  = 0xF00000;
constexpr u32 WS_WT   = 0x1600000;
constexpr u32 WS_BYTES = 0x1A00000;
constexpr int KV_TILE = 2048, KV_HEAD = 64 * KV_TILE;
constexpr int LKV = 2080;

template <int I> struct IC { static constexpr int value = I; };
template <int B, int E, class F> DI void static_for(F&& f) { if constexpr (B < E) { f(IC<B>{}); static_for<B + 1, E>(f); } }
#define SFOR(n, B, E) static_for<B, E>([&](auto ic_) __attribute__((always_inline)) { constexpr int n = decltype(ic_)::value;
#define SEND });

DI float bf_lo(u32 u) { return __uint_as_float(u << 16); }
DI float bf_hi(u32 u) { return __uint_as_float(u & 0xffff0000u); }
DI u32 pack2(float a, float b) { bf16x2 r = {(__bf16)a, (__bf16)b}; return __builtin_bit_cast(u32, r); }
DI float bfround(float a) { return (float)(__bf16)a; }
DI float ln_rstd(float var) { return __builtin_amdgcn_rcpf(bfround(__builtin_amdgcn_sqrtf(bfround(bfround(var) + 1e-5f)))); }
DI float xor32_sum(float v) {
  const auto r = __builtin_amdgcn_permlane32_swap(__float_as_uint(v), __float_as_uint(v), false, false);
  return __uint_as_float(r[0]) + __uint_as_float(r[1]);
}
DI float dpp_xor1(float v) { return __int_as_float(__builtin_amdgcn_mov_dpp(__float_as_int(v), 0xB1, 0xF, 0xF, true)); }
DI float dpp_xor2(float v) { return __int_as_float(__builtin_amdgcn_mov_dpp(__float_as_int(v), 0x4E, 0xF, 0xF, true)); }
DI float shfl_idx(float v, int srcLane) { return __int_as_float(__builtin_amdgcn_ds_bpermute(srcLane << 2, __float_as_int(v))); }

DI u32x4 make_srd(const void* p, u32 bytes) {
  const size_t a = (size_t)p;
  u32x4 r = {(u32)a, (u32)(a >> 32) & 0xffffu, bytes, 0x20000u};
  return r;
}
#define GLDS(voff, srd, sb, GO, lb, LO)                                                                                   \
  do { u32 tmp_;                                                                                                          \
    asm volatile("s_add_u32 m0, %4, %6\n\ts_add_u32 %0, %3, %5\n\tbuffer_load_dwordx4 %1, %2, %0 offen lds"            \
                 : "=&s"(tmp_) : "v"(voff), "s"(srd), "s"(sb), "s"(lb), "i"(GO), "i"(LO) : "memory", "scc"); } while (0)
#define GLD(W, dst, voff, srd, sb, GO)                                                                                    \
  do { u32 tmp_;                                                                                                          \
    asm volatile("s_add_u32 %1, %4, %5\n\tbuffer_load_dword" W " %0, %2, %3, %1 offen"                                   \
                 : "=v"(dst), "=&s"(tmp_) : "v"(voff), "s"(srd), "s"(sb), "i"(GO) : "memory", "scc"); } while (0)
#define GLD128(dst, voff, srd, sb, GO) GLD("x4", dst, voff, srd, sb, GO)
#define GLD64(dst, voff, srd, sb, GO) GLD("x2", dst, voff, srd, sb, GO)
#define WAITV(N) asm volatile("s_waitcnt vmcnt(" #N ")" ::: "memory")
#define PIN(x) asm volatile("" : "+v"(x))
#define BAR() do { asm volatile("s_waitcnt lgkmcnt(0)" ::: "memory"); __builtin_amdgcn_s_barrier(); asm volatile("" ::: "memory"); } while (0)

DI bf16x8 lds_frag(const char* smem, u32 off) { return *(const LAS bf16x8*)((const LAS char*)smem + off); }
DI bf16x8 lds_tr2(const char* smem, u32 off0, u32 off1) {
  s16x4 a = __builtin_amdgcn_ds_read_tr16_b64_v4i16((LAS s16x4*)((LAS char*)smem + off0));
  s16x4 b = __builtin_amdgcn_ds_read_tr16_b64_v4i16((LAS s16x4*)((LAS char*)smem + off1));
  s16x8 r = __builtin_shufflevector(a, b, 0, 1, 2, 3, 4, 5, 6, 7);
  return __builtin_bit_cast(bf16x8, r);
}
DI u32 lds_base(const char* smem) { return (u32)(size_t)(const LAS char*)smem; }
#define SB() __builtin_amdgcn_sched_barrier(0)
#define RAWBAR() do { asm volatile("" ::: "memory"); __builtin_amdgcn_s_barrier(); asm volatile("" ::: "memory"); } while (0)
#define MFMA32(a, b, c) __builtin_amdgcn_mfma_f32_32x32x16_bf16((a), (b), (c), 0, 0, 0)

DI u32x4 modulate8(const f32x2* n, const f32x2* c1, const f32x2* sh) {
  u32x4 o;
#pragma unroll
  for (int e = 0; e < 4; ++e) { const f32x2 r = __builtin_elementwise_fma(n[e], c1[e], sh[e]); o[e] = pack2(r[0], r[1]); }
  return o;
}

#define GLDS_SC1(voff, srd, sb, GO, lb, LO)                                                                               \
  do { u32 tmp_;                                                                                                          \
    asm volatile("s_add_u32 m0, %4, %6\n\ts_add_u32 %0, %3, %5\n\tbuffer_load_dwordx4 %1, %2, %0 offen sc1 lds"         \
                 : "=&s"(tmp_) : "v"(voff), "s"(srd), "s"(sb), "s"(lb), "i"(GO), "i"(LO) : "memory", "scc"); } while (0)
#define GLD128_SC1(dst, voff, srd, sb, GO)                                                                                \
  do { u32 tmp_;                                                                                                          \
    asm volatile("s_add_u32 %1, %4, %5\n\tbuffer_load_dwordx4 %0, %2, %3, %1 offen sc1"                                 \
                 : "=v"(dst), "=&s"(tmp_) : "v"(voff), "s"(srd), "s"(sb), "i"(GO) : "memory", "scc"); } while (0)
#define GST_SC1(W, val, voff, srd, sb, GO)                                                                                \
  do { u32 tmp_;                                                                                                          \
    asm volatile("s_add_u32 %0, %4, %5\n\tbuffer_store_dword" W " %1, %2, %3, %0 offen sc1\n\ts_nop 1"                  \
                 : "=&s"(tmp_) : "v"(val), "v"(voff), "s"(srd), "s"(sb), "i"(GO) : "memory", "scc"); } while (0)
#define GTOUCH(voff, srd, sb, GO, lb)                                                                                     \
  do { u32 tmp_;                                                                                                          \
    asm volatile("s_mov_b32 m0, %4\n\ts_add_u32 %0, %3, %5\n\tbuffer_load_dword %1, %2, %0 offen lds"                   \
                 : "=&s"(tmp_) : "v"(voff), "s"(srd), "s"(sb), "s"(lb), "i"(GO) : "memory", "scc"); } while (0)


DI void wave_sum8(const float* v, float* z) {
  float u[4];
#pragma unroll
  for (int i = 0; i < 4; ++i) {
    const auto a = __builtin_amdgcn_permlane32_swap(__float_as_uint(v[i]), __float_as_uint(v[i + 4]), false, false);
    u[i] = __uint_as_float(a[0]) + __uint_as_float(a[1]);
  }
#pragma unroll
  for (int i = 0; i < 2; ++i) {
    const auto a = __builtin_amdgcn_permlane16_swap(__float_as_uint(u[i]), __float_as_uint(u[i + 2]), false, false);
    float s = __uint_as_float(a[0]) + __uint_as_float(a[1]);
    s += dpp_xor1(s); s += dpp_xor2(s);
    s += __int_as_float(__builtin_amdgcn_mov_dpp(__float_as_int(s), 0x141, 0xF, 0xF, true));
    s += __int_as_float(__builtin_amdgcn_mov_dpp(__float_as_int(s), 0x140, 0xF, 0xF, true));
    z[i] = s;
  }
}
#define ROW_OF(z, r) __int_as_float(__builtin_amdgcn_readlane(__float_as_int(z[(r) & 1]), ((r) >> 1) * 16))
DI float col8_sum(float v) {
  v += __int_as_float(__builtin_amdgcn_mov_dpp(__float_as_int(v), 0x128, 0xF, 0xF, true));
  const auto a = __builtin_amdgcn_permlane16_swap(__float_as_uint(v), __float_as_uint(v), false, false);
  return xor32_sum(__uint_as_float(a[0]) + __uint_as_float(a[1]));
}

DI void signal_cnt(char* ws, int idx) {
  if (threadIdx.x == 0) __hip_atomic_fetch_add((u32*)(ws + WS_CNT) + idx * 64, 1u, __ATOMIC_RELAXED, __HIP_MEMORY_SCOPE_AGENT);
}
DI void wait_cnt(char* ws, int idx, u32 want, int w) {
  if (w == 0) {
    u32* c = (u32*)(ws + WS_CNT) + idx * 64;
#pragma unroll 1
    for (u32 spins = 0; spins < (1u << 17); ++spins) {
      if ((u32)__builtin_amdgcn_readfirstlane(__hip_atomic_load(c, __ATOMIC_RELAXED, __HIP_MEMORY_SCOPE_AGENT)) >= want) break;
      __builtin_amdgcn_s_sleep(4);
    }
    asm volatile("" ::: "memory");
  }
}
constexpr int WT0 = 18;
#define SIGNAL_ASM(idx, sel)                                                                                         \
  do { unsigned long long ex_;                                                                                       \
    asm volatile("s_cmp_eq_u32 %5, 0\n\ts_cbranch_scc0 1f\n\ts_mov_b64 %0, exec\n\ts_mov_b64 exec, 1\n\t"            \
                 "buffer_atomic_add %1, %2, %3, %4 offen\n\ts_mov_b64 exec, %0\n1:"                                  \
                 : "=&s"(ex_) : "v"(1u), "v"(0u), "s"(sws), "s"(WS_CNT + (u32)(idx) * 256), "s"(sel) : "memory", "scc"); } while (0)
DI float gsum(const u32x4 a) { return __uint_as_float(a[0]) + __uint_as_float(a[2]); }
DI bool gok(const u32x4 a, u32 tag) { return a[1] == tag && a[3] == tag; }
#define SMF(v, p, o) __builtin_amdgcn_smfmac_f32_32x32x32_bf16((v), __builtin_bit_cast(bf16x16, (p)), (o), 0xDDDD, 0, 0)
#define LO8(s) __builtin_shufflevector(s, s, 0, 1, 2, 3, 4, 5, 6, 7)
#define HI8(s) __builtin_shufflevector(s, s, 8, 9, 10, 11, 12, 13, 14, 15)

extern "C" __global__ __launch_bounds__(512, 2) void adaln_block(const u16* __restrict__ x, const u16* __restrict__ wqkv, const u16* __restrict__ wo,
                                                              const u16* __restrict__ wg, const u16* __restrict__ wu, const u16* __restrict__ wd,
                                                              const u16* __restrict__ c, const u16* __restrict__ wmod, char* __restrict__ ws,
                                                              u16* __restrict__ out) {
  __shared__ __attribute__((aligned(16))) char smem[163840];
  const int bid = blockIdx.x, xcd = bid & 7, loc = bid >> 3;
  const int t = threadIdx.x, l = t & 63;
  const int w = __builtin_amdgcn_readfirstlane(t >> 6);
  const u32 lb = lds_base(smem);
  {
    unsigned long long pc;
    u32 len;
    asm volatile("s_getpc_b64 %0\n.Lcode_mark:\n\ts_mov_b32 %1, .Lcode_end-.Lcode_mark" : "=s"(pc), "=s"(len));
    const u32x4 scode = make_srd((const void*)pc, loc < 8 ? len : 0u);
    GTOUCH((u32)(loc * 512 + t) * 16, scode, 0u, 0, lb + 57344);
    SB();
  }
  const u32x4 sws = make_srd(ws, WS_BYTES), sx = make_srd(x, 2048 * 1024);
  const u32 lo16 = (u32)l * 16, zs = 0;
  const int hb = l >> 5;
  const int pm3 = loc >> 3, pn3 = loc & 7;
  constexpr u32 TAB = 159744, MT = TAB + 2048;
  u32 tag;
  asm volatile("" :: "s"(x), "s"(wqkv), "s"(wo), "s"(wg), "s"(wu), "s"(wd), "s"(c), "s"(wmod), "s"(ws), "s"(out));

  {
    constexpr u32 WSLOT = 24576, RED = 49152, MTAB = 53248;
    const int pm = (xcd >> 1) * 8 + (loc >> 2), cg = (xcd & 1) * 4 + (loc & 3);
    const u32x4 sw = make_srd(wqkv, 512 * 3072), sm = make_srd(wmod, bid < 96 ? 512 * 6144 : 0), sc = make_srd(c, bid < 96 ? 1024 : 0);
    const int mcb = bid >> 1, mkq = bid & 1;
    u32 ev;
    u32x2 cv;
    u32x4 mv[4], xa[8];
    if (bid < 32 && w == 7) GST_SC1("", zs, l < 3 ? (u32)(bid * 3 + l) * 256 : 0x40000000u, sws, zs, WS_CNT);
    asm volatile("buffer_load_dword %0, %1, %2, %3 offen sc1" : "=v"(ev) : "v"(zs), "s"(sws), "s"(WS_EPOCH) : "memory");
#pragma unroll
    for (int i = 0; i < 4; ++i) GLD128(mv[i], (u32)((t >> 3) * 4 * 6144 + (t & 7) * 16), sm, (u32)(mkq * 256 * 6144 + mcb * 128), i * 6144);
    GLD64(cv, (u32)((t >> 3) * 8), sc, (u32)(mkq * 512), 0);
    RAWBAR();
    const int rx = loc & 3, rw = loc >> 2;
    const u32 xsb = (u32)(pm * 64 + w * 2) * 1024;
#define XROW(i) GLD128(xa[i], lo16, sx, xsb + (u32)(((i) >> 1) ^ rx) * 16384, ((i) & 1) * 1024)
    const u32 wvo = (u32)((l >> 3) * 3072 + ((((l >> 2) ^ (l >> 4)) & 1) * 32 + (l & 3) * 8) * 2);
    const u32 wsb = (u32)((w & 3) * 8 * 3072 + cg * 128), wlb = lb + (w & 3) * 1024;
#define KA_SLOT(kc) (((kc) >> 1 == 0 ? 0 : ((kc) >> 1 == 2 ? 65536 + 2 * WSLOT : 65536)) + ((kc) & 1) * WSLOT)
#define KA_CHUNK(kc) SFOR(n_, 0, 6) GLDS(wvo, sw, wsb + (u32)(((kc) + rw) & 7) * (64 * 3072), (n_ >> 1) * 1024 + (n_ & 1) * 32 * 3072, wlb, KA_SLOT(kc) + (n_ >> 1) * 8192 + (n_ & 1) * 4096); SEND
    __builtin_amdgcn_s_setprio(2);
    XROW(0); XROW(1);
    __builtin_amdgcn_s_setprio(1);
    XROW(2); XROW(3);
    __builtin_amdgcn_s_setprio(0);
    XROW(4);
    WAITV(5);
    PIN(ev); PIN(cv);
#pragma unroll
    for (int i = 0; i < 4; ++i) PIN(mv[i]);
    tag = (u32)__builtin_amdgcn_readfirstlane(ev) + 1u;
    if (bid < 96) {
      f32x2 a2[4];
#pragma unroll
      for (int i = 0; i < 4; ++i) {
        const float cc = (i & 1) ? bf_hi(cv[i >> 1]) : bf_lo(cv[i >> 1]);
        const float sv = bfround(cc * __builtin_amdgcn_rcpf(1.f + __builtin_amdgcn_exp2f(-1.4426950408889634f * cc)));
#pragma unroll
        for (int e = 0; e < 4; ++e) {
          const f32x2 wv = {bf_lo(mv[i][e]), bf_hi(mv[i][e])};
          a2[e] = i == 0 ? wv * sv : __builtin_elementwise_fma(wv, (f32x2){sv, sv}, a2[e]);
        }
      }
      float acc[8];
#pragma unroll
      for (int e = 0; e < 8; ++e) acc[e] = a2[e >> 1][e & 1];
      float u[4], z[2];
#pragma unroll
      for (int i = 0; i < 4; ++i) {
        const auto a = __builtin_amdgcn_permlane32_swap(__float_as_uint(acc[i]), __float_as_uint(acc[i + 4]), false, false);
        u[i] = __uint_as_float(a[0]) + __uint_as_float(a[1]);
      }
#pragma unroll
      for (int i = 0; i < 2; ++i) {
        const auto a = __builtin_amdgcn_permlane16_swap(__float_as_uint(u[i]), __float_as_uint(u[i + 2]), false, false);
        z[i] = __uint_as_float(a[0]) + __uint_as_float(a[1]);
        z[i] += __int_as_float(__builtin_amdgcn_mov_dpp(__float_as_int(z[i]), 0x128, 0xF, 0xF, true));
      }
      *(LAS f32x2*)((LAS char*)smem + RED + w * 256 + (l & 7) * 32 + (l >> 4) * 8) = (f32x2){z[0], z[1]};
    }
    XROW(5); XROW(6); XROW(7);
    BAR();
    if (bid < 96 && w == 7) {
      __builtin_amdgcn_s_setprio(3);
      const LAS float* rp = (const LAS float*)((LAS char*)smem + RED) + l;
      const float v = ((rp[0] + rp[64]) + (rp[128] + rp[192])) + ((rp[256] + rp[320]) + (rp[384] + rp[448]));
      const u32x2 gr = {__float_as_uint(v), tag};
      GST_SC1("x2", gr, lo16, sws, (u32)(mcb * 1024 + mkq * 8), WS_GRAN);
      __builtin_amdgcn_s_setprio(0);
    }
    u32x4 gq[2];
#define GATHER(tail)                                                                                                            \
    asm volatile("buffer_load_dwordx4 %0, %2, %3, %4 offen sc1\n\tbuffer_load_dwordx4 %1, %2, %3, %5 offen sc1" tail            \
                 : "=&v"(gq[0]), "=&v"(gq[1]) : "v"((u32)t * 16), "s"(sws), "s"(WS_GRAN), "s"(WS_GRAN + 8192) : "memory")
    const int kind = w < 4 ? (w >> 1) : 2, hp = w < 4 ? (w & 1) : (w >> 1 & 1);
    u32 brd0 = (u32)(kind * 8192 + (hb * 8 + (l >> 2 & 3)) * 128 + ((hp ^ (l >> 3)) & 1) * 64 + (l >> 4 & 1) * 32 + (l & 3) * 8);
    u32 brd1 = brd0 + 65536, brd2 = brd0 + 65536 + 2 * WSLOT;
    PIN(brd1); PIN(brd2);
    bf16x8 wq[32];
#define KA_FRAG(kc) _Pragma("unroll") for (int c_ = 0; c_ < 4; ++c_) {                                                    \
      const u32 o_ = ((kc) >> 1 == 0 ? brd0 : ((kc) >> 1 == 2 ? brd2 : brd1)) + ((kc) & 1) * WSLOT + c_ * 2048; wq[(kc) * 4 + c_] = lds_tr2(smem, o_, o_ + 512); }
#define KA_FRAG1(f) do { const u32 o_ = ((f) >> 3 == 2 ? brd2 : brd1) + ((f) >> 2 & 1) * WSLOT + ((f) & 3) * 2048; wq[f] = lds_tr2(smem, o_, o_ + 512); } while (0)
    if (w < 4) {
      KA_CHUNK(0) KA_CHUNK(1) KA_CHUNK(2) KA_CHUNK(3)
      WAITV(24);
    } else if (bid < 96 && w == 7) WAITV(1); else WAITV(0);
    f32x2 nv[8][4];
    {
      float v[8], v2[8], z[2], q[2];
      const bf16x2 one2 = __builtin_bit_cast(bf16x2, 0x3f803f80u);
#pragma unroll
      for (int i = 0; i < 8; ++i) {
        PIN(xa[i]);
        v[i] = 0.f; v2[i] = 0.f;
#pragma unroll
        for (int e = 0; e < 4; ++e) {
          const u32 pw = xa[i][e];
          const bf16x2 p2 = __builtin_bit_cast(bf16x2, pw);
          v[i] = __builtin_amdgcn_fdot2_f32_bf16(p2, one2, v[i], false);
          v2[i] = __builtin_amdgcn_fdot2_f32_bf16(p2, p2, v2[i], false);
        }
      }
      wave_sum8(v, z);
      wave_sum8(v2, q);
#pragma unroll
      for (int j = 0; j < 2; ++j) {
        const float mu = z[j] * (1.f / 512.f);
        q[j] = ln_rstd(fmaxf(__builtin_fmaf(-mu, mu, q[j] * (1.f / 512.f)), 0.f));
        z[j] = -bfround(mu) * q[j];
        PIN(q[j]); PIN(z[j]);
      }
      SFOR(i, 0, 8)
        const float rs = ROW_OF(q, i), mr = ROW_OF(z, i);
        _Pragma("unroll") for (int e = 0; e < 4; ++e)
          nv[i][e] = __builtin_elementwise_fma((f32x2){bf_lo(xa[i][e]), bf_hi(xa[i][e])}, (f32x2){rs, rs}, (f32x2){mr, mr});
      SEND
#pragma unroll
      for (int i = 0; i < 8; ++i)
#pragma unroll
        for (int e = 0; e < 4; ++e) PIN(nv[i][e]);
    }
    GATHER("");
    if (w >= 4) { KA_CHUNK(4) KA_CHUNK(5) } else WAITV(14);
    RAWBAR();
    KA_FRAG(0); KA_FRAG(1);
    if (w >= 4) WAITV(12); else WAITV(0);
    PIN(gq[0]); PIN(gq[1]);
#pragma unroll 1
    for (u32 spins = 0; spins < (1u << 17); ++spins) {
      if (__all(gok(gq[0], tag) && gok(gq[1], tag))) break;
      __builtin_amdgcn_s_sleep(2);
      GATHER("\n\ts_waitcnt vmcnt(0)");
    }
    ((LAS float*)((LAS char*)smem + MTAB))[t] = bfround(1.f + bfround(gsum(gq[1])));
    ((LAS float*)((LAS char*)smem + MTAB))[512 + t] = bfround(gsum(gq[0]));
    BAR();
    f32x2 c1[4], sh[4];
    {
      const LAS f32x4* tc = (const LAS f32x4*)((LAS char*)smem + MTAB + l * 32);
      const f32x4 a0 = tc[0], a1 = tc[1], b0 = tc[128], b1 = tc[129];
#pragma unroll
      for (int e = 0; e < 2; ++e) {
        c1[e] = (f32x2){a0[2 * e], a0[2 * e + 1]}; c1[2 + e] = (f32x2){a1[2 * e], a1[2 * e + 1]};
        sh[e] = (f32x2){b0[2 * e], b0[2 * e + 1]}; sh[2 + e] = (f32x2){b1[2 * e], b1[2 * e + 1]};
      }
    }
    KA_FRAG(2);
    u32 awr[4];
#pragma unroll
    for (int i = 0; i < 4; ++i) awr[i] = (u32)((((l >> 1) - 4 * rw) & 31) * 1024 + (l & 1) * 512 + (((i >> 1) ^ rx) & 1) * 256 + (((w * 2 + (i & 1)) ^ (l & 7)) << 4));
#define KA_PUT(i) *(LAS u32x4*)((LAS char*)smem + awr[(i) & 3] + ((i) >> 2) * 32768) = hv[i]
    u32x4 hv[8];
#pragma unroll
    for (int i = 0; i < 4; ++i) { hv[i] = modulate8(nv[i], c1, sh); KA_PUT(i); }
    KA_FRAG(3);
#pragma unroll
    for (int i = 4; i < 8; ++i) { hv[i] = modulate8(nv[i], c1, sh); PIN(hv[i]); }
    const int ta = w < 4 ? 0 : (w & 1), tx = rx >> 1;
    u32 ard[4];
#pragma unroll
    for (int i = 0; i < 4; ++i) ard[i] = (u32)(ta * 32768 + i * 1024 + hb * 512 + (((l & 31) ^ (i * 2 + hb)) << 4));
#define HFRAG(n) lds_frag(smem, ard[(n) & 3] + ((n) >> 2) * 4096)
    const int head = cg * 4 + hp * 2;
    const u32 kvb = WS_KV + (u32)(head * KV_HEAD + pm * 2 * KV_TILE);
    f32x16 z16;
#pragma unroll
    for (int i = 0; i < 16; ++i) z16[i] = 0.f;
    BAR();
    if (w < 4) {
      const float qs = kind == 0 ? 0.25f * 1.4426950408889634f : 1.f;
      const u32 so = kind == 0 ? WS_QF + (u32)(head * 64 + pm * 2) * 1024 + lo16 : kvb + (u32)(hb * 1024 + ((l & 19) | (l & 4) << 1 | (l & 8) >> 1) * 16);
      const u32 sh_ = kind == 0 ? 65536u : (u32)KV_HEAD, st_ = kind == 0 ? 1024u : (u32)KV_TILE;
#define AIDX(n) ((n) < 24 ? (n) : ((n) < 48 ? (n) + 8 : ((n) < 56 ? (n) - 24 : (n))))
      bf16x8 hf[64];
      f32x16 acc[2] = {z16, z16};
      u32x4 ov[2];
#pragma unroll
      for (int i = 0; i < 4; ++i) hf[i] = HFRAG(i);
      SB();
      SFOR(n, 0, 72)
        if constexpr (n < 64) { acc[AIDX(n) >> 5] = MFMA32(wq[AIDX(n) & 31], hf[n], acc[AIDX(n) >> 5]); SB(); }
        if constexpr (n < 4) KA_PUT(4 + n);
        if constexpr (n == 8) RAWBAR();
        if constexpr (n >= 8 && n < 16) KA_FRAG1(8 + n);
        if constexpr (n == 38) RAWBAR();
        if constexpr (n >= 38 && n < 46) KA_FRAG1(n - 14);
        if constexpr (n + 4 < 64) hf[n + 4] = HFRAG(AIDX(n + 4));
        if constexpr (n >= 58 && (n & 7) >= 2 && (n & 7) < 6) {
          constexpr int e = ((n & 7) - 2) * 2, ps = n >= 64;
          ov[e >> 2][e & 3] = pack2(acc[ps][2 * e] * qs, acc[ps][2 * e + 1] * qs);
          ov[e >> 2][(e & 3) + 1] = pack2(acc[ps][2 * e + 2] * qs, acc[ps][2 * e + 3] * qs);
        }
        if constexpr (n == 62 || n == 70) GST_SC1("x4", ov[0], so, sws, st_ * ((n >= 64) ^ tx), 0);
        if constexpr (n == 63 || n == 71) GST_SC1("x4", ov[1], so, sws, st_ * ((n >= 64) ^ tx) + sh_, 0);
        if (n < 64) SB();
      SEND
    } else {
      bf16x8 hf[32];
      f32x16 acc = z16;
#pragma unroll
      for (int i = 4; i < 8; ++i) KA_PUT(i);
      WAITV(0);
      BAR();
      KA_CHUNK(6) KA_CHUNK(7)
#pragma unroll
      for (int i = 0; i < 4; ++i) hf[i] = HFRAG(i);
      SB();
      SFOR(n, 0, 32)
        acc = MFMA32(hf[n], wq[n], acc);
        SB();
        if constexpr (n < 8) KA_FRAG1(16 + n);
        if constexpr (n == 8) { WAITV(0); RAWBAR(); }
        if constexpr (n >= 8 && n < 16) KA_FRAG1(16 + n);
        if constexpr (n + 4 < 32) hf[n + 4] = HFRAG(n + 4);
        SB();
      SEND
      const u32 kvt = kvb + (u32)((ta ^ tx) * KV_TILE);
      const u32 vo = kvt + (u32)((l >> 4 & 1) * KV_HEAD + 512 + hb * 256 + (l & 15) * 16);
#pragma unroll
      for (int e = 0; e < 2; ++e) {
        const u32x4 ov = {pack2(acc[8 * e], acc[8 * e + 1]), pack2(acc[8 * e + 2], acc[8 * e + 3]), pack2(acc[8 * e + 4], acc[8 * e + 5]), pack2(acc[8 * e + 6], acc[8 * e + 7])};
        GST_SC1("x4", ov, vo, sws, zs, e * 1024);
      }
    }
    u32x4 tq[3];
    const u32 msb = WS_GRAN + (u32)(((w == 0 ? 1024 : (w == 1 ? 2048 : 2560)) + pn3 * 64) * 16);
#define TGATHER(tail)                                                                                                           \
    asm volatile("buffer_load_dwordx4 %0, %3, %5, %6 offen sc1\n\tbuffer_load_dwordx4 %1, %3, %5, %7 offen sc1\n\t"             \
                 "buffer_load_dwordx4 %2, %4, %5, %8 offen sc1" tail                                                            \
                 : "=&v"(tq[0]), "=&v"(tq[1]), "=&v"(tq[2])                                                                     \
                 : "v"((u32)t * 16), "v"(lo16), "s"(sws), "s"(WS_GRAN + 1536 * 16), "s"(WS_GRAN + 2048 * 16), "s"(msb) : "memory")
    TGATHER("");
    WAITV(3);
    BAR();
    signal_cnt(ws, cg);
    WAITV(0);
#pragma unroll
    for (int i = 0; i < 3; ++i) PIN(tq[i]);
#pragma unroll 1
    for (u32 spins = 0; spins < (1u << 17); ++spins) {
      if (__all(gok(tq[0], tag) && gok(tq[1], tag) && gok(tq[2], tag))) break;
      __builtin_amdgcn_s_sleep(2);
      TGATHER("\n\ts_waitcnt vmcnt(0)");
    }
    *(LAS u16*)((LAS char*)smem + TAB + t * 2) = (u16)(__float_as_uint(bfround(1.f + bfround(gsum(tq[1])))) >> 16);
    *(LAS u16*)((LAS char*)smem + TAB + 1024 + t * 2) = (u16)(__float_as_uint(bfround(gsum(tq[0]))) >> 16);
    if (t < 128) *(LAS u32x4*)((LAS char*)smem + t * 1040 + 1024) = (u32x4){0x3f803f80u, 0x3f803f80u, 0x3f803f80u, 0x3f803f80u};
    const float mv_ = bfround(gsum(tq[2]));
    ((LAS float*)((LAS char*)smem + MT))[t] = (w == 1) ? bfround(1.f + mv_) : mv_;
  }

  const int lq = loc >= 28 ? 27 : loc;
  const int pan4 = (xcd >> 1) * 4 + lq / 7, pn4 = (xcd & 1) * 7 + lq % 7;
  constexpr u32 STG = 135168;
  f32x16 zero;
#pragma unroll
  for (int i = 0; i < 16; ++i) zero[i] = 0.f;
  bf16x8 wf[32];
  const u32x4 so = make_srd(wo, 512 * 1024), sd = make_srd(wd, 1792 * 1024);

  const int rte = xcd * 8 + (pm3 >> 1) * 4 + (pm3 & 1) + (w >> 2) * 2;
  const int erow = w * 8 + (l & 7), ecol = l >> 3;
  u32x4 xr;

  {
    const u32x4 sg = make_srd(wg, 512 * 3584), su = make_srd(wu, 512 * 3584);
    const u32 rsb = (u32)((pan4 * 32 + w * 4) * 3584 + pn4 * 256);
    u32 rrd = STG + (u32)((l >> 4 & 1) * 8192 + (hb * 8 + (l >> 2 & 3)) * 256 + ((w ^ (2 * (l >> 2 & 3) + (l >> 4 & 1))) * 32) + (l & 3) * 8);
    PIN(rrd);
    const u32 wtb = (u32)(pn4 * 8 + w) * 32768;
    u32x4 wr[32];
    u32 pv;
    bf16x8 rf;
    const int head = xcd * 4 + (loc >> 3), qt = (loc & 7) * 8 + w;
    const u32 ksb = (u32)(head * KV_HEAD + w * 1024), wlb = lb + w * 1024;
    const int r = l & 31;
    u32 krd = (u32)(hb * 1040 + r * 16);
    u32 vrd = (u32)(r < 16 ? 512 + hb * 256 + r * 16 : (r == 16 ? 1024 : 0x40000));
    wait_cnt(ws, xcd, 32, w);
    BAR();
#define KVR(r) GLDS_SC1(lo16, sws, ksb, WS_KV + (r) * 8192, lb + w * 1040, (r) * 8320)
    u32x4 qraw;
    __builtin_amdgcn_s_setprio(2);
    GLD128_SC1(qraw, lo16, sws, (u32)(head * 64 + qt) * 1024, WS_QF);
    KVR(0); KVR(1);
    __builtin_amdgcn_s_setprio(1);
    KVR(2); KVR(3);
    __builtin_amdgcn_s_setprio(0);
    GLDS((u32)((l >> 4) * 3584 + (((l >> 1 & 7) ^ (2 * (l >> 4))) * 16 + (l & 1) * 8) * 2), sg, rsb, 0, wlb, STG);
    GLDS((u32)((l >> 4) * 3584 + (((l >> 1 & 7) ^ (2 * (l >> 4) + 1)) * 16 + (l & 1) * 8) * 2), su, rsb, 0, wlb, STG + 8192);
    WAITV(4);
    PIN(qraw);
    RAWBAR();
    const bf16x8 qf = __builtin_bit_cast(bf16x8, qraw);
    float m;
    bf16x8 kf = lds_frag(smem, krd);
    {
      f32x16 s0 = MFMA32(kf, qf, zero);
      m = s0[0];
#pragma unroll
      for (int i = 1; i < 16; ++i) m = fmaxf(m, s0[i]);
      const auto a = __builtin_amdgcn_permlane32_swap(__float_as_uint(m), __float_as_uint(m), false, false);
      m = fmaxf(__uint_as_float(a[0]), __uint_as_float(a[1]));
    }
    f32x16 negm;
#pragma unroll
    for (int i = 0; i < 16; ++i) negm[i] = -m;
    f32x16 o = zero;
    f32x16 sc = MFMA32(kf, qf, negm);
    kf = lds_frag(smem, krd + LKV);
    bf16x8 v0 = lds_frag(smem, vrd), v1;
    const u32 tvo2 = (u32)((l < 14 ? l : 13) * 1024 + pn3 * 128), tvo3 = (u32)((l & 15) * 1024 + pn3 * 128);
    const u32 tsb = (u32)(pm3 * 8 + w) * 8192;
    SFOR(j, 0, 64)
      __builtin_amdgcn_s_setprio(3 - (j & 3));
      if (j == 5) { WAITV(10); RAWBAR(); }
      if (j == 13) { WAITV(0); RAWBAR(); SIGNAL_ASM(64 + pn4, w + (loc >= 28 ? 1 : 0)); }
      if (j == 28 || j == 56) { krd += 28 * LKV; vrd += 28 * LKV; PIN(krd); PIN(vrd); }
      const u32 jo = (u32)(j % 28) * LKV;
#define EXPS(a, b) _Pragma("unroll") for (int i = (a); i < (b); ++i) sc[i] = __builtin_amdgcn_exp2f(sc[i])
      f32x16 sn;
#define QKNEXT() do { if (j < 63) asm volatile("v_mfma_f32_32x32x16_bf16 %0, %1, %2, %3" : "=&v"(sn) : "v"(kf), "v"(qf), "v"(negm)); } while (0)
#define KREAD() do { if (j < 62) kf = lds_frag(smem, krd + jo + 2 * LKV); } while (0)
#define SIDE()                                                                                                             \
      do {                                                                                                                 \
        if (j == 7 || j == 8) GST_SC1("x4", rf, loc < 28 ? lo16 : 0x40000000u, sws, wtb + (u32)pan4 * 2048, WS_WT + (j - 7) * 1024); \
        if (j == 6 || j == 7) rf = lds_tr2(smem, rrd + (j & 1) * 4096, rrd + (j & 1) * 4096 + 1024);                        \
        if (j == WT0 - 2) asm volatile("buffer_load_dword %0, %1, %2, %3 offen sc1" : "=v"(pv) : "v"(zs), "s"(sws), "s"(WS_CNT + (u32)(64 + pn4) * 256) : "memory"); \
        if (j == WT0) {                                              \
          u32 c_, n_;                                                                                                      \
          asm volatile("s_mov_b32 %2, 0x20000\n1:\n\ts_waitcnt vmcnt(0)\n\tv_readfirstlane_b32 %1, %0\n\ts_nop 0\n\t"      \
                       "s_cmp_ge_u32 %1, 16\n\ts_cbranch_scc1 2f\n\ts_sleep 4\n\tbuffer_load_dword %0, %3, %4, %5 offen sc1\n\t" \
                       "s_sub_u32 %2, %2, 1\n\ts_cbranch_scc0 1b\n\ts_waitcnt vmcnt(0)\n2:"                               \
                       : "+v"(pv), "=&s"(c_), "=&s"(n_) : "v"(zs), "s"(sws), "s"(WS_CNT + (u32)(64 + pn4) * 256) : "memory", "scc"); \
        }                                                                                                                  \
        if constexpr (j >= WT0 && j < WT0 + 32) GLD128_SC1(wr[j - WT0], lo16, sws, wtb, WS_WT + (j - WT0) * 1024);         \
                                                        \
        if constexpr (j >= WT0 + 33 && j < WT0 + 41 && (j - WT0) % 2 == 1) GTOUCH(tvo2, sd, tsb * 7, (j - WT0 - 33) / 2 * 14336, lb + STG);          \
        if constexpr (j == WT0 + 32) GTOUCH(tvo3, so, tsb * 2, 0, lb + STG);                                               \
        if (j == WT0 + 31) GLD128(xr, (u32)((erow & 31) * 1024 + ecol * 16), sx, (u32)(rte * 32768 + pn3 * 128), 0);       \
        if constexpr (j < 6) { KVR(4 + 2 * j); KVR(5 + 2 * j); }                                                           \
      } while (0)
      v0 = lds_frag(smem, vrd + jo); v1 = lds_frag(smem, vrd + jo + 1040);
      SB();
      EXPS(j == 0 ? 0 : 2, 5);
      PIN(sc);
      QKNEXT(); SB(); KREAD(); SB();
      EXPS(5, 9);
      SB();
      o = SMF(v0, LO8(sc), o);
      SIDE();
      SB();
      EXPS(9, 16);
      PIN(sc);
      if (j < 63) { sn[0] = __builtin_amdgcn_exp2f(sn[0]); sn[1] = __builtin_amdgcn_exp2f(sn[1]); PIN(sn); }
      SB();
      o = SMF(v1, HI8(sc), o);
      PIN(o);
      SB();
      if (j < 63) sc = sn;
    SEND
    WAITV(5);
#pragma unroll
    for (int i = 0; i < 32; ++i) { PIN(wr[i]); wf[i] = __builtin_bit_cast(bf16x8, wr[i]); }
    PIN(xr);
    float lsum = __uint_as_float(__builtin_amdgcn_permlane32_swap(__float_as_uint(o[8]), __float_as_uint(o[8]), false, false)[0]);
    bool ok = lsum < 1e18f;
#pragma unroll
    for (int i = 0; i < 8; ++i) ok = ok && (fabsf(o[i]) < 3e38f);
    if (!__all(ok)) {
      asm volatile("" ::: "memory");
      const __amdgpu_buffer_rsrc_t rk = __builtin_amdgcn_make_buffer_rsrc(ws + WS_KV + (size_t)head * KV_HEAD, 0, KV_HEAD, 0x20000);
      const int kg = hb * 1024 + r * 16, vg = 512 + hb * 256 + (r & 15) * 16;
      const bf16x8 ones = __builtin_bit_cast(bf16x8, (u32x4){0x3f803f80u, 0x3f803f80u, 0x3f803f80u, 0x3f803f80u});
#define KVLD(off, j) __builtin_bit_cast(bf16x8, __builtin_amdgcn_raw_buffer_load_b128(rk, (off), (j) * KV_TILE, 16))
#pragma unroll 1
      for (int j = 0; j < 64; ++j) {
        const f32x16 sv = MFMA32(KVLD(kg, j), qf, zero);
#pragma unroll
        for (int i = 0; i < 16; ++i) m = fmaxf(m, sv[i]);
      }
      m = fmaxf(m, __shfl_xor(m, 32));
#pragma unroll
      for (int i = 0; i < 16; ++i) negm[i] = -m;
      o = zero;
#pragma unroll 1
      for (int j = 0; j < 64; ++j) {
        f32x16 sv = MFMA32(KVLD(kg, j), qf, negm);
        const bf16x8 a0 = KVLD(vg, j), a1 = KVLD(vg + 1024, j);
        const bf16x8 v0 = r < 16 ? a0 : ones, v1 = r < 16 ? a1 : ones;
#pragma unroll
        for (int i = 0; i < 16; ++i) sv[i] = __builtin_amdgcn_exp2f(sv[i]);
        o = SMF(v0, LO8(sv), o);
        o = SMF(v1, HI8(sv), o);
      }
      lsum = shfl_idx(o[8], r);
    }
    const float inv = 1.f / lsum;
    u32x4 ov;
#pragma unroll
    for (int e = 0; e < 4; ++e) ov[e] = pack2(o[2 * e] * inv, o[2 * e + 1] * inv);
    GST_SC1("x4", ov, lo16, sws, (u32)(qt * 32 + head) * 1024, WS_OF);
    WAITV(0);
    BAR();
    signal_cnt(ws, 8 + (loc & 7));
  }

  const int tm = w & 1;
  const u32 wvo = (u32)((l >> 3) * 1024 + ((((l >> 2) ^ (l >> 4)) & 1) * 32 + (l & 3) * 8) * 2);
  const u32 wrd0 = (u32)((hb * 4 + (l >> 2 & 3)) * 128 + (l >> 3 & 1) * 64 + (l >> 4 & 1) * 32 + (l & 3) * 8);
  f32x16 uv = zero;
  const u32 trd = TAB + (u32)((l >> 4 & 1) * 1024 + hb * 16);
#define UV_PART(k0)                                                                                          \
  do { bf16x8 tf_[16];                                                                                       \
    _Pragma("unroll") for (int i_ = 0; i_ < 4; ++i_) tf_[i_] = lds_frag(smem, trd + ((k0) + i_) * 32);       \
    _Pragma("unroll") for (int i_ = 0; i_ < 16; ++i_) {                                                      \
      if (i_ + 4 < 16) tf_[i_ + 4] = lds_frag(smem, trd + ((k0) + i_ + 4) * 32);                             \
      uv = MFMA32(wf[(k0) + i_], tf_[i_], uv); SB(); }                                                       \
    PIN(uv); } while (0)

  {
    const int ro = pn3 & 3;
    {
      bf16x8 tf[16];
#pragma unroll
      for (int i = 0; i < 4; ++i) tf[i] = lds_frag(smem, trd + i * 32);
      SFOR(j, 0, 16)
        uv = MFMA32(wf[j], tf[j], uv);
        SB();
        if constexpr (j + 4 < 16) tf[j + 4] = lds_frag(smem, trd + (j + 4) * 32);
        if constexpr (j < 8) GLDS(wvo, so, (u32)(w * 8192 + pn3 * 128 + ((j + 2 * ro) & 7) * 65536), 0, lb + w * 1024, j * 8192);
        SB();
      SEND
      PIN(uv);
    }
    WAITV(0);
    wait_cnt(ws, 8 + xcd, 32, w);
    RAWBAR();
    if (bid == 0 && t == 0) __hip_atomic_store((u32*)(ws + WS_EPOCH), tag, __ATOMIC_RELAXED, __HIP_MEMORY_SCOPE_AGENT);
    const u32 olo = (u32)((w >> 2) * 32768 + (w & 3) * 2048);
    const u32 osb = (u32)(rte * 32768 + (w & 3) * 2048), olb = lb + olo;
    SFOR(j, 0, 8) if ((j & 1) == 0) __builtin_amdgcn_s_setprio(3 - j / 2); GLDS_SC1(lo16, sws, osb + (u32)(((j >> 1) + ro) & 3) * 8192, WS_OF + (j & 1) * 1024, olb, 65536 + (j >> 1) * 8192 + (j & 1) * 1024); SEND
    const int tn = w >> 1 & 1, kh = w >> 2;
    u32 ord = 65536 + (u32)(tm * 32768 + kh * 4096) + lo16;
    PIN(ord);
    const u32 wrd = (wrd0 ^ (u32)(tn * 64)) + kh * 8192;
    f32x16 acc = zero;
    bf16x8 wq[16];
#define WOFRAG(n) wq[n] = lds_tr2(smem, wrd + ((n) >> 2) * 16384 + ((n) & 3) * 2048, wrd + ((n) >> 2) * 16384 + ((n) & 3) * 2048 + 1024)
#pragma unroll
    for (int n = 0; n < 8; ++n) WOFRAG(n);
    SFOR(s, 0, 4)
      if (s == 0) WAITV(6); else if (s == 1) WAITV(4); else if (s == 2) WAITV(2); else WAITV(0);
      RAWBAR();
      bf16x8 oq[4];
#pragma unroll
      for (int j = 0; j < 4; ++j) oq[j] = lds_frag(smem, ord + s * 8192 + j * 1024);
      if constexpr (s < 2) {
#pragma unroll
        for (int j = 0; j < 4; ++j) WOFRAG(8 + s * 4 + j);
      }
      SB();
#pragma unroll
      for (int j = 0; j < 4; ++j) acc = MFMA32(wq[s * 4 + j], oq[j], acc);
      SB();
    SEND
    {
      LAS char* eb = (LAS char*)smem + kh * 17408 + (tm * 32 + (l & 31)) * 272 + (tn * 32 + hb * 4) * 4;
#pragma unroll
      for (int e = 0; e < 4; ++e) *(LAS f32x4*)(eb + e * 32) = (f32x4){acc[4 * e], acc[4 * e + 1], acc[4 * e + 2], acc[4 * e + 3]};
    }
    f32x4 ga[2], cb[2];
#pragma unroll
    for (int e = 0; e < 2; ++e) { ga[e] = *(const LAS f32x4*)((LAS char*)smem + MT + ecol * 32 + e * 16); cb[e] = *(const LAS f32x4*)((LAS char*)smem + MT + 256 + ecol * 32 + e * 16); }
    BAR();
    u32x4 x1p, yp;
#pragma unroll
    for (int e = 0; e < 2; ++e) {
      const LAS char* eb = (const LAS char*)smem + erow * 272 + ecol * 32 + e * 16;
      const f32x4 a = *(const LAS f32x4*)eb + *(const LAS f32x4*)(eb + 17408);
#pragma unroll
      for (int h = 0; h < 2; ++h) {
        const u32 pk = pack2(bf_lo(xr[2 * e + h]) + ga[e][2 * h] * a[2 * h], bf_hi(xr[2 * e + h]) + ga[e][2 * h + 1] * a[2 * h + 1]);
        x1p[2 * e + h] = pk;
        yp[2 * e + h] = pack2(bf_lo(pk) * cb[e][2 * h], bf_hi(pk) * cb[e][2 * h + 1]);
      }
    }
    GST_SC1("x4", yp, (u32)((ecol >> 1) * 1024 + ((ecol & 1) * 32 + (erow & 31)) * 16), sws, (u32)(rte * 32 + pn3 * 4) * 1024, WS_Y);
    float sum = 0.f, sq = 0.f;
#pragma unroll
    for (int e = 0; e < 4; ++e) {
      const u32 pw = x1p[e];
      const bf16x2 p2 = __builtin_bit_cast(bf16x2, pw);
      sum = __builtin_amdgcn_fdot2_f32_bf16(p2, __builtin_bit_cast(bf16x2, 0x3f803f80u), sum, false);
      sq = __builtin_amdgcn_fdot2_f32_bf16(p2, p2, sq, false);
    }
    sum = col8_sum(sum); sq = col8_sum(sq);
    const float mean = sum * (1.f / 64.f);
    const f32x2 o2 = {mean, fmaxf(__builtin_fmaf(-mean, sum, sq), 0.f)};
    GST_SC1("x2", o2, l < 8 ? (u32)((erow & 31) * 8) : 0x40000000u, sws, (u32)(rte * 2048 + pn3 * 256), WS_ST2);
    WAITV(0);
    RAWBAR();
    signal_cnt(ws, 16 + xcd * 2 + (pm3 >> 1));
    GST_SC1("x4", x1p, (u32)(erow * 8 + ecol) * 16, sws, (u32)bid * 8192, WS_X1);
  }

  const u32 dsb = (u32)(w * 16384 + pn3 * 128), dlb = lb + w * 2048;
  const int r5 = (pn3 * 7) >> 2;
#define CH5(c) (u32)((c) + r5 >= 14 ? (c) + r5 - 14 : (c) + r5)
#define WDPIECE(c, slot, h) GLDS(wvo, sd, dsb + CH5(c) * 131072, (h) * 8192, dlb, (slot) * 16384 + (h) * 1024)
#define WDISSUE(c, slot) do { WDPIECE(c, slot, 0); WDPIECE(c, slot, 1); } while (0)
  const int kq = w & 3;
  const u32 wr5 = wrd0 + kq * 4096;
  bf16x8 dq[14][4];
#define DFRAG1(c, i) dq[c][i] = lds_tr2(smem, (wr5 ^ (((i) & 1) * 64u)) + (c) * 16384 + ((i) >> 1) * 2048, (wr5 ^ (((i) & 1) * 64u)) + (c) * 16384 + ((i) >> 1) * 2048 + 1024)
  if (loc < 28) {
    constexpr u32 RST4 = 157696;
    UV_PART(16);
    wait_cnt(ws, 16 + pan4, 16, w);
    RAWBAR();
    const int rt4 = pan4 * 4;
    const u32 ysb = (u32)(rt4 * 32768 + w * 1024), ylb = lb + w * 1024;
#define YPIECE(i) GLDS_SC1(lo16, sws, ysb, WS_Y + (i) * 8192, ylb, (i) * 8192)
    __builtin_amdgcn_s_setprio(2);
    YPIECE(0); YPIECE(1);
    __builtin_amdgcn_s_setprio(1);
    YPIECE(2); YPIECE(3);
    GLDS_SC1(lo16, sws, (u32)(rt4 * 2048 + w * 1024), WS_ST2, ylb, 131072);
    __builtin_amdgcn_s_setprio(0);
    YPIECE(4); YPIECE(5);
    float uu[16], vv[16];
#pragma unroll
    for (int i = 0; i < 16; ++i) {
      const auto r = __builtin_amdgcn_permlane16_swap(__float_as_uint(uv[i]), __float_as_uint(uv[i]), false, false);
      uu[i] = __uint_as_float(r[0]); vv[i] = __uint_as_float(r[1]);
      PIN(uu[i]); PIN(vv[i]);
    }
    u32 yrd0 = lo16, yrd1 = lo16 + 65536;
    PIN(yrd1);
    const u32 asb = (u32)(rt4 * 112 + pn4 * 8 + w) * 1024;
#define YFRAG(n) lds_frag(smem, ((n) < 64 ? yrd0 : yrd1) + ((n) & 63) * 1024)
    WAITV(5);
    RAWBAR();
    bf16x8 yf[128];
#pragma unroll
    for (int n = 0; n < 4; ++n) yf[n] = YFRAG(n);
    SB();
    f32x16 acc = zero, accp = zero;
    f32x2 stp = {0.f, 0.f};
    float eg[8], eu[8], et[8];
    u32x4 ov;
    SFOR(n, 0, 160)
      constexpr int a = n >> 5, ks = n & 31;
      if (ks == 0 && a > 0) { accp = acc; acc = zero; }
      if constexpr (n < 128) { acc = MFMA32(wf[ks], yf[n], acc); SB(); }
      if (n == 10) { WAITV(7); RAWBAR(); }
      if (n == 26) { WAITV(8); BAR(); }
      if (n == 56) { WAITV(4); RAWBAR(); }
      if (n == 100) WAITV(3);
      if (n == 121) __builtin_amdgcn_s_setprio(1);
      if (n == 121) { WAITV(4); RAWBAR(); SIGNAL_ASM(48 + pan4, w); }
      if constexpr (n < 20 && n % 2 == 0) YPIECE(6 + n / 2);
      if (n == 36 || n == 68 || n == 100) RAWBAR();
      if constexpr (n >= 36 && n < 52 && n % 4 == 0) WDPIECE((n - 36) / 8, (n - 36) / 8, (n - 36) / 4 & 1);
      if constexpr (n >= 68 && n < 84 && n % 4 == 0) WDPIECE(2 + (n - 68) / 8, 2 + (n - 68) / 8, (n - 68) / 4 & 1);
      if constexpr (n == 84 || n == 88) WDPIECE(8, 8, (n - 84) / 4);
      if constexpr (n >= 100 && n < 116 && n % 4 == 0) WDPIECE(4 + (n - 100) / 8, 4 + (n - 100) / 8, (n - 100) / 4 & 1);
      if constexpr (n >= 100 && n < 116) DFRAG1((n - 100) >> 2, (n - 100) & 3);
      if constexpr (n == 122 || n == 125) WDPIECE(10, 0, n & 1);
      if constexpr (n == 12) {
        const LAS f32x2* sp = (const LAS f32x2*)((LAS char*)smem + 131072 + (t >> 7) * 2048 + (t & 3) * 512 + (t >> 2 & 31) * 8);
        const f32x2 p0 = sp[0], p1 = sp[32];
        float mu = p0[0] + p1[0];
        mu += dpp_xor1(mu); mu += dpp_xor2(mu);
        mu *= 0.125f;
        float q = p0[1] + p1[1] + 64.f * ((p0[0] - mu) * (p0[0] - mu) + (p1[0] - mu) * (p1[0] - mu));
        q += dpp_xor1(q); q += dpp_xor2(q);
        const float rs = __builtin_amdgcn_rsqf(q * (1.f / 512.f) + 1e-5f);
        ((LAS f32x2*)((LAS char*)smem + RST4))[t >> 2] = (f32x2){rs, -mu * rs};
      }
      if (ks == 28 && a < 4) stp = ((const LAS f32x2*)((LAS char*)smem + RST4))[a * 32 + (l & 31)];
      if constexpr (n + 4 < 128) yf[n + 4] = YFRAG(n + 4);
      if constexpr (a > 0 && ks >= 2 && ks < 26) {
        constexpr int i = (ks - 2) / 3, ph = (ks - 2) % 3;
        if (ph == 0) {
          eg[i] = __builtin_fmaf(accp[i], stp[0], __builtin_fmaf(uu[i], stp[1], vv[i]));
          eu[i] = __builtin_fmaf(accp[8 + i], stp[0], __builtin_fmaf(uu[8 + i], stp[1], vv[8 + i]));
          et[i] = -1.4426950408889634f * eg[i];
          if (a < 4) { PIN(eg[i]); PIN(eu[i]); PIN(et[i]); }
        } else if (ph == 1) {
          et[i] = 1.f + __builtin_amdgcn_exp2f(et[i]);
          eg[i] *= eu[i];
          if (a < 4) { PIN(eg[i]); PIN(et[i]); }
        } else {
          eg[i] *= __builtin_amdgcn_rcpf(et[i]);
          if (a < 4) PIN(eg[i]);
          if constexpr (i & 1) ov[i >> 1] = pack2(eg[i - 1], eg[i]);
        }
      }
      if constexpr (a > 0 && ks == 26) GST_SC1("x4", ov, lo16, sws, asb, WS_ACT + (a - 1) * 112 * 1024);
      if (n < 128) SB();
    SEND
    __builtin_amdgcn_s_setprio(0);
    WDISSUE(11, 1); WDISSUE(12, 2); WDISSUE(13, 3);
  } else {
    WDISSUE(0, 0); WDISSUE(1, 1); WDISSUE(2, 2); WDISSUE(3, 3); WDISSUE(8, 8); WDISSUE(4, 4); WDISSUE(5, 5);
    WAITV(0);
    RAWBAR();
    SFOR(n, 0, 16) DFRAG1(n >> 2, n & 3); SEND
    BAR();
    WDISSUE(10, 0); WDISSUE(11, 1); WDISSUE(12, 2); WDISSUE(13, 3);
  }

  {
    u32 wra[3][2];
#pragma unroll
    for (int i = 0; i < 3; ++i) { wra[i][0] = wr5 + i * 65536; wra[i][1] = wra[i][0] ^ 64u; PIN(wra[i][0]); PIN(wra[i][1]); }
#define DFRAG(c) _Pragma("unroll") for (int i_ = 0; i_ < 4; ++i_) {                                              \
      const u32 o_ = wra[((c) % 10) >> 2][i_ & 1] + (((c) % 10) & 3) * 16384 + (i_ >> 1) * 2048; dq[c][i_] = lds_tr2(smem, o_, o_ + 1024); }
    f32x4 gb[2];
#pragma unroll
    for (int e = 0; e < 2; ++e) gb[e] = *(const LAS f32x4*)((LAS char*)smem + MT + 512 + (l & 7) * 32 + e * 16);
    const int pan5 = xcd * 2 + (pm3 >> 1);
    wait_cnt(ws, 48 + pan5, 14, w);
    WAITV(0);
    BAR();
    if (loc < 28) signal_cnt(ws, 32 + pan4);
#define WD2(c, slot) do { WDISSUE(c, slot); WDPIECE(c, slot, -8); WDPIECE(c, slot, -7); } while (0)
#define BAR_IF(lo) asm volatile("s_cmp_lt_u32 %0, 4\n\ts_cbranch_scc" lo " 1f\n\ts_barrier\n1:" :: "s"(w) : "memory", "scc")
    if (w >= 4) {
      __builtin_amdgcn_s_setprio(1);
      WD2(6, 6); WD2(7, 7); WD2(9, 9);
      WAITV(0);
      RAWBAR();
      wait_cnt(ws, 32 + pan5, 14, 0);
    }
    const u32 asb = (u32)(rte * 112 + kq * 2) * 1024;
    u32x4 af[14][2];
#define AISSUE(c) do { GLD128_SC1(af[c][0], lo16, sws, asb + CH5(c) * 8192, WS_ACT); GLD128_SC1(af[c][1], lo16, sws, asb + CH5(c) * 8192, WS_ACT + 1024); } while (0)
    constexpr int AWIN = 8;
    SFOR(c, 0, AWIN) AISSUE(c); SEND
    f32x16 acc[2] = {zero, zero};
    u32x4 x1p;
    SFOR(c, 0, 14)
      constexpr int young = c + AWIN <= 14 ? 2 * (AWIN - 1) : (c < 13 ? 2 * (13 - c) + 1 : 0);
      asm volatile("s_waitcnt vmcnt(%0)" :: "i"(young) : "memory");
      if (c == 4) BAR_IF("0");
      if (c == 10) BAR_IF("1");
      if constexpr (c + AWIN < 14) AISSUE(c + AWIN);
      if (c + AWIN == 14) GLD128_SC1(x1p, (u32)t * 16, sws, (u32)bid * 8192, WS_X1);
      if constexpr (c >= 3 && c < 13) { DFRAG(c + 1); }
      PIN(af[c][0]); PIN(af[c][1]);
      SB();
#pragma unroll
      for (int i = 0; i < 4; ++i) acc[i & 1] = MFMA32(dq[c][i], __builtin_bit_cast(bf16x8, af[c][i >> 1]), acc[i & 1]);
      SB();
    SEND
    BAR_IF("0");
    {
      LAS char* eb = (LAS char*)smem + 65536 + kq * 17408 + ((w >> 2) * 32 + (l & 31)) * 272 + hb * 16;
#pragma unroll
      for (int tn = 0; tn < 2; ++tn)
#pragma unroll
        for (int e = 0; e < 4; ++e)
          *(LAS f32x4*)(eb + tn * 128 + e * 32) = (f32x4){acc[tn][4 * e], acc[tn][4 * e + 1], acc[tn][4 * e + 2], acc[tn][4 * e + 3]};
    }
    BAR();
    PIN(x1p);
    u32x4 ov;
#pragma unroll
    for (int e = 0; e < 2; ++e) {
      const LAS char* eb = (const LAS char*)smem + 65536 + (t >> 3) * 272 + (l & 7) * 32 + e * 16;
      const f32x4 s = (*(const LAS f32x4*)eb + *(const LAS f32x4*)(eb + 17408)) + (*(const LAS f32x4*)(eb + 2 * 17408) + *(const LAS f32x4*)(eb + 3 * 17408));
      ov[2 * e] = pack2(bf_lo(x1p[2 * e]) + gb[e][0] * s[0], bf_hi(x1p[2 * e]) + gb[e][1] * s[1]);
      ov[2 * e + 1] = pack2(bf_lo(x1p[2 * e + 1]) + gb[e][2] * s[2], bf_hi(x1p[2 * e + 1]) + gb[e][3] * s[3]);
    }
    const u32x4 sout = make_srd(out, 2048 * 1024);
    GST_SC1("x4", ov, (u32)((t >> 3 & 31) * 1024 + (l & 7) * 16), sout, (u32)(rte * 32768 + pn3 * 128), 0);
  }
  asm volatile(".Lcode_end:");
}

extern "C" void kernel_launch(void* const* d_in, const int* in_sizes, int n_in, void* d_out, int out_size, void* d_ws, size_t ws_size,
                              hipStream_t stream) {
  adaln_block<<<256, 512, 0, stream>>>((const u16*)d_in[0], (const u16*)d_in[1], (const u16*)d_in[2], (const u16*)d_in[4], (const u16*)d_in[5],
                                       (const u16*)d_in[6], (const u16*)d_in[7], (const u16*)d_in[8], (char*)d_ws, (u16*)d_out);
}
```
